```python
import torch

def _softmax(x, axis=-1):
    return torch.softmax(x, dim=axis)
def _gelu(x):
    return torch.nn.functional.gelu(x, approximate="tanh")
def _silu(x):
    return torch.nn.functional.silu(x)
def _sigmoid(x):
    return torch.sigmoid(x)
def _layernorm(x, axis=-1, eps=1e-5):
    m = x.mean(dim=axis, keepdim=True)
    v = x.var(dim=axis, keepdim=True, unbiased=False)
    return (x - m) / torch.sqrt(v + eps)
def _scatter_add(tgt, idx, src, axis=0):
    return tgt.clone().index_add_(axis, idx, src)
def _segment_sum(src, seg, n):
    out = torch.zeros((n,) + src.shape[1:], dtype=src.dtype)
    return out.index_add_(0, seg, src)
def _pad(x, pw):
    flat = tuple(v for pair in reversed(pw) for v in pair)
    return torch.nn.functional.pad(x, flat)

kernel_name = 'recurrent_scan__gqa_block_cached__mixer_channel_mlp__decode'

def setup_inputs():

    _w = (lambda a: (lambda t: t.to(torch.bfloat16) if t.is_floating_point() else t)(torch.as_tensor(a)))
    def _A_setup():
        B, Sq, D = 2, 1, 512
        Hq, Hkv, Sk, Dh = 32, 8, 8192, 128
        assert Hq % Hkv == 0, (Hq, Hkv)
        return {
            "x": _w((lambda *s: torch.randn(*s, dtype=torch.bfloat16) * 0.1)(B, Sq, D)),
            "k_cache": _w((lambda *s: torch.randn(*s, dtype=torch.bfloat16) * 0.8)(B, Hkv, Sk, Dh)),
            "v_cache": _w((lambda *s: torch.randn(*s, dtype=torch.bfloat16) * 3.2)(B, Hkv, Sk, Dh)),
            "wq": _w((lambda *s: torch.randn(*s, dtype=torch.bfloat16) * 0.8)(D, Hq * Dh) * (D ** -0.5)),
            "wo": _w((lambda *s: torch.randn(*s, dtype=torch.bfloat16) * 3.2)(Hq * Dh, D) * ((Hq * Dh) ** -0.5)),
        }
    _A_in = _A_setup()

    def _B_setup():
        B, S, D, F = 2, 1, 512, 1792
        return {
            "x": _w((lambda *s: torch.randn(*s, dtype=torch.bfloat16) * 0.1)(B, S, D)),
            "w1": _w((lambda *s: torch.randn(*s, dtype=torch.bfloat16) * 3.2)(D, F) * (D ** -0.5)),
            "b1": _w((lambda *s: torch.randn(*s, dtype=torch.bfloat16) * 3.2)(F)),
            "w2": _w((lambda *s: torch.randn(*s, dtype=torch.bfloat16) * 3.2)(F, D) * (F ** -0.5)),
            "b2": _w((lambda *s: torch.randn(*s, dtype=torch.bfloat16) * 3.2)(D)),
        }
    _B_in = _B_setup()

    return {
        "A_x": _A_in["x"],
        "A_k_cache": _A_in["k_cache"],
        "A_v_cache": _A_in["v_cache"],
        "A_wq": _A_in["wq"],
        "A_wo": _A_in["wo"],
        "B_x": _B_in["x"],
        "B_w1": _B_in["w1"],
        "B_b1": _B_in["b1"],
        "B_w2": _B_in["w2"],
        "B_b2": _B_in["b2"],
    }

def reference(A_x, A_k_cache, A_v_cache, A_wq, A_wo, B_x, B_w1, B_b1, B_w2, B_b2):
    def _A_ref(x, k_cache, v_cache, wq, wo):
        B, Sq, D = x.shape
        Hq, Dh = 32, 128
        Hkv = k_cache.shape[1]
        rep = Hq // Hkv
        q = (lambda x, *d: x.permute(*d))((x @ wq).reshape(B, Sq, Hq, Dh), 0, 2, 1, 3)
        k = (lambda x, n, axis: torch.repeat_interleave(x, n, dim=axis))(k_cache, rep, axis=1)
        v = (lambda x, n, axis: torch.repeat_interleave(x, n, dim=axis))(v_cache, rep, axis=1)
        scores = torch.einsum("bhqd,bhkd->bhqk", q, k) * (Dh ** -0.5)
        p = _softmax(scores, axis=-1)
        o = torch.einsum("bhqk,bhkd->bhqd", p, v)
        return (lambda x, *d: x.permute(*d))(o, 0, 2, 1, 3).reshape(B, Sq, Hq * Dh) @ wo

    def _B_ref(x, w1, b1, w2, b2):
        y = _layernorm(x, axis=-1)
        h = _gelu(torch.einsum("bsd,df->bsf", y, w1) + b1)
        return torch.einsum("bsf,fd->bsd", h, w2) + b2

    x = A_x
    xn = _layernorm(x, axis=-1)
    s = _A_ref(xn, A_k_cache, A_v_cache, A_wq, A_wo)
    g = _silu(_B_ref(xn, B_w1, B_b1, B_w2, B_b2))
    return x + s * g
```

```cpp
#include <hip/hip_runtime.h>
#include <hip/hip_bf16.h>
#include <stdint.h>

typedef unsigned short u16;
typedef unsigned u32x4 __attribute__((ext_vector_type(4)));
__device__ __forceinline__ float bf2f(unsigned b) { return __builtin_bit_cast(float, b << 16); }
__device__ __forceinline__ float rbf(float f) { return (float)(__bf16)f; }
__device__ __forceinline__ u16 f2bf(float f) { return __builtin_bit_cast(u16, (__bf16)f); }

constexpr int D = 512, HQ = 32, HKV = 8, SK = 8192, DH = 128, F = 1792, NSPLIT = 16;

__device__ void ln512(const float* in, float* out, float* red, int tid) {
  float a = in[tid], b = in[tid + 256];
  float s = a + b;
  for (int o = 32; o; o >>= 1) s += __shfl_xor(s, o);
  if ((tid & 63) == 0) red[tid >> 6] = s;
  __syncthreads();
  float mean = (red[0] + red[1] + red[2] + red[3]) * (1.f / 512.f);
  __syncthreads();
  float v = (a - mean) * (a - mean) + (b - mean) * (b - mean);
  for (int o = 32; o; o >>= 1) v += __shfl_xor(v, o);
  if ((tid & 63) == 0) red[tid >> 6] = v;
  __syncthreads();
  float var = (red[0] + red[1] + red[2] + red[3]) * (1.f / 512.f);
  __syncthreads();
  float mb = rbf(mean), den = rbf(sqrtf(rbf(rbf(var) + 1e-5f)));
  out[tid] = rbf(rbf(a - mb) / den);
  out[tid + 256] = rbf(rbf(b - mb) / den);
  __syncthreads();
}

__global__ void __launch_bounds__(256) k1(const u16* x, const u16* wq, const u16* w1, const u16* b1, float* q, float* h) {
  __shared__ float xs[2][512], xn[2][512], y[2][512], red[4], part[4][2][64];
  int tid = threadIdx.x;
  for (int i = tid; i < 1024; i += 256) xs[i >> 9][i & 511] = bf2f(x[i]);
  __syncthreads();
  ln512(xs[0], xn[0], red, tid); ln512(xs[1], xn[1], red, tid);
  ln512(xn[0], y[0], red, tid); ln512(xn[1], y[1], red, tid);
  int blk = blockIdx.x, c = tid & 63, w = tid >> 6;
  bool isq = blk < 64;
  const u16* W = isq ? wq + blk * 64 : w1 + (blk - 64) * 64;
  int ld = isq ? 4096 : F;
  float (*in)[512] = isq ? xn : y;
  float a0 = 0, a1 = 0;
  for (int k = w * 128; k < w * 128 + 128; ++k) { float wv = bf2f(W[(size_t)k * ld + c]); a0 += in[0][k] * wv; a1 += in[1][k] * wv; }
  part[w][0][c] = a0; part[w][1][c] = a1;
  __syncthreads();
  if (tid < 128) {
    int b = tid >> 6;
    float s = part[0][b][c] + part[1][b][c] + part[2][b][c] + part[3][b][c];
    if (isq) q[b * 4096 + blk * 64 + c] = rbf(s);
    else {
      int col = (blk - 64) * 64 + c;
      float t = rbf(rbf(s) + bf2f(b1[col]));
      float g = 0.5f * t * (1.f + tanhf(0.7978845608028654f * (t + 0.044715f * t * t * t)));
      h[b * F + col] = rbf(g);
    }
  }
}

__global__ void __launch_bounds__(256) k2(const u16* K, const u16* V, const float* q, float* part) {
  __shared__ float qs[4][128], sc[4][512], red[4][4], osum[8][4][128];
  int tid = threadIdx.x, sp = blockIdx.x, kvh = blockIdx.y, b = blockIdx.z;
  for (int i = tid; i < 512; i += 256) qs[i >> 7][i & 127] = q[b * 4096 + kvh * 512 + i];
  __syncthreads();
  const u16* Kb = K + ((size_t)(b * HKV + kvh) * SK + sp * 512) * DH;
  const u16* Vb = V + ((size_t)(b * HKV + kvh) * SK + sp * 512) * DH;
  int c = tid & 15, r = tid >> 4;
  for (int it = 0; it < 32; ++it) {
    int key = it * 16 + r;
    u32x4 kv = *(const u32x4*)(Kb + (size_t)key * DH + c * 8);
    float a[4] = {0, 0, 0, 0};
    for (int j = 0; j < 4; ++j) {
      float lo = bf2f(kv[j] & 0xffff), hi = bf2f(kv[j] >> 16);
      for (int hh = 0; hh < 4; ++hh) a[hh] += lo * qs[hh][c * 8 + 2 * j] + hi * qs[hh][c * 8 + 2 * j + 1];
    }
    for (int hh = 0; hh < 4; ++hh) { for (int o = 8; o; o >>= 1) a[hh] += __shfl_xor(a[hh], o); if (c == 0) sc[hh][key] = a[hh] * 0.08838834764831845f; }
  }
  __syncthreads();
  int w = tid >> 6, l = tid & 63;
  float m = -INFINITY;
  for (int i = l; i < 512; i += 64) m = fmaxf(m, sc[w][i]);
  for (int o = 32; o; o >>= 1) m = fmaxf(m, __shfl_xor(m, o));
  float s = 0;
  for (int i = l; i < 512; i += 64) { float p = __expf(sc[w][i] - m); sc[w][i] = p; s += p; }
  for (int o = 32; o; o >>= 1) s += __shfl_xor(s, o);
  if (l == 0) { red[w][0] = m; red[w][1] = s; }
  __syncthreads();
  float acc[4][8] = {};
  for (int it = 0; it < 32; ++it) {
    int key = it * 16 + r;
    u32x4 vv = *(const u32x4*)(Vb + (size_t)key * DH + c * 8);
    for (int hh = 0; hh < 4; ++hh) { float p = sc[hh][key];
      for (int j = 0; j < 4; ++j) { acc[hh][2 * j] += p * bf2f(vv[j] & 0xffff); acc[hh][2 * j + 1] += p * bf2f(vv[j] >> 16); } }
  }
  for (int hh = 0; hh < 4; ++hh) for (int j = 0; j < 8; ++j) { float t = acc[hh][j]; t += __shfl_xor(t, 16); t += __shfl_xor(t, 32); acc[hh][j] = t; }
  if (l < 16) for (int hh = 0; hh < 4; ++hh) for (int j = 0; j < 8; ++j) osum[w][hh][c * 8 + j] = acc[hh][j];
  __syncthreads();
  float* P = part + (size_t)(((b * HKV + kvh) * NSPLIT + sp) * 4) * 130;
  for (int i = tid; i < 512; i += 256) { int hh = i >> 7, d = i & 127; P[hh * 130 + d] = osum[0][hh][d] + osum[1][hh][d] + osum[2][hh][d] + osum[3][hh][d]; }
  if (tid < 4) { P[tid * 130 + 128] = red[tid][0]; P[tid * 130 + 129] = red[tid][1]; }
}

__global__ void __launch_bounds__(256) k3(const float* part, const float* h, const u16* wo, const u16* w2, float* sp) {
  __shared__ float in[2][128];
  int tid = threadIdx.x, blk = blockIdx.x;
  bool iso = blk < 32;
  int nk = iso ? 128 : 64;
  if (iso) {
    int b = tid >> 7, d = tid & 127, kvh = blk >> 2, hh = blk & 3;
    const float* P = part + (size_t)((b * HKV + kvh) * NSPLIT * 4 + hh) * 130;
    float M = -INFINITY;
    for (int s = 0; s < NSPLIT; ++s) M = fmaxf(M, P[s * 520 + 128]);
    float L = 0, o = 0;
    for (int s = 0; s < NSPLIT; ++s) { float wgt = __expf(P[s * 520 + 128] - M); L += wgt * P[s * 520 + 129]; o += wgt * P[s * 520 + d]; }
    in[b][d] = rbf(o / L);
  } else if (tid < 128) { int b = tid >> 6, d = tid & 63; in[b][d] = h[b * F + (blk - 32) * 64 + d]; }
  __syncthreads();
  const u16* W = iso ? wo + (size_t)blk * 128 * 512 : w2 + (size_t)(blk - 32) * 64 * 512;
  float a[2][2] = {};
  for (int k = 0; k < nk; ++k) {
    unsigned wv = *(const unsigned*)(W + (size_t)k * 512 + tid * 2);
    float lo = bf2f(wv & 0xffff), hi = bf2f(wv >> 16);
    a[0][0] += in[0][k] * lo; a[0][1] += in[0][k] * hi; a[1][0] += in[1][k] * lo; a[1][1] += in[1][k] * hi;
  }
  for (int b = 0; b < 2; ++b) for (int j = 0; j < 2; ++j) sp[(size_t)blk * 1024 + b * 512 + tid * 2 + j] = a[b][j];
}

__global__ void __launch_bounds__(256) k4(const float* sp, const u16* x, const u16* b2, u16* out) {
  int i = blockIdx.x * 256 + threadIdx.x;
  float s = 0, m = 0;
  for (int k = 0; k < 32; ++k) s += sp[k * 1024 + i];
  for (int k = 32; k < 60; ++k) m += sp[k * 1024 + i];
  s = rbf(s);
  float t = rbf(rbf(m) + bf2f(b2[i & 511]));
  float g = rbf(t / (1.f + __expf(-t)));
  out[i] = f2bf(bf2f(x[i]) + rbf(s * g));
}

extern "C" void kernel_launch(void* const* d_in, const int* in_sizes, int n_in, void* d_out, int out_size, void* d_ws, size_t ws_size, hipStream_t stream) {
  const u16* x = (const u16*)d_in[0]; const u16* K = (const u16*)d_in[1]; const u16* V = (const u16*)d_in[2];
  const u16* wq = (const u16*)d_in[3]; const u16* wo = (const u16*)d_in[4];
  const u16* w1 = (const u16*)d_in[6]; const u16* b1 = (const u16*)d_in[7]; const u16* w2 = (const u16*)d_in[8]; const u16* b2 = (const u16*)d_in[9];
  float* ws = (float*)d_ws;
  float* q = ws; float* h = ws + 8192; float* part = ws + 16384; float* sp = part + 256 * 520;
  k1<<<92, 256, 0, stream>>>(x, wq, w1, b1, q, h);
  k2<<<dim3(NSPLIT, HKV, 2), 256, 0, stream>>>(K, V, q, part);
  k3<<<60, 256, 0, stream>>>(part, h, wo, w2, sp);
  k4<<<4, 256, 0, stream>>>(sp, x, b2, (u16*)d_out);
}
```

```cpp
#include <hip/hip_runtime.h>
#include <stdint.h>

#define K_NT 0
#define V_NT 1
#define STAMP 0

#define GAS __attribute__((address_space(1)))
#define LAS __attribute__((address_space(3)))
#define DI __device__ __forceinline__
#define RLX_AGENT __ATOMIC_RELAXED, __HIP_MEMORY_SCOPE_AGENT
typedef unsigned short u16;
typedef unsigned u32;
typedef unsigned long long u64;
typedef u32 u32x2 __attribute__((ext_vector_type(2)));
typedef u32 u32x4 __attribute__((ext_vector_type(4)));
typedef float f32x2 __attribute__((ext_vector_type(2)));
typedef float f32x4 __attribute__((ext_vector_type(4)));
typedef __bf16 bf16x2 __attribute__((ext_vector_type(2)));
typedef __bf16 bf16x8 __attribute__((ext_vector_type(8)));

constexpr int D = 512, SK = 8192, DH = 128, FF = 1792, NQ = 4096;
constexpr u32 SENT = 0xFFFFFFFFu;
constexpr int SC1 = 16;
constexpr int SPIN_CAP = 1 << 18;

constexpr int WS_EPOCH = 0;
constexpr int WS_QG = 256;
constexpr int WS_ML = WS_QG + 32768;
constexpr int WS_OP = WS_ML + 8192;
constexpr int WS_S2 = WS_OP + 256 * 2048;
constexpr int WS_MP = WS_S2 + 256 * 1024;
constexpr int WS_END = WS_MP + 256 * 1024;
constexpr int WS_STAMP = 4 << 20;

constexpr int L_FLAG = 0, L_XN = 16, L_Y = L_XN + 4096, L_Q = L_Y + 2048, L_MW = L_Q + 1024, L_LW = L_MW + 256, L_HP = L_LW + 256,
              L_HS = L_HP + 4096, L_OW = L_HS + 128, OW_STRIDE = 2064, L_END = L_OW + 16 * OW_STRIDE;

DI float bflo(u32 v) { return __builtin_bit_cast(float, v << 16); }
DI float bfhi(u32 v) { return __builtin_bit_cast(float, v & 0xffff0000u); }
DI float rbf(float f) { return (float)(__bf16)f; }
DI u32 pk(float a, float b) { return __builtin_bit_cast(u32, __builtin_convertvector((f32x2){a, b}, bf16x2)); }
DI u32 f2u(float f) { return __builtin_bit_cast(u32, f); }
DI float u2f(u32 u) { return __builtin_bit_cast(float, u); }
DI u32 pub(float f) { return min(f2u(f), SENT - 1u); }
DI u32x4 pub4(f32x4 v) { return (u32x4){pub(v[0]), pub(v[1]), pub(v[2]), pub(v[3])}; }
DI bool ok4(u32x4 v) { return v[0] != SENT && v[1] != SENT && v[2] != SENT && v[3] != SENT; }
DI f32x4 mfma(u32x4 a, u32x4 b, f32x4 c) {
  return __builtin_amdgcn_mfma_f32_16x16x32_bf16(__builtin_bit_cast(bf16x8, a), __builtin_bit_cast(bf16x8, b), c, 0, 0, 0);
}
template <int CTRL> DI float dpp(float x) { return u2f(__builtin_amdgcn_mov_dpp(f2u(x), CTRL, 0xf, 0xf, true)); }
constexpr int XOR1 = 0xB1, XOR2 = 0x4E, ROR1 = 0x121, ROR2 = 0x122, ROR4 = 0x124, ROR8 = 0x128;
DI float half32(float a, float b) { auto r = __builtin_amdgcn_permlane32_swap(f2u(a), f2u(b), false, false); return u2f(r[0]) + u2f(r[1]); }
DI float half16(float a, float b) { auto r = __builtin_amdgcn_permlane16_swap(f2u(a), f2u(b), false, false); return u2f(r[0]) + u2f(r[1]); }
DI float max32(float a) { auto r = __builtin_amdgcn_permlane32_swap(f2u(a), f2u(a), false, false); return fmaxf(u2f(r[0]), u2f(r[1])); }
DI float max16(float a) { auto r = __builtin_amdgcn_permlane16_swap(f2u(a), f2u(a), false, false); return fmaxf(u2f(r[0]), u2f(r[1])); }
DI float wave_sum(float v) {
  v += dpp<XOR1>(v); v += dpp<XOR2>(v); v += dpp<ROR4>(v); v += dpp<ROR8>(v);
  v = half16(v, v); return half32(v, v);
}
DI u32 plo(u32 k1, u32 k0) { return __builtin_amdgcn_perm(k1, k0, 0x05040100u); }
DI u32 phi(u32 k1, u32 k0) { return __builtin_amdgcn_perm(k1, k0, 0x07060302u); }
DI u32x4 sel4(bool c, u32x4 v) { return (u32x4){c ? v[0] : 0u, c ? v[1] : 0u, c ? v[2] : 0u, c ? v[3] : 0u}; }
template <int NT> DI u32x4 ldg(const GAS u32x4* p) { if (NT) return __builtin_nontemporal_load(p); else return *p; }
#define PIN() do { asm volatile("" ::: "memory"); __builtin_amdgcn_sched_barrier(0); } while (0)
#define LDS_BARRIER() do { asm volatile("s_waitcnt lgkmcnt(0)\n\ts_barrier" ::: "memory"); __builtin_amdgcn_sched_barrier(0); } while (0)

template <int N> struct IC { static constexpr int value = N; };

DI void layernorm8(const float (&v)[8], float (&o)[8]) {
  float s = 0.f;
#pragma unroll
  for (int e = 0; e < 8; ++e) s += v[e];
  const float mean = wave_sum(s) * (1.f / 512.f);
  float q = 0.f;
#pragma unroll
  for (int e = 0; e < 8; ++e) { const float d = v[e] - mean; q = fmaf(d, d, q); }
  const float var = wave_sum(q) * (1.f / 512.f);
  const float mb = rbf(mean), inv = 1.f / rbf(sqrtf(rbf(rbf(var) + 1e-5f)));
#pragma unroll
  for (int e = 0; e < 8; ++e) o[e] = rbf(rbf(v[e] - mb) * inv);
}

#define ST(i)

__global__ void __launch_bounds__(1024, 4)
fused_decode(const u16* x_, const u16* k_, const u16* v_, const u16* wq_, const u16* wo_, const u16* w1_, const u16* b1_,
             const u16* w2_, const u16* b2_, u16* out_, unsigned char* ws_) {
  __shared__ __attribute__((aligned(16))) unsigned char lds_raw[L_END];
  LAS unsigned char* lds = (LAS unsigned char*)lds_raw;
  ST(0);
  const int tid = threadIdx.x, lane = tid & 63, w = __builtin_amdgcn_readfirstlane(tid >> 6);
  const int g = lane >> 4, c = lane & 15;
  const int bid = blockIdx.x, kvh = bid & 7, j = bid >> 3, b = j >> 4, sp = j & 15, rb = j >> 2, cb = j & 3;
  const int prod = cb * 64 + kvh * 8 + rb;
  const __amdgpu_buffer_rsrc_t ws = __builtin_amdgcn_make_buffer_rsrc(ws_, 0, WS_END, 0x00020000);
  u32 tag = 0;

  if (w == 0) {
    *(LAS u32*)(lds + L_FLAG) = 0u;
    const u32 ep = __hip_atomic_load((GAS u32*)(ws_ + WS_EPOCH), RLX_AGENT);
    const u32x4 x0 = ((const GAS u32x4*)x_)[lane], x1 = ((const GAS u32x4*)x_)[64 + lane];
    PIN();
    u32x4 wqv[16];
    const GAS u32x4* wqp = (const GAS u32x4*)(wq_ + (size_t)(lane >> 1) * NQ + kvh * 512 + j * 16 + (lane & 1) * 8);
#pragma unroll
    for (int i = 0; i < 16; ++i) wqv[i] = wqp[(size_t)i * 32 * (NQ / 8)];
    const u32x4 sent = {SENT, SENT, SENT, SENT};
    const int opoff = WS_OP + ((kvh * 2 + b) * 16 + sp) * 2048 + lane * 16;
    __builtin_amdgcn_raw_buffer_store_b128(sent, ws, opoff, 0, SC1);
    __builtin_amdgcn_raw_buffer_store_b128(sent, ws, opoff + 1024, 0, SC1);
    __builtin_amdgcn_raw_buffer_store_b128(sent, ws, WS_S2 + prod * 1024 + lane * 16, 0, SC1);
    __builtin_amdgcn_raw_buffer_store_b128(sent, ws, WS_MP + prod * 1024 + lane * 16, 0, SC1);
    if (lane < 4) __builtin_amdgcn_raw_buffer_store_b64((u32x2){SENT, SENT}, ws, WS_ML + (((kvh * 4 + lane) * 2 + b) * 16 + sp) * 8, 0, SC1);
    PIN();
    LDS_BARRIER();
    ST(1);
    float xa[8], xb[8], na[8], nb[8];
#pragma unroll
    for (int e = 0; e < 4; ++e) { xa[2 * e] = bflo(x0[e]); xa[2 * e + 1] = bfhi(x0[e]); xb[2 * e] = bflo(x1[e]); xb[2 * e + 1] = bfhi(x1[e]); }
    layernorm8(xa, na); layernorm8(xb, nb);
#pragma unroll
    for (int e = 0; e < 8; e += 2) *(LAS f32x4*)(lds + L_XN + (lane * 8 + e) * 8) = (f32x4){na[e], nb[e], na[e + 1], nb[e + 1]};
    f32x2 acc[8];
#pragma unroll
    for (int e = 0; e < 8; ++e) acc[e] = (f32x2){0.f, 0.f};
#pragma unroll
    for (int i = 0; i < 16; ++i) {
      const f32x2 xv = *(LAS f32x2*)(lds + L_XN + (i * 32 + (lane >> 1)) * 8);
#pragma unroll
      for (int e = 0; e < 4; ++e) {
        const float lo = bflo(wqv[i][e]), hi = bfhi(wqv[i][e]);
        acc[2 * e] = __builtin_elementwise_fma(xv, (f32x2){lo, lo}, acc[2 * e]);
        acc[2 * e + 1] = __builtin_elementwise_fma(xv, (f32x2){hi, hi}, acc[2 * e + 1]);
      }
    }
    float r8[8], r4[4], r2[2];
#pragma unroll
    for (int e = 0; e < 8; ++e) r8[e] = half32(acc[e][0], acc[e][1]);
#pragma unroll
    for (int e = 0; e < 4; ++e) r4[e] = half16(r8[e], r8[e + 4]);
#pragma unroll
    for (int e = 0; e < 2; ++e) { const float t0 = r4[e] + dpp<XOR2>(r4[e]), t1 = r4[e + 2] + dpp<XOR2>(r4[e + 2]); r2[e] = (lane & 2) ? t1 : t0; }
#pragma unroll
    for (int e = 0; e < 2; ++e) { r2[e] += dpp<ROR4>(r2[e]); r2[e] += dpp<ROR8>(r2[e]); }
    tag = __builtin_amdgcn_readfirstlane(ep) + 1u;
    asm volatile("s_waitcnt vmcnt(0)" ::: "memory");
    if ((lane & 12) == 0) {
      const int bq = lane >> 5, e = (lane & 1) * 4 + ((lane >> 4) & 1) * 2 + ((lane >> 1) & 1);
      __hip_atomic_store((GAS u64*)(ws_ + WS_QG) + ((bq * 8 + kvh) * 32 + j) * 8 + e, ((u64)tag << 32) | pk(r2[0], r2[1]), RLX_AGENT);
    }
    ST(2);
    float ya[8], yb[8];
    layernorm8(na, ya); layernorm8(nb, yb);
    *(LAS u32x4*)(lds + L_Y + lane * 16) = (u32x4){pk(ya[0], ya[1]), pk(ya[2], ya[3]), pk(ya[4], ya[5]), pk(ya[6], ya[7])};
    *(LAS u32x4*)(lds + L_Y + 1024 + lane * 16) = (u32x4){pk(yb[0], yb[1]), pk(yb[2], yb[3]), pk(yb[4], yb[5]), pk(yb[6], yb[7])};
    u32x4 g0, g1;
    const int qoff = WS_QG + (b * 8 + kvh) * 2048 + lane * 16;
#pragma unroll 1
    for (int spin = 0; spin < SPIN_CAP; ++spin) {
      g0 = __builtin_amdgcn_raw_buffer_load_b128(ws, qoff, 0, SC1);
      g1 = __builtin_amdgcn_raw_buffer_load_b128(ws, qoff + 1024, 0, SC1);
      if (__all(g0[1] == tag && g0[3] == tag && g1[1] == tag && g1[3] == tag)) break;
      asm volatile("" ::: "memory");
    }
    *(LAS u32x2*)(lds + L_Q + lane * 8) = (u32x2){g0[0], g0[2]};
    *(LAS u32x2*)(lds + L_Q + 512 + lane * 8) = (u32x2){g1[0], g1[2]};
    asm volatile("s_waitcnt lgkmcnt(0)" ::: "memory");
    *(volatile LAS u32*)(lds + L_FLAG) = 1u;
    ST(3);
  } else {
    LDS_BARRIER();
  }
  PIN();

  u32 w1v[8];
  {
    const GAS u32* p = (const GAS u32*)(w1_ + (size_t)(w * 32 + g * 8) * FF + (kvh * 8 + rb) * 28 + min(c, 13) * 2);
#pragma unroll
    for (int i = 0; i < 8; ++i) w1v[i] = p[i * (FF / 2)];
  }
  const size_t kvbase = ((size_t)(b * 8 + kvh) * SK + sp * 512 + w * 32) * (DH / 8);
  u32x4 kf[8], vf[8];
  {
    const GAS u32x4* kp = (const GAS u32x4*)k_ + kvbase + (4 * (c & 3) + (c >> 2)) * 16 + g;
#pragma unroll
    for (int i = 0; i < 8; ++i) { kf[i] = ldg<K_NT>(kp + (i >> 2) * 256 + (i & 3) * 4); __builtin_amdgcn_sched_barrier(0); }
    const GAS u32x4* vp = (const GAS u32x4*)v_ + kvbase + lane;
#pragma unroll
    for (int i = 0; i < 8; ++i) { vf[i] = ldg<V_NT>(vp + i * 64); __builtin_amdgcn_sched_barrier(0); }
  }
  PIN();
  ST(4);
  if (w != 0) {
#pragma unroll 1
    for (int spin = 0; spin < (SPIN_CAP << 4); ++spin) {
      if (__builtin_amdgcn_readfirstlane(*(volatile LAS u32*)(lds + L_FLAG)) != 0u) break;
      __builtin_amdgcn_s_sleep(2);
    }
  }
  PIN();
  ST(5);

  {
    const u32x4 ya = *(LAS u32x4*)(lds + L_Y + (c & 1) * 1024 + (w * 32 + g * 8) * 2);
    const f32x4 z = {0.f, 0.f, 0.f, 0.f};
    const f32x4 d0 = mfma(ya, (u32x4){plo(w1v[1], w1v[0]), plo(w1v[3], w1v[2]), plo(w1v[5], w1v[4]), plo(w1v[7], w1v[6])}, z);
    const f32x4 d1 = mfma(ya, (u32x4){phi(w1v[1], w1v[0]), phi(w1v[3], w1v[2]), phi(w1v[5], w1v[4]), phi(w1v[7], w1v[6])}, z);
    if (g == 0) {
      *(LAS f32x2*)(lds + L_HP + (w * 64 + 2 * c) * 4) = (f32x2){d0[0], d1[0]};
      *(LAS f32x2*)(lds + L_HP + (w * 64 + 32 + 2 * c) * 4) = (f32x2){d0[1], d1[1]};
    }
  }
  f32x4 sa = {0.f, 0.f, 0.f, 0.f}, sb = sa;
#pragma unroll
  for (int s = 0; s < 4; ++s) {
    const u32x4 qf = *(LAS u32x4*)(lds + L_Q + (c & 3) * 256 + s * 64 + g * 16);
    sa = mfma(kf[s], qf, sa);
    sb = mfma(kf[4 + s], qf, sb);
  }
  constexpr float C2 = 0.08838834764831845f * 1.4426950408889634f;
  sa *= C2; sb *= C2;
  float m = fmaxf(fmaxf(fmaxf(sa[0], sa[1]), fmaxf(sa[2], sa[3])), fmaxf(fmaxf(sb[0], sb[1]), fmaxf(sb[2], sb[3])));
  m = max32(max16(m));
  if (lane < 4) *(LAS float*)(lds + L_MW + (w * 4 + lane) * 4) = m;
  PIN();
  auto rest = [&](auto role) __attribute__((always_inline)) {
  constexpr int ROLE = decltype(role)::value;
  u32x4 wt[8];
  u32 b1v = 0;
  if (ROLE == 1) {
    const int r0 = (kvh * 8 + rb) * 28;
#pragma unroll
    for (int i = 0; i < 8; ++i) wt[i] = *(const GAS u32x4*)(w2_ + (size_t)(r0 + min(g * 8 + i, 27)) * D + cb * 128 + c * 8);
    b1v = ((const GAS u16*)b1_)[r0 + min(lane & 31, 27)];
  } else if (ROLE == 2) {
    const GAS u16* p = (const GAS u16*)wo_ + (size_t)(kvh * 512 + rb * 64 + g * 4) * D + cb * 128 + (w - 14) * 64 + c * 4;
#pragma unroll
    for (int i = 0; i < 8; ++i) {
      const u32x2 t0 = *(const GAS u32x2*)(p + (size_t)(32 * (i >> 2) + (i & 3)) * D);
      const u32x2 t1 = *(const GAS u32x2*)(p + (size_t)(32 * (i >> 2) + 16 + (i & 3)) * D);
      wt[i] = (u32x4){t0[0], t0[1], t1[0], t1[1]};
    }
  }
  PIN();
  ST(6);
  LDS_BARRIER();
  ST(7);
  float M = *(LAS float*)(lds + L_MW + lane * 4);
  M = fmaxf(M, dpp<ROR4>(M)); M = fmaxf(M, dpp<ROR8>(M)); M = max32(max16(M));
  float pa[4], pb[4], l = 0.f;
#pragma unroll
  for (int e = 0; e < 4; ++e) { pa[e] = __builtin_amdgcn_exp2f(sa[e] - M); pb[e] = __builtin_amdgcn_exp2f(sb[e] - M); l += pa[e] + pb[e]; }
  l = half16(l, l); l = half32(l, l);
  if (lane < 4) *(LAS float*)(lds + L_LW + (w * 4 + lane) * 4) = l;
  const u32x4 pf = {pk(pa[0], pa[1]), pk(pa[2], pa[3]), pk(pb[0], pb[1]), pk(pb[2], pb[3])};
  f32x4 oe = {0.f, 0.f, 0.f, 0.f}, oo = oe;
#pragma unroll
  for (int u = 0; u < 4; ++u) {
    const u32x4 a = sel4((c >> 2) == u, pf);
    oe = mfma(a, (u32x4){plo(vf[1][u], vf[0][u]), plo(vf[3][u], vf[2][u]), plo(vf[5][u], vf[4][u]), plo(vf[7][u], vf[6][u])}, oe);
    oo = mfma(a, (u32x4){phi(vf[1][u], vf[0][u]), phi(vf[3][u], vf[2][u]), phi(vf[5][u], vf[4][u]), phi(vf[7][u], vf[6][u])}, oo);
  }
#pragma unroll
  for (int h = 0; h < 4; ++h) *(LAS f32x2*)(lds + L_OW + w * OW_STRIDE + h * 512 + c * 32 + g * 8) = (f32x2){oe[h], oo[h]};
  PIN();
  ST(8);
  if (ROLE == 1) {
    float t = 0.f;
#pragma unroll
    for (int i = 0; i < 16; ++i) t += *(LAS float*)(lds + L_HP + (i * 64 + lane) * 4);
    t = rbf(rbf(t) + bflo(b1v));
    const float uu = 0.7978845608028654f * (t + 0.044715f * t * t * t);
    float hv = t / (1.f + __builtin_amdgcn_exp2f(-2.f * 1.4426950408889634f * uu));
    hv = (lane & 31) < 28 ? hv : 0.f;
    *(LAS u16*)(lds + L_HS + lane * 2) = (u16)pk(hv, hv);
    const u32x4 ha = *(LAS u32x4*)(lds + L_HS + ((c >> 2) & 1) * 64 + g * 16);
    f32x4 r = {0.f, 0.f, 0.f, 0.f};
#pragma unroll
    for (int d = 0; d < 8; ++d) {
      const u32x4 a = sel4((c >> 3) == (d >> 2) && (c & 3) == (d & 3), ha);
      const int q = d >> 1;
      const u32x4 bb = (d & 1) ? (u32x4){phi(wt[1][q], wt[0][q]), phi(wt[3][q], wt[2][q]), phi(wt[5][q], wt[4][q]), phi(wt[7][q], wt[6][q])}
                               : (u32x4){plo(wt[1][q], wt[0][q]), plo(wt[3][q], wt[2][q]), plo(wt[5][q], wt[4][q]), plo(wt[7][q], wt[6][q])};
      r = mfma(a, bb, r);
    }
    __builtin_amdgcn_raw_buffer_store_b128(pub4(r), ws, WS_MP + prod * 1024 + (g & 1) * 512 + (c * 8 + (g >> 1) * 4) * 4, 0, SC1);
  }
  LDS_BARRIER();
  ST(9);

  if (ROLE == 0 && w >= 2 && w < 10) {
    const int f = (w - 2) * 16 + (lane >> 2), p4 = lane & 3;
    f32x4 s = *(LAS f32x4*)(lds + L_OW + (p4 * 4) * OW_STRIDE + f * 16);
#pragma unroll
    for (int i = 1; i < 4; ++i) s += *(LAS f32x4*)(lds + L_OW + (p4 * 4 + i) * OW_STRIDE + f * 16);
#pragma unroll
    for (int e = 0; e < 4; ++e) { s[e] += dpp<XOR1>(s[e]); s[e] += dpp<XOR2>(s[e]); }
    if (p4 == 0) __builtin_amdgcn_raw_buffer_store_b128(pub4(s), ws, WS_OP + ((kvh * 2 + b) * 16 + sp) * 2048 + f * 16, 0, SC1);
  } else if (ROLE == 0 && w == 10) {
    float L = *(LAS float*)(lds + L_LW + lane * 4);
    L += dpp<ROR4>(L); L += dpp<ROR8>(L); L = half16(L, L); L = half32(L, L);
    if (lane < 4) __builtin_amdgcn_raw_buffer_store_b64((u32x2){pub(M), pub(L)}, ws, WS_ML + (((kvh * 4 + lane) * 2 + b) * 16 + sp) * 8, 0, SC1);
  } else if (ROLE == 2) {
    u32x4 bw[8];
#pragma unroll
    for (int t = 0; t < 2; ++t)
#pragma unroll
      for (int d = 0; d < 4; ++d) {
        const int q = t * 2 + (d >> 1);
        bw[t * 4 + d] = (d & 1) ? (u32x4){phi(wt[1][q], wt[0][q]), phi(wt[3][q], wt[2][q]), phi(wt[5][q], wt[4][q]), phi(wt[7][q], wt[6][q])}
                                : (u32x4){plo(wt[1][q], wt[0][q]), plo(wt[3][q], wt[2][q]), plo(wt[5][q], wt[4][q]), plo(wt[7][q], wt[6][q])};
      }
    const int tt = c >> 3, bb = (c >> 2) & 1, sl = c & 3;
    const int ooff = WS_OP + ((kvh * 2 + bb) * 16 + sl * 4) * 2048 + (rb * 64 + tt * 16 + g * 4) * 4;
    const int moff = WS_ML + (((kvh * 4 + (rb >> 1)) * 2 + bb) * 16 + sl * 4) * 8;
    u32x4 ov[8], ml0, ml1;
#pragma unroll 1
    for (int spin = 0; spin < SPIN_CAP; ++spin) {
      ml0 = __builtin_amdgcn_raw_buffer_load_b128(ws, moff, 0, SC1);
      ml1 = __builtin_amdgcn_raw_buffer_load_b128(ws, moff + 16, 0, SC1);
#pragma unroll
      for (int i = 0; i < 8; ++i) ov[i] = __builtin_amdgcn_raw_buffer_load_b128(ws, ooff + (i >> 1) * 2048 + (i & 1) * 128, 0, SC1);
      bool ok = ok4(ml0) && ok4(ml1);
#pragma unroll
      for (int i = 0; i < 8; ++i) ok = ok && ok4(ov[i]);
      if (__all(ok)) break;
      asm volatile("" ::: "memory");
    }
    ST(10);
    const float ms[4] = {u2f(ml0[0]), u2f(ml0[2]), u2f(ml1[0]), u2f(ml1[2])}, ls[4] = {u2f(ml0[1]), u2f(ml0[3]), u2f(ml1[1]), u2f(ml1[3])};
    float G = fmaxf(fmaxf(ms[0], ms[1]), fmaxf(ms[2], ms[3]));
    G = fmaxf(G, dpp<XOR1>(G)); G = fmaxf(G, dpp<XOR2>(G));
    float wg[4], L = 0.f;
#pragma unroll
    for (int u = 0; u < 4; ++u) { wg[u] = __builtin_amdgcn_exp2f(ms[u] - G); L = fmaf(wg[u], ls[u], L); }
    L += dpp<XOR1>(L); L += dpp<XOR2>(L);
    const float inv = 1.f / L;
    float o[8];
#pragma unroll
    for (int e = 0; e < 8; ++e) {
      float t = 0.f;
#pragma unroll
      for (int u = 0; u < 4; ++u) t = fmaf(wg[u], u2f(ov[u * 2 + (e >> 2)][e & 3]), t);
      t += dpp<XOR1>(t); t += dpp<XOR2>(t);
      o[e] = t * inv;
    }
    const u32x4 of = {pk(o[0], o[1]), pk(o[2], o[3]), pk(o[4], o[5]), pk(o[6], o[7])};
    f32x4 r = {0.f, 0.f, 0.f, 0.f};
#pragma unroll
    for (int i = 0; i < 8; ++i) r = mfma(sel4(tt == (i >> 2) && sl == (i & 3), of), bw[i], r);
#pragma unroll
    for (int e = 0; e < 4; ++e) r[e] = half32(r[e], r[e]);
    if (g < 2) __builtin_amdgcn_raw_buffer_store_b128(pub4(r), ws, WS_S2 + prod * 1024 + g * 512 + ((w - 14) * 64 + c * 4) * 4, 0, SC1);
  } else if (ROLE == 0 && w == 0) {
    const int cf = bid & 3, bf = (bid >> 2) & 1, quad = bid >> 3;
    const int e4 = (lane >> 5) * 2 + ((lane >> 4) & 1), col = cf * 128 + quad * 4 + e4;
    const u32 xv = ((const GAS u16*)x_)[bf * D + col], b2v = ((const GAS u16*)b2_)[col];
    const GAS u64* ev = (const GAS u64*)(ws_ + WS_QG) + ((lane >> 3) * 32 + (lane & 7) * 4 + cf) * 8;
#pragma unroll 1
    for (int spin = 0; spin < SPIN_CAP; ++spin) {
      const u64 t = __hip_atomic_load(ev, RLX_AGENT);
      if (__all((u32)(t >> 32) == tag)) break;
    }
    asm volatile("" ::: "memory");
    const int off = (cf * 64 + lane) * 1024 + bf * 512 + quad * 16;
    u32x4 v;
#pragma unroll 1
    for (int spin = 0; spin < SPIN_CAP; ++spin) {
      v = __builtin_amdgcn_raw_buffer_load_b128(ws, WS_MP + off, 0, SC1);
      if (__all(ok4(v))) break;
      asm volatile("" ::: "memory");
    }
    float t = half16(half32(u2f(v[0]), u2f(v[2])), half32(u2f(v[1]), u2f(v[3])));
    t += dpp<XOR1>(t); t += dpp<XOR2>(t); t += dpp<ROR4>(t); t += dpp<ROR8>(t);
    t = rbf(rbf(t) + bflo(b2v));
    const float gate = rbf(t / (1.f + __builtin_amdgcn_exp2f(-1.4426950408889634f * t)));
    ST(10);
#pragma unroll 1
    for (int spin = 0; spin < SPIN_CAP; ++spin) {
      v = __builtin_amdgcn_raw_buffer_load_b128(ws, WS_S2 + off, 0, SC1);
      if (__all(ok4(v))) break;
      asm volatile("" ::: "memory");
    }
    float s = half16(half32(u2f(v[0]), u2f(v[2])), half32(u2f(v[1]), u2f(v[3])));
    s += dpp<XOR1>(s); s += dpp<XOR2>(s); s += dpp<ROR4>(s); s += dpp<ROR8>(s);
    const float res = bflo(xv) + rbf(rbf(s) * gate);
    if (c == 0) ((GAS u16*)out_)[bf * D + col] = (u16)pk(res, res);
    if (bid == 0 && lane == 0) __hip_atomic_store((GAS u32*)(ws_ + WS_EPOCH), tag, RLX_AGENT);
  }
  };
  if (w >= 14) rest(IC<2>{});
  else if (w == 1) rest(IC<1>{});
  else rest(IC<0>{});
  ST(11);
}

extern "C" void kernel_launch(void* const* d_in, const int* in_sizes, int n_in, void* d_out, int out_size, void* d_ws, size_t ws_size,
                              hipStream_t stream) {
  fused_decode<<<256, 1024, 0, stream>>>((const u16*)d_in[0], (const u16*)d_in[1], (const u16*)d_in[2], (const u16*)d_in[3],
                                         (const u16*)d_in[4], (const u16*)d_in[6], (const u16*)d_in[7], (const u16*)d_in[8],
                                         (const u16*)d_in[9], (u16*)d_out, (unsigned char*)d_ws);
}
```

```cpp
#include <hip/hip_runtime.h>
#include <stdint.h>

#define WS_AT_END 0
#define SLEEP_Q 8
#define SLEEP_O 0
#define SLEEP_S 0
#define WO_MODE 0
#define WO_SLEEP 0
#define ABL 0
#define STAMP 0

#define GAS __attribute__((address_space(1)))
#define LAS __attribute__((address_space(3)))
#define DI __device__ __forceinline__
#define RLX_AGENT __ATOMIC_RELAXED, __HIP_MEMORY_SCOPE_AGENT
typedef unsigned short u16;
typedef unsigned u32;
typedef unsigned long long u64;
typedef u32 u32x2 __attribute__((ext_vector_type(2)));
typedef u32 u32x4 __attribute__((ext_vector_type(4)));
typedef float f32x2 __attribute__((ext_vector_type(2)));
typedef float f32x4 __attribute__((ext_vector_type(4)));
typedef __bf16 bf16x2 __attribute__((ext_vector_type(2)));
typedef __bf16 bf16x8 __attribute__((ext_vector_type(8)));

constexpr int D = 512, SK = 8192, DH = 128, FF = 1792, NQ = 4096;
constexpr int SC1 = 16;
constexpr int SPIN_CAP = 1 << 18;
constexpr float LOG2E = 1.4426950408889634f;

constexpr int WS_EPOCH = 0;
constexpr int WS_QP = 256;
constexpr int WS_PA = WS_QP + 256 * 1024;
constexpr int WS_OP = WS_PA + 224 * 1024;
constexpr int WS_S2 = WS_OP + 256 * 2048;
constexpr int WS_MP = WS_S2 + 256 * 2048;
constexpr int WS_END = WS_MP + 224 * 2048;
constexpr int WS_SPAN = 4 << 20;
constexpr int WS_STAMP = 8 << 20;

constexpr int L_FLAG = 0, L_CNT = 16, L_XN = 32, L_Y = L_XN + 2048, L_Q = L_Y + 2048, L_MW = L_Q + 1024, L_LW = L_MW + 256,
              L_HP = L_LW + 256, L_HS = L_HP + 1024, L_GP = L_HS + 128, GP_STRIDE = 272, L_FP = L_GP + 8 * GP_STRIDE,
              L_MF = L_FP + 512, L_W2 = L_MF + 256, L_OW = L_W2 + 8192, OW_STRIDE = 2064, L_END = L_OW + 16 * OW_STRIDE;

DI float bflo(u32 v) { return __builtin_bit_cast(float, v << 16); }
DI float bfhi(u32 v) { return __builtin_bit_cast(float, v & 0xffff0000u); }
DI float rbf(float f) { return (float)(__bf16)f; }
DI u32 pk(float a, float b) { return __builtin_bit_cast(u32, __builtin_convertvector((f32x2){a, b}, bf16x2)); }
DI u32 f2u(float f) { return __builtin_bit_cast(u32, f); }
DI float u2f(u32 u) { return __builtin_bit_cast(float, u); }
DI f32x4 mfma(u32x4 a, u32x4 b, f32x4 c) {
  return __builtin_amdgcn_mfma_f32_16x16x32_bf16(__builtin_bit_cast(bf16x8, a), __builtin_bit_cast(bf16x8, b), c, 0, 0, 0);
}
template <int CTRL> DI float dpp(float x) { return u2f(__builtin_amdgcn_mov_dpp(f2u(x), CTRL, 0xf, 0xf, true)); }
constexpr int XOR1 = 0xB1, XOR2 = 0x4E, ROR4 = 0x124, ROR8 = 0x128;
DI float half32(float a, float b) { auto r = __builtin_amdgcn_permlane32_swap(f2u(a), f2u(b), false, false); return u2f(r[0]) + u2f(r[1]); }
DI float half16(float a, float b) { auto r = __builtin_amdgcn_permlane16_swap(f2u(a), f2u(b), false, false); return u2f(r[0]) + u2f(r[1]); }
DI float max32(float a) { auto r = __builtin_amdgcn_permlane32_swap(f2u(a), f2u(a), false, false); return fmaxf(u2f(r[0]), u2f(r[1])); }
DI float max16(float a) { auto r = __builtin_amdgcn_permlane16_swap(f2u(a), f2u(a), false, false); return fmaxf(u2f(r[0]), u2f(r[1])); }
DI float half_sum(float v) {
  v += dpp<XOR1>(v); v += dpp<XOR2>(v); v += dpp<ROR4>(v); v += dpp<ROR8>(v);
  return half16(v, v);
}
DI u32 plo(u32 k1, u32 k0) { return __builtin_amdgcn_perm(k1, k0, 0x05040100u); }
DI u32 phi(u32 k1, u32 k0) { return __builtin_amdgcn_perm(k1, k0, 0x07060302u); }
DI u32x4 colfrag(const u32x4 (&r)[8], int d) {
  const int q = d >> 1;
  return (d & 1) ? (u32x4){phi(r[1][q], r[0][q]), phi(r[3][q], r[2][q]), phi(r[5][q], r[4][q]), phi(r[7][q], r[6][q])}
                 : (u32x4){plo(r[1][q], r[0][q]), plo(r[3][q], r[2][q]), plo(r[5][q], r[4][q]), plo(r[7][q], r[6][q])};
}
DI u32x4 sel4(bool c, u32x4 v) { return (u32x4){c ? v[0] : 0u, c ? v[1] : 0u, c ? v[2] : 0u, c ? v[3] : 0u}; }
template <typename T> DI T ldnt(const GAS T* p) { return __builtin_nontemporal_load(p); }
DI u32x4 ldws(__amdgpu_buffer_rsrc_t ws, int off) { return __builtin_amdgcn_raw_buffer_load_b128(ws, off, 0, SC1); }
DI void stws(__amdgpu_buffer_rsrc_t ws, int off, u32 a, u32 b, u32 tag) { __builtin_amdgcn_raw_buffer_store_b128((u32x4){a, tag, b, tag}, ws, off, 0, SC1); }
DI bool tagged(u32x4 v, u32 tag) { return v[1] == tag && v[3] == tag; }
#define PIN() do { asm volatile("" ::: "memory"); __builtin_amdgcn_sched_barrier(0); } while (0)
#define LDS_BARRIER() do { asm volatile("s_waitcnt lgkmcnt(0)\n\ts_barrier" ::: "memory"); __builtin_amdgcn_sched_barrier(0); } while (0)
#define RAW_BARRIER() do { __builtin_amdgcn_sched_barrier(0); asm volatile("s_barrier" ::: "memory"); __builtin_amdgcn_sched_barrier(0); } while (0)
DI void dma16(const u16* src, u32 dst) {
  u32 keep;
  asm volatile("s_mov_b32 %0, m0\n\ts_mov_b32 m0, %2\n\ts_nop 0\n\tglobal_load_lds_dwordx4 %1, off nt\n\ts_mov_b32 m0, %0"
               : "=&s"(keep) : "v"(src), "s"(dst) : "memory");
}
template <int N> struct IC { static constexpr int value = N; };

DI void layernorm(u32 (&x)[8]) {
  float v[16], s = 0.f;
#pragma unroll
  for (int e = 0; e < 8; ++e) { v[2 * e] = bflo(x[e]); v[2 * e + 1] = bfhi(x[e]); s += v[2 * e] + v[2 * e + 1]; }
  const float mean = half_sum(s) * (1.f / 512.f);
  float q = 0.f;
#pragma unroll
  for (int e = 0; e < 16; ++e) { const float d = v[e] - mean; q = fmaf(d, d, q); }
  const float var = half_sum(q) * (1.f / 512.f);
  const float mb = rbf(mean), inv = __builtin_amdgcn_rcpf(rbf(__builtin_amdgcn_sqrtf(rbf(rbf(var) + 1e-5f))));
#pragma unroll
  for (int e = 0; e < 8; ++e) { const u32 d = pk(v[2 * e] - mb, v[2 * e + 1] - mb); x[e] = pk(bflo(d) * inv, bfhi(d) * inv); }
}

DI void tile_mfma(const u32x4 (&wv)[8], LAS unsigned char* xin, int g, int c, f32x4& lo, f32x4& hi) {
  const u32x4 xf = *(LAS u32x4*)(xin + ((c >> 2) & 1) * 1024 + ((c >> 3) * 32 + g * 8) * 2);
#pragma unroll
  for (int d = 0; d < 4; ++d) {
    const u32x4 am = sel4((c & 3) == d, xf);
    lo = mfma(am, colfrag(wv, d), lo);
    hi = mfma(am, colfrag(wv, 4 + d), hi);
  }
}
DI f32x2 tile_reduce(f32x4 lo, f32x4 hi, int g, int c) {
  const bool valid = (g >> 1) == (c >> 3);
  float t[4];
#pragma unroll
  for (int e = 0; e < 4; ++e) { t[e] = half32(valid ? lo[e] : 0.f, valid ? hi[e] : 0.f); t[e] += dpp<ROR8>(t[e]); }
  return (c & 8) ? (f32x2){t[2], t[3]} : (f32x2){t[0], t[1]};
}

#define ST(i)

__global__ void __launch_bounds__(1024, 4)
fused_decode(const u16* x_, const u16* k_, const u16* v_, const u16* wq_, const u16* wo_, const u16* w1_, const u16* b1_,
             const u16* w2_, const u16* b2_, u16* out_, unsigned char* ws_
             ) {
  __shared__ __attribute__((aligned(16))) unsigned char lds_raw[L_END];
  LAS unsigned char* lds = (LAS unsigned char*)lds_raw;
  ST(0);
  asm volatile("" :: "s"(x_), "s"(k_), "s"(v_), "s"(wq_), "s"(wo_), "s"(w1_), "s"(b1_), "s"(w2_), "s"(b2_), "s"(out_), "s"(ws_));
  const int tid = threadIdx.x, lane = tid & 63, w = __builtin_amdgcn_readfirstlane(tid >> 6);
  const int g = lane >> 4, c = lane & 15;
  const int bid = blockIdx.x, kvh = bid & 7, j = bid >> 3, b = j >> 4, sp = j & 15, rb = j >> 2, cb = j & 3;
  const bool mlp = bid < 224;
  const __amdgpu_buffer_rsrc_t ws = __builtin_amdgcn_make_buffer_rsrc(ws_, 0, WS_END, 0x00020000);
  const u32 ep = __hip_atomic_load((GAS u32*)(ws_ + WS_EPOCH), RLX_AGENT);
  const size_t kvbase = ((size_t)(b * 8 + kvh) * SK + sp * 512 + w * 32) * (DH / 8);
  const GAS u32x4* kp = (const GAS u32x4*)k_ + kvbase + (((c >> 1) & 1) * 4 + (c >> 2)) * 16 + g * 2 + (c & 1);
  u32x4 kf[8], vf[8];
  u32 tag;

  const int r2 = bid >> 2, c2 = bid & 3;
  const int cq = j & 7, kq = j >> 3;
  const int c1 = bid >> 3, k8 = bid & 7;
  if (w == 0 || (w == 1 && mlp)) {
    __builtin_amdgcn_s_setprio(3);
    if (w == 0 && lane < 8) *(LAS u32*)(lds + L_FLAG + lane * 4) = 0u;
    const GAS u32x4* xp = (const GAS u32x4*)x_ + lane * 2;
    const u32x4 x0 = xp[0], x1 = xp[1];
    PIN();
    const int rs = w == 0 ? NQ / 8 : FF / 8;
    const u16* tile = w == 0 ? wq_ + (size_t)kq * 128 * NQ + kvh * 512 + cq * 64 : w1_ + k8 * 64 * FF + c1 * 64;
    const GAS u32x4* wp = (const GAS u32x4*)tile + ((c >> 3) * 32 + g * 8) * rs + (c & 7);
    u32x4 wv[2][8];
#pragma unroll
    for (int i = 0; i < 8; ++i) wv[0][i] = ldnt(wp + i * rs);
    if (w == 0) {
#pragma unroll
      for (int i = 0; i < 8; ++i) wv[1][i] = ldnt(wp + (64 + i) * (NQ / 8));
    }
    PIN();
    LDS_BARRIER();
    ST(1);
    u32 xs[8] = {x0[0], x0[1], x0[2], x0[3], x1[0], x1[1], x1[2], x1[3]};
    layernorm(xs);
    if (w == 1) layernorm(xs);
    LAS unsigned char* xl = lds + L_XN + w * 2048;
    *(LAS u32x4*)(xl + lane * 32) = (u32x4){xs[0], xs[1], xs[2], xs[3]};
    *(LAS u32x4*)(xl + lane * 32 + 16) = (u32x4){xs[4], xs[5], xs[6], xs[7]};
    f32x4 lo = {0.f, 0.f, 0.f, 0.f}, hi = lo;
    tile_mfma(wv[0], xl + (w == 0 ? kq * 256 : k8 * 128), g, c, lo, hi);
    __builtin_amdgcn_s_waitcnt(0x0F70);
    if (w == 0) tile_mfma(wv[1], xl + kq * 256 + 128, g, c, lo, hi);
    const f32x2 r = tile_reduce(lo, hi, g, c);
    tag = __builtin_amdgcn_readfirstlane(ep) + 1u;
    const int unit = (g >> 1) * 2 + (c >> 3);
    const int off = w == 0 ? WS_QP + ((((g & 1) * 8 + kvh) * 8 + cq) * 4 + kq) * 512 + ((c & 7) * 4 + unit) * 16
                           : WS_PA + (c1 * 8 + k8) * 1024 + ((c >> 2) & 1) * 512 + (g & 1) * 256 + ((c & 3) * 4 + unit) * 16;
    stws(ws, off, f2u(r[0]), f2u(r[1]), tag);
    ST(2);
  } else {
    __builtin_amdgcn_s_setprio(1);
    if (w == 2 && mlp) {
      const u16* p = w2_ + (r2 * 32 + g * 8) * D + c2 * 128 + c * 8;
#pragma unroll
      for (int i = 0; i < 8; ++i) dma16(p + i * D, (u32)(size_t)(lds + L_W2 + i * 1024));
    }
    RAW_BARRIER();
  }

#pragma unroll
  for (int i = 0; i < 8; ++i) { kf[i] = ldnt(kp + (i >> 1) * 128 + (i & 1) * 8); __builtin_amdgcn_sched_barrier(0); }
  PIN();
  u32 b1v = 0;
  if (w < 4) {
    __builtin_amdgcn_s_setprio(3);
    tag = __builtin_amdgcn_readfirstlane(ep) + 1u;
    const int qoff = WS_QP + (b * 8 + kvh) * 16384 + w * 4096 + lane * 16;
    const int paoff = mlp ? WS_PA + ((r2 >> 1) * 8 + w * 2 + (lane >> 5)) * 1024 + (r2 & 1) * 512 + (lane & 31) * 16 : qoff;
    b1v = *(const GAS u32*)(b1_ + (mlp ? r2 * 32 : 0) + c * 2);
    u32x4 qv[4], pa;
#pragma unroll 1
    for (int spin = 0; spin < SPIN_CAP; ++spin) {
#pragma unroll
      for (int i = 0; i < 4; ++i) qv[i] = ldws(ws, qoff + i * 1024);
      pa = ldws(ws, paoff);
      if (__all(tagged(qv[0], tag) && tagged(qv[1], tag) && tagged(qv[2], tag) && tagged(qv[3], tag) && tagged(pa, tag))) break;
      __builtin_amdgcn_s_sleep(SLEEP_Q);
      asm volatile("" ::: "memory");
    }
    const float q0 = half32(u2f(qv[0][0]) + u2f(qv[1][0]), u2f(qv[2][0]) + u2f(qv[3][0]));
    const float q1 = half32(u2f(qv[0][2]) + u2f(qv[1][2]), u2f(qv[2][2]) + u2f(qv[3][2]));
    *(LAS u32*)(lds + L_Q + w * 256 + lane * 4) = pk(q0, q1);
    *(LAS float*)(lds + L_HP + w * 256 + (lane & 31) * 8 + (lane >> 5) * 4) = half32(u2f(pa[0]), u2f(pa[2]));
    asm volatile("s_waitcnt lgkmcnt(0)" ::: "memory");
    *(volatile LAS u32*)(lds + L_FLAG + w * 4) = 1u;
    ST(3);
  }
  __builtin_amdgcn_s_setprio(0);
  PIN();
  {
    const GAS u32x4* vp = (const GAS u32x4*)v_ + kvbase + lane;
#pragma unroll
    for (int i = 0; i < 8; ++i) { vf[i] = ldnt(vp + i * 64); __builtin_amdgcn_sched_barrier(0); }
  }
  PIN();
  ST(4);
  tag = __builtin_amdgcn_readfirstlane(ep) + 1u;
#pragma unroll 1
  for (int spin = 0; spin < (SPIN_CAP << 4); ++spin) {
    if (__all(*(volatile LAS u32*)(lds + L_FLAG + (lane & 3) * 4) != 0u)) break;
    __builtin_amdgcn_s_sleep(2);
  }
  PIN();
  if (w == 2 && mlp) {
    f32x2 t2 = *(LAS f32x2*)(lds + L_HP + (lane & 31) * 8);
#pragma unroll
    for (int i = 1; i < 4; ++i) t2 += *(LAS f32x2*)(lds + L_HP + i * 256 + (lane & 31) * 8);
    float hv[2];
#pragma unroll
    for (int e = 0; e < 2; ++e) {
      const float t = rbf(rbf(t2[e]) + (e ? bfhi(b1v) : bflo(b1v)));
      const float uu = 0.7978845608028654f * (t + 0.044715f * t * t * t);
      hv[e] = t / (1.f + __builtin_amdgcn_exp2f(-2.f * LOG2E * uu));
    }
    *(LAS u32*)(lds + L_HS + (lane & 31) * 4) = pk(hv[0], hv[1]);
    const u32x4 ha = *(LAS u32x4*)(lds + L_HS + ((c >> 2) & 1) * 64 + g * 16);
    u32x4 w2t[8];
#pragma unroll
    for (int i = 0; i < 8; ++i) w2t[i] = *(LAS u32x4*)(lds + L_W2 + i * 1024 + lane * 16);
    f32x4 r = {0.f, 0.f, 0.f, 0.f};
#pragma unroll
    for (int d = 0; d < 8; ++d) r = mfma(sel4((c >> 3) == (d >> 2) && (c & 3) == (d & 3), ha), colfrag(w2t, d), r);
    const int off = WS_MP + (c2 * 56 + r2) * 2048 + (g & 1) * 1024 + (c * 8 + (g >> 1) * 4) * 8;
    stws(ws, off, f2u(r[0]), f2u(r[1]), tag);
    stws(ws, off + 16, f2u(r[2]), f2u(r[3]), tag);
    ST(2);
  }
  PIN();

  float sc[8];
  {
    const int x0 = (c >> 2) & 1;
    const u32x4 q0 = *(LAS u32x4*)(lds + L_Q + (c & 3) * 256 + (g * 2 + x0) * 16);
    const u32x4 q1 = *(LAS u32x4*)(lds + L_Q + (c & 3) * 256 + 128 + (g * 2 + x0) * 16);
    constexpr float C2 = 0.08838834764831845f * LOG2E;
#pragma unroll
    for (int o = 0; o < 4; ++o) {
      f32x4 s = mfma(kf[2 * o], q0, (f32x4){0.f, 0.f, 0.f, 0.f});
      s = mfma(kf[2 * o + 1], q1, s);
#pragma unroll
      for (int a = 0; a < 2; ++a) {
        float e1 = s[2 * a + 1];
        asm("" : "+v"(e1));
        const float t = x0 ? e1 : s[2 * a];
        sc[2 * o + a] = (t + dpp<ROR4>(t)) * C2;
      }
    }
  }
  PIN();

    float m = fmaxf(fmaxf(fmaxf(sc[0], sc[1]), fmaxf(sc[2], sc[3])), fmaxf(fmaxf(sc[4], sc[5]), fmaxf(sc[6], sc[7])));
    m = max32(max16(m));
    float p[8], l = 0.f;
#pragma unroll
    for (int e = 0; e < 8; ++e) { p[e] = __builtin_amdgcn_exp2f(sc[e] - m); l += p[e]; }
    l = half16(l, l); l = half32(l, l);
    if (lane < 4) { *(LAS float*)(lds + L_MW + (lane * 16 + w) * 4) = m; *(LAS float*)(lds + L_LW + (lane * 16 + w) * 4) = l; }
    PIN();
    if (lane == 0) __hip_atomic_fetch_add((LAS u32*)(lds + L_CNT), 1u, __ATOMIC_RELAXED, __HIP_MEMORY_SCOPE_WORKGROUP);
    const u32x4 pf = {pk(p[0], p[1]), pk(p[2], p[3]), pk(p[4], p[5]), pk(p[6], p[7])};
    u32x4 am[4];
#pragma unroll
    for (int u = 0; u < 4; ++u) am[u] = sel4((c >> 2) == u, pf);
    PIN();
    u32x4 ve[4], vo[4];
    u32 slo, shi;
    asm volatile("s_mov_b32 %0, 0x05040100\n\ts_mov_b32 %1, 0x07060302" : "=s"(slo), "=s"(shi) : "v"(am[0][0]), "v"(am[1][1]), "v"(am[2][2]), "v"(am[3][3]));
#pragma unroll
    for (int r = 0; r < 4; ++r) {
#pragma unroll
      for (int u = 0; u < 4; ++u) {
        ve[u][r] = __builtin_amdgcn_perm(vf[2 * r + 1][u], vf[2 * r][u], slo);
        vo[u][r] = __builtin_amdgcn_perm(vf[2 * r + 1][u], vf[2 * r][u], shi);
      }
      if (r < 3)
        asm volatile("" : "+s"(slo), "+s"(shi) : "v"(ve[0][r]), "v"(ve[1][r]), "v"(ve[2][r]), "v"(ve[3][r]), "v"(vo[0][r]), "v"(vo[1][r]), "v"(vo[2][r]), "v"(vo[3][r]));
    }
    f32x4 oe = {0.f, 0.f, 0.f, 0.f}, oo = oe;
#pragma unroll
    for (int u = 0; u < 4; ++u) { oe = mfma(am[u], ve[u], oe); oo = mfma(am[u], vo[u], oo); }
#pragma unroll
    for (int h = 0; h < 4; ++h) *(LAS f32x2*)(lds + L_OW + w * OW_STRIDE + h * 512 + c * 32 + g * 8) = (f32x2){oe[h], oo[h]};
    PIN();
    ST(5);

  const bool gath = (w & 4) == 0;
  const int v = (w & 3) + (w >> 3) * 4;
  auto wait_ml = [&]() __attribute__((always_inline)) {
#pragma unroll 1
    for (int spin = 0; spin < (SPIN_CAP << 4); ++spin) {
      if (__builtin_amdgcn_readfirstlane(*(volatile LAS u32*)(lds + L_CNT)) == 16u) break;
      __builtin_amdgcn_s_sleep(1);
    }
    PIN();
  };
  auto load_wo = [&](u32x4 (&wt)[8]) __attribute__((always_inline)) {
    const GAS u16* p = (const GAS u16*)wo_ + (size_t)(kvh * 512 + rb * 64 + g * 4) * D + cb * 128 + (w - 4) * 64 + c * 4;
#pragma unroll
    for (int i = 0; i < 8; ++i) {
      const u32x2 t0 = ldnt((const GAS u32x2*)(p + (size_t)(32 * (i >> 2) + (i & 3)) * D));
      const u32x2 t1 = ldnt((const GAS u32x2*)(p + (size_t)(32 * (i >> 2) + 16 + (i & 3)) * D));
      wt[i] = (u32x4){t0[0], t0[1], t1[0], t1[1]};
    }
    PIN();
  };
  const int cf = bid & 3, bf = (bid >> 2) & 1, seg = bid >> 3;
  if (gath) {
    if (w < 4 && bid < 64) {
      const int moff = WS_MP + (cf * 56 + w * 14 + (lane >> 3)) * 2048 + bf * 1024 + seg * 128 + (lane & 7) * 16;
      const int moff2 = lane < 48 ? moff + 16384 : moff;
      u32x4 m0, m1;
#pragma unroll 1
      for (int spin = 0; spin < SPIN_CAP; ++spin) {
        m0 = ldws(ws, moff); m1 = ldws(ws, moff2);
        if (__all(tagged(m0, tag) && tagged(m1, tag))) break;
        __builtin_amdgcn_s_sleep(SLEEP_Q);
        asm volatile("" ::: "memory");
      }
      m1 = sel4(lane < 48, m1);
      float t = half32(u2f(m0[0]) + u2f(m1[0]), u2f(m0[2]) + u2f(m1[2]));
      t = half16(t, t);
      t += dpp<ROR8>(t);
      if ((lane & 24) == 0) *(LAS float*)(lds + L_MF + w * 64 + (lane & 7) * 8 + (lane >> 5) * 4) = t;
    }
    {
      const int f = v * 16 + (lane >> 2), p4 = lane & 3, hd = v >> 1;
      wait_ml();
      const f32x4 mv = *(LAS f32x4*)(lds + L_MW + hd * 64 + p4 * 16), lv = *(LAS f32x4*)(lds + L_LW + hd * 64 + p4 * 16);
      float M = fmaxf(fmaxf(mv[0], mv[1]), fmaxf(mv[2], mv[3]));
      M = fmaxf(M, dpp<XOR1>(M)); M = fmaxf(M, dpp<XOR2>(M));
      float L = 0.f, fw[4];
#pragma unroll
      for (int i = 0; i < 4; ++i) { fw[i] = __builtin_amdgcn_exp2f(mv[i] - M); L = fmaf(fw[i], lv[i], L); }
      L += dpp<XOR1>(L); L += dpp<XOR2>(L);
      const float inv = __builtin_amdgcn_rcpf(L);
#pragma unroll
      for (int i = 0; i < 4; ++i) fw[i] *= inv;
      const u32 lam = f2u(M + __builtin_amdgcn_logf(L)) >> ((lane >> 5) * 16);
      u32 t0 = (tag << 8) | (lam & 255u), t1 = (tag << 8) | ((lam >> 8) & 255u);
      int ooff = WS_OP + ((kvh * 2 + b) * 16 + sp) * 2048 + f * 16;
      int laddr = L_OW + p4 * 4 * OW_STRIDE + f * 16;
      asm volatile("" : "+v"(fw[0]), "+v"(fw[1]), "+v"(fw[2]), "+v"(fw[3]), "+v"(t0), "+v"(t1), "+v"(ooff), "+v"(laddr));
      LDS_BARRIER();
      ST(6);
      f32x4 s = fw[0] * *(LAS f32x4*)(lds + laddr);
#pragma unroll
      for (int i = 1; i < 4; ++i) s += fw[i] * *(LAS f32x4*)(lds + laddr + i * OW_STRIDE);
#pragma unroll
      for (int e = 0; e < 4; ++e) s[e] += dpp<XOR1>(s[e]);
#pragma unroll
      for (int e = 0; e < 4; ++e) s[e] += dpp<XOR2>(s[e]);
      if (p4 == 0) __builtin_amdgcn_raw_buffer_store_b128((u32x4){pk(s[0], s[1]), t0, pk(s[2], s[3]), t1}, ws, ooff, 0, SC1);
    }
    PIN();
    ST(7);
    {
      const int ooff = WS_OP + (kvh * 32 + v * 4 + g) * 2048 + rb * 256 + c * 16;
      u32x4 ov;
      bool ok = false;
#pragma unroll 1
      for (int spin = 0; spin < SPIN_CAP; ++spin) {
        if (!ok) { ov = ldws(ws, ooff); ok = ((ov[1] ^ (tag << 8)) | (ov[3] ^ (tag << 8))) < 256u; }
        if (__all(ok)) break;
        if (SLEEP_O) __builtin_amdgcn_s_sleep(SLEEP_O);
        asm volatile("" ::: "memory");
      }
      ST(8);
      const u32 h16 = __builtin_amdgcn_perm(ov[3], ov[1], 0x04000400u), oth = f2u(dpp<ROR8>(u2f(h16)));
      const float lam = u2f((c & 8) ? plo(h16, oth) : plo(oth, h16));
      const float G = max32(max16(lam));
      const float wg = __builtin_amdgcn_exp2f(lam - G);
      float L = half16(wg, wg);
      L = half32(L, L);
      const float t = half16(half32(wg * bflo(ov[0]), wg * bflo(ov[2])), half32(wg * bfhi(ov[0]), wg * bfhi(ov[2])));
      *(LAS float*)(lds + L_GP + v * GP_STRIDE + (c * 4 + (g >> 1) * 2 + (g & 1)) * 4) = t;
      if (lane == 0) *(LAS f32x2*)(lds + L_GP + v * GP_STRIDE + 256) = (f32x2){G, L};
    }
    LDS_BARRIER();
    ST(9);
    if (bid < 64) {
      const int soff = WS_S2 + (cf * 64 + v * 8 + (lane >> 3)) * 2048 + bf * 1024 + seg * 128 + (lane & 7) * 16;
      u32x4 sv;
      bool ok = false;
#pragma unroll 1
      for (int spin = 0; spin < SPIN_CAP; ++spin) {
        if (!ok) { sv = ldws(ws, soff); ok = tagged(sv, tag); }
        if (__all(ok)) break;
        if (SLEEP_S) __builtin_amdgcn_s_sleep(SLEEP_S);
        asm volatile("" ::: "memory");
      }
      ST(10);
      float t = half32(u2f(sv[0]), u2f(sv[2]));
      t = half16(t, t);
      t += dpp<ROR8>(t);
      if ((lane & 24) == 0) *(LAS float*)(lds + L_FP + v * 64 + (lane & 7) * 8 + (lane >> 5) * 4) = t;
      LDS_BARRIER();
    }
  } else if (w < 6) {
    u32x4 wt[8], bw[8];
    if (WO_MODE == 1) wait_ml();
    if (WO_MODE < 2) load_wo(wt);
    LDS_BARRIER();
    ST(6);
    if (WO_MODE == 2) { if (WO_SLEEP) __builtin_amdgcn_s_sleep(WO_SLEEP); load_wo(wt); }
#pragma unroll
    for (int i = 0; i < 8; ++i) bw[i] = colfrag(wt, i);
    asm volatile("" :: "v"(bw[0]), "v"(bw[1]), "v"(bw[2]), "v"(bw[3]), "v"(bw[4]), "v"(bw[5]), "v"(bw[6]), "v"(bw[7]));
    LDS_BARRIER();
    ST(9);
    const int tt = c >> 3;
    LAS unsigned char* gp = lds + L_GP + (c & 7) * GP_STRIDE;
    const f32x2 gl = *(LAS f32x2*)(gp + 256);
    const f32x4 o0 = *(LAS f32x4*)(gp + (tt * 16 + g * 4) * 4), o1 = *(LAS f32x4*)(gp + (32 + tt * 16 + g * 4) * 4);
    float G = fmaxf(gl[0], dpp<XOR1>(gl[0]));
    G = fmaxf(G, dpp<XOR2>(G));
    const float fw = __builtin_amdgcn_exp2f(gl[0] - G);
    float L = fw * gl[1];
    L += dpp<XOR1>(L); L += dpp<XOR2>(L);
    const float sc2 = fw * __builtin_amdgcn_rcpf(L);
    float o[8];
#pragma unroll
    for (int e = 0; e < 8; ++e) { float t = sc2 * (e < 4 ? o0[e & 3] : o1[e & 3]); t += dpp<XOR1>(t); o[e] = t + dpp<XOR2>(t); }
    const u32x4 of = {pk(o[0], o[1]), pk(o[2], o[3]), pk(o[4], o[5]), pk(o[6], o[7])};
    float r[4];
#pragma unroll
    for (int d = 0; d < 4; ++d) {
      const float a0 = mfma(of, bw[d], (f32x4){0.f, 0.f, 0.f, 0.f})[0], a1 = mfma(of, bw[4 + d], (f32x4){0.f, 0.f, 0.f, 0.f})[0];
      r[d] = (g >> 1) ? a1 : a0;
    }
    const float r0 = half32(r[0], r[2]), r1 = half32(r[1], r[3]);
    stws(ws, WS_S2 + (cb * 64 + kvh * 8 + rb) * 2048 + (g & 1) * 1024 + ((w - 4) * 64 + c * 4 + (g >> 1) * 2) * 8, f2u(r0), f2u(r1), tag);
    ST(10);
  } else if (w == 6 && bid < 64) {
    const int col = cf * 128 + seg * 16 + c;
    const u32 xv = ((const GAS u16*)x_)[bf * D + col], b2v = ((const GAS u16*)b2_)[col];
    LDS_BARRIER();
    ST(6);
    float t = *(LAS float*)(lds + L_MF + lane * 4);
    t = half16(t, t);
    t = half32(t, t);
    t = rbf(rbf(t) + bflo(b2v));
    float gate = rbf(t / (1.f + __builtin_amdgcn_exp2f(-LOG2E * t))), xf = bflo(xv);
    asm volatile("" : "+v"(gate), "+v"(xf));
    LDS_BARRIER();
    LDS_BARRIER();
    ST(10);
    float s = *(LAS float*)(lds + L_FP + lane * 4) + *(LAS float*)(lds + L_FP + 256 + lane * 4);
    s = half16(s, s);
    s = half32(s, s);
    const float res = xf + rbf(rbf(s) * gate);
    if (g == 0) ((GAS u16*)out_)[bf * D + col] = (u16)pk(res, res);
    if (bid == 0 && lane == 0) __hip_atomic_store((GAS u32*)(ws_ + WS_EPOCH), tag, RLX_AGENT);
  } else {
    LDS_BARRIER();
    ST(6);
  }
  ST(11);
}

extern "C" void kernel_launch(void* const* d_in, const int* in_sizes, int n_in, void* d_out, int out_size, void* d_ws, size_t ws_size,
                              hipStream_t stream) {
  unsigned char* ws = (unsigned char*)d_ws + (WS_AT_END ? ((ws_size - WS_SPAN) & ~(size_t)4095) : 0);
  fused_decode<<<256, 1024, 0, stream>>>((const u16*)d_in[0], (const u16*)d_in[1], (const u16*)d_in[2], (const u16*)d_in[3],
                                         (const u16*)d_in[4], (const u16*)d_in[6], (const u16*)d_in[7], (const u16*)d_in[8],
                                         (const u16*)d_in[9], (u16*)d_out, ws
                                         );
}
```

```cpp
#define STAMP 1
#define PROBE 0
#define STMASK (1 | (1 << 0))
#include <hip/hip_runtime.h>
#include <stdint.h>

#define WS_AT_END 0
#define SLEEP_Q 8
#define SLEEP_O 0
#define SLEEP_S 0
#define WO_MODE 1
#define WO_SLEEP 0
#define ABL 0

#define GAS __attribute__((address_space(1)))
#define LAS __attribute__((address_space(3)))
#define DI __device__ __forceinline__
#define RLX_AGENT __ATOMIC_RELAXED, __HIP_MEMORY_SCOPE_AGENT
typedef unsigned short u16;
typedef unsigned u32;
typedef unsigned long long u64;
typedef u32 u32x2 __attribute__((ext_vector_type(2)));
typedef u32 u32x4 __attribute__((ext_vector_type(4)));
typedef float f32x2 __attribute__((ext_vector_type(2)));
typedef float f32x4 __attribute__((ext_vector_type(4)));
typedef __bf16 bf16x2 __attribute__((ext_vector_type(2)));
typedef __bf16 bf16x8 __attribute__((ext_vector_type(8)));

constexpr int D = 512, SK = 8192, DH = 128, FF = 1792, NQ = 4096;
constexpr int SC1 = 16;
constexpr int SPIN_CAP = 1 << 18;
constexpr float LOG2E = 1.4426950408889634f;

constexpr int WS_EPOCH = 0;
constexpr int WS_QP = 256;
constexpr int WS_PA = WS_QP + 256 * 1024;
constexpr int WS_OP = WS_PA + 224 * 1024;
constexpr int WS_S2 = WS_OP + 256 * 2048;
constexpr int WS_MP = WS_S2 + 256 * 2048;
constexpr int WS_END = WS_MP + 224 * 2048;
constexpr int WS_SPAN = 4 << 20;
constexpr int WS_STAMP = 8 << 20;

constexpr int L_FLAG = 0, L_CNT = 16, L_XN = 32, L_Y = L_XN + 2048, L_Q = L_Y + 2048, L_MW = L_Q + 1024, L_LW = L_MW + 256,
              L_HP = L_LW + 256, L_HS = L_HP + 1024, L_GP = L_HS + 128, GP_STRIDE = 272, L_FP = L_GP + 8 * GP_STRIDE,
              L_MF = L_FP + 512, L_W2 = L_MF + 256, L_OW = L_W2 + 8192, OW_STRIDE = 2064, L_END = L_OW + 16 * OW_STRIDE;

DI float bflo(u32 v) { return __builtin_bit_cast(float, v << 16); }
DI float bfhi(u32 v) { return __builtin_bit_cast(float, v & 0xffff0000u); }
DI float rbf(float f) { return (float)(__bf16)f; }
DI u32 pk(float a, float b) { return __builtin_bit_cast(u32, __builtin_convertvector((f32x2){a, b}, bf16x2)); }
DI u32 f2u(float f) { return __builtin_bit_cast(u32, f); }
DI float u2f(u32 u) { return __builtin_bit_cast(float, u); }
DI f32x4 mfma(u32x4 a, u32x4 b, f32x4 c) {
  return __builtin_amdgcn_mfma_f32_16x16x32_bf16(__builtin_bit_cast(bf16x8, a), __builtin_bit_cast(bf16x8, b), c, 0, 0, 0);
}
template <int CTRL> DI float dpp(float x) { return u2f(__builtin_amdgcn_mov_dpp(f2u(x), CTRL, 0xf, 0xf, true)); }
constexpr int XOR1 = 0xB1, XOR2 = 0x4E, ROR4 = 0x124, ROR8 = 0x128;
DI float half32(float a, float b) { auto r = __builtin_amdgcn_permlane32_swap(f2u(a), f2u(b), false, false); return u2f(r[0]) + u2f(r[1]); }
DI float half16(float a, float b) { auto r = __builtin_amdgcn_permlane16_swap(f2u(a), f2u(b), false, false); return u2f(r[0]) + u2f(r[1]); }
DI float max32(float a) { auto r = __builtin_amdgcn_permlane32_swap(f2u(a), f2u(a), false, false); return fmaxf(u2f(r[0]), u2f(r[1])); }
DI float max16(float a) { auto r = __builtin_amdgcn_permlane16_swap(f2u(a), f2u(a), false, false); return fmaxf(u2f(r[0]), u2f(r[1])); }
DI float half_sum(float v) {
  v += dpp<XOR1>(v); v += dpp<XOR2>(v); v += dpp<ROR4>(v); v += dpp<ROR8>(v);
  return half16(v, v);
}
DI u32 plo(u32 k1, u32 k0) { return __builtin_amdgcn_perm(k1, k0, 0x05040100u); }
DI u32 phi(u32 k1, u32 k0) { return __builtin_amdgcn_perm(k1, k0, 0x07060302u); }
DI u32x4 colfrag(const u32x4 (&r)[8], int d) {
  const int q = d >> 1;
  return (d & 1) ? (u32x4){phi(r[1][q], r[0][q]), phi(r[3][q], r[2][q]), phi(r[5][q], r[4][q]), phi(r[7][q], r[6][q])}
                 : (u32x4){plo(r[1][q], r[0][q]), plo(r[3][q], r[2][q]), plo(r[5][q], r[4][q]), plo(r[7][q], r[6][q])};
}
DI u32x4 sel4(bool c, u32x4 v) { return (u32x4){c ? v[0] : 0u, c ? v[1] : 0u, c ? v[2] : 0u, c ? v[3] : 0u}; }
template <typename T> DI T ldnt(const GAS T* p) { return __builtin_nontemporal_load(p); }
DI u32x4 ldws(__amdgpu_buffer_rsrc_t ws, int off) { return __builtin_amdgcn_raw_buffer_load_b128(ws, off, 0, SC1); }
DI void stws(__amdgpu_buffer_rsrc_t ws, int off, u32 a, u32 b, u32 tag) { __builtin_amdgcn_raw_buffer_store_b128((u32x4){a, tag, b, tag}, ws, off, 0, SC1); }
DI bool tagged(u32x4 v, u32 tag) { return v[1] == tag && v[3] == tag; }
#define PIN() do { asm volatile("" ::: "memory"); __builtin_amdgcn_sched_barrier(0); } while (0)
#define LDS_BARRIER() do { asm volatile("s_waitcnt lgkmcnt(0)\n\ts_barrier" ::: "memory"); __builtin_amdgcn_sched_barrier(0); } while (0)
#define RAW_BARRIER() do { __builtin_amdgcn_sched_barrier(0); asm volatile("s_barrier" ::: "memory"); __builtin_amdgcn_sched_barrier(0); } while (0)
DI void dma16(const u16* src, u32 dst) {
  u32 keep;
  asm volatile("s_mov_b32 %0, m0\n\ts_mov_b32 m0, %2\n\ts_nop 0\n\tglobal_load_lds_dwordx4 %1, off nt\n\ts_mov_b32 m0, %0"
               : "=&s"(keep) : "v"(src), "s"(dst) : "memory");
}
template <int N> struct IC { static constexpr int value = N; };

DI void layernorm(u32 (&x)[8]) {
  float v[16], s = 0.f;
#pragma unroll
  for (int e = 0; e < 8; ++e) { v[2 * e] = bflo(x[e]); v[2 * e + 1] = bfhi(x[e]); s += v[2 * e] + v[2 * e + 1]; }
  const float mean = half_sum(s) * (1.f / 512.f);
  float q = 0.f;
#pragma unroll
  for (int e = 0; e < 16; ++e) { const float d = v[e] - mean; q = fmaf(d, d, q); }
  const float var = half_sum(q) * (1.f / 512.f);
  const float mb = rbf(mean), inv = __builtin_amdgcn_rcpf(rbf(__builtin_amdgcn_sqrtf(rbf(rbf(var) + 1e-5f))));
#pragma unroll
  for (int e = 0; e < 8; ++e) { const u32 d = pk(v[2 * e] - mb, v[2 * e + 1] - mb); x[e] = pk(bflo(d) * inv, bfhi(d) * inv); }
}

DI void tile_mfma(const u32x4 (&wv)[8], LAS unsigned char* xin, int g, int c, f32x4& lo, f32x4& hi) {
  const u32x4 xf = *(LAS u32x4*)(xin + ((c >> 2) & 1) * 1024 + ((c >> 3) * 32 + g * 8) * 2);
#pragma unroll
  for (int d = 0; d < 4; ++d) {
    const u32x4 am = sel4((c & 3) == d, xf);
    lo = mfma(am, colfrag(wv, d), lo);
    hi = mfma(am, colfrag(wv, 4 + d), hi);
  }
}
DI f32x2 tile_reduce(f32x4 lo, f32x4 hi, int g, int c) {
  const bool valid = (g >> 1) == (c >> 3);
  float t[4];
#pragma unroll
  for (int e = 0; e < 4; ++e) { t[e] = half32(valid ? lo[e] : 0.f, valid ? hi[e] : 0.f); t[e] += dpp<ROR8>(t[e]); }
  return (c & 8) ? (f32x2){t[2], t[3]} : (f32x2){t[0], t[1]};
}

#define ST(i) do { if (!((STMASK >> (i)) & 1)) break; __builtin_amdgcn_sched_barrier(0); ts[i] = __builtin_amdgcn_s_memrealtime(); __builtin_amdgcn_s_waitcnt(0xC07F); __builtin_amdgcn_sched_barrier(0); } while (0)

__global__ void __launch_bounds__(1024, 4)
fused_decode(const u16* x_, const u16* k_, const u16* v_, const u16* wq_, const u16* wo_, const u16* w1_, const u16* b1_,
             const u16* w2_, const u16* b2_, u16* out_, unsigned char* ws_
             ) {
  __shared__ __attribute__((aligned(16))) unsigned char lds_raw[L_END];
  LAS unsigned char* lds = (LAS unsigned char*)lds_raw;
  u64 ts[12];
  for (int i = 0; i < 12; ++i) ts[i] = 0;
  ST(0);
  asm volatile("" :: "s"(x_), "s"(k_), "s"(v_), "s"(wq_), "s"(wo_), "s"(w1_), "s"(b1_), "s"(w2_), "s"(b2_), "s"(out_), "s"(ws_));
  const int tid = threadIdx.x, lane = tid & 63, w = __builtin_amdgcn_readfirstlane(tid >> 6);
  const int g = lane >> 4, c = lane & 15;
  const int bid = blockIdx.x, kvh = bid & 7, j = bid >> 3, b = j >> 4, sp = j & 15, rb = j >> 2, cb = j & 3;
  const bool mlp = bid < 224;
  const __amdgpu_buffer_rsrc_t ws = __builtin_amdgcn_make_buffer_rsrc(ws_, 0, WS_END, 0x00020000);
  const u32 ep = __hip_atomic_load((GAS u32*)(ws_ + WS_EPOCH), RLX_AGENT);
  const size_t kvbase = ((size_t)(b * 8 + kvh) * SK + sp * 512 + w * 32) * (DH / 8);
  const GAS u32x4* kp = (const GAS u32x4*)k_ + kvbase + (((c >> 1) & 1) * 4 + (c >> 2)) * 16 + g * 2 + (c & 1);
  u32x4 kf[8], vf[8];
  u32 tag;

  const int r2 = bid >> 2, c2 = bid & 3;
  const int cq = j & 7, kq = j >> 3;
  const int c1 = bid >> 3, k8 = bid & 7;
  if (w == 0 || (w == 1 && mlp)) {
    __builtin_amdgcn_s_setprio(3);
    if (w == 0 && lane < 8) *(LAS u32*)(lds + L_FLAG + lane * 4) = 0u;
    const GAS u32x4* xp = (const GAS u32x4*)x_ + lane * 2;
    const u32x4 x0 = xp[0], x1 = xp[1];
    PIN();
    const int rs = w == 0 ? NQ / 8 : FF / 8;
    const u16* tile = w == 0 ? wq_ + (size_t)kq * 128 * NQ + kvh * 512 + cq * 64 : w1_ + k8 * 64 * FF + c1 * 64;
    const GAS u32x4* wp = (const GAS u32x4*)tile + ((c >> 3) * 32 + g * 8) * rs + (c & 7);
    u32x4 wv[2][8];
#pragma unroll
    for (int i = 0; i < 8; ++i) wv[0][i] = ldnt(wp + i * rs);
    if (w == 0) {
#pragma unroll
      for (int i = 0; i < 8; ++i) wv[1][i] = ldnt(wp + (64 + i) * (NQ / 8));
    }
    PIN();
    LDS_BARRIER();
    ST(1);
    u32 xs[8] = {x0[0], x0[1], x0[2], x0[3], x1[0], x1[1], x1[2], x1[3]};
    layernorm(xs);
    if (w == 1) layernorm(xs);
    LAS unsigned char* xl = lds + L_XN + w * 2048;
    *(LAS u32x4*)(xl + lane * 32) = (u32x4){xs[0], xs[1], xs[2], xs[3]};
    *(LAS u32x4*)(xl + lane * 32 + 16) = (u32x4){xs[4], xs[5], xs[6], xs[7]};
    f32x4 lo = {0.f, 0.f, 0.f, 0.f}, hi = lo;
    tile_mfma(wv[0], xl + (w == 0 ? kq * 256 : k8 * 128), g, c, lo, hi);
    __builtin_amdgcn_s_waitcnt(0x0F70);
    if (w == 0) tile_mfma(wv[1], xl + kq * 256 + 128, g, c, lo, hi);
    const f32x2 r = tile_reduce(lo, hi, g, c);
    tag = __builtin_amdgcn_readfirstlane(ep) + 1u;
    const int unit = (g >> 1) * 2 + (c >> 3);
    const int off = w == 0 ? WS_QP + ((((g & 1) * 8 + kvh) * 8 + cq) * 4 + kq) * 512 + ((c & 7) * 4 + unit) * 16
                           : WS_PA + (c1 * 8 + k8) * 1024 + ((c >> 2) & 1) * 512 + (g & 1) * 256 + ((c & 3) * 4 + unit) * 16;
    stws(ws, off, f2u(r[0]), f2u(r[1]), tag);
    ST(2);
  } else {
    __builtin_amdgcn_s_setprio(1);
    if (w == 2 && mlp) {
      const u16* p = w2_ + (r2 * 32 + g * 8) * D + c2 * 128 + c * 8;
#pragma unroll
      for (int i = 0; i < 8; ++i) dma16(p + i * D, (u32)(size_t)(lds + L_W2 + i * 1024));
    }
    RAW_BARRIER();
  }

#pragma unroll
  for (int i = 0; i < 8; ++i) { kf[i] = ldnt(kp + (i >> 1) * 128 + (i & 1) * 8); __builtin_amdgcn_sched_barrier(0); }
  PIN();
  u32 b1v = 0;
  if (w < 4) {
    __builtin_amdgcn_s_setprio(3);
    tag = __builtin_amdgcn_readfirstlane(ep) + 1u;
    const int qoff = WS_QP + (b * 8 + kvh) * 16384 + w * 4096 + lane * 16;
    const int paoff = mlp ? WS_PA + ((r2 >> 1) * 8 + w * 2 + (lane >> 5)) * 1024 + (r2 & 1) * 512 + (lane & 31) * 16 : qoff;
    b1v = *(const GAS u32*)(b1_ + (mlp ? r2 * 32 : 0) + c * 2);
    u32x4 qv[4], pa;
#pragma unroll 1
    for (int spin = 0; spin < SPIN_CAP; ++spin) {
#pragma unroll
      for (int i = 0; i < 4; ++i) qv[i] = ldws(ws, qoff + i * 1024);
      pa = ldws(ws, paoff);
      if (__all(tagged(qv[0], tag) && tagged(qv[1], tag) && tagged(qv[2], tag) && tagged(qv[3], tag) && tagged(pa, tag))) break;
      __builtin_amdgcn_s_sleep(SLEEP_Q);
      asm volatile("" ::: "memory");
    }
    const float q0 = half32(u2f(qv[0][0]) + u2f(qv[1][0]), u2f(qv[2][0]) + u2f(qv[3][0]));
    const float q1 = half32(u2f(qv[0][2]) + u2f(qv[1][2]), u2f(qv[2][2]) + u2f(qv[3][2]));
    *(LAS u32*)(lds + L_Q + w * 256 + lane * 4) = pk(q0, q1);
    *(LAS float*)(lds + L_HP + w * 256 + (lane & 31) * 8 + (lane >> 5) * 4) = half32(u2f(pa[0]), u2f(pa[2]));
    asm volatile("s_waitcnt lgkmcnt(0)" ::: "memory");
    *(volatile LAS u32*)(lds + L_FLAG + w * 4) = 1u;
    ST(3);
  }
  __builtin_amdgcn_s_setprio(0);
  PIN();
  {
    const GAS u32x4* vp = (const GAS u32x4*)v_ + kvbase + lane;
#pragma unroll
    for (int i = 0; i < 8; ++i) { vf[i] = ldnt(vp + i * 64); __builtin_amdgcn_sched_barrier(0); }
  }
  PIN();
  ST(4);
  tag = __builtin_amdgcn_readfirstlane(ep) + 1u;
#pragma unroll 1
  for (int spin = 0; spin < (SPIN_CAP << 4); ++spin) {
    if (__all(*(volatile LAS u32*)(lds + L_FLAG + (lane & 3) * 4) != 0u)) break;
    __builtin_amdgcn_s_sleep(2);
  }
  PIN();
  if (w == 2 && mlp) {
    f32x2 t2 = *(LAS f32x2*)(lds + L_HP + (lane & 31) * 8);
#pragma unroll
    for (int i = 1; i < 4; ++i) t2 += *(LAS f32x2*)(lds + L_HP + i * 256 + (lane & 31) * 8);
    float hv[2];
#pragma unroll
    for (int e = 0; e < 2; ++e) {
      const float t = rbf(rbf(t2[e]) + (e ? bfhi(b1v) : bflo(b1v)));
      const float uu = 0.7978845608028654f * (t + 0.044715f * t * t * t);
      hv[e] = t / (1.f + __builtin_amdgcn_exp2f(-2.f * LOG2E * uu));
    }
    *(LAS u32*)(lds + L_HS + (lane & 31) * 4) = pk(hv[0], hv[1]);
    const u32x4 ha = *(LAS u32x4*)(lds + L_HS + ((c >> 2) & 1) * 64 + g * 16);
    u32x4 w2t[8];
#pragma unroll
    for (int i = 0; i < 8; ++i) w2t[i] = *(LAS u32x4*)(lds + L_W2 + i * 1024 + lane * 16);
    f32x4 r = {0.f, 0.f, 0.f, 0.f};
#pragma unroll
    for (int d = 0; d < 8; ++d) r = mfma(sel4((c >> 3) == (d >> 2) && (c & 3) == (d & 3), ha), colfrag(w2t, d), r);
    const int off = WS_MP + (c2 * 56 + r2) * 2048 + (g & 1) * 1024 + (c * 8 + (g >> 1) * 4) * 8;
    stws(ws, off, f2u(r[0]), f2u(r[1]), tag);
    stws(ws, off + 16, f2u(r[2]), f2u(r[3]), tag);
    ST(2);
  }
  PIN();

  float sc[8];
  {
    const int x0 = (c >> 2) & 1;
    const u32x4 q0 = *(LAS u32x4*)(lds + L_Q + (c & 3) * 256 + (g * 2 + x0) * 16);
    const u32x4 q1 = *(LAS u32x4*)(lds + L_Q + (c & 3) * 256 + 128 + (g * 2 + x0) * 16);
    constexpr float C2 = 0.08838834764831845f * LOG2E;
#pragma unroll
    for (int o = 0; o < 4; ++o) {
      f32x4 s = mfma(kf[2 * o], q0, (f32x4){0.f, 0.f, 0.f, 0.f});
      s = mfma(kf[2 * o + 1], q1, s);
#pragma unroll
      for (int a = 0; a < 2; ++a) {
        float e1 = s[2 * a + 1];
        asm("" : "+v"(e1));
        const float t = x0 ? e1 : s[2 * a];
        sc[2 * o + a] = (t + dpp<ROR4>(t)) * C2;
      }
    }
  }
  PIN();

    float m = fmaxf(fmaxf(fmaxf(sc[0], sc[1]), fmaxf(sc[2], sc[3])), fmaxf(fmaxf(sc[4], sc[5]), fmaxf(sc[6], sc[7])));
    m = max32(max16(m));
    float p[8], l = 0.f;
#pragma unroll
    for (int e = 0; e < 8; ++e) { p[e] = __builtin_amdgcn_exp2f(sc[e] - m); l += p[e]; }
    l = half16(l, l); l = half32(l, l);
    if (lane < 4) { *(LAS float*)(lds + L_MW + (lane * 16 + w) * 4) = m; *(LAS float*)(lds + L_LW + (lane * 16 + w) * 4) = l; }
    PIN();
    if (lane == 0) __hip_atomic_fetch_add((LAS u32*)(lds + L_CNT), 1u, __ATOMIC_RELAXED, __HIP_MEMORY_SCOPE_WORKGROUP);
    const u32x4 pf = {pk(p[0], p[1]), pk(p[2], p[3]), pk(p[4], p[5]), pk(p[6], p[7])};
    u32x4 am[4];
#pragma unroll
    for (int u = 0; u < 4; ++u) am[u] = sel4((c >> 2) == u, pf);
    PIN();
    u32x4 ve[4], vo[4];
    u32 slo, shi;
    asm volatile("s_mov_b32 %0, 0x05040100\n\ts_mov_b32 %1, 0x07060302" : "=s"(slo), "=s"(shi) : "v"(am[0][0]), "v"(am[1][1]), "v"(am[2][2]), "v"(am[3][3]));
#pragma unroll
    for (int r = 0; r < 4; ++r) {
#pragma unroll
      for (int u = 0; u < 4; ++u) {
        ve[u][r] = __builtin_amdgcn_perm(vf[2 * r + 1][u], vf[2 * r][u], slo);
        vo[u][r] = __builtin_amdgcn_perm(vf[2 * r + 1][u], vf[2 * r][u], shi);
      }
      if (r < 3)
        asm volatile("" : "+s"(slo), "+s"(shi) : "v"(ve[0][r]), "v"(ve[1][r]), "v"(ve[2][r]), "v"(ve[3][r]), "v"(vo[0][r]), "v"(vo[1][r]), "v"(vo[2][r]), "v"(vo[3][r]));
    }
    f32x4 oe = {0.f, 0.f, 0.f, 0.f}, oo = oe;
#pragma unroll
    for (int u = 0; u < 4; ++u) { oe = mfma(am[u], ve[u], oe); oo = mfma(am[u], vo[u], oo); }
#pragma unroll
    for (int h = 0; h < 4; ++h) *(LAS f32x2*)(lds + L_OW + w * OW_STRIDE + h * 512 + c * 32 + g * 8) = (f32x2){oe[h], oo[h]};
    PIN();
    ST(5);

  const bool gath = (w & 4) == 0;
  const int v = (w & 3) + (w >> 3) * 4;
  auto wait_ml = [&]() __attribute__((always_inline)) {
#pragma unroll 1
    for (int spin = 0; spin < (SPIN_CAP << 4); ++spin) {
      if (__builtin_amdgcn_readfirstlane(*(volatile LAS u32*)(lds + L_CNT)) == 16u) break;
      __builtin_amdgcn_s_sleep(1);
    }
    PIN();
  };
  auto load_wo = [&](u32x4 (&wt)[8]) __attribute__((always_inline)) {
    const GAS u16* p = (const GAS u16*)wo_ + (size_t)(kvh * 512 + rb * 64 + g * 4) * D + cb * 128 + (w - 4) * 64 + c * 4;
#pragma unroll
    for (int i = 0; i < 8; ++i) {
      const u32x2 t0 = ldnt((const GAS u32x2*)(p + (size_t)(32 * (i >> 2) + (i & 3)) * D));
      const u32x2 t1 = ldnt((const GAS u32x2*)(p + (size_t)(32 * (i >> 2) + 16 + (i & 3)) * D));
      wt[i] = (u32x4){t0[0], t0[1], t1[0], t1[1]};
    }
    PIN();
  };
  const int cf = bid & 3, bf = (bid >> 2) & 1, seg = bid >> 3;
  if (gath) {
    if (w < 4 && bid < 64) {
      const int moff = WS_MP + (cf * 56 + w * 14 + (lane >> 3)) * 2048 + bf * 1024 + seg * 128 + (lane & 7) * 16;
      const int moff2 = lane < 48 ? moff + 16384 : moff;
      u32x4 m0, m1;
#pragma unroll 1
      for (int spin = 0; spin < SPIN_CAP; ++spin) {
        m0 = ldws(ws, moff); m1 = ldws(ws, moff2);
        if (__all(tagged(m0, tag) && tagged(m1, tag))) break;
        __builtin_amdgcn_s_sleep(SLEEP_Q);
        asm volatile("" ::: "memory");
      }
      m1 = sel4(lane < 48, m1);
      float t = half32(u2f(m0[0]) + u2f(m1[0]), u2f(m0[2]) + u2f(m1[2]));
      t = half16(t, t);
      t += dpp<ROR8>(t);
      if ((lane & 24) == 0) *(LAS float*)(lds + L_MF + w * 64 + (lane & 7) * 8 + (lane >> 5) * 4) = t;
    }
    {
      const int f = v * 16 + (lane >> 2), p4 = lane & 3, hd = v >> 1;
      wait_ml();
      const f32x4 mv = *(LAS f32x4*)(lds + L_MW + hd * 64 + p4 * 16), lv = *(LAS f32x4*)(lds + L_LW + hd * 64 + p4 * 16);
      float M = fmaxf(fmaxf(mv[0], mv[1]), fmaxf(mv[2], mv[3]));
      M = fmaxf(M, dpp<XOR1>(M)); M = fmaxf(M, dpp<XOR2>(M));
      float L = 0.f, fw[4];
#pragma unroll
      for (int i = 0; i < 4; ++i) { fw[i] = __builtin_amdgcn_exp2f(mv[i] - M); L = fmaf(fw[i], lv[i], L); }
      L += dpp<XOR1>(L); L += dpp<XOR2>(L);
      const float inv = __builtin_amdgcn_rcpf(L);
#pragma unroll
      for (int i = 0; i < 4; ++i) fw[i] *= inv;
      const u32 lam = f2u(M + __builtin_amdgcn_logf(L)) >> ((lane >> 5) * 16);
      u32 t0 = (tag << 8) | (lam & 255u), t1 = (tag << 8) | ((lam >> 8) & 255u);
      int ooff = WS_OP + ((kvh * 2 + b) * 16 + sp) * 2048 + f * 16;
      int laddr = L_OW + p4 * 4 * OW_STRIDE + f * 16;
      asm volatile("" : "+v"(fw[0]), "+v"(fw[1]), "+v"(fw[2]), "+v"(fw[3]), "+v"(t0), "+v"(t1), "+v"(ooff), "+v"(laddr));
      LDS_BARRIER();
      ST(6);
      f32x4 s = fw[0] * *(LAS f32x4*)(lds + laddr);
#pragma unroll
      for (int i = 1; i < 4; ++i) s += fw[i] * *(LAS f32x4*)(lds + laddr + i * OW_STRIDE);
#pragma unroll
      for (int e = 0; e < 4; ++e) s[e] += dpp<XOR1>(s[e]);
#pragma unroll
      for (int e = 0; e < 4; ++e) s[e] += dpp<XOR2>(s[e]);
      if (p4 == 0) __builtin_amdgcn_raw_buffer_store_b128((u32x4){pk(s[0], s[1]), t0, pk(s[2], s[3]), t1}, ws, ooff, 0, SC1);
    }
    PIN();
    ST(7);
    {
      const int ooff = WS_OP + (kvh * 32 + v * 4 + g) * 2048 + rb * 256 + c * 16;
      u32x4 ov;
      bool ok = false;
#pragma unroll 1
      for (int spin = 0; spin < SPIN_CAP; ++spin) {
        if (!ok) { ov = ldws(ws, ooff); ok = ((ov[1] ^ (tag << 8)) | (ov[3] ^ (tag << 8))) < 256u; }
        if (__all(ok)) break;
        if (SLEEP_O) __builtin_amdgcn_s_sleep(SLEEP_O);
        asm volatile("" ::: "memory");
      }
      ST(8);
      const u32 h16 = __builtin_amdgcn_perm(ov[3], ov[1], 0x04000400u), oth = f2u(dpp<ROR8>(u2f(h16)));
      const float lam = u2f((c & 8) ? plo(h16, oth) : plo(oth, h16));
      const float G = max32(max16(lam));
      const float wg = __builtin_amdgcn_exp2f(lam - G);
      float L = half16(wg, wg);
      L = half32(L, L);
      const float t = half16(half32(wg * bflo(ov[0]), wg * bflo(ov[2])), half32(wg * bfhi(ov[0]), wg * bfhi(ov[2])));
      *(LAS float*)(lds + L_GP + v * GP_STRIDE + (c * 4 + (g >> 1) * 2 + (g & 1)) * 4) = t;
      if (lane == 0) *(LAS f32x2*)(lds + L_GP + v * GP_STRIDE + 256) = (f32x2){G, L};
    }
    LDS_BARRIER();
    ST(9);
    if (bid < 64) {
      const int soff = WS_S2 + (cf * 64 + v * 8 + (lane >> 3)) * 2048 + bf * 1024 + seg * 128 + (lane & 7) * 16;
      u32x4 sv;
      bool ok = false;
#pragma unroll 1
      for (int spin = 0; spin < SPIN_CAP; ++spin) {
        if (!ok) { sv = ldws(ws, soff); ok = tagged(sv, tag); }
        if (__all(ok)) break;
        if (SLEEP_S) __builtin_amdgcn_s_sleep(SLEEP_S);
        asm volatile("" ::: "memory");
      }
      ST(10);
      float t = half32(u2f(sv[0]), u2f(sv[2]));
      t = half16(t, t);
      t += dpp<ROR8>(t);
      if ((lane & 24) == 0) *(LAS float*)(lds + L_FP + v * 64 + (lane & 7) * 8 + (lane >> 5) * 4) = t;
      LDS_BARRIER();
    }
  } else if (w < 6) {
    u32x4 wt[8], bw[8];
    if (WO_MODE == 1) wait_ml();
    if (WO_MODE < 2) load_wo(wt);
    LDS_BARRIER();
    ST(6);
    if (WO_MODE == 2) { if (WO_SLEEP) __builtin_amdgcn_s_sleep(WO_SLEEP); load_wo(wt); }
#pragma unroll
    for (int i = 0; i < 8; ++i) bw[i] = colfrag(wt, i);
    asm volatile("" :: "v"(bw[0]), "v"(bw[1]), "v"(bw[2]), "v"(bw[3]), "v"(bw[4]), "v"(bw[5]), "v"(bw[6]), "v"(bw[7]));
    LDS_BARRIER();
    ST(9);
    const int tt = c >> 3;
    LAS unsigned char* gp = lds + L_GP + (c & 7) * GP_STRIDE;
    const f32x2 gl = *(LAS f32x2*)(gp + 256);
    const f32x4 o0 = *(LAS f32x4*)(gp + (tt * 16 + g * 4) * 4), o1 = *(LAS f32x4*)(gp + (32 + tt * 16 + g * 4) * 4);
    float G = fmaxf(gl[0], dpp<XOR1>(gl[0]));
    G = fmaxf(G, dpp<XOR2>(G));
    const float fw = __builtin_amdgcn_exp2f(gl[0] - G);
    float L = fw * gl[1];
    L += dpp<XOR1>(L); L += dpp<XOR2>(L);
    const float sc2 = fw * __builtin_amdgcn_rcpf(L);
    float o[8];
#pragma unroll
    for (int e = 0; e < 8; ++e) { float t = sc2 * (e < 4 ? o0[e & 3] : o1[e & 3]); t += dpp<XOR1>(t); o[e] = t + dpp<XOR2>(t); }
    const u32x4 of = {pk(o[0], o[1]), pk(o[2], o[3]), pk(o[4], o[5]), pk(o[6], o[7])};
    float r[4];
#pragma unroll
    for (int d = 0; d < 4; ++d) {
      const float a0 = mfma(of, bw[d], (f32x4){0.f, 0.f, 0.f, 0.f})[0], a1 = mfma(of, bw[4 + d], (f32x4){0.f, 0.f, 0.f, 0.f})[0];
      r[d] = (g >> 1) ? a1 : a0;
    }
    const float r0 = half32(r[0], r[2]), r1 = half32(r[1], r[3]);
    stws(ws, WS_S2 + (cb * 64 + kvh * 8 + rb) * 2048 + (g & 1) * 1024 + ((w - 4) * 64 + c * 4 + (g >> 1) * 2) * 8, f2u(r0), f2u(r1), tag);
    ST(10);
  } else if (w == 6 && bid < 64) {
    const int col = cf * 128 + seg * 16 + c;
    const u32 xv = ((const GAS u16*)x_)[bf * D + col], b2v = ((const GAS u16*)b2_)[col];
    LDS_BARRIER();
    ST(6);
    float t = *(LAS float*)(lds + L_MF + lane * 4);
    t = half16(t, t);
    t = half32(t, t);
    t = rbf(rbf(t) + bflo(b2v));
    float gate = rbf(t / (1.f + __builtin_amdgcn_exp2f(-LOG2E * t))), xf = bflo(xv);
    asm volatile("" : "+v"(gate), "+v"(xf));
    LDS_BARRIER();
    LDS_BARRIER();
    ST(10);
    float s = *(LAS float*)(lds + L_FP + lane * 4) + *(LAS float*)(lds + L_FP + 256 + lane * 4);
    s = half16(s, s);
    s = half32(s, s);
    const float res = xf + rbf(rbf(s) * gate);
    if (g == 0) ((GAS u16*)out_)[bf * D + col] = (u16)pk(res, res);
    if (bid == 0 && lane == 0) __hip_atomic_store((GAS u32*)(ws_ + WS_EPOCH), tag, RLX_AGENT);
  } else {
    LDS_BARRIER();
    ST(6);
  }
  ST(11);
  if (ts[PROBE] && (w & 4) == 0) {
    const u64 until = ts[0] + 4000 + (ts[PROBE] - ts[0]);
    while (__builtin_amdgcn_s_memrealtime() < until) __builtin_amdgcn_s_sleep(1);
  }
}

extern "C" void kernel_launch(void* const* d_in, const int* in_sizes, int n_in, void* d_out, int out_size, void* d_ws, size_t ws_size,
                              hipStream_t stream) {
  unsigned char* ws = (unsigned char*)d_ws + (WS_AT_END ? ((ws_size - WS_SPAN) & ~(size_t)4095) : 0);
  fused_decode<<<256, 1024, 0, stream>>>((const u16*)d_in[0], (const u16*)d_in[1], (const u16*)d_in[2], (const u16*)d_in[3],
                                         (const u16*)d_in[4], (const u16*)d_in[6], (const u16*)d_in[7], (const u16*)d_in[8],
                                         (const u16*)d_in[9], (u16*)d_out, ws
                                         );
}
```

```cpp
#define STAMP 1
#define PROBE 0
#define STMASK 1
#include <hip/hip_runtime.h>
#include <stdint.h>

#define WS_AT_END 0
#define SLEEP_Q 8
#define SLEEP_O 0
#define SLEEP_S 0
#define WO_MODE 1
#define WO_SLEEP 0
#define ABL 0

#define GAS __attribute__((address_space(1)))
#define LAS __attribute__((address_space(3)))
#define DI __device__ __forceinline__
#define RLX_AGENT __ATOMIC_RELAXED, __HIP_MEMORY_SCOPE_AGENT
typedef unsigned short u16;
typedef unsigned u32;
typedef unsigned long long u64;
typedef u32 u32x2 __attribute__((ext_vector_type(2)));
typedef u32 u32x4 __attribute__((ext_vector_type(4)));
typedef float f32x2 __attribute__((ext_vector_type(2)));
typedef float f32x4 __attribute__((ext_vector_type(4)));
typedef __bf16 bf16x2 __attribute__((ext_vector_type(2)));
typedef __bf16 bf16x8 __attribute__((ext_vector_type(8)));

constexpr int D = 512, SK = 8192, DH = 128, FF = 1792, NQ = 4096;
constexpr int SC1 = 16;
constexpr int SPIN_CAP = 1 << 18;
constexpr float LOG2E = 1.4426950408889634f;

constexpr int WS_EPOCH = 0;
constexpr int WS_QP = 256;
constexpr int WS_PA = WS_QP + 256 * 1024;
constexpr int WS_OP = WS_PA + 224 * 1024;
constexpr int WS_S2 = WS_OP + 256 * 2048;
constexpr int WS_MP = WS_S2 + 256 * 2048;
constexpr int WS_END = WS_MP + 224 * 2048;
constexpr int WS_SPAN = 4 << 20;
constexpr int WS_STAMP = 8 << 20;

constexpr int L_FLAG = 0, L_CNT = 16, L_XN = 32, L_Y = L_XN + 2048, L_Q = L_Y + 2048, L_MW = L_Q + 1024, L_LW = L_MW + 256,
              L_HP = L_LW + 256, L_HS = L_HP + 1024, L_GP = L_HS + 128, GP_STRIDE = 272, L_FP = L_GP + 8 * GP_STRIDE,
              L_MF = L_FP + 512, L_W2 = L_MF + 256, L_OW = L_W2 + 8192, OW_STRIDE = 2064, L_END = L_OW + 16 * OW_STRIDE;

DI float bflo(u32 v) { return __builtin_bit_cast(float, v << 16); }
DI float bfhi(u32 v) { return __builtin_bit_cast(float, v & 0xffff0000u); }
DI float rbf(float f) { return (float)(__bf16)f; }
DI u32 pk(float a, float b) { return __builtin_bit_cast(u32, __builtin_convertvector((f32x2){a, b}, bf16x2)); }
DI u32 f2u(float f) { return __builtin_bit_cast(u32, f); }
DI float u2f(u32 u) { return __builtin_bit_cast(float, u); }
DI f32x4 mfma(u32x4 a, u32x4 b, f32x4 c) {
  return __builtin_amdgcn_mfma_f32_16x16x32_bf16(__builtin_bit_cast(bf16x8, a), __builtin_bit_cast(bf16x8, b), c, 0, 0, 0);
}
template <int CTRL> DI float dpp(float x) { return u2f(__builtin_amdgcn_mov_dpp(f2u(x), CTRL, 0xf, 0xf, true)); }
constexpr int XOR1 = 0xB1, XOR2 = 0x4E, ROR4 = 0x124, ROR8 = 0x128;
DI float half32(float a, float b) { auto r = __builtin_amdgcn_permlane32_swap(f2u(a), f2u(b), false, false); return u2f(r[0]) + u2f(r[1]); }
DI float half16(float a, float b) { auto r = __builtin_amdgcn_permlane16_swap(f2u(a), f2u(b), false, false); return u2f(r[0]) + u2f(r[1]); }
DI float max32(float a) { auto r = __builtin_amdgcn_permlane32_swap(f2u(a), f2u(a), false, false); return fmaxf(u2f(r[0]), u2f(r[1])); }
DI float max16(float a) { auto r = __builtin_amdgcn_permlane16_swap(f2u(a), f2u(a), false, false); return fmaxf(u2f(r[0]), u2f(r[1])); }
DI float half_sum(float v) {
  v += dpp<XOR1>(v); v += dpp<XOR2>(v); v += dpp<ROR4>(v); v += dpp<ROR8>(v);
  return half16(v, v);
}
DI u32 plo(u32 k1, u32 k0) { return __builtin_amdgcn_perm(k1, k0, 0x05040100u); }
DI u32 phi(u32 k1, u32 k0) { return __builtin_amdgcn_perm(k1, k0, 0x07060302u); }
DI u32x4 colfrag(const u32x4 (&r)[8], int d) {
  const int q = d >> 1;
  return (d & 1) ? (u32x4){phi(r[1][q], r[0][q]), phi(r[3][q], r[2][q]), phi(r[5][q], r[4][q]), phi(r[7][q], r[6][q])}
                 : (u32x4){plo(r[1][q], r[0][q]), plo(r[3][q], r[2][q]), plo(r[5][q], r[4][q]), plo(r[7][q], r[6][q])};
}
DI u32x4 sel4(bool c, u32x4 v) { return (u32x4){c ? v[0] : 0u, c ? v[1] : 0u, c ? v[2] : 0u, c ? v[3] : 0u}; }
template <typename T> DI T ldnt(const GAS T* p) { return __builtin_nontemporal_load(p); }
DI u32x4 ldws(__amdgpu_buffer_rsrc_t ws, int off) { return __builtin_amdgcn_raw_buffer_load_b128(ws, off, 0, SC1); }
DI void stws(__amdgpu_buffer_rsrc_t ws, int off, u32 a, u32 b, u32 tag) { __builtin_amdgcn_raw_buffer_store_b128((u32x4){a, tag, b, tag}, ws, off, 0, SC1); }
DI bool tagged(u32x4 v, u32 tag) { return v[1] == tag && v[3] == tag; }
#define PIN() do { asm volatile("" ::: "memory"); __builtin_amdgcn_sched_barrier(0); } while (0)
#define LDS_BARRIER() do { asm volatile("s_waitcnt lgkmcnt(0)\n\ts_barrier" ::: "memory"); __builtin_amdgcn_sched_barrier(0); } while (0)
#define RAW_BARRIER() do { __builtin_amdgcn_sched_barrier(0); asm volatile("s_barrier" ::: "memory"); __builtin_amdgcn_sched_barrier(0); } while (0)
DI void dma16(const u16* src, u32 dst) {
  u32 keep;
  asm volatile("s_mov_b32 %0, m0\n\ts_mov_b32 m0, %2\n\ts_nop 0\n\tglobal_load_lds_dwordx4 %1, off nt\n\ts_mov_b32 m0, %0"
               : "=&s"(keep) : "v"(src), "s"(dst) : "memory");
}
template <int N> struct IC { static constexpr int value = N; };

DI void layernorm(u32 (&x)[8]) {
  float v[16], s = 0.f;
#pragma unroll
  for (int e = 0; e < 8; ++e) { v[2 * e] = bflo(x[e]); v[2 * e + 1] = bfhi(x[e]); s += v[2 * e] + v[2 * e + 1]; }
  const float mean = half_sum(s) * (1.f / 512.f);
  float q = 0.f;
#pragma unroll
  for (int e = 0; e < 16; ++e) { const float d = v[e] - mean; q = fmaf(d, d, q); }
  const float var = half_sum(q) * (1.f / 512.f);
  const float mb = rbf(mean), inv = __builtin_amdgcn_rcpf(rbf(__builtin_amdgcn_sqrtf(rbf(rbf(var) + 1e-5f))));
#pragma unroll
  for (int e = 0; e < 8; ++e) { const u32 d = pk(v[2 * e] - mb, v[2 * e + 1] - mb); x[e] = pk(bflo(d) * inv, bfhi(d) * inv); }
}

DI void tile_mfma(const u32x4 (&wv)[8], LAS unsigned char* xin, int g, int c, f32x4& lo, f32x4& hi) {
  const u32x4 xf = *(LAS u32x4*)(xin + ((c >> 2) & 1) * 1024 + ((c >> 3) * 32 + g * 8) * 2);
#pragma unroll
  for (int d = 0; d < 4; ++d) {
    const u32x4 am = sel4((c & 3) == d, xf);
    lo = mfma(am, colfrag(wv, d), lo);
    hi = mfma(am, colfrag(wv, 4 + d), hi);
  }
}
DI f32x2 tile_reduce(f32x4 lo, f32x4 hi, int g, int c) {
  const bool valid = (g >> 1) == (c >> 3);
  float t[4];
#pragma unroll
  for (int e = 0; e < 4; ++e) { t[e] = half32(valid ? lo[e] : 0.f, valid ? hi[e] : 0.f); t[e] += dpp<ROR8>(t[e]); }
  return (c & 8) ? (f32x2){t[2], t[3]} : (f32x2){t[0], t[1]};
}

#define ST(i) do { if (!((STMASK >> (i)) & 1)) break; __builtin_amdgcn_sched_barrier(0); ts[i] = __builtin_amdgcn_s_memrealtime(); __builtin_amdgcn_s_waitcnt(0xC07F); __builtin_amdgcn_sched_barrier(0); } while (0)

__global__ void __launch_bounds__(1024, 4)
fused_decode(const u16* x_, const u16* k_, const u16* v_, const u16* wq_, const u16* wo_, const u16* w1_, const u16* b1_,
             const u16* w2_, const u16* b2_, u16* out_, unsigned char* ws_
             ) {
  __shared__ __attribute__((aligned(16))) unsigned char lds_raw[L_END];
  LAS unsigned char* lds = (LAS unsigned char*)lds_raw;
  u64 ts[12];
  for (int i = 0; i < 12; ++i) ts[i] = 0;
  ST(0);
  asm volatile("" :: "s"(x_), "s"(k_), "s"(v_), "s"(wq_), "s"(wo_), "s"(w1_), "s"(b1_), "s"(w2_), "s"(b2_), "s"(out_), "s"(ws_));
  const int tid = threadIdx.x, lane = tid & 63, w = __builtin_amdgcn_readfirstlane(tid >> 6);
  const int g = lane >> 4, c = lane & 15;
  const int bid = blockIdx.x, kvh = bid & 7, j = bid >> 3, b = j >> 4, sp = j & 15, rb = j >> 2, cb = j & 3;
  const bool mlp = bid < 224;
  const __amdgpu_buffer_rsrc_t ws = __builtin_amdgcn_make_buffer_rsrc(ws_, 0, WS_END, 0x00020000);
  const u32 ep = __hip_atomic_load((GAS u32*)(ws_ + WS_EPOCH), RLX_AGENT);
  const size_t kvbase = ((size_t)(b * 8 + kvh) * SK + sp * 512 + w * 32) * (DH / 8);
  const GAS u32x4* kp = (const GAS u32x4*)k_ + kvbase + (((c >> 1) & 1) * 4 + (c >> 2)) * 16 + g * 2 + (c & 1);
  u32x4 kf[8], vf[8];
  u32 tag;

  const int r2 = bid >> 2, c2 = bid & 3;
  const int cq = j & 7, kq = j >> 3;
  const int c1 = bid >> 3, k8 = bid & 7;
  if (w == 0 || (w == 1 && mlp)) {
    __builtin_amdgcn_s_setprio(3);
    if (w == 0 && lane < 8) *(LAS u32*)(lds + L_FLAG + lane * 4) = 0u;
    const GAS u32x4* xp = (const GAS u32x4*)x_ + lane * 2;
    const u32x4 x0 = xp[0], x1 = xp[1];
    PIN();
    const int rs = w == 0 ? NQ / 8 : FF / 8;
    const u16* tile = w == 0 ? wq_ + (size_t)kq * 128 * NQ + kvh * 512 + cq * 64 : w1_ + k8 * 64 * FF + c1 * 64;
    const GAS u32x4* wp = (const GAS u32x4*)tile + ((c >> 3) * 32 + g * 8) * rs + (c & 7);
    u32x4 wv[2][8];
#pragma unroll
    for (int i = 0; i < 8; ++i) wv[0][i] = ldnt(wp + i * rs);
    if (w == 0) {
#pragma unroll
      for (int i = 0; i < 8; ++i) wv[1][i] = ldnt(wp + (64 + i) * (NQ / 8));
    }
    PIN();
    LDS_BARRIER();
    ST(1);
    u32 xs[8] = {x0[0], x0[1], x0[2], x0[3], x1[0], x1[1], x1[2], x1[3]};
    layernorm(xs);
    if (w == 1) layernorm(xs);
    LAS unsigned char* xl = lds + L_XN + w * 2048;
    *(LAS u32x4*)(xl + lane * 32) = (u32x4){xs[0], xs[1], xs[2], xs[3]};
    *(LAS u32x4*)(xl + lane * 32 + 16) = (u32x4){xs[4], xs[5], xs[6], xs[7]};
    f32x4 lo = {0.f, 0.f, 0.f, 0.f}, hi = lo;
    tile_mfma(wv[0], xl + (w == 0 ? kq * 256 : k8 * 128), g, c, lo, hi);
    __builtin_amdgcn_s_waitcnt(0x0F70);
    if (w == 0) tile_mfma(wv[1], xl + kq * 256 + 128, g, c, lo, hi);
    const f32x2 r = tile_reduce(lo, hi, g, c);
    tag = __builtin_amdgcn_readfirstlane(ep) + 1u;
    const int unit = (g >> 1) * 2 + (c >> 3);
    const int off = w == 0 ? WS_QP + ((((g & 1) * 8 + kvh) * 8 + cq) * 4 + kq) * 512 + ((c & 7) * 4 + unit) * 16
                           : WS_PA + (c1 * 8 + k8) * 1024 + ((c >> 2) & 1) * 512 + (g & 1) * 256 + ((c & 3) * 4 + unit) * 16;
    stws(ws, off, f2u(r[0]), f2u(r[1]), tag);
    ST(2);
  } else {
    __builtin_amdgcn_s_setprio(1);
    if (w == 2 && mlp) {
      const u16* p = w2_ + (r2 * 32 + g * 8) * D + c2 * 128 + c * 8;
#pragma unroll
      for (int i = 0; i < 8; ++i) dma16(p + i * D, (u32)(size_t)(lds + L_W2 + i * 1024));
    }
    RAW_BARRIER();
  }

#pragma unroll
  for (int i = 0; i < 8; ++i) { kf[i] = ldnt(kp + (i >> 1) * 128 + (i & 1) * 8); __builtin_amdgcn_sched_barrier(0); }
  PIN();
  u32 b1v = 0;
  if (w < 4) {
    __builtin_amdgcn_s_setprio(3);
    tag = __builtin_amdgcn_readfirstlane(ep) + 1u;
    const int qoff = WS_QP + (b * 8 + kvh) * 16384 + w * 4096 + lane * 16;
    const int paoff = mlp ? WS_PA + ((r2 >> 1) * 8 + w * 2 + (lane >> 5)) * 1024 + (r2 & 1) * 512 + (lane & 31) * 16 : qoff;
    b1v = *(const GAS u32*)(b1_ + (mlp ? r2 * 32 : 0) + c * 2);
    u32x4 qv[4], pa;
#pragma unroll 1
    for (int spin = 0; spin < SPIN_CAP; ++spin) {
#pragma unroll
      for (int i = 0; i < 4; ++i) qv[i] = ldws(ws, qoff + i * 1024);
      pa = ldws(ws, paoff);
      if (__all(tagged(qv[0], tag) && tagged(qv[1], tag) && tagged(qv[2], tag) && tagged(qv[3], tag) && tagged(pa, tag))) break;
      __builtin_amdgcn_s_sleep(SLEEP_Q);
      asm volatile("" ::: "memory");
    }
    const float q0 = half32(u2f(qv[0][0]) + u2f(qv[1][0]), u2f(qv[2][0]) + u2f(qv[3][0]));
    const float q1 = half32(u2f(qv[0][2]) + u2f(qv[1][2]), u2f(qv[2][2]) + u2f(qv[3][2]));
    *(LAS u32*)(lds + L_Q + w * 256 + lane * 4) = pk(q0, q1);
    *(LAS float*)(lds + L_HP + w * 256 + (lane & 31) * 8 + (lane >> 5) * 4) = half32(u2f(pa[0]), u2f(pa[2]));
    asm volatile("s_waitcnt lgkmcnt(0)" ::: "memory");
    *(volatile LAS u32*)(lds + L_FLAG + w * 4) = 1u;
    ST(3);
  }
  __builtin_amdgcn_s_setprio(0);
  PIN();
  {
    const GAS u32x4* vp = (const GAS u32x4*)v_ + kvbase + lane;
#pragma unroll
    for (int i = 0; i < 8; ++i) { vf[i] = ldnt(vp + i * 64); __builtin_amdgcn_sched_barrier(0); }
  }
  PIN();
  ST(4);
  tag = __builtin_amdgcn_readfirstlane(ep) + 1u;
#pragma unroll 1
  for (int spin = 0; spin < (SPIN_CAP << 4); ++spin) {
    if (__all(*(volatile LAS u32*)(lds + L_FLAG + (lane & 3) * 4) != 0u)) break;
    __builtin_amdgcn_s_sleep(2);
  }
  PIN();
  if (w == 2 && mlp) {
    f32x2 t2 = *(LAS f32x2*)(lds + L_HP + (lane & 31) * 8);
#pragma unroll
    for (int i = 1; i < 4; ++i) t2 += *(LAS f32x2*)(lds + L_HP + i * 256 + (lane & 31) * 8);
    float hv[2];
#pragma unroll
    for (int e = 0; e < 2; ++e) {
      const float t = rbf(rbf(t2[e]) + (e ? bfhi(b1v) : bflo(b1v)));
      const float uu = 0.7978845608028654f * (t + 0.044715f * t * t * t);
      hv[e] = t / (1.f + __builtin_amdgcn_exp2f(-2.f * LOG2E * uu));
    }
    *(LAS u32*)(lds + L_HS + (lane & 31) * 4) = pk(hv[0], hv[1]);
    const u32x4 ha = *(LAS u32x4*)(lds + L_HS + ((c >> 2) & 1) * 64 + g * 16);
    u32x4 w2t[8];
#pragma unroll
    for (int i = 0; i < 8; ++i) w2t[i] = *(LAS u32x4*)(lds + L_W2 + i * 1024 + lane * 16);
    f32x4 r = {0.f, 0.f, 0.f, 0.f};
#pragma unroll
    for (int d = 0; d < 8; ++d) r = mfma(sel4((c >> 3) == (d >> 2) && (c & 3) == (d & 3), ha), colfrag(w2t, d), r);
    const int off = WS_MP + (c2 * 56 + r2) * 2048 + (g & 1) * 1024 + (c * 8 + (g >> 1) * 4) * 8;
    stws(ws, off, f2u(r[0]), f2u(r[1]), tag);
    stws(ws, off + 16, f2u(r[2]), f2u(r[3]), tag);
    ST(2);
  }
  PIN();

  float sc[8];
  {
    const int x0 = (c >> 2) & 1;
    const u32x4 q0 = *(LAS u32x4*)(lds + L_Q + (c & 3) * 256 + (g * 2 + x0) * 16);
    const u32x4 q1 = *(LAS u32x4*)(lds + L_Q + (c & 3) * 256 + 128 + (g * 2 + x0) * 16);
    constexpr float C2 = 0.08838834764831845f * LOG2E;
#pragma unroll
    for (int o = 0; o < 4; ++o) {
      f32x4 s = mfma(kf[2 * o], q0, (f32x4){0.f, 0.f, 0.f, 0.f});
      s = mfma(kf[2 * o + 1], q1, s);
#pragma unroll
      for (int a = 0; a < 2; ++a) {
        float e1 = s[2 * a + 1];
        asm("" : "+v"(e1));
        const float t = x0 ? e1 : s[2 * a];
        sc[2 * o + a] = (t + dpp<ROR4>(t)) * C2;
      }
    }
  }
  PIN();

    float m = fmaxf(fmaxf(fmaxf(sc[0], sc[1]), fmaxf(sc[2], sc[3])), fmaxf(fmaxf(sc[4], sc[5]), fmaxf(sc[6], sc[7])));
    m = max32(max16(m));
    float p[8], l = 0.f;
#pragma unroll
    for (int e = 0; e < 8; ++e) { p[e] = __builtin_amdgcn_exp2f(sc[e] - m); l += p[e]; }
    l = half16(l, l); l = half32(l, l);
    if (lane < 4) { *(LAS float*)(lds + L_MW + (lane * 16 + w) * 4) = m; *(LAS float*)(lds + L_LW + (lane * 16 + w) * 4) = l; }
    PIN();
    if (lane == 0) __hip_atomic_fetch_add((LAS u32*)(lds + L_CNT), 1u, __ATOMIC_RELAXED, __HIP_MEMORY_SCOPE_WORKGROUP);
    const u32x4 pf = {pk(p[0], p[1]), pk(p[2], p[3]), pk(p[4], p[5]), pk(p[6], p[7])};
    u32x4 am[4];
#pragma unroll
    for (int u = 0; u < 4; ++u) am[u] = sel4((c >> 2) == u, pf);
    PIN();
    u32x4 ve[4], vo[4];
    u32 slo, shi;
    asm volatile("s_mov_b32 %0, 0x05040100\n\ts_mov_b32 %1, 0x07060302" : "=s"(slo), "=s"(shi) : "v"(am[0][0]), "v"(am[1][1]), "v"(am[2][2]), "v"(am[3][3]));
#pragma unroll
    for (int r = 0; r < 4; ++r) {
#pragma unroll
      for (int u = 0; u < 4; ++u) {
        ve[u][r] = __builtin_amdgcn_perm(vf[2 * r + 1][u], vf[2 * r][u], slo);
        vo[u][r] = __builtin_amdgcn_perm(vf[2 * r + 1][u], vf[2 * r][u], shi);
      }
      if (r < 3)
        asm volatile("" : "+s"(slo), "+s"(shi) : "v"(ve[0][r]), "v"(ve[1][r]), "v"(ve[2][r]), "v"(ve[3][r]), "v"(vo[0][r]), "v"(vo[1][r]), "v"(vo[2][r]), "v"(vo[3][r]));
    }
    f32x4 oe = {0.f, 0.f, 0.f, 0.f}, oo = oe;
#pragma unroll
    for (int u = 0; u < 4; ++u) { oe = mfma(am[u], ve[u], oe); oo = mfma(am[u], vo[u], oo); }
#pragma unroll
    for (int h = 0; h < 4; ++h) *(LAS f32x2*)(lds + L_OW + w * OW_STRIDE + h * 512 + c * 32 + g * 8) = (f32x2){oe[h], oo[h]};
    PIN();
    ST(5);

  const bool gath = (w & 4) == 0;
  const int v = (w & 3) + (w >> 3) * 4;
  auto wait_ml = [&]() __attribute__((always_inline)) {
#pragma unroll 1
    for (int spin = 0; spin < (SPIN_CAP << 4); ++spin) {
      if (__builtin_amdgcn_readfirstlane(*(volatile LAS u32*)(lds + L_CNT)) == 16u) break;
      __builtin_amdgcn_s_sleep(1);
    }
    PIN();
  };
  auto load_wo = [&](u32x4 (&wt)[8]) __attribute__((always_inline)) {
    const GAS u16* p = (const GAS u16*)wo_ + (size_t)(kvh * 512 + rb * 64 + g * 4) * D + cb * 128 + (w - 4) * 64 + c * 4;
#pragma unroll
    for (int i = 0; i < 8; ++i) {
      const u32x2 t0 = ldnt((const GAS u32x2*)(p + (size_t)(32 * (i >> 2) + (i & 3)) * D));
      const u32x2 t1 = ldnt((const GAS u32x2*)(p + (size_t)(32 * (i >> 2) + 16 + (i & 3)) * D));
      wt[i] = (u32x4){t0[0], t0[1], t1[0], t1[1]};
    }
    PIN();
  };
  const int cf = bid & 3, bf = (bid >> 2) & 1, seg = bid >> 3;
  if (gath) {
    if (w < 4 && bid < 64) {
      const int moff = WS_MP + (cf * 56 + w * 14 + (lane >> 3)) * 2048 + bf * 1024 + seg * 128 + (lane & 7) * 16;
      const int moff2 = lane < 48 ? moff + 16384 : moff;
      u32x4 m0, m1;
#pragma unroll 1
      for (int spin = 0; spin < SPIN_CAP; ++spin) {
        m0 = ldws(ws, moff); m1 = ldws(ws, moff2);
        if (__all(tagged(m0, tag) && tagged(m1, tag))) break;
        __builtin_amdgcn_s_sleep(SLEEP_Q);
        asm volatile("" ::: "memory");
      }
      m1 = sel4(lane < 48, m1);
      float t = half32(u2f(m0[0]) + u2f(m1[0]), u2f(m0[2]) + u2f(m1[2]));
      t = half16(t, t);
      t += dpp<ROR8>(t);
      if ((lane & 24) == 0) *(LAS float*)(lds + L_MF + w * 64 + (lane & 7) * 8 + (lane >> 5) * 4) = t;
    }
    {
      const int f = v * 16 + (lane >> 2), p4 = lane & 3, hd = v >> 1;
      wait_ml();
      const f32x4 mv = *(LAS f32x4*)(lds + L_MW + hd * 64 + p4 * 16), lv = *(LAS f32x4*)(lds + L_LW + hd * 64 + p4 * 16);
      float M = fmaxf(fmaxf(mv[0], mv[1]), fmaxf(mv[2], mv[3]));
      M = fmaxf(M, dpp<XOR1>(M)); M = fmaxf(M, dpp<XOR2>(M));
      float L = 0.f, fw[4];
#pragma unroll
      for (int i = 0; i < 4; ++i) { fw[i] = __builtin_amdgcn_exp2f(mv[i] - M); L = fmaf(fw[i], lv[i], L); }
      L += dpp<XOR1>(L); L += dpp<XOR2>(L);
      const float inv = __builtin_amdgcn_rcpf(L);
#pragma unroll
      for (int i = 0; i < 4; ++i) fw[i] *= inv;
      const u32 lam = f2u(M + __builtin_amdgcn_logf(L)) >> ((lane >> 5) * 16);
      u32 t0 = (tag << 8) | (lam & 255u), t1 = (tag << 8) | ((lam >> 8) & 255u);
      int ooff = WS_OP + ((kvh * 2 + b) * 16 + sp) * 2048 + f * 16;
      int laddr = L_OW + p4 * 4 * OW_STRIDE + f * 16;
      asm volatile("" : "+v"(fw[0]), "+v"(fw[1]), "+v"(fw[2]), "+v"(fw[3]), "+v"(t0), "+v"(t1), "+v"(ooff), "+v"(laddr));
      LDS_BARRIER();
      ST(6);
      f32x4 s = fw[0] * *(LAS f32x4*)(lds + laddr);
#pragma unroll
      for (int i = 1; i < 4; ++i) s += fw[i] * *(LAS f32x4*)(lds + laddr + i * OW_STRIDE);
#pragma unroll
      for (int e = 0; e < 4; ++e) s[e] += dpp<XOR1>(s[e]);
#pragma unroll
      for (int e = 0; e < 4; ++e) s[e] += dpp<XOR2>(s[e]);
      if (p4 == 0) __builtin_amdgcn_raw_buffer_store_b128((u32x4){pk(s[0], s[1]), t0, pk(s[2], s[3]), t1}, ws, ooff, 0, SC1);
    }
    PIN();
    ST(7);
    {
      const int ooff = WS_OP + (kvh * 32 + v * 4 + g) * 2048 + rb * 256 + c * 16;
      u32x4 ov;
      bool ok = false;
#pragma unroll 1
      for (int spin = 0; spin < SPIN_CAP; ++spin) {
        if (!ok) { ov = ldws(ws, ooff); ok = ((ov[1] ^ (tag << 8)) | (ov[3] ^ (tag << 8))) < 256u; }
        if (__all(ok)) break;
        if (SLEEP_O) __builtin_amdgcn_s_sleep(SLEEP_O);
        asm volatile("" ::: "memory");
      }
      ST(8);
      const u32 h16 = __builtin_amdgcn_perm(ov[3], ov[1], 0x04000400u), oth = f2u(dpp<ROR8>(u2f(h16)));
      const float lam = u2f((c & 8) ? plo(h16, oth) : plo(oth, h16));
      const float G = max32(max16(lam));
      const float wg = __builtin_amdgcn_exp2f(lam - G);
      float L = half16(wg, wg);
      L = half32(L, L);
      const float t = half16(half32(wg * bflo(ov[0]), wg * bflo(ov[2])), half32(wg * bfhi(ov[0]), wg * bfhi(ov[2])));
      *(LAS float*)(lds + L_GP + v * GP_STRIDE + (c * 4 + (g >> 1) * 2 + (g & 1)) * 4) = t;
      if (lane == 0) *(LAS f32x2*)(lds + L_GP + v * GP_STRIDE + 256) = (f32x2){G, L};
    }
    LDS_BARRIER();
    ST(9);
    if (bid < 64) {
      const int soff = WS_S2 + (cf * 64 + v * 8 + (lane >> 3)) * 2048 + bf * 1024 + seg * 128 + (lane & 7) * 16;
      u32x4 sv;
      bool ok = false;
#pragma unroll 1
      for (int spin = 0; spin < SPIN_CAP; ++spin) {
        if (!ok) { sv = ldws(ws, soff); ok = tagged(sv, tag); }
        if (__all(ok)) break;
        if (SLEEP_S) __builtin_amdgcn_s_sleep(SLEEP_S);
        asm volatile("" ::: "memory");
      }
      ST(10);
      float t = half32(u2f(sv[0]), u2f(sv[2]));
      t = half16(t, t);
      t += dpp<ROR8>(t);
      if ((lane & 24) == 0) *(LAS float*)(lds + L_FP + v * 64 + (lane & 7) * 8 + (lane >> 5) * 4) = t;
      LDS_BARRIER();
    }
  } else if (w < 6) {
    u32x4 wt[8], bw[8];
    if (WO_MODE == 1) wait_ml();
    if (WO_MODE < 2) load_wo(wt);
    LDS_BARRIER();
    ST(6);
    if (WO_MODE == 2) { if (WO_SLEEP) __builtin_amdgcn_s_sleep(WO_SLEEP); load_wo(wt); }
#pragma unroll
    for (int i = 0; i < 8; ++i) bw[i] = colfrag(wt, i);
    asm volatile("" :: "v"(bw[0]), "v"(bw[1]), "v"(bw[2]), "v"(bw[3]), "v"(bw[4]), "v"(bw[5]), "v"(bw[6]), "v"(bw[7]));
    LDS_BARRIER();
    ST(9);
    const int tt = c >> 3;
    LAS unsigned char* gp = lds + L_GP + (c & 7) * GP_STRIDE;
    const f32x2 gl = *(LAS f32x2*)(gp + 256);
    const f32x4 o0 = *(LAS f32x4*)(gp + (tt * 16 + g * 4) * 4), o1 = *(LAS f32x4*)(gp + (32 + tt * 16 + g * 4) * 4);
    float G = fmaxf(gl[0], dpp<XOR1>(gl[0]));
    G = fmaxf(G, dpp<XOR2>(G));
    const float fw = __builtin_amdgcn_exp2f(gl[0] - G);
    float L = fw * gl[1];
    L += dpp<XOR1>(L); L += dpp<XOR2>(L);
    const float sc2 = fw * __builtin_amdgcn_rcpf(L);
    float o[8];
#pragma unroll
    for (int e = 0; e < 8; ++e) { float t = sc2 * (e < 4 ? o0[e & 3] : o1[e & 3]); t += dpp<XOR1>(t); o[e] = t + dpp<XOR2>(t); }
    const u32x4 of = {pk(o[0], o[1]), pk(o[2], o[3]), pk(o[4], o[5]), pk(o[6], o[7])};
    float r[4];
#pragma unroll
    for (int d = 0; d < 4; ++d) {
      const float a0 = mfma(of, bw[d], (f32x4){0.f, 0.f, 0.f, 0.f})[0], a1 = mfma(of, bw[4 + d], (f32x4){0.f, 0.f, 0.f, 0.f})[0];
      r[d] = (g >> 1) ? a1 : a0;
    }
    const float r0 = half32(r[0], r[2]), r1 = half32(r[1], r[3]);
    stws(ws, WS_S2 + (cb * 64 + kvh * 8 + rb) * 2048 + (g & 1) * 1024 + ((w - 4) * 64 + c * 4 + (g >> 1) * 2) * 8, f2u(r0), f2u(r1), tag);
    ST(10);
  } else if (w == 6 && bid < 64) {
    const int col = cf * 128 + seg * 16 + c;
    const u32 xv = ((const GAS u16*)x_)[bf * D + col], b2v = ((const GAS u16*)b2_)[col];
    LDS_BARRIER();
    ST(6);
    float t = *(LAS float*)(lds + L_MF + lane * 4);
    t = half16(t, t);
    t = half32(t, t);
    t = rbf(rbf(t) + bflo(b2v));
    float gate = rbf(t / (1.f + __builtin_amdgcn_exp2f(-LOG2E * t))), xf = bflo(xv);
    asm volatile("" : "+v"(gate), "+v"(xf));
    LDS_BARRIER();
    LDS_BARRIER();
    ST(10);
    float s = *(LAS float*)(lds + L_FP + lane * 4) + *(LAS float*)(lds + L_FP + 256 + lane * 4);
    s = half16(s, s);
    s = half32(s, s);
    const float res = xf + rbf(rbf(s) * gate);
    if (g == 0) ((GAS u16*)out_)[bf * D + col] = (u16)pk(res, res);
    if (bid == 0 && lane == 0) __hip_atomic_store((GAS u32*)(ws_ + WS_EPOCH), tag, RLX_AGENT);
  } else {
    LDS_BARRIER();
    ST(6);
  }
  ST(11);
  if (ts[PROBE] && (w & 4) == 0) {
    const u64 until = 0; if (bid == 0 && tid == 0) *(GAS u64*)(ws_ + (6 << 20)) = ts[0];
    while (__builtin_amdgcn_s_memrealtime() < until) __builtin_amdgcn_s_sleep(1);
  }
}

__global__ void tiny(unsigned char* p) { const u64 t0 = __builtin_amdgcn_s_memrealtime(); const u64 until = min(*(const u64*)(p + (6 << 20)) + 10000, t0 + 20000); while (__builtin_amdgcn_s_memrealtime() < until) __builtin_amdgcn_s_sleep(1); }
extern "C" void kernel_launch(void* const* d_in, const int* in_sizes, int n_in, void* d_out, int out_size, void* d_ws, size_t ws_size,
                              hipStream_t stream) {
  unsigned char* ws = (unsigned char*)d_ws + (WS_AT_END ? ((ws_size - WS_SPAN) & ~(size_t)4095) : 0);
  fused_decode<<<256, 1024, 0, stream>>>((const u16*)d_in[0], (const u16*)d_in[1], (const u16*)d_in[2], (const u16*)d_in[3],
                                         (const u16*)d_in[4], (const u16*)d_in[6], (const u16*)d_in[7], (const u16*)d_in[8],
                                         (const u16*)d_in[9], (u16*)d_out, ws
                                         );
  tiny<<<1, 64, 0, stream>>>(ws);
}
```

```cpp
#include <hip/hip_runtime.h>
#include <stdint.h>

#define WS_AT_END 0
#define SLEEP_Q 8
#define SLEEP_O 3
#define SLEEP_S 0
#define WO_MODE 1
#define Q_DELAY 16
#define MP_LATE 1
#define SLEEP_M 4
#define WO_AT 1150
#define WO_SLEEP 0
#define ABL 0
#define STAMP 0

#define GAS __attribute__((address_space(1)))
#define LAS __attribute__((address_space(3)))
#define DI __device__ __forceinline__
#define RLX_AGENT __ATOMIC_RELAXED, __HIP_MEMORY_SCOPE_AGENT
typedef unsigned short u16;
typedef unsigned u32;
typedef unsigned long long u64;
typedef u32 u32x2 __attribute__((ext_vector_type(2)));
typedef u32 u32x4 __attribute__((ext_vector_type(4)));
typedef float f32x2 __attribute__((ext_vector_type(2)));
typedef float f32x4 __attribute__((ext_vector_type(4)));
typedef __bf16 bf16x2 __attribute__((ext_vector_type(2)));
typedef __bf16 bf16x8 __attribute__((ext_vector_type(8)));

constexpr int D = 512, SK = 8192, DH = 128, FF = 1792, NQ = 4096;
constexpr int SC1 = 16;
constexpr int SPIN_CAP = 1 << 18;
constexpr int NFIN = 32;
constexpr float LOG2E = 1.4426950408889634f;

constexpr int WS_EPOCH = 0;
constexpr int WS_QP = 256;
constexpr int WS_PA = WS_QP + 512 * 256;
constexpr int WS_OP = WS_PA + 112 * 512;
constexpr int WS_S2 = WS_OP + 256 * 2048;
constexpr int WS_MP = WS_S2 + 256 * 1024;
constexpr int WS_END = WS_MP + 112 * 1024;
constexpr int WS_SPAN = 4 << 20;
constexpr int WS_STAMP = 8 << 20;

constexpr int F_Q = 0, F_HALF = 4, F_W2 = 6;
constexpr int L_FLAG = 0, L_CNT = 48, L_XN = 64, L_HALF = L_XN + 8192, L_Q = L_HALF + 1024, L_MW = L_Q + 1024, L_LW = L_MW + 256,
              L_HS = L_LW + 256, L_GP = L_HS + 256, GP_STRIDE = 272, L_FP = L_GP + 8 * GP_STRIDE, L_MF = L_FP + 1024,
              L_W2 = L_MF + 512, L_OW = L_W2 + 16384, OW_STRIDE = 2064, L_END = L_OW + 16 * OW_STRIDE;

DI float bflo(u32 v) { return __builtin_bit_cast(float, v << 16); }
DI float bfhi(u32 v) { return __builtin_bit_cast(float, v & 0xffff0000u); }
DI float rbf(float f) { return (float)(__bf16)f; }
DI u32 pk(float a, float b) { return __builtin_bit_cast(u32, __builtin_convertvector((f32x2){a, b}, bf16x2)); }
DI u32 f2u(float f) { return __builtin_bit_cast(u32, f); }
DI float u2f(u32 u) { return __builtin_bit_cast(float, u); }
DI f32x4 mfma(u32x4 a, u32x4 b, f32x4 c) {
  return __builtin_amdgcn_mfma_f32_16x16x32_bf16(__builtin_bit_cast(bf16x8, a), __builtin_bit_cast(bf16x8, b), c, 0, 0, 0);
}
template <int CTRL> DI float dpp(float x) { return u2f(__builtin_amdgcn_mov_dpp(f2u(x), CTRL, 0xf, 0xf, true)); }
constexpr int XOR1 = 0xB1, XOR2 = 0x4E, ROR4 = 0x124, ROR8 = 0x128;
DI float half32(float a, float b) { auto r = __builtin_amdgcn_permlane32_swap(f2u(a), f2u(b), false, false); return u2f(r[0]) + u2f(r[1]); }
DI float half16(float a, float b) { auto r = __builtin_amdgcn_permlane16_swap(f2u(a), f2u(b), false, false); return u2f(r[0]) + u2f(r[1]); }
DI float max32(float a) { auto r = __builtin_amdgcn_permlane32_swap(f2u(a), f2u(a), false, false); return __builtin_amdgcn_fmed3f(u2f(r[0]), u2f(r[1]), __builtin_inff()); }
DI float max16(float a) { auto r = __builtin_amdgcn_permlane16_swap(f2u(a), f2u(a), false, false); return __builtin_amdgcn_fmed3f(u2f(r[0]), u2f(r[1]), __builtin_inff()); }
DI float half_sum(float v) {
  v += dpp<XOR1>(v); v += dpp<XOR2>(v); v += dpp<ROR4>(v); v += dpp<ROR8>(v);
  return half16(v, v);
}
DI u32 plo(u32 k1, u32 k0) { return __builtin_amdgcn_perm(k1, k0, 0x05040100u); }
DI u32 phi(u32 k1, u32 k0) { return __builtin_amdgcn_perm(k1, k0, 0x07060302u); }
DI u32x4 colfrag(const u32x4 (&r)[8], int d) {
  const int q = d >> 1;
  return (d & 1) ? (u32x4){phi(r[1][q], r[0][q]), phi(r[3][q], r[2][q]), phi(r[5][q], r[4][q]), phi(r[7][q], r[6][q])}
                 : (u32x4){plo(r[1][q], r[0][q]), plo(r[3][q], r[2][q]), plo(r[5][q], r[4][q]), plo(r[7][q], r[6][q])};
}
DI u32x4 sel4(bool c, u32x4 v) { return (u32x4){c ? v[0] : 0u, c ? v[1] : 0u, c ? v[2] : 0u, c ? v[3] : 0u}; }
template <typename T> DI T ldnt(const GAS T* p) { return __builtin_nontemporal_load(p); }
#define LAUX 16
#define SAUX 16
DI u32x4 ldws(__amdgpu_buffer_rsrc_t ws, int off) { return __builtin_amdgcn_raw_buffer_load_b128(ws, off, 0, LAUX); }
DI void stws(__amdgpu_buffer_rsrc_t ws, int off, u32 a, u32 b, u32 tag) { __builtin_amdgcn_raw_buffer_store_b128((u32x4){a, tag, b, tag}, ws, off, 0, SAUX); }
DI void stws1(__amdgpu_buffer_rsrc_t ws, int off, u32 a, u32 tag) { __builtin_amdgcn_raw_buffer_store_b64((u32x2){a, tag}, ws, off, 0, SAUX); }
DI bool tagged(u32x4 v, u32 tag) { return v[1] == tag && v[3] == tag; }
#define PIN() do { asm volatile("" ::: "memory"); __builtin_amdgcn_sched_barrier(0); } while (0)
#define LDS_BARRIER() do { asm volatile("s_waitcnt lgkmcnt(0)\n\ts_barrier" ::: "memory"); __builtin_amdgcn_sched_barrier(0); } while (0)
#define RAW_BARRIER() do { __builtin_amdgcn_sched_barrier(0); asm volatile("s_barrier" ::: "memory"); __builtin_amdgcn_sched_barrier(0); } while (0)
DI void dma16(const u16* src, u32 dst) {
  u32 keep;
  asm volatile("s_mov_b32 %0, m0\n\ts_mov_b32 m0, %2\n\ts_nop 0\n\tglobal_load_lds_dwordx4 %1, off nt\n\ts_mov_b32 m0, %0"
               : "=&s"(keep) : "v"(src), "s"(dst) : "memory");
}
DI void set_flag(LAS unsigned char* lds, int f) {
  asm volatile("s_waitcnt lgkmcnt(0)" ::: "memory");
  *(volatile LAS u32*)(lds + L_FLAG + f * 4) = 1u;
}
DI void wait_flags(LAS unsigned char* lds, int first, int mask, int lane) {
#pragma unroll 1
  for (int spin = 0; spin < (SPIN_CAP << 4); ++spin) {
    if (__all(*(volatile LAS u32*)(lds + L_FLAG + (first + (lane & mask)) * 4) != 0u)) break;
    __builtin_amdgcn_s_sleep(1);
  }
  PIN();
}
DI u32 read_tag(const unsigned char* ws) {
  u32 e;
  asm volatile("s_load_dword %0, %1, 0x0\n\ts_waitcnt lgkmcnt(0)" : "=s"(e) : "s"(ws) : "memory");
  return e + 1u;
}
template <int LOW> DI u32x4 poll16(__amdgpu_buffer_rsrc_t ws, int off, u32 want) {
  u32x4 v;
  u64 keep;
  u32 left = SPIN_CAP, t;
  asm volatile("s_mov_b64 %[keep], exec\n\t"
               "s_branch 2f\n"
               "1:\n\t"
               "s_sub_u32 %[left], %[left], 1\n\t"
               "s_cbranch_scc1 3f\n"
               "2:\n\t"
               "buffer_load_dwordx4 v[100:103], %[off], %[ws], 0 offen sc1\n\t"
               "s_waitcnt vmcnt(0)\n\t"
               "v_bitop3_b32 %[t], v101, v103, %[want] bitop3:0x7e\n\t"
               "v_cmp_lt_u32 vcc, %[low], %[t]\n\t"
               "s_and_b64 exec, exec, vcc\n\t"
               "s_cbranch_scc1 1b\n"
               "3:\n\t"
               "s_mov_b64 exec, %[keep]"
               : "={v[100:103]}"(v), [keep] "=&s"(keep), [left] "+s"(left), [t] "=&v"(t)
               : [off] "v"(off), [ws] "s"(ws), [want] "s"(want), [low] "i"(LOW)
               : "vcc", "scc", "memory");
  return v;
}
DI void quad_sum4(float& a, float& b, float& c, float& d) {
  asm("s_nop 1\n\t"
      "v_add_f32_dpp %0, %0, %0 quad_perm:[1,0,3,2] row_mask:0xf bank_mask:0xf\n\t"
      "v_add_f32_dpp %1, %1, %1 quad_perm:[1,0,3,2] row_mask:0xf bank_mask:0xf\n\t"
      "v_add_f32_dpp %2, %2, %2 quad_perm:[1,0,3,2] row_mask:0xf bank_mask:0xf\n\t"
      "v_add_f32_dpp %3, %3, %3 quad_perm:[1,0,3,2] row_mask:0xf bank_mask:0xf\n\t"
      "v_add_f32_dpp %0, %0, %0 quad_perm:[2,3,0,1] row_mask:0xf bank_mask:0xf\n\t"
      "v_add_f32_dpp %1, %1, %1 quad_perm:[2,3,0,1] row_mask:0xf bank_mask:0xf\n\t"
      "v_add_f32_dpp %2, %2, %2 quad_perm:[2,3,0,1] row_mask:0xf bank_mask:0xf\n\t"
      "v_add_f32_dpp %3, %3, %3 quad_perm:[2,3,0,1] row_mask:0xf bank_mask:0xf"
      : "+v"(a), "+v"(b), "+v"(c), "+v"(d));
}
DI float sum_records(u32x4 v) {
  const float t = half16(half32(bflo(v[0]), bflo(v[2])), half32(bfhi(v[0]), bfhi(v[2])));
  return t + dpp<ROR8>(t);
}

DI void layernorm(u32 (&x)[8]) {
  float v[16], s = 0.f;
#pragma unroll
  for (int e = 0; e < 8; ++e) { v[2 * e] = bflo(x[e]); v[2 * e + 1] = bfhi(x[e]); s += v[2 * e] + v[2 * e + 1]; }
  const float mean = half_sum(s) * (1.f / 512.f);
  float q = 0.f;
#pragma unroll
  for (int e = 0; e < 16; ++e) { const float d = v[e] - mean; q = fmaf(d, d, q); }
  const float var = half_sum(q) * (1.f / 512.f);
  const float mb = rbf(mean), inv = __builtin_amdgcn_rcpf(rbf(__builtin_amdgcn_sqrtf(rbf(rbf(var) + 1e-5f))));
#pragma unroll
  for (int e = 0; e < 8; ++e) { const u32 d = pk(v[2 * e] - mb, v[2 * e + 1] - mb); x[e] = pk(bflo(d) * inv, bfhi(d) * inv); }
}

DI f32x2 tile_matvec(const u32x4 (&wv)[8], LAS unsigned char* xin, int g, int c) {
  const u32x4 xf = *(LAS u32x4*)(xin + ((c >> 2) & 1) * 1024 + ((c >> 3) * 32 + g * 8) * 2);
  f32x4 lo = {0.f, 0.f, 0.f, 0.f}, hi = lo;
#pragma unroll
  for (int d = 0; d < 4; ++d) {
    const u32x4 am = sel4((c & 3) == d, xf);
    lo = mfma(am, colfrag(wv, d), lo);
    hi = mfma(am, colfrag(wv, 4 + d), hi);
  }
  const bool valid = (g >> 1) == (c >> 3);
  float t[4];
#pragma unroll
  for (int e = 0; e < 4; ++e) { t[e] = half32(valid ? lo[e] : 0.f, valid ? hi[e] : 0.f); t[e] += dpp<ROR8>(t[e]); }
  return (c & 8) ? (f32x2){t[2], t[3]} : (f32x2){t[0], t[1]};
}

#define ST(i)

__global__ void __launch_bounds__(1024, 4)
fused_decode(const u16* x_, const u16* k_, const u16* v_, const u16* wq_, const u16* wo_, const u16* w1_, const u16* b1_,
             const u16* w2_, const u16* b2_, u16* out_, unsigned char* ws_
             ) {
  __shared__ __attribute__((aligned(16))) unsigned char lds_raw[L_END];
  LAS unsigned char* lds = (LAS unsigned char*)lds_raw;
  ST(0);
  const u64 t_start = WO_MODE == 3 ? __builtin_amdgcn_s_memrealtime() : 0;
  asm volatile("" :: "s"(x_), "s"(k_), "s"(v_), "s"(wq_), "s"(wo_), "s"(w1_), "s"(b1_), "s"(w2_), "s"(b2_), "s"(out_), "s"(ws_));
  const int tid = threadIdx.x, lane = tid & 63, w = __builtin_amdgcn_readfirstlane(tid >> 6);
  const int g = lane >> 4, c = lane & 15;
  const int bid = blockIdx.x, kvh = bid & 7, j = bid >> 3, b = j >> 4, sp = j & 15, rb = j >> 2, cb = j & 3;
  const bool mlp1 = j < 14, mlp2 = j >= 14 && j < 28;
  const __amdgpu_buffer_rsrc_t ws = __builtin_amdgcn_make_buffer_rsrc(ws_, 0, WS_END, 0x00020000);
  u32 tag;
  const size_t kvbase = ((size_t)(b * 8 + kvh) * SK + sp * 512 + w * 32) * (DH / 8);
  const GAS u32x4* kp = (const GAS u32x4*)k_ + kvbase + (((c >> 1) & 1) * 4 + (c >> 2)) * 16 + g * 2 + (c & 1);
  u32x4 kf[8], vf[8];

  const int cq = j & 7, kq = j >> 3;
  const int mt = (mlp1 ? j : j - 14) * 8 + kvh;
  const int mhi = mt >> 2, mlo = mt & 3;
  if (w < 2 || (w < 4 && mlp1)) {
    __builtin_amdgcn_s_setprio(3);
    if (w == 0 && lane < 16) *(LAS u32*)(lds + L_FLAG + lane * 4) = 0u;
    const GAS u32x4* xp = (const GAS u32x4*)x_ + lane * 2;
    const u32x4 x0 = xp[0], x1 = xp[1];
    PIN();
    const int rs = w < 2 ? NQ / 8 : FF / 8, row0 = (w < 2 ? kq : mlo) * 128 + (w & 1) * 64;
    const u16* tile = w < 2 ? wq_ + kvh * 512 + cq * 64 : w1_ + mhi * 64;
    const GAS u32x4* wp = (const GAS u32x4*)tile + (row0 + (c >> 3) * 32 + g * 8) * rs + (c & 7);
    u32x4 wv[8];
#pragma unroll
    for (int i = 0; i < 8; ++i) wv[i] = ldnt(wp + i * rs);
    PIN();
    LDS_BARRIER();
    ST(1);
    tag = read_tag(ws_);
    PIN();
    u32 xs[8] = {x0[0], x0[1], x0[2], x0[3], x1[0], x1[1], x1[2], x1[3]};
    layernorm(xs);
    if (w >= 2) layernorm(xs);
    LAS unsigned char* xl = lds + L_XN + w * 2048;
    *(LAS u32x4*)(xl + lane * 32) = (u32x4){xs[0], xs[1], xs[2], xs[3]};
    *(LAS u32x4*)(xl + lane * 32 + 16) = (u32x4){xs[4], xs[5], xs[6], xs[7]};
    f32x2 r = tile_matvec(wv, xl + row0 * 2, g, c);
    LAS f32x2* half = (LAS f32x2*)(lds + L_HALF + (w >> 1) * 512 + lane * 8);
    if (w & 1) {
      *half = r;
      set_flag(lds, F_HALF + (w >> 1));
    } else {
      wait_flags(lds, F_HALF + (w >> 1), 0, lane);
      r += *half;
      const int rec = w == 0 ? WS_QP + ((((g & 1) * 8 + kvh) * 8 + cq) * 4 + kq) * 256 : WS_PA + mt * 512 + (g & 1) * 256;
      stws1(ws, rec + ((c & 7) * 4 + (g >> 1) * 2 + (c >> 3)) * 8, pk(r[0], r[1]), tag);
    }
    ST(2);
    __builtin_amdgcn_s_setprio(1);
  } else {
    if (w < 4 && mlp2) {
      const u16* p = w2_ + (mhi * 64 + (w & 1) * 32 + g * 8) * D + mlo * 128 + c * 8;
#pragma unroll
      for (int i = 0; i < 8; ++i) dma16(p + i * D, (u32)(size_t)(lds + L_W2 + (w & 1) * 8192 + i * 1024));
    }
    RAW_BARRIER();
    if ((w >> 2) == 1) __builtin_amdgcn_s_setprio(2); else __builtin_amdgcn_s_setprio(1);
  }
  PIN();

#pragma unroll
  for (int i = 0; i < 8; ++i) { kf[i] = ldnt(kp + (i >> 1) * 128 + (i & 1) * 8); __builtin_amdgcn_sched_barrier(0); }
  if ((w >> 2) != 1) __builtin_amdgcn_s_setprio(0);
  PIN();
  {
    const GAS u32x4* vp = (const GAS u32x4*)v_ + kvbase + lane;
#pragma unroll
    for (int i = 0; i < 8; ++i) { vf[i] = ldnt(vp + i * 64); __builtin_amdgcn_sched_barrier(0); }
  }
  PIN();
  ST(4);
  tag = read_tag(ws_);
  const int paoff = WS_PA + mhi * 2048 + lane * 16;
  u32x4 p0, p1;
  u32 b1v;
  if ((w >> 2) == 1) {
    const int u = w & 3;
    const int qoff = WS_QP + (b * 8 + kvh) * 8192 + u * 2048 + lane * 16;
    asm volatile("s_waitcnt vmcnt(0)" ::: "memory");
    if (Q_DELAY) __builtin_amdgcn_s_sleep(Q_DELAY);
    u32x4 q0, q1;
#pragma unroll 1
    for (int spin = 0; spin < SPIN_CAP; ++spin) {
      q0 = ldws(ws, qoff); q1 = ldws(ws, qoff + 1024);
      if (__all(tagged(q0, tag) && tagged(q1, tag))) break;
      __builtin_amdgcn_s_sleep(SLEEP_Q);
      asm volatile("" ::: "memory");
    }
    const float qa = half16(half32(bflo(q0[0]), bflo(q1[0])), half32(bflo(q0[2]), bflo(q1[2])));
    const float qb = half16(half32(bfhi(q0[0]), bfhi(q1[0])), half32(bfhi(q0[2]), bfhi(q1[2])));
    *(LAS u32*)(lds + L_Q + u * 256 + ((g >> 1) * 32 + c * 2 + (g & 1)) * 4) = pk(qa, qb);
    set_flag(lds, F_Q + u);
    ST(3);
    if (w == 7 && mlp2) {
      p0 = ldws(ws, paoff); p1 = ldws(ws, paoff + 1024);
      b1v = *(const GAS u32*)(b1_ + mhi * 64 + c * 4 + (g >> 1) * 2);
      PIN();
    }
    __builtin_amdgcn_s_setprio(0);
  }
  wait_flags(lds, F_Q, 3, lane);

  float sc[8];
  {
    const int x0 = (c >> 2) & 1;
    const u32x4 q0 = *(LAS u32x4*)(lds + L_Q + (c & 3) * 256 + (g * 2 + x0) * 16);
    const u32x4 q1 = *(LAS u32x4*)(lds + L_Q + (c & 3) * 256 + 128 + (g * 2 + x0) * 16);
    constexpr float C2 = 0.08838834764831845f * LOG2E;
#pragma unroll
    for (int o = 0; o < 4; ++o) {
      f32x4 s = mfma(kf[2 * o], q0, (f32x4){0.f, 0.f, 0.f, 0.f});
      s = mfma(kf[2 * o + 1], q1, s);
#pragma unroll
      for (int a = 0; a < 2; ++a) {
        float e1 = s[2 * a + 1];
        asm("" : "+v"(e1));
        const float t = x0 ? e1 : s[2 * a];
        sc[2 * o + a] = (t + dpp<ROR4>(t)) * C2;
      }
    }
  }
  PIN();

    float m = fmaxf(fmaxf(fmaxf(sc[0], sc[1]), fmaxf(sc[2], sc[3])), fmaxf(fmaxf(sc[4], sc[5]), fmaxf(sc[6], sc[7])));
    m = max32(max16(m));
    float p[8], l = 0.f;
#pragma unroll
    for (int e = 0; e < 8; ++e) { p[e] = __builtin_amdgcn_exp2f(sc[e] - m); l += p[e]; }
    l = half16(l, l); l = half32(l, l);
    if (lane < 4) { *(LAS float*)(lds + L_MW + (lane * 16 + w) * 4) = m; *(LAS float*)(lds + L_LW + (lane * 16 + w) * 4) = l; }
    PIN();
    if ((w >> 1) == 1) set_flag(lds, F_W2 + (w & 1));
    if (lane == 0) __hip_atomic_fetch_add((LAS u32*)(lds + L_CNT), 1u, __ATOMIC_RELAXED, __HIP_MEMORY_SCOPE_WORKGROUP);
    const u32x4 pf = {pk(p[0], p[1]), pk(p[2], p[3]), pk(p[4], p[5]), pk(p[6], p[7])};
    u32x4 am[4];
#pragma unroll
    for (int u = 0; u < 4; ++u) am[u] = sel4((c >> 2) == u, pf);
    PIN();
    u32x4 ve[4], vo[4];
    u32 slo, shi;
    asm volatile("s_mov_b32 %0, 0x05040100\n\ts_mov_b32 %1, 0x07060302" : "=s"(slo), "=s"(shi) : "v"(am[0][0]), "v"(am[1][1]), "v"(am[2][2]), "v"(am[3][3]));
#pragma unroll
    for (int r = 0; r < 4; ++r) {
#pragma unroll
      for (int u = 0; u < 4; ++u) {
        ve[u][r] = __builtin_amdgcn_perm(vf[2 * r + 1][u], vf[2 * r][u], slo);
        vo[u][r] = __builtin_amdgcn_perm(vf[2 * r + 1][u], vf[2 * r][u], shi);
      }
      if (r < 3)
        asm volatile("" : "+s"(slo), "+s"(shi) : "v"(ve[0][r]), "v"(ve[1][r]), "v"(ve[2][r]), "v"(ve[3][r]), "v"(vo[0][r]), "v"(vo[1][r]), "v"(vo[2][r]), "v"(vo[3][r]));
    }
    f32x4 oe = {0.f, 0.f, 0.f, 0.f}, oo = oe;
#pragma unroll
    for (int u = 0; u < 4; ++u) { oe = mfma(am[u], ve[u], oe); oo = mfma(am[u], vo[u], oo); }
#pragma unroll
    for (int h = 0; h < 4; ++h) *(LAS f32x2*)(lds + L_OW + w * OW_STRIDE + h * 512 + c * 32 + g * 8) = (f32x2){oe[h], oo[h]};
    PIN();
    ST(5);

  const bool gath = (w & 4) == 0;
  const int v = (w & 3) + (w >> 3) * 4;
  auto wait_ml = [&]() __attribute__((always_inline)) {
#pragma unroll 1
    for (int spin = 0; spin < (SPIN_CAP << 4); ++spin) {
      if (__builtin_amdgcn_readfirstlane(*(volatile LAS u32*)(lds + L_CNT)) == 16u) break;
      __builtin_amdgcn_s_sleep(1);
    }
    PIN();
  };
  auto load_wo = [&](u32x4 (&wt)[8]) __attribute__((always_inline)) {
    const GAS u16* p = (const GAS u16*)wo_ + (size_t)(kvh * 512 + rb * 64 + g * 4) * D + cb * 128 + (w - 4) * 64 + c * 4;
#pragma unroll
    for (int i = 0; i < 8; ++i) {
      const u32x2 t0 = ldnt((const GAS u32x2*)(p + (size_t)(32 * (i >> 2) + (i & 3)) * D));
      const u32x2 t1 = ldnt((const GAS u32x2*)(p + (size_t)(32 * (i >> 2) + 16 + (i & 3)) * D));
      wt[i] = (u32x4){t0[0], t0[1], t1[0], t1[1]};
    }
    PIN();
  };
  const int cf = bid & 3, bf = (bid >> 2) & 1, seg = bid >> 3;
  if (gath) {
    {
      const int f = v * 16 + (lane >> 2), p4 = lane & 3, hd = v >> 1;
      wait_ml();
      const f32x4 mv = *(LAS f32x4*)(lds + L_MW + hd * 64 + p4 * 16), lv = *(LAS f32x4*)(lds + L_LW + hd * 64 + p4 * 16);
      float M = fmaxf(fmaxf(mv[0], mv[1]), fmaxf(mv[2], mv[3]));
      M = fmaxf(M, dpp<XOR1>(M)); M = fmaxf(M, dpp<XOR2>(M));
      float L = 0.f, fw[4];
#pragma unroll
      for (int i = 0; i < 4; ++i) { fw[i] = __builtin_amdgcn_exp2f(mv[i] - M); L = fmaf(fw[i], lv[i], L); }
      L += dpp<XOR1>(L); L += dpp<XOR2>(L);
      const float inv = __builtin_amdgcn_rcpf(L);
#pragma unroll
      for (int i = 0; i < 4; ++i) fw[i] *= inv;
      const u32 lam = f2u(M + __builtin_amdgcn_logf(L)) >> ((lane >> 5) * 16);
      u32 t0 = (tag << 8) | (lam & 255u), t1 = (tag << 8) | ((lam >> 8) & 255u);
      int ooff = WS_OP + ((kvh * 2 + b) * 16 + sp) * 2048 + f * 16;
      int laddr = L_OW + p4 * 4 * OW_STRIDE + f * 16;
      asm volatile("" : "+v"(fw[0]), "+v"(fw[1]), "+v"(fw[2]), "+v"(fw[3]), "+v"(t0), "+v"(t1), "+v"(ooff), "+v"(laddr));
      ST(3);
      LDS_BARRIER();
      ST(6);
      f32x4 s = fw[0] * *(LAS f32x4*)(lds + laddr);
#pragma unroll
      for (int i = 1; i < 4; ++i) s += fw[i] * *(LAS f32x4*)(lds + laddr + i * OW_STRIDE);
      float s0 = s[0], s1 = s[1], s2 = s[2], s3 = s[3];
      quad_sum4(s0, s1, s2, s3);
      if (p4 == 0) __builtin_amdgcn_raw_buffer_store_b128((u32x4){pk(s0, s1), t0, pk(s2, s3), t1}, ws, ooff, 0, SAUX);
    }
    PIN();
    ST(7);
    {
      u32 gaddr2 = (u32)(size_t)(lds + L_GP + v * GP_STRIDE + 256), gaddr = gaddr2 - 256 + (c * 4 + g) * 4;
      u32 lsel = (c & 8) ? 0x05040100u : 0x01000504u;
      asm volatile("" : "+v"(gaddr), "+v"(gaddr2), "+v"(lsel));
      if (SLEEP_O) __builtin_amdgcn_s_sleep(SLEEP_O);
      const u32x4 ov = poll16<255>(ws, WS_OP + (kvh * 32 + v * 4 + g) * 2048 + rb * 256 + c * 16, tag << 8);
      ST(8);
      u32 a, lam, m1, x0, x1, x2, x3;
      f32x2 gl;
      asm volatile("v_perm_b32 %[a], %[t1], %[t0], %[sel]\n\t"
                   "v_lshlrev_b32 %[x0], 16, %[d0]\n\t"
                   "v_lshlrev_b32 %[x2], 16, %[d1]\n\t"
                   "v_mov_b32_dpp %[m1], %[a] row_ror:8 row_mask:0xf bank_mask:0xf\n\t"
                   "v_and_b32 %[x1], 0xffff0000, %[d0]\n\t"
                   "v_perm_b32 %[lam], %[a], %[m1], %[lsel]\n\t"
                   "v_mov_b32 %[m1], %[lam]\n\t"
                   "v_mov_b32 %[a], %[lam]\n\t"
                   "v_and_b32 %[x3], 0xffff0000, %[d1]\n\t"
                   "s_nop 0\n\t"
                   "v_permlane16_swap_b32 %[m1], %[a]\n\t"
                   "v_max_f32 %[m1], %[m1], %[a]\n\t"
                   "v_mov_b32 %[a], %[m1]\n\t"
                   "s_nop 1\n\t"
                   "v_permlane32_swap_b32 %[m1], %[a]\n\t"
                   "v_max_f32 v104, %[m1], %[a]\n\t"
                   "v_sub_f32 %[lam], %[lam], v104\n\t"
                   "v_exp_f32 %[lam], %[lam]\n\t"
                   "s_nop 0\n\t"
                   "v_mul_f32 %[x0], %[lam], %[x0]\n\t"
                   "v_mul_f32 %[x2], %[lam], %[x2]\n\t"
                   "v_mul_f32 %[x1], %[lam], %[x1]\n\t"
                   "v_mul_f32 %[x3], %[lam], %[x3]\n\t"
                   "v_mov_b32 %[a], %[lam]\n\t"
                   "v_permlane32_swap_b32 %[x0], %[x2]\n\t"
                   "v_permlane32_swap_b32 %[x1], %[x3]\n\t"
                   "v_permlane16_swap_b32 %[lam], %[a]\n\t"
                   "v_add_f32 %[x0], %[x0], %[x2]\n\t"
                   "v_add_f32 %[x1], %[x1], %[x3]\n\t"
                   "v_add_f32 %[lam], %[lam], %[a]\n\t"
                   "v_mov_b32 %[a], %[lam]\n\t"
                   "v_permlane16_swap_b32 %[x0], %[x1]\n\t"
                   "v_add_f32 %[x0], %[x0], %[x1]\n\t"
                   "v_permlane32_swap_b32 %[lam], %[a]\n\t"
                   "ds_write_b32 %[ga], %[x0]\n\t"
                   "v_add_f32 v105, %[lam], %[a]\n\t"
                   "ds_write_b64 %[gb], v[104:105]"
                   : [a] "=&v"(a), [lam] "=&v"(lam), [m1] "=&v"(m1), [x0] "=&v"(x0), [x1] "=&v"(x1), [x2] "=&v"(x2), [x3] "=&v"(x3), "=&{v[104:105]}"(gl)
                   : [d0] "v"(ov[0]), [t0] "v"(ov[1]), [d1] "v"(ov[2]), [t1] "v"(ov[3]), [sel] "s"(0x04000400u), [lsel] "v"(lsel), [ga] "v"(gaddr), [gb] "v"(gaddr2)
                   : "memory");
    }
    LDS_BARRIER();
    ST(9);
    if (bid < NFIN) {
      int faddr = L_FP + ((lane & 7) * 4 + g) * 32 + v * 4;
      asm volatile("" : "+v"(faddr));
      const u32x4 sv = poll16<0>(ws, WS_S2 + (cf * 64 + v * 8 + (lane >> 3)) * 1024 + bf * 512 + seg * 128 + (lane & 7) * 16, tag);
      ST(10);
      *(LAS float*)(lds + faddr) = sum_records(sv);
      LDS_BARRIER();
    }
  } else if (w < 6) {
    u32x4 wt[8], bw[8];
    if (WO_MODE == 3) {
      wait_ml();
      const bool now = __builtin_amdgcn_s_memrealtime() >= t_start + WO_AT;
      if (!now) {
        LDS_BARRIER();
        ST(6);
        while (__builtin_amdgcn_s_memrealtime() < t_start + WO_AT) __builtin_amdgcn_s_sleep(1);
      }
      load_wo(wt);
      if (now) { LDS_BARRIER(); ST(6); }
    } else {
    if (WO_MODE == 1) wait_ml();
    if (WO_MODE < 2) load_wo(wt);
    ST(7);
    LDS_BARRIER();
    ST(6);
    if (WO_MODE == 2) { if (WO_SLEEP) __builtin_amdgcn_s_sleep(WO_SLEEP); load_wo(wt); }
    }
#pragma unroll
    for (int i = 0; i < 8; ++i) bw[i] = colfrag(wt, i);
    int gp = L_GP + (c & 7) * GP_STRIDE, go = gp + ((c >> 3) * 16 + g * 4) * 4;
    int soff = WS_S2 + (cb * 64 + kvh * 8 + rb) * 1024 + (g & 1) * 512 + ((w - 4) * 32 + c * 2 + (g >> 1)) * 8;
    asm volatile("" : "+v"(gp), "+v"(go), "+v"(soff) : "v"(bw[0]), "v"(bw[1]), "v"(bw[2]), "v"(bw[3]), "v"(bw[4]), "v"(bw[5]), "v"(bw[6]), "v"(bw[7]));
    LDS_BARRIER();
    ST(9);
    const f32x2 gl = *(LAS f32x2*)(lds + gp + 256);
    const f32x4 o0 = *(LAS f32x4*)(lds + go), o1 = *(LAS f32x4*)(lds + go + 128);
    float o[8] = {o0[0], o0[1], o0[2], o0[3], o1[0], o1[1], o1[2], o1[3]}, fw, L = gl[1];
    float L0, L1;
#define QP1(r) "v_add_f32_dpp " r ", " r ", " r " quad_perm:[1,0,3,2] row_mask:0xf bank_mask:0xf\n\t"
#define QP2(r) "v_add_f32_dpp " r ", " r ", " r " quad_perm:[2,3,0,1] row_mask:0xf bank_mask:0xf\n\t"
    asm volatile("v_max_f32_dpp %[fw], %[G], %[G] quad_perm:[1,0,3,2] row_mask:0xf bank_mask:0xf\n\t"
                 "s_nop 1\n\t"
                 "v_max_f32_dpp %[fw], %[fw], %[fw] quad_perm:[2,3,0,1] row_mask:0xf bank_mask:0xf\n\t"
                 "v_sub_f32 %[fw], %[G], %[fw]\n\t"
                 "v_exp_f32 %[fw], %[fw]\n\t"
                 "s_nop 0\n\t"
                 "v_mul_f32 %[L], %[fw], %[L]\n\t"
                 "v_mul_f32 %0, %[fw], %0\n\tv_mul_f32 %1, %[fw], %1\n\tv_mul_f32 %2, %[fw], %2\n\tv_mul_f32 %3, %[fw], %3\n\t"
                 "v_mul_f32 %4, %[fw], %4\n\tv_mul_f32 %5, %[fw], %5\n\tv_mul_f32 %6, %[fw], %6\n\tv_mul_f32 %7, %[fw], %7\n\t"
                 QP1("%[L]") QP1("%0") QP1("%1") QP1("%2") QP1("%3") QP1("%4") QP1("%5") QP1("%6") QP1("%7")
                 QP2("%[L]") QP2("%0") QP2("%1") QP2("%2") QP2("%3") QP2("%4") QP2("%5") QP2("%6") QP2("%7")
                 "v_readlane_b32 %[L0], %[L], 0\n\t"
                 "v_readlane_b32 %[L1], %[L], 4"
                 : "+v"(o[0]), "+v"(o[1]), "+v"(o[2]), "+v"(o[3]), "+v"(o[4]), "+v"(o[5]), "+v"(o[6]), "+v"(o[7]),
                   [fw] "=&v"(fw), [L] "+v"(L), [L0] "=s"(L0), [L1] "=s"(L1)
                 : [G] "v"(gl[0]));
    const u32x4 of = {pk(o[0], o[1]), pk(o[2], o[3]), pk(o[4], o[5]), pk(o[6], o[7])};
    f32x4 acc[8];
#pragma unroll
    for (int i = 0; i < 8; ++i) acc[i] = mfma(of, bw[i], (f32x4){0.f, 0.f, 0.f, 0.f});
    __builtin_amdgcn_sched_barrier(0);
    const float inv = __builtin_amdgcn_rcpf((g & 1) ? L1 : L0);
    float r[4];
#pragma unroll
    for (int d = 0; d < 4; ++d) r[d] = (g >> 1) ? acc[4 + d][0] : acc[d][0];
    stws1(ws, soff, pk(half32(r[0], r[2]) * inv, half32(r[1], r[3]) * inv), tag);
    ST(10);
  } else if (w == 7 && mlp2) {
#pragma unroll 1
    for (int spin = 0; spin < SPIN_CAP; ++spin) {
      if (__all(tagged(p0, tag) && tagged(p1, tag))) break;
      __builtin_amdgcn_s_sleep(SLEEP_Q);
      asm volatile("" ::: "memory");
      p0 = ldws(ws, paoff); p1 = ldws(ws, paoff + 1024);
    }
    const float t2[2] = {half32(bflo(p0[0]) + bflo(p1[0]), bflo(p0[2]) + bflo(p1[2])), half32(bfhi(p0[0]) + bfhi(p1[0]), bfhi(p0[2]) + bfhi(p1[2]))};
    float hv[2];
#pragma unroll
    for (int e = 0; e < 2; ++e) {
      const float t = rbf(rbf(t2[e]) + (e ? bfhi(b1v) : bflo(b1v)));
      const float uu = 0.7978845608028654f * (t + 0.044715f * t * t * t);
      hv[e] = t / (1.f + __builtin_amdgcn_exp2f(-2.f * LOG2E * uu));
    }
    *(LAS u32*)(lds + L_HS + (g & 1) * 128 + (c * 2 + (g >> 1)) * 4) = pk(hv[0], hv[1]);
    wait_flags(lds, F_W2, 1, lane);
    f32x4 r = {0.f, 0.f, 0.f, 0.f};
#pragma unroll
    for (int s = 0; s < 2; ++s) {
      const u32x4 ha = *(LAS u32x4*)(lds + L_HS + ((c >> 2) & 1) * 128 + s * 64 + g * 16);
      u32x4 w2t[8];
#pragma unroll
      for (int i = 0; i < 8; ++i) w2t[i] = *(LAS u32x4*)(lds + L_W2 + s * 8192 + i * 1024 + lane * 16);
#pragma unroll
      for (int d = 0; d < 8; ++d) r = mfma(sel4((c >> 3) == (d >> 2) && (c & 3) == (d & 3), ha), colfrag(w2t, d), r);
    }
    u32 m0 = pk(r[0], r[1]), m1 = pk(r[2], r[3]);
    asm volatile("" : "+v"(m0), "+v"(m1));
    ST(2);
    if (MP_LATE) LDS_BARRIER();
    stws(ws, WS_MP + (mlo * 28 + mhi) * 1024 + (g & 1) * 512 + (c * 2 + (g >> 1)) * 16, m0, m1, tag);
    if (!MP_LATE) LDS_BARRIER();
    ST(6);
  } else if (w == 6 && bid < NFIN) {
    const int col = cf * 128 + seg * 32 + (lane & 31);
    const u32 xv = ((const GAS u16*)x_)[bf * D + col], b2v = ((const GAS u16*)b2_)[col];
    LDS_BARRIER();
    ST(6);
    LDS_BARRIER();
    LAS float* mf = (LAS float*)(lds + L_MF + (lane >> 5) * 256 + (lane & 31) * 4);
    float t = mf[0] + mf[32];
    t = half32(t, t);
    t = rbf(rbf(t) + bflo(b2v));
    float gate = rbf(t / (1.f + __builtin_amdgcn_exp2f(-LOG2E * t))), xf = bflo(xv);
    int faddr = L_FP + (lane & 31) * 32 + (lane >> 5) * 16;
    GAS u16* op = (GAS u16*)out_ + bf * D + col;
    asm volatile("" : "+v"(gate), "+v"(xf), "+v"(faddr), "+v"(op));
    LDS_BARRIER();
    ST(10);
    const f32x4 fp = *(LAS f32x4*)(lds + faddr);
    float s = (fp[0] + fp[1]) + (fp[2] + fp[3]);
    s = half32(s, s);
    const float res = xf + rbf(rbf(s) * gate);
    if (lane < 32) __hip_atomic_store(op, (u16)pk(res, res), RLX_AGENT);
    if (bid == 0 && lane == 0) __hip_atomic_store((GAS u32*)(ws_ + WS_EPOCH), tag, RLX_AGENT);
  } else {
    LDS_BARRIER();
    ST(6);
    if (w >= 12 && bid < NFIN) {
      const int row = (w - 12) * 8 + (lane >> 3);
      const int moff = WS_MP + (cf * 28 + min(row, 27)) * 1024 + bf * 512 + seg * 128 + (lane & 7) * 16;
      u32x4 mv;
#pragma unroll 1
      for (int spin = 0; spin < SPIN_CAP; ++spin) {
        mv = ldws(ws, moff);
        if (__all(tagged(mv, tag))) break;
        __builtin_amdgcn_s_sleep(SLEEP_M);
        asm volatile("" ::: "memory");
      }
      const float t = sum_records(sel4(row < 28, mv));
      if ((lane & 8) == 0) *(LAS float*)(lds + L_MF + (w - 12) * 128 + ((lane & 7) * 4 + g) * 4) = t;
      LDS_BARRIER();
    }
  }
  ST(11);
}

extern "C" void kernel_launch(void* const* d_in, const int* in_sizes, int n_in, void* d_out, int out_size, void* d_ws, size_t ws_size,
                              hipStream_t stream) {
  unsigned char* ws = (unsigned char*)d_ws + (WS_AT_END ? ((ws_size - WS_SPAN) & ~(size_t)4095) : 0);
  fused_decode<<<256, 1024, 0, stream>>>((const u16*)d_in[0], (const u16*)d_in[1], (const u16*)d_in[2], (const u16*)d_in[3],
                                         (const u16*)d_in[4], (const u16*)d_in[6], (const u16*)d_in[7], (const u16*)d_in[8],
                                         (const u16*)d_in[9], (u16*)d_out, ws
                                         );
}
```

```cpp
#include <hip/hip_runtime.h>
#include <stdint.h>

#define WS_AT_END 0
#define SLEEP_Q 8
#define SLEEP_O 8
#define SLEEP_S 8
#define WO_MODE 1
#define PRIO_K 1
#define ROT 1
#define CODE_LINES 100
#define Q_DELAY 16
#define MP_LATE 0
#define SLEEP_M 4
#define WO_AT 1150
#define WO_SLEEP 0
#define ABL 0
#define STAMP 0

#define GAS __attribute__((address_space(1)))
#define LAS __attribute__((address_space(3)))
#define DI __device__ __forceinline__
#define RLX_AGENT __ATOMIC_RELAXED, __HIP_MEMORY_SCOPE_AGENT
typedef unsigned short u16;
typedef unsigned u32;
typedef unsigned long long u64;
typedef u32 u32x2 __attribute__((ext_vector_type(2)));
typedef u32 u32x4 __attribute__((ext_vector_type(4)));
typedef float f32x2 __attribute__((ext_vector_type(2)));
typedef float f32x4 __attribute__((ext_vector_type(4)));
typedef __bf16 bf16x2 __attribute__((ext_vector_type(2)));
typedef __bf16 bf16x8 __attribute__((ext_vector_type(8)));

constexpr int D = 512, SK = 8192, DH = 128, FF = 1792, NQ = 4096;
constexpr int SC1 = 16;
constexpr int SPIN_CAP = 1 << 18;
constexpr int NFIN = 32;
constexpr float LOG2E = 1.4426950408889634f;

constexpr int WS_EPOCH = 0;
constexpr int WS_QP = 256;
constexpr int WS_PA = WS_QP + 512 * 256;
constexpr int WS_OP = WS_PA + 112 * 512;
constexpr int WS_S2 = WS_OP + 256 * 2048;
constexpr int WS_MP = WS_S2 + 256 * 1024;
constexpr int WS_END = WS_MP + 112 * 1024;
constexpr int WS_SPAN = 4 << 20;
constexpr int WS_STAMP = 8 << 20;

constexpr int F_Q = 0, F_HALF = 4, F_W2 = 6;
constexpr int L_FLAG = 0, L_CNT = 48, L_XN = 64, L_HALF = L_XN + 8192, L_Q = L_HALF + 1024, L_MW = L_Q + 1024, L_LW = L_MW + 256,
              L_HS = L_LW + 256, L_GP = L_HS + 256, GP_STRIDE = 272, L_FP = L_GP + 8 * GP_STRIDE, L_MF = L_FP + 1024,
              L_TOUCH = L_MF + 512, L_W2 = L_TOUCH + 256, L_OW = L_W2 + 16384, OW_STRIDE = 2064, L_END = L_OW + 16 * OW_STRIDE;

DI float bflo(u32 v) { return __builtin_bit_cast(float, v << 16); }
DI float bfhi(u32 v) { return __builtin_bit_cast(float, v & 0xffff0000u); }
DI float rbf(float f) { return (float)(__bf16)f; }
DI u32 pk(float a, float b) { return __builtin_bit_cast(u32, __builtin_convertvector((f32x2){a, b}, bf16x2)); }
DI u32 f2u(float f) { return __builtin_bit_cast(u32, f); }
DI float u2f(u32 u) { return __builtin_bit_cast(float, u); }
DI f32x4 mfma(u32x4 a, u32x4 b, f32x4 c) {
  return __builtin_amdgcn_mfma_f32_16x16x32_bf16(__builtin_bit_cast(bf16x8, a), __builtin_bit_cast(bf16x8, b), c, 0, 0, 0);
}
template <int CTRL> DI float dpp(float x) { return u2f(__builtin_amdgcn_mov_dpp(f2u(x), CTRL, 0xf, 0xf, true)); }
constexpr int XOR1 = 0xB1, XOR2 = 0x4E, ROR4 = 0x124, ROR8 = 0x128;
DI float half32(float a, float b) { auto r = __builtin_amdgcn_permlane32_swap(f2u(a), f2u(b), false, false); return u2f(r[0]) + u2f(r[1]); }
DI float half16(float a, float b) { auto r = __builtin_amdgcn_permlane16_swap(f2u(a), f2u(b), false, false); return u2f(r[0]) + u2f(r[1]); }
DI float max32(float a) { auto r = __builtin_amdgcn_permlane32_swap(f2u(a), f2u(a), false, false); return __builtin_amdgcn_fmed3f(u2f(r[0]), u2f(r[1]), __builtin_inff()); }
DI float max16(float a) { auto r = __builtin_amdgcn_permlane16_swap(f2u(a), f2u(a), false, false); return __builtin_amdgcn_fmed3f(u2f(r[0]), u2f(r[1]), __builtin_inff()); }
DI float half_sum(float v) {
  v += dpp<XOR1>(v); v += dpp<XOR2>(v); v += dpp<ROR4>(v); v += dpp<ROR8>(v);
  return half16(v, v);
}
DI u32 plo(u32 k1, u32 k0) { return __builtin_amdgcn_perm(k1, k0, 0x05040100u); }
DI u32 phi(u32 k1, u32 k0) { return __builtin_amdgcn_perm(k1, k0, 0x07060302u); }
DI u32x4 colfrag(const u32x4 (&r)[8], int d) {
  const int q = d >> 1;
  return (d & 1) ? (u32x4){phi(r[1][q], r[0][q]), phi(r[3][q], r[2][q]), phi(r[5][q], r[4][q]), phi(r[7][q], r[6][q])}
                 : (u32x4){plo(r[1][q], r[0][q]), plo(r[3][q], r[2][q]), plo(r[5][q], r[4][q]), plo(r[7][q], r[6][q])};
}
DI u32x4 sel4(bool c, u32x4 v) { return (u32x4){c ? v[0] : 0u, c ? v[1] : 0u, c ? v[2] : 0u, c ? v[3] : 0u}; }
template <typename T> DI T ldnt(const GAS T* p) { return __builtin_nontemporal_load(p); }
#define LAUX 16
#define SAUX 16
DI u32x4 ldws(__amdgpu_buffer_rsrc_t ws, int off) { return __builtin_amdgcn_raw_buffer_load_b128(ws, off, 0, LAUX); }
DI void stws(__amdgpu_buffer_rsrc_t ws, int off, u32x2 a, u32x2 b) { __builtin_amdgcn_raw_buffer_store_b128((u32x4){a[0], a[1], b[0], b[1]}, ws, off, 0, SAUX); }
DI void stws1(__amdgpu_buffer_rsrc_t ws, int off, u32x2 a) { __builtin_amdgcn_raw_buffer_store_b64(a, ws, off, 0, SAUX); }
DI u32x2 pack2(float a, float b, u32 tag8) {
  const u32 ar = f2u(a) + 0x800u;
  const u32 u = (f2u(b) & 0xf000u) | ((ar >> 4) & ~0xf000u);
  return (u32x2){phi(f2u(b), ar), __builtin_amdgcn_perm(tag8, u, 0x07060501u)};
}
DI float first2(u32 w0, u32 w1) { return u2f(__builtin_amdgcn_perm(w0, w1 << 4, 0x0504000cu)); }
DI float second2(u32 w0, u32 w1) { return u2f(__builtin_amdgcn_perm(w0, w1, 0x0706000cu)); }
DI bool tagged(u32x4 v, u32 tag8) { return ((v[1] ^ tag8) | (v[3] ^ tag8)) < 256u; }
#define PIN() do { asm volatile("" ::: "memory"); __builtin_amdgcn_sched_barrier(0); } while (0)
#define LDS_BARRIER() do { asm volatile("s_waitcnt lgkmcnt(0)\n\ts_barrier" ::: "memory"); __builtin_amdgcn_sched_barrier(0); } while (0)
#define RAW_BARRIER() do { __builtin_amdgcn_sched_barrier(0); asm volatile("s_barrier" ::: "memory"); __builtin_amdgcn_sched_barrier(0); } while (0)
DI void dma16(const u16* src, u32 dst) {
  u32 keep;
  asm volatile("s_mov_b32 %0, m0\n\ts_mov_b32 m0, %2\n\ts_nop 0\n\tglobal_load_lds_dwordx4 %1, off nt\n\ts_mov_b32 m0, %0"
               : "=&s"(keep) : "v"(src), "s"(dst) : "memory");
}
DI void touch(const void* src, u32 dst) {
  u32 keep;
  asm volatile("s_mov_b32 %0, m0\n\ts_mov_b32 m0, %2\n\ts_nop 0\n\tglobal_load_lds_dword %1, off\n\ts_mov_b32 m0, %0"
               : "=&s"(keep) : "v"(src), "s"(dst) : "memory");
}
DI void set_flag(LAS unsigned char* lds, int f) {
  asm volatile("s_waitcnt lgkmcnt(0)" ::: "memory");
  *(volatile LAS u32*)(lds + L_FLAG + f * 4) = 1u;
}
DI void wait_flags(LAS unsigned char* lds, int first, int mask, int lane) {
#pragma unroll 1
  for (int spin = 0; spin < (SPIN_CAP << 4); ++spin) {
    if (__all(*(volatile LAS u32*)(lds + L_FLAG + (first + (lane & mask)) * 4) != 0u)) break;
    __builtin_amdgcn_s_sleep(1);
  }
  PIN();
}
DI u32 read_tag(const unsigned char* ws) {
  u32 e;
  asm volatile("s_load_dword %0, %1, 0x0\n\ts_waitcnt lgkmcnt(0)" : "=s"(e) : "s"(ws) : "memory");
  return (e + 1u) << 8;
}
template <int LOW> DI u32x4 poll16(__amdgpu_buffer_rsrc_t ws, int off, u32 want) {
  u32x4 v;
  u64 keep;
  u32 left = SPIN_CAP, t;
  asm volatile("s_mov_b64 %[keep], exec\n\t"
               "s_branch 2f\n"
               "1:\n\t"
               "s_sub_u32 %[left], %[left], 1\n\t"
               "s_cbranch_scc1 3f\n"
               "2:\n\t"
               "buffer_load_dwordx4 v[100:103], %[off], %[ws], 0 offen sc1\n\t"
               "s_waitcnt vmcnt(0)\n\t"
               "v_bitop3_b32 %[t], v101, v103, %[want] bitop3:0x7e\n\t"
               "v_cmp_lt_u32 vcc, %[low], %[t]\n\t"
               "s_and_b64 exec, exec, vcc\n\t"
               "s_cbranch_scc1 1b\n"
               "3:\n\t"
               "s_mov_b64 exec, %[keep]"
               : "={v[100:103]}"(v), [keep] "=&s"(keep), [left] "+s"(left), [t] "=&v"(t)
               : [off] "v"(off), [ws] "s"(ws), [want] "s"(want), [low] "i"(LOW)
               : "vcc", "scc", "memory");
  return v;
}
DI void quad_sum4(float& a, float& b, float& c, float& d) {
  asm("s_nop 1\n\t"
      "v_add_f32_dpp %0, %0, %0 quad_perm:[1,0,3,2] row_mask:0xf bank_mask:0xf\n\t"
      "v_add_f32_dpp %1, %1, %1 quad_perm:[1,0,3,2] row_mask:0xf bank_mask:0xf\n\t"
      "v_add_f32_dpp %2, %2, %2 quad_perm:[1,0,3,2] row_mask:0xf bank_mask:0xf\n\t"
      "v_add_f32_dpp %3, %3, %3 quad_perm:[1,0,3,2] row_mask:0xf bank_mask:0xf\n\t"
      "v_add_f32_dpp %0, %0, %0 quad_perm:[2,3,0,1] row_mask:0xf bank_mask:0xf\n\t"
      "v_add_f32_dpp %1, %1, %1 quad_perm:[2,3,0,1] row_mask:0xf bank_mask:0xf\n\t"
      "v_add_f32_dpp %2, %2, %2 quad_perm:[2,3,0,1] row_mask:0xf bank_mask:0xf\n\t"
      "v_add_f32_dpp %3, %3, %3 quad_perm:[2,3,0,1] row_mask:0xf bank_mask:0xf"
      : "+v"(a), "+v"(b), "+v"(c), "+v"(d));
}
DI float sum_records(u32x4 v) {
  const float t = half16(half32(first2(v[0], v[1]), first2(v[2], v[3])), half32(second2(v[0], v[1]), second2(v[2], v[3])));
  return t + dpp<ROR8>(t);
}

DI void layernorm(u32 (&x)[8]) {
  float v[16], s = 0.f;
#pragma unroll
  for (int e = 0; e < 8; ++e) { v[2 * e] = bflo(x[e]); v[2 * e + 1] = bfhi(x[e]); s += v[2 * e] + v[2 * e + 1]; }
  const float mean = half_sum(s) * (1.f / 512.f);
  float q = 0.f;
#pragma unroll
  for (int e = 0; e < 16; ++e) { const float d = v[e] - mean; q = fmaf(d, d, q); }
  const float var = half_sum(q) * (1.f / 512.f);
  const float mb = rbf(mean), inv = __builtin_amdgcn_rcpf(rbf(__builtin_amdgcn_sqrtf(rbf(rbf(var) + 1e-5f))));
#pragma unroll
  for (int e = 0; e < 8; ++e) { const u32 d = pk(v[2 * e] - mb, v[2 * e + 1] - mb); x[e] = pk(bflo(d) * inv, bfhi(d) * inv); }
}

DI f32x2 tile_matvec(const u32x4 (&wv)[8], LAS unsigned char* xin, int g, int c) {
  const u32x4 xf = *(LAS u32x4*)(xin + ((c >> 2) & 1) * 1024 + ((c >> 3) * 32 + g * 8) * 2);
  f32x4 lo = {0.f, 0.f, 0.f, 0.f}, hi = lo;
#pragma unroll
  for (int d = 0; d < 4; ++d) {
    const u32x4 am = sel4((c & 3) == d, xf);
    lo = mfma(am, colfrag(wv, d), lo);
    hi = mfma(am, colfrag(wv, 4 + d), hi);
  }
  const bool valid = (g >> 1) == (c >> 3);
  float t[4];
#pragma unroll
  for (int e = 0; e < 4; ++e) { t[e] = half32(valid ? lo[e] : 0.f, valid ? hi[e] : 0.f); t[e] += dpp<ROR8>(t[e]); }
  return (c & 8) ? (f32x2){t[2], t[3]} : (f32x2){t[0], t[1]};
}

#define ST(i)

__global__ void __launch_bounds__(1024, 4)
fused_decode(const u16* x_, const u16* k_, const u16* v_, const u16* wq_, const u16* wo_, const u16* w1_, const u16* b1_,
             const u16* w2_, const u16* b2_, u16* out_, unsigned char* ws_
             ) {
  __shared__ __attribute__((aligned(16))) unsigned char lds_raw[L_END];
  LAS unsigned char* lds = (LAS unsigned char*)lds_raw;
  ST(0);
  const u64 t_start = WO_MODE == 3 ? __builtin_amdgcn_s_memrealtime() : 0;
  asm volatile("" :: "s"(x_), "s"(k_), "s"(v_), "s"(wq_), "s"(wo_), "s"(w1_), "s"(b1_), "s"(w2_), "s"(b2_), "s"(out_), "s"(ws_));
  const int tid = threadIdx.x, lane = tid & 63, w = __builtin_amdgcn_readfirstlane(tid >> 6);
  const int g = lane >> 4, c = lane & 15;
  const int bid = blockIdx.x, kvh = bid & 7, j = bid >> 3, b = j >> 4, sp = j & 15, rb = j >> 2, cb = j & 3;
  const bool mlp1 = j < 14, mlp2 = j >= 14 && j < 28;
  const __amdgpu_buffer_rsrc_t ws = __builtin_amdgcn_make_buffer_rsrc(ws_, 0, WS_END, 0x00020000);
  u32 tag;
  const size_t kvbase = ((size_t)(b * 8 + kvh) * SK + sp * 512 + ((w + (ROT == 1 ? j : ROT == 2 ? bid * 5 : ROT == 3 ? kvh * 2 : 0)) & 15) * 32) * (DH / 8);
  const GAS u32x4* kp = (const GAS u32x4*)k_ + kvbase + (((c >> 1) & 1) * 4 + (c >> 2)) * 16 + g * 2 + (c & 1);
  u32x4 kf[8], vf[8];

  const int cq = j & 7, kq = j >> 3;
  const int mt = (mlp1 ? j : j - 14) * 8 + kvh;
  const int mhi = mt >> 2, mlo = mt & 3;
  if (w < 2 || (w < 4 && mlp1)) {
    __builtin_amdgcn_s_setprio(3);
    if (w == 0 && lane < 16) *(LAS u32*)(lds + L_FLAG + lane * 4) = 0u;
    const GAS u32x4* xp = (const GAS u32x4*)x_ + lane * 2;
    const u32x4 x0 = xp[0], x1 = xp[1];
    PIN();
    const int rs = w < 2 ? NQ / 8 : FF / 8, row0 = (w < 2 ? kq : mlo) * 128 + (w & 1) * 64;
    const u16* tile = w < 2 ? wq_ + kvh * 512 + cq * 64 : w1_ + mhi * 64;
    const GAS u32x4* wp = (const GAS u32x4*)tile + (row0 + (c >> 3) * 32 + g * 8) * rs + (c & 7);
    u32x4 wv[8];
#pragma unroll
    for (int i = 0; i < 8; ++i) wv[i] = ldnt(wp + i * rs);
    PIN();
    LDS_BARRIER();
    ST(1);
    tag = read_tag(ws_);
    PIN();
    u32 xs[8] = {x0[0], x0[1], x0[2], x0[3], x1[0], x1[1], x1[2], x1[3]};
    layernorm(xs);
    if (w >= 2) layernorm(xs);
    LAS unsigned char* xl = lds + L_XN + w * 2048;
    *(LAS u32x4*)(xl + lane * 32) = (u32x4){xs[0], xs[1], xs[2], xs[3]};
    *(LAS u32x4*)(xl + lane * 32 + 16) = (u32x4){xs[4], xs[5], xs[6], xs[7]};
    f32x2 r = tile_matvec(wv, xl + row0 * 2, g, c);
    LAS f32x2* half = (LAS f32x2*)(lds + L_HALF + (w >> 1) * 512 + lane * 8);
    if (w & 1) {
      *half = r;
      set_flag(lds, F_HALF + (w >> 1));
    } else {
      wait_flags(lds, F_HALF + (w >> 1), 0, lane);
      r += *half;
      const int rec = w == 0 ? WS_QP + ((((g & 1) * 8 + kvh) * 8 + cq) * 4 + kq) * 256 : WS_PA + mt * 512 + (g & 1) * 256;
      stws1(ws, rec + ((c & 7) * 4 + (g >> 1) * 2 + (c >> 3)) * 8, pack2(r[0], r[1], tag));
    }
    ST(2);
    __builtin_amdgcn_s_setprio(PRIO_K);
  } else {
    if (w < 4 && mlp2) {
      const u16* p = w2_ + (mhi * 64 + (w & 1) * 32 + g * 8) * D + mlo * 128 + c * 8;
#pragma unroll
      for (int i = 0; i < 8; ++i) dma16(p + i * D, (u32)(size_t)(lds + L_W2 + (w & 1) * 8192 + i * 1024));
    }
    if (CODE_LINES && w == 4) {
      const unsigned char* pc;
      asm volatile("s_getpc_b64 %0" : "=s"(pc));
      if (lane < 4 && j * 4 + lane < CODE_LINES) touch(pc + (j * 4 + lane) * 128, (u32)(size_t)(lds + L_TOUCH));
    }
    RAW_BARRIER();
    if ((w >> 2) == 1) __builtin_amdgcn_s_setprio(2); else __builtin_amdgcn_s_setprio(PRIO_K);
  }
  PIN();

#pragma unroll
  for (int i = 0; i < 8; ++i) { kf[i] = ldnt(kp + (i >> 1) * 128 + (i & 1) * 8); __builtin_amdgcn_sched_barrier(0); }
  if ((w >> 2) != 1) __builtin_amdgcn_s_setprio(0);
  PIN();
  {
    const GAS u32x4* vp = (const GAS u32x4*)v_ + kvbase + lane;
#pragma unroll
    for (int i = 0; i < 8; ++i) { vf[i] = ldnt(vp + i * 64); __builtin_amdgcn_sched_barrier(0); }
  }
  PIN();
  ST(4);
  tag = read_tag(ws_);
  const int paoff = WS_PA + mhi * 2048 + lane * 16;
  u32x4 p0, p1;
  u32 b1v;
  if ((w >> 2) == 1) {
    const int u = w & 3;
    const int qoff = WS_QP + (b * 8 + kvh) * 8192 + u * 2048 + lane * 16;
    asm volatile("s_waitcnt vmcnt(0)" ::: "memory");
    if (Q_DELAY) __builtin_amdgcn_s_sleep(Q_DELAY);
    u32x4 q0, q1;
#pragma unroll 1
    for (int spin = 0; spin < SPIN_CAP; ++spin) {
      q0 = ldws(ws, qoff); q1 = ldws(ws, qoff + 1024);
      if (__all(tagged(q0, tag) && tagged(q1, tag))) break;
      __builtin_amdgcn_s_sleep(SLEEP_Q);
      asm volatile("" ::: "memory");
    }
    const float qa = half16(half32(first2(q0[0], q0[1]), first2(q1[0], q1[1])), half32(first2(q0[2], q0[3]), first2(q1[2], q1[3])));
    const float qb = half16(half32(second2(q0[0], q0[1]), second2(q1[0], q1[1])), half32(second2(q0[2], q0[3]), second2(q1[2], q1[3])));
    *(LAS u32*)(lds + L_Q + u * 256 + ((g >> 1) * 32 + c * 2 + (g & 1)) * 4) = pk(qa, qb);
    set_flag(lds, F_Q + u);
    ST(3);
    if (w == 7 && mlp2) {
      p0 = ldws(ws, paoff); p1 = ldws(ws, paoff + 1024);
      b1v = *(const GAS u32*)(b1_ + mhi * 64 + c * 4 + (g >> 1) * 2);
      PIN();
    }
    __builtin_amdgcn_s_setprio(0);
  }
  wait_flags(lds, F_Q, 3, lane);

  float sc[8];
  {
    const int x0 = (c >> 2) & 1;
    const u32x4 q0 = *(LAS u32x4*)(lds + L_Q + (c & 3) * 256 + (g * 2 + x0) * 16);
    const u32x4 q1 = *(LAS u32x4*)(lds + L_Q + (c & 3) * 256 + 128 + (g * 2 + x0) * 16);
    constexpr float C2 = 0.08838834764831845f * LOG2E;
#pragma unroll
    for (int o = 0; o < 4; ++o) {
      f32x4 s = mfma(kf[2 * o], q0, (f32x4){0.f, 0.f, 0.f, 0.f});
      s = mfma(kf[2 * o + 1], q1, s);
#pragma unroll
      for (int a = 0; a < 2; ++a) {
        float e1 = s[2 * a + 1];
        asm("" : "+v"(e1));
        const float t = x0 ? e1 : s[2 * a];
        sc[2 * o + a] = (t + dpp<ROR4>(t)) * C2;
      }
    }
  }
  PIN();

    float m = fmaxf(fmaxf(fmaxf(sc[0], sc[1]), fmaxf(sc[2], sc[3])), fmaxf(fmaxf(sc[4], sc[5]), fmaxf(sc[6], sc[7])));
    m = max32(max16(m));
    float p[8], l = 0.f;
#pragma unroll
    for (int e = 0; e < 8; ++e) { p[e] = __builtin_amdgcn_exp2f(sc[e] - m); l += p[e]; }
    l = half16(l, l); l = half32(l, l);
    if (lane < 4) { *(LAS float*)(lds + L_MW + (lane * 16 + w) * 4) = m; *(LAS float*)(lds + L_LW + (lane * 16 + w) * 4) = l; }
    PIN();
    if ((w >> 1) == 1) set_flag(lds, F_W2 + (w & 1));
    if (lane == 0) __hip_atomic_fetch_add((LAS u32*)(lds + L_CNT), 1u, __ATOMIC_RELAXED, __HIP_MEMORY_SCOPE_WORKGROUP);
    const u32x4 pf = {pk(p[0], p[1]), pk(p[2], p[3]), pk(p[4], p[5]), pk(p[6], p[7])};
    u32x4 am[4];
#pragma unroll
    for (int u = 0; u < 4; ++u) am[u] = sel4((c >> 2) == u, pf);
    PIN();
    u32x4 ve[4], vo[4];
    u32 slo, shi;
    asm volatile("s_mov_b32 %0, 0x05040100\n\ts_mov_b32 %1, 0x07060302" : "=s"(slo), "=s"(shi) : "v"(am[0][0]), "v"(am[1][1]), "v"(am[2][2]), "v"(am[3][3]));
#pragma unroll
    for (int r = 0; r < 4; ++r) {
#pragma unroll
      for (int u = 0; u < 4; ++u) {
        ve[u][r] = __builtin_amdgcn_perm(vf[2 * r + 1][u], vf[2 * r][u], slo);
        vo[u][r] = __builtin_amdgcn_perm(vf[2 * r + 1][u], vf[2 * r][u], shi);
      }
      if (r < 3)
        asm volatile("" : "+s"(slo), "+s"(shi) : "v"(ve[0][r]), "v"(ve[1][r]), "v"(ve[2][r]), "v"(ve[3][r]), "v"(vo[0][r]), "v"(vo[1][r]), "v"(vo[2][r]), "v"(vo[3][r]));
    }
    f32x4 oe = {0.f, 0.f, 0.f, 0.f}, oo = oe;
#pragma unroll
    for (int u = 0; u < 4; ++u) { oe = mfma(am[u], ve[u], oe); oo = mfma(am[u], vo[u], oo); }
#pragma unroll
    for (int h = 0; h < 4; ++h) *(LAS f32x2*)(lds + L_OW + w * OW_STRIDE + h * 512 + c * 32 + g * 8) = (f32x2){oe[h], oo[h]};
    PIN();
    ST(5);

  const bool gath = (w & 4) == 0;
  const int v = (w & 3) + (w >> 3) * 4;
  auto wait_ml = [&]() __attribute__((always_inline)) {
#pragma unroll 1
    for (int spin = 0; spin < (SPIN_CAP << 4); ++spin) {
      if (__builtin_amdgcn_readfirstlane(*(volatile LAS u32*)(lds + L_CNT)) == 16u) break;
      __builtin_amdgcn_s_sleep(1);
    }
    PIN();
  };
  auto load_wo = [&](u32x4 (&wt)[8]) __attribute__((always_inline)) {
    const GAS u16* p = (const GAS u16*)wo_ + (size_t)(kvh * 512 + rb * 64 + g * 4) * D + cb * 128 + (w - 4) * 64 + c * 4;
#pragma unroll
    for (int i = 0; i < 8; ++i) {
      const u32x2 t0 = ldnt((const GAS u32x2*)(p + (size_t)(32 * (i >> 2) + (i & 3)) * D));
      const u32x2 t1 = ldnt((const GAS u32x2*)(p + (size_t)(32 * (i >> 2) + 16 + (i & 3)) * D));
      wt[i] = (u32x4){t0[0], t0[1], t1[0], t1[1]};
    }
    PIN();
  };
  const int cf = bid & 3, bf = (bid >> 2) & 1, seg = bid >> 3;
  if (w >= 12) {
    LDS_BARRIER();
    ST(6);
    if (bid < NFIN) {
      const int row = (w - 12) * 8 + (lane >> 3);
      const int moff = WS_MP + (cf * 28 + min(row, 27)) * 1024 + bf * 512 + seg * 128 + (lane & 7) * 16;
      u32x4 mv;
#pragma unroll 1
      for (int spin = 0; spin < SPIN_CAP; ++spin) {
        mv = ldws(ws, moff);
        if (__all(tagged(mv, tag))) break;
        __builtin_amdgcn_s_sleep(SLEEP_M);
        asm volatile("" ::: "memory");
      }
      const float t = sum_records(sel4(row < 28, mv));
      if ((lane & 8) == 0) *(LAS float*)(lds + L_MF + (w - 12) * 128 + ((lane & 7) * 4 + g) * 4) = t;
      LDS_BARRIER();
    }
  } else if (gath) {
    {
      const int f = v * 16 + (lane >> 2), p4 = lane & 3, hd = v >> 1;
      wait_ml();
      const f32x4 mv = *(LAS f32x4*)(lds + L_MW + hd * 64 + p4 * 16), lv = *(LAS f32x4*)(lds + L_LW + hd * 64 + p4 * 16);
      float M = fmaxf(fmaxf(mv[0], mv[1]), fmaxf(mv[2], mv[3]));
      M = fmaxf(M, dpp<XOR1>(M)); M = fmaxf(M, dpp<XOR2>(M));
      float L = 0.f, fw[4];
#pragma unroll
      for (int i = 0; i < 4; ++i) { fw[i] = __builtin_amdgcn_exp2f(mv[i] - M); L = fmaf(fw[i], lv[i], L); }
      L += dpp<XOR1>(L); L += dpp<XOR2>(L);
      const float inv = __builtin_amdgcn_rcpf(L);
#pragma unroll
      for (int i = 0; i < 4; ++i) fw[i] *= inv;
      const u32 lam = f2u(M + __builtin_amdgcn_logf(L)) >> ((lane >> 5) * 16);
      u32 t0 = tag | (lam & 255u), t1 = tag | ((lam >> 8) & 255u);
      int ooff = WS_OP + ((kvh * 2 + b) * 16 + sp) * 2048 + f * 16;
      int laddr = L_OW + p4 * 4 * OW_STRIDE + f * 16;
      asm volatile("" : "+v"(fw[0]), "+v"(fw[1]), "+v"(fw[2]), "+v"(fw[3]), "+v"(t0), "+v"(t1), "+v"(ooff), "+v"(laddr));
      ST(3);
      LDS_BARRIER();
      ST(6);
      f32x4 s = fw[0] * *(LAS f32x4*)(lds + laddr);
#pragma unroll
      for (int i = 1; i < 4; ++i) s += fw[i] * *(LAS f32x4*)(lds + laddr + i * OW_STRIDE);
      float s0 = s[0], s1 = s[1], s2 = s[2], s3 = s[3];
      quad_sum4(s0, s1, s2, s3);
      if (p4 == 0) __builtin_amdgcn_raw_buffer_store_b128((u32x4){pk(s0, s1), t0, pk(s2, s3), t1}, ws, ooff, 0, SAUX);
    }
    PIN();
    ST(7);
    {
      u32 gaddr2 = (u32)(size_t)(lds + L_GP + v * GP_STRIDE + 256), gaddr = gaddr2 - 256 + (c * 4 + g) * 4;
      u32 lsel = (c & 8) ? 0x05040100u : 0x01000504u;
      asm volatile("" : "+v"(gaddr), "+v"(gaddr2), "+v"(lsel));
      if (SLEEP_O) __builtin_amdgcn_s_sleep(SLEEP_O);
      const u32x4 ov = poll16<255>(ws, WS_OP + (kvh * 32 + v * 4 + g) * 2048 + rb * 256 + c * 16, tag);
      ST(8);
      u32 a, lam, m1, x0, x1, x2, x3;
      f32x2 gl;
      asm volatile("v_perm_b32 %[a], %[t1], %[t0], %[sel]\n\t"
                   "v_lshlrev_b32 %[x0], 16, %[d0]\n\t"
                   "v_lshlrev_b32 %[x2], 16, %[d1]\n\t"
                   "v_mov_b32_dpp %[m1], %[a] row_ror:8 row_mask:0xf bank_mask:0xf\n\t"
                   "v_and_b32 %[x1], 0xffff0000, %[d0]\n\t"
                   "v_perm_b32 %[lam], %[a], %[m1], %[lsel]\n\t"
                   "v_mov_b32 %[m1], %[lam]\n\t"
                   "v_mov_b32 %[a], %[lam]\n\t"
                   "v_and_b32 %[x3], 0xffff0000, %[d1]\n\t"
                   "s_nop 0\n\t"
                   "v_permlane16_swap_b32 %[m1], %[a]\n\t"
                   "v_max_f32 %[m1], %[m1], %[a]\n\t"
                   "v_mov_b32 %[a], %[m1]\n\t"
                   "s_nop 1\n\t"
                   "v_permlane32_swap_b32 %[m1], %[a]\n\t"
                   "v_max_f32 v104, %[m1], %[a]\n\t"
                   "v_sub_f32 %[lam], %[lam], v104\n\t"
                   "v_exp_f32 %[lam], %[lam]\n\t"
                   "s_nop 0\n\t"
                   "v_mul_f32 %[x0], %[lam], %[x0]\n\t"
                   "v_mul_f32 %[x2], %[lam], %[x2]\n\t"
                   "v_mul_f32 %[x1], %[lam], %[x1]\n\t"
                   "v_mul_f32 %[x3], %[lam], %[x3]\n\t"
                   "v_mov_b32 %[a], %[lam]\n\t"
                   "v_permlane32_swap_b32 %[x0], %[x2]\n\t"
                   "v_permlane32_swap_b32 %[x1], %[x3]\n\t"
                   "v_permlane16_swap_b32 %[lam], %[a]\n\t"
                   "v_add_f32 %[x0], %[x0], %[x2]\n\t"
                   "v_add_f32 %[x1], %[x1], %[x3]\n\t"
                   "v_add_f32 %[lam], %[lam], %[a]\n\t"
                   "v_mov_b32 %[a], %[lam]\n\t"
                   "v_permlane16_swap_b32 %[x0], %[x1]\n\t"
                   "v_add_f32 %[x0], %[x0], %[x1]\n\t"
                   "v_permlane32_swap_b32 %[lam], %[a]\n\t"
                   "ds_write_b32 %[ga], %[x0]\n\t"
                   "v_add_f32 v105, %[lam], %[a]\n\t"
                   "ds_write_b64 %[gb], v[104:105]"
                   : [a] "=&v"(a), [lam] "=&v"(lam), [m1] "=&v"(m1), [x0] "=&v"(x0), [x1] "=&v"(x1), [x2] "=&v"(x2), [x3] "=&v"(x3), "=&{v[104:105]}"(gl)
                   : [d0] "v"(ov[0]), [t0] "v"(ov[1]), [d1] "v"(ov[2]), [t1] "v"(ov[3]), [sel] "s"(0x04000400u), [lsel] "v"(lsel), [ga] "v"(gaddr), [gb] "v"(gaddr2)
                   : "memory");
    }
    LDS_BARRIER();
    ST(9);
    if (bid < NFIN) {
      int faddr = L_FP + ((lane & 7) * 4 + g) * 32 + v * 4;
      asm volatile("" : "+v"(faddr));
      if (SLEEP_S) __builtin_amdgcn_s_sleep(SLEEP_S);
      const u32x4 sv = poll16<255>(ws, WS_S2 + (cf * 64 + v * 8 + (lane >> 3)) * 1024 + bf * 512 + seg * 128 + (lane & 7) * 16, tag);
      ST(10);
      *(LAS float*)(lds + faddr) = sum_records(sv);
      LDS_BARRIER();
    }
  } else if (w < 6) {
    u32x4 wt[8], bw[8];
    if (WO_MODE == 3) {
      wait_ml();
      const bool now = __builtin_amdgcn_s_memrealtime() >= t_start + WO_AT;
      if (!now) {
        LDS_BARRIER();
        ST(6);
        while (__builtin_amdgcn_s_memrealtime() < t_start + WO_AT) __builtin_amdgcn_s_sleep(1);
      }
      load_wo(wt);
      if (now) { LDS_BARRIER(); ST(6); }
    } else {
    if (WO_MODE == 1) wait_ml();
    if (WO_MODE < 2) load_wo(wt);
    ST(7);
    LDS_BARRIER();
    ST(6);
    if (WO_MODE == 2) { if (WO_SLEEP) __builtin_amdgcn_s_sleep(WO_SLEEP); load_wo(wt); }
    }
#pragma unroll
    for (int i = 0; i < 8; ++i) bw[i] = colfrag(wt, i);
    int gp = L_GP + (c & 7) * GP_STRIDE, go = gp + ((c >> 3) * 16 + g * 4) * 4;
    int soff = WS_S2 + (cb * 64 + kvh * 8 + rb) * 1024 + (g & 1) * 512 + ((w - 4) * 32 + c * 2 + (g >> 1)) * 8;
    asm volatile("" : "+v"(gp), "+v"(go), "+v"(soff) : "v"(bw[0]), "v"(bw[1]), "v"(bw[2]), "v"(bw[3]), "v"(bw[4]), "v"(bw[5]), "v"(bw[6]), "v"(bw[7]));
    LDS_BARRIER();
    ST(9);
    f32x2 gl = *(LAS f32x2*)(lds + gp + 256);
    u32x4 of = *(LAS u32x4*)(lds + go), o1 = *(LAS u32x4*)(lds + go + 128);
#define QP1(r) "v_add_f32_dpp " r ", " r ", " r " quad_perm:[1,0,3,2] row_mask:0xf bank_mask:0xf\n\t"
#define QP2(r) "v_add_f32_dpp " r ", " r ", " r " quad_perm:[2,3,0,1] row_mask:0xf bank_mask:0xf\n\t"
#define PKM(r) "v_pk_mul_f32 " r ", v[118:119], " r " op_sel_hi:[0,1]\n\t"
    asm volatile("v_max_f32_dpp v118, v116, v116 quad_perm:[1,0,3,2] row_mask:0xf bank_mask:0xf\n\t"
                 "s_nop 1\n\t"
                 "v_max_f32_dpp v118, v118, v118 quad_perm:[2,3,0,1] row_mask:0xf bank_mask:0xf\n\t"
                 "v_sub_f32 v118, v116, v118\n\t"
                 "v_exp_f32 v118, v118\n\t"
                 "s_nop 0\n\t"
                 "v_mul_f32 v117, v118, v117\n\t"
                 PKM("v[108:109]") PKM("v[110:111]") PKM("v[112:113]") PKM("v[114:115]")
                 QP1("v117") QP1("v108") QP1("v109") QP1("v110") QP1("v111") QP1("v112") QP1("v113") QP1("v114") QP1("v115")
                 QP2("v117") QP2("v108") QP2("v109")
                 "v_rcp_f32 v118, v117\n\t"
                 QP2("v110") QP2("v111") QP2("v112") QP2("v113") QP2("v114") QP2("v115")
                 PKM("v[108:109]") PKM("v[110:111]") PKM("v[112:113]") PKM("v[114:115]")
                 "v_cvt_pk_bf16_f32 v108, v108, v109\n\t"
                 "v_cvt_pk_bf16_f32 v109, v110, v111\n\t"
                 "v_cvt_pk_bf16_f32 v110, v112, v113\n\t"
                 "v_cvt_pk_bf16_f32 v111, v114, v115\n\t"
                 "s_nop 1"
                 : "+{v[108:111]}"(of), "+{v[112:115]}"(o1), "+{v[116:117]}"(gl)
                 :
                 : "v118", "v119");
    f32x4 acc[8];
#pragma unroll
    for (int i = 0; i < 8; ++i) { const int k = (i & 3) * 2 + (i >> 2); acc[k] = mfma(of, bw[k], (f32x4){0.f, 0.f, 0.f, 0.f}); }
    __builtin_amdgcn_sched_barrier(0);
    float r[4];
#pragma unroll
    for (int d = 0; d < 4; ++d) r[d] = (g >> 1) ? acc[4 + d][0] : acc[d][0];
    stws1(ws, soff, pack2(half32(r[0], r[2]), half32(r[1], r[3]), tag));
    ST(10);
  } else if (w == 7 && mlp2) {
#pragma unroll 1
    for (int spin = 0; spin < SPIN_CAP; ++spin) {
      if (__all(tagged(p0, tag) && tagged(p1, tag))) break;
      __builtin_amdgcn_s_sleep(SLEEP_Q);
      asm volatile("" ::: "memory");
      p0 = ldws(ws, paoff); p1 = ldws(ws, paoff + 1024);
    }
    const float t2[2] = {half32(first2(p0[0], p0[1]) + first2(p1[0], p1[1]), first2(p0[2], p0[3]) + first2(p1[2], p1[3])),
                         half32(second2(p0[0], p0[1]) + second2(p1[0], p1[1]), second2(p0[2], p0[3]) + second2(p1[2], p1[3]))};
    float hv[2];
#pragma unroll
    for (int e = 0; e < 2; ++e) {
      const float t = rbf(rbf(t2[e]) + (e ? bfhi(b1v) : bflo(b1v)));
      const float uu = 0.7978845608028654f * (t + 0.044715f * t * t * t);
      hv[e] = t / (1.f + __builtin_amdgcn_exp2f(-2.f * LOG2E * uu));
    }
    *(LAS u32*)(lds + L_HS + (g & 1) * 128 + (c * 2 + (g >> 1)) * 4) = pk(hv[0], hv[1]);
    wait_flags(lds, F_W2, 1, lane);
    f32x4 r = {0.f, 0.f, 0.f, 0.f};
#pragma unroll
    for (int s = 0; s < 2; ++s) {
      const u32x4 ha = *(LAS u32x4*)(lds + L_HS + ((c >> 2) & 1) * 128 + s * 64 + g * 16);
      u32x4 w2t[8];
#pragma unroll
      for (int i = 0; i < 8; ++i) w2t[i] = *(LAS u32x4*)(lds + L_W2 + s * 8192 + i * 1024 + lane * 16);
#pragma unroll
      for (int d = 0; d < 8; ++d) r = mfma(sel4((c >> 3) == (d >> 2) && (c & 3) == (d & 3), ha), colfrag(w2t, d), r);
    }
    u32x2 m0 = pack2(r[0], r[1], tag), m1 = pack2(r[2], r[3], tag);
    asm volatile("" : "+v"(m0), "+v"(m1));
    ST(2);
    if (MP_LATE) LDS_BARRIER();
    stws(ws, WS_MP + (mlo * 28 + mhi) * 1024 + (g & 1) * 512 + (c * 2 + (g >> 1)) * 16, m0, m1);
    if (!MP_LATE) LDS_BARRIER();
    ST(6);
  } else if (w == 6 && bid < NFIN) {
    const int col = cf * 128 + seg * 32 + (lane & 31);
    const u32 xv = ((const GAS u16*)x_)[bf * D + col], b2v = ((const GAS u16*)b2_)[col];
    LDS_BARRIER();
    ST(6);
    LDS_BARRIER();
    LAS float* mf = (LAS float*)(lds + L_MF + (lane >> 5) * 256 + (lane & 31) * 4);
    float t = mf[0] + mf[32];
    t = half32(t, t);
    t = rbf(rbf(t) + bflo(b2v));
    float gate = rbf(t / (1.f + __builtin_amdgcn_exp2f(-LOG2E * t))), xf = bflo(xv);
    int faddr = L_FP + (lane & 31) * 32 + (lane >> 5) * 16;
    GAS u16* op = (GAS u16*)out_ + bf * D + col;
    asm volatile("" : "+v"(gate), "+v"(xf), "+v"(faddr), "+v"(op));
    LDS_BARRIER();
    ST(10);
    const f32x4 fp = *(LAS f32x4*)(lds + faddr);
    float s = (fp[0] + fp[1]) + (fp[2] + fp[3]);
    s = half32(s, s);
    const float res = xf + rbf(rbf(s) * gate);
    if (lane < 32) __hip_atomic_store(op, (u16)pk(res, res), RLX_AGENT);
    if (bid == 0 && lane == 0) __hip_atomic_store((GAS u32*)(ws_ + WS_EPOCH), tag >> 8, RLX_AGENT);
  } else {
    LDS_BARRIER();
    ST(6);
  }
  ST(11);
}

extern "C" void kernel_launch(void* const* d_in, const int* in_sizes, int n_in, void* d_out, int out_size, void* d_ws, size_t ws_size,
                              hipStream_t stream) {
  unsigned char* ws = (unsigned char*)d_ws + (WS_AT_END ? ((ws_size - WS_SPAN) & ~(size_t)4095) : 0);
  fused_decode<<<256, 1024, 0, stream>>>((const u16*)d_in[0], (const u16*)d_in[1], (const u16*)d_in[2], (const u16*)d_in[3],
                                         (const u16*)d_in[4], (const u16*)d_in[6], (const u16*)d_in[7], (const u16*)d_in[8],
                                         (const u16*)d_in[9], (u16*)d_out, ws
                                         );
}
```

```cpp
#include <hip/hip_runtime.h>
#include <stdint.h>

#define GAS __attribute__((address_space(1)))
#define LAS __attribute__((address_space(3)))
#define DI __device__ __forceinline__
#define RLX_AGENT __ATOMIC_RELAXED, __HIP_MEMORY_SCOPE_AGENT
typedef unsigned short u16;
typedef unsigned u32;
typedef unsigned long long u64;
typedef u32 u32x2 __attribute__((ext_vector_type(2)));
typedef u32 u32x4 __attribute__((ext_vector_type(4)));
typedef float f32x2 __attribute__((ext_vector_type(2)));
typedef float f32x4 __attribute__((ext_vector_type(4)));
typedef __bf16 bf16x2 __attribute__((ext_vector_type(2)));
typedef __bf16 bf16x8 __attribute__((ext_vector_type(8)));

constexpr int D = 512, SK = 8192, DH = 128, FF = 1792, NQ = 4096;
constexpr int SC1 = 16, NT = 2;
constexpr int SPIN_CAP = 1 << 18;
constexpr int NFIN = 32;
constexpr int NAP_Q = 32, NAP_O = 8, NAP_S = 8, NAP_RETRY = 8;
constexpr float LOG2E = 1.4426950408889634f;

constexpr int WS_EPOCH = 0;
constexpr int WS_QP = 256;
constexpr int WS_PA = WS_QP + 512 * 256;
constexpr int WS_OP = WS_PA + 112 * 512;
constexpr int WS_S2 = WS_OP + 256 * 2048;
constexpr int WS_MP = WS_S2 + 256 * 1024;
constexpr int WS_END = WS_MP + 112 * 1024;

constexpr int F_Q = 0, F_HALF = 4, F_W2 = 6;
constexpr int L_FLAG = 0, L_CNT = 48, L_XN = 64, L_HALF = L_XN + 8192, L_Q = L_HALF + 1024, L_MW = L_Q + 1024, L_LW = L_MW + 256,
              L_HS = L_LW + 256, L_GP = L_HS + 256, GP_STRIDE = 272, L_FP = L_GP + 8 * GP_STRIDE, L_MF = L_FP + 1024,
              L_W2 = L_MF + 512, L_OW = L_W2 + 16384, OW_STRIDE = 2064, L_END = L_OW + 16 * OW_STRIDE;

DI float bflo(u32 v) { return __builtin_bit_cast(float, v << 16); }
DI float bfhi(u32 v) { return __builtin_bit_cast(float, v & 0xffff0000u); }
DI float rbf(float f) { return (float)(__bf16)f; }
DI u32 pk(float a, float b) { return __builtin_bit_cast(u32, __builtin_convertvector((f32x2){a, b}, bf16x2)); }
DI u32 f2u(float f) { return __builtin_bit_cast(u32, f); }
DI float u2f(u32 u) { return __builtin_bit_cast(float, u); }
DI f32x4 mfma(u32x4 a, u32x4 b, f32x4 c) {
  return __builtin_amdgcn_mfma_f32_16x16x32_bf16(__builtin_bit_cast(bf16x8, a), __builtin_bit_cast(bf16x8, b), c, 0, 0, 0);
}
template <int CTRL> DI float dpp(float x) { return u2f(__builtin_amdgcn_mov_dpp(f2u(x), CTRL, 0xf, 0xf, true)); }
constexpr int XOR1 = 0xB1, XOR2 = 0x4E, ROR4 = 0x124, ROR8 = 0x128;
DI float half32(float a, float b) { auto r = __builtin_amdgcn_permlane32_swap(f2u(a), f2u(b), false, false); return u2f(r[0]) + u2f(r[1]); }
DI float half16(float a, float b) { auto r = __builtin_amdgcn_permlane16_swap(f2u(a), f2u(b), false, false); return u2f(r[0]) + u2f(r[1]); }
DI float max32(float a) { auto r = __builtin_amdgcn_permlane32_swap(f2u(a), f2u(a), false, false); return fmaxf(u2f(r[0]), u2f(r[1])); }
DI float max16(float a) { auto r = __builtin_amdgcn_permlane16_swap(f2u(a), f2u(a), false, false); return fmaxf(u2f(r[0]), u2f(r[1])); }
DI float half_sum(float v) {
  v += dpp<XOR1>(v); v += dpp<XOR2>(v); v += dpp<ROR4>(v); v += dpp<ROR8>(v);
  return half16(v, v);
}
DI u32 plo(u32 k1, u32 k0) { return __builtin_amdgcn_perm(k1, k0, 0x05040100u); }
DI u32 phi(u32 k1, u32 k0) { return __builtin_amdgcn_perm(k1, k0, 0x07060302u); }
DI u32x4 colfrag(const u32x4 (&r)[8], int d) {
  const int q = d >> 1;
  return (d & 1) ? (u32x4){phi(r[1][q], r[0][q]), phi(r[3][q], r[2][q]), phi(r[5][q], r[4][q]), phi(r[7][q], r[6][q])}
                 : (u32x4){plo(r[1][q], r[0][q]), plo(r[3][q], r[2][q]), plo(r[5][q], r[4][q]), plo(r[7][q], r[6][q])};
}
DI u32x4 sel4(bool c, u32x4 v) { return (u32x4){c ? v[0] : 0u, c ? v[1] : 0u, c ? v[2] : 0u, c ? v[3] : 0u}; }
template <typename T> DI T ldnt(const GAS T* p) { return __builtin_nontemporal_load(p); }
DI u32x4 ldws(__amdgpu_buffer_rsrc_t ws, int off) { return __builtin_amdgcn_raw_buffer_load_b128(ws, off, 0, SC1); }
DI void stws(__amdgpu_buffer_rsrc_t ws, int off, u32x2 a, u32x2 b) { __builtin_amdgcn_raw_buffer_store_b128((u32x4){a[0], a[1], b[0], b[1]}, ws, off, 0, SC1); }
DI void stws1(__amdgpu_buffer_rsrc_t ws, int off, u32x2 a) { __builtin_amdgcn_raw_buffer_store_b64(a, ws, off, 0, SC1); }
DI u32x2 pack2(float a, float b, u32 tag8) {
  const u32 ar = f2u(a) + 0x800u;
  const u32 u = (f2u(b) & 0xf000u) | ((ar >> 4) & ~0xf000u);
  return (u32x2){phi(f2u(b), ar), __builtin_amdgcn_perm(tag8, u, 0x07060501u)};
}
DI float first2(u32 w0, u32 w1) { return u2f(__builtin_amdgcn_perm(w0, w1 << 4, 0x0504000cu)); }
DI float second2(u32 w0, u32 w1) { return u2f(__builtin_amdgcn_perm(w0, w1, 0x0706000cu)); }
DI bool tagged(u32x4 v, u32 tag8) { return ((v[1] ^ tag8) | (v[3] ^ tag8)) < 256u; }
#define PIN() do { asm volatile("" ::: "memory"); __builtin_amdgcn_sched_barrier(0); } while (0)
#define LDS_BARRIER() do { asm volatile("s_waitcnt lgkmcnt(0)\n\ts_barrier" ::: "memory"); __builtin_amdgcn_sched_barrier(0); } while (0)
#define RAW_BARRIER() do { __builtin_amdgcn_sched_barrier(0); asm volatile("s_barrier" ::: "memory"); __builtin_amdgcn_sched_barrier(0); } while (0)
DI void dma16(const u16* src, u32 dst) {
  u32 keep;
  asm volatile("s_mov_b32 %0, m0\n\ts_mov_b32 m0, %2\n\ts_nop 0\n\tglobal_load_lds_dwordx4 %1, off nt\n\ts_mov_b32 m0, %0"
               : "=&s"(keep) : "v"(src), "s"(dst) : "memory");
}
DI void set_flag(LAS unsigned char* lds, int f) {
  asm volatile("s_waitcnt lgkmcnt(0)" ::: "memory");
  *(volatile LAS u32*)(lds + L_FLAG + f * 4) = 1u;
}
DI void wait_flags(LAS unsigned char* lds, int first, int mask, int lane) {
#pragma unroll 1
  for (int spin = 0; spin < (SPIN_CAP << 4); ++spin) {
    if (__all(*(volatile LAS u32*)(lds + L_FLAG + (first + (lane & mask)) * 4) != 0u)) break;
    __builtin_amdgcn_s_sleep(1);
  }
  PIN();
}
DI u32 read_tag(const unsigned char* ws) {
  u32 e;
  asm volatile("s_load_dword %0, %1, 0x0\n\ts_waitcnt lgkmcnt(0)" : "=s"(e) : "s"(ws) : "memory");
  return (e + 1u) << 8;
}
DI u32x4 poll16(__amdgpu_buffer_rsrc_t ws, int off, u32 want) {
  u32x4 v;
  u64 keep;
  u32 left = SPIN_CAP, t;
  asm volatile("s_mov_b64 %[keep], exec\n\t"
               "s_branch 2f\n"
               "1:\n\t"
               "s_sub_u32 %[left], %[left], 1\n\t"
               "s_cbranch_scc1 3f\n"
               "2:\n\t"
               "buffer_load_dwordx4 v[100:103], %[off], %[ws], 0 offen sc1\n\t"
               "s_waitcnt vmcnt(0)\n\t"
               "v_bitop3_b32 %[t], v101, v103, %[want] bitop3:0x7e\n\t"
               "v_cmp_lt_u32 vcc, 0xff, %[t]\n\t"
               "s_and_b64 exec, exec, vcc\n\t"
               "s_cbranch_scc1 1b\n"
               "3:\n\t"
               "s_mov_b64 exec, %[keep]"
               : "=&{v[100:103]}"(v), [keep] "=&s"(keep), [left] "+s"(left), [t] "=&v"(t)
               : [off] "v"(off), [ws] "s"(ws), [want] "s"(want)
               : "vcc", "scc", "memory");
  return v;
}
DI void quad_sum4(float& a, float& b, float& c, float& d) {
  asm volatile("s_nop 1\n\t"
      "v_add_f32_dpp %0, %0, %0 quad_perm:[1,0,3,2] row_mask:0xf bank_mask:0xf\n\t"
      "v_add_f32_dpp %1, %1, %1 quad_perm:[1,0,3,2] row_mask:0xf bank_mask:0xf\n\t"
      "v_add_f32_dpp %2, %2, %2 quad_perm:[1,0,3,2] row_mask:0xf bank_mask:0xf\n\t"
      "v_add_f32_dpp %3, %3, %3 quad_perm:[1,0,3,2] row_mask:0xf bank_mask:0xf\n\t"
      "v_add_f32_dpp %0, %0, %0 quad_perm:[2,3,0,1] row_mask:0xf bank_mask:0xf\n\t"
      "v_add_f32_dpp %1, %1, %1 quad_perm:[2,3,0,1] row_mask:0xf bank_mask:0xf\n\t"
      "v_add_f32_dpp %2, %2, %2 quad_perm:[2,3,0,1] row_mask:0xf bank_mask:0xf\n\t"
      "v_add_f32_dpp %3, %3, %3 quad_perm:[2,3,0,1] row_mask:0xf bank_mask:0xf"
      : "+v"(a), "+v"(b), "+v"(c), "+v"(d));
}
DI float sum_records(u32x4 v) {
  const float t = half16(half32(first2(v[0], v[1]), first2(v[2], v[3])), half32(second2(v[0], v[1]), second2(v[2], v[3])));
  return t + dpp<ROR8>(t);
}

DI void layernorm(u32 (&x)[8]) {
  float v[16], s = 0.f;
#pragma unroll
  for (int e = 0; e < 8; ++e) { v[2 * e] = bflo(x[e]); v[2 * e + 1] = bfhi(x[e]); s += v[2 * e] + v[2 * e + 1]; }
  const float mean = half_sum(s) * (1.f / 512.f);
  float q = 0.f;
#pragma unroll
  for (int e = 0; e < 16; ++e) { const float d = v[e] - mean; q = fmaf(d, d, q); }
  const float var = half_sum(q) * (1.f / 512.f);
  const float mb = rbf(mean), inv = __builtin_amdgcn_rcpf(rbf(__builtin_amdgcn_sqrtf(rbf(rbf(var) + 1e-5f))));
#pragma unroll
  for (int e = 0; e < 8; ++e) { const u32 d = pk(v[2 * e] - mb, v[2 * e + 1] - mb); x[e] = pk(bflo(d) * inv, bfhi(d) * inv); }
}

DI f32x2 tile_matvec(const u32x4 (&wv)[8], LAS unsigned char* xin, int g, int c) {
  const u32x4 xf = *(LAS u32x4*)(xin + ((c >> 2) & 1) * 1024 + ((c >> 3) * 32 + g * 8) * 2);
  f32x4 lo = {0.f, 0.f, 0.f, 0.f}, hi = lo;
#pragma unroll
  for (int d = 0; d < 4; ++d) {
    const u32x4 am = sel4((c & 3) == d, xf);
    lo = mfma(am, colfrag(wv, d), lo);
    hi = mfma(am, colfrag(wv, 4 + d), hi);
  }
  const bool valid = (g >> 1) == (c >> 3);
  float t[4];
#pragma unroll
  for (int e = 0; e < 4; ++e) { t[e] = half32(valid ? lo[e] : 0.f, valid ? hi[e] : 0.f); t[e] += dpp<ROR8>(t[e]); }
  return (c & 8) ? (f32x2){t[2], t[3]} : (f32x2){t[0], t[1]};
}

__global__ void __launch_bounds__(1024, 4)
fused_decode(const u16* x_, const u16* k_, const u16* v_, const u16* wq_, const u16* wo_, const u16* w1_, const u16* b1_,
             const u16* w2_, const u16* b2_, u16* out_, unsigned char* ws_) {
  __shared__ __attribute__((aligned(16))) unsigned char lds_raw[L_END];
  LAS unsigned char* lds = (LAS unsigned char*)lds_raw;
  asm volatile("" :: "s"(x_), "s"(k_), "s"(v_), "s"(wq_), "s"(wo_), "s"(w1_), "s"(b1_), "s"(w2_), "s"(b2_), "s"(out_), "s"(ws_));
  const int tid = threadIdx.x, lane = tid & 63, w = __builtin_amdgcn_readfirstlane(tid >> 6);
  const int g = lane >> 4, c = lane & 15;
  const int bid = blockIdx.x, kvh = bid & 7, j = bid >> 3, b = j >> 4, sp = j & 15, rb = j >> 2, cb = j & 3;
  const bool mlp1 = j < 14, mlp2 = j >= 14 && j < 28;
  const __amdgpu_buffer_rsrc_t ws = __builtin_amdgcn_make_buffer_rsrc(ws_, 0, WS_END, 0x00020000);
  u32 tag;
  const size_t kvseq = (size_t)(b * 8 + kvh) * SK * DH;
  const __amdgpu_buffer_rsrc_t kr = __builtin_amdgcn_make_buffer_rsrc((void*)(k_ + kvseq), 0, SK * DH * 2, 0x00020000);
  const __amdgpu_buffer_rsrc_t vr = __builtin_amdgcn_make_buffer_rsrc((void*)(v_ + kvseq), 0, SK * DH * 2, 0x00020000);
  const int kvoff = (sp * 16 + ((w + j) & 15)) * 8192;
  const int klane = (((c >> 1) & 1) * 4 + (c >> 2)) * 256 + (g * 2 + (c & 1)) * 16;
  u32x4 kf[8], vf[8];

  const int cq = j & 7, kq = j >> 3;
  const int mt = (mlp1 ? j : j - 14) * 8 + kvh;
  const int mhi = mt >> 2, mlo = mt & 3;
  if (w < 2 || (w < 4 && mlp1)) {
    __builtin_amdgcn_s_setprio(3);
    if (w == 0 && lane < 16) *(LAS u32*)(lds + L_FLAG + lane * 4) = 0u;
    const GAS u32x4* xp = (const GAS u32x4*)x_ + lane * 2;
    const u32x4 x0 = xp[0], x1 = xp[1];
    PIN();
    const int rs = w < 2 ? NQ / 8 : FF / 8, row0 = (w < 2 ? kq : mlo) * 128 + (w & 1) * 64;
    const u16* tile = w < 2 ? wq_ + kvh * 512 + cq * 64 : w1_ + mhi * 64;
    const GAS u32x4* wp = (const GAS u32x4*)tile + (row0 + (c >> 3) * 32 + g * 8) * rs + (c & 7);
    u32x4 wv[8];
#pragma unroll
    for (int i = 0; i < 8; ++i) wv[i] = ldnt(wp + i * rs);
    PIN();
    LDS_BARRIER();
    tag = read_tag(ws_);
    PIN();
    u32 xs[8] = {x0[0], x0[1], x0[2], x0[3], x1[0], x1[1], x1[2], x1[3]};
    layernorm(xs);
    if (w >= 2) layernorm(xs);
    LAS unsigned char* xl = lds + L_XN + w * 2048;
    *(LAS u32x4*)(xl + lane * 32) = (u32x4){xs[0], xs[1], xs[2], xs[3]};
    *(LAS u32x4*)(xl + lane * 32 + 16) = (u32x4){xs[4], xs[5], xs[6], xs[7]};
    f32x2 r = tile_matvec(wv, xl + row0 * 2, g, c);
    LAS f32x2* half = (LAS f32x2*)(lds + L_HALF + (w >> 1) * 512 + lane * 8);
    if (w & 1) {
      *half = r;
      set_flag(lds, F_HALF + (w >> 1));
    } else {
      wait_flags(lds, F_HALF + (w >> 1), 0, lane);
      r += *half;
      const int rec = w == 0 ? WS_QP + ((((g & 1) * 8 + kvh) * 8 + cq) * 4 + kq) * 256 : WS_PA + mt * 512 + (g & 1) * 256;
      stws1(ws, rec + ((c & 7) * 4 + (g >> 1) * 2 + (c >> 3)) * 8, pack2(r[0], r[1], tag));
    }
    __builtin_amdgcn_s_setprio(1);
  } else {
    if (w < 4 && mlp2) {
      const u16* p = w2_ + (mhi * 64 + (w & 1) * 32 + g * 8) * D + mlo * 128 + c * 8;
#pragma unroll
      for (int i = 0; i < 8; ++i) dma16(p + i * D, (u32)(size_t)(lds + L_W2 + (w & 1) * 8192 + i * 1024));
    }
    RAW_BARRIER();
    if ((w >> 2) == 1) __builtin_amdgcn_s_setprio(2); else __builtin_amdgcn_s_setprio(1);
  }
  PIN();

#pragma unroll
  for (int i = 0; i < 8; ++i) {
    kf[i] = __builtin_amdgcn_raw_buffer_load_b128(kr, klane + (i & 1) * 128, kvoff + (i >> 1) * 2048, NT);
    __builtin_amdgcn_sched_barrier(0);
  }
  if ((w >> 2) != 1) __builtin_amdgcn_s_setprio(0);
  PIN();
#pragma unroll
  for (int i = 0; i < 8; ++i) {
    vf[i] = __builtin_amdgcn_raw_buffer_load_b128(vr, lane * 16, kvoff + i * 1024, NT);
    __builtin_amdgcn_sched_barrier(0);
  }
  PIN();
  tag = read_tag(ws_);
  const int paoff = WS_PA + mhi * 2048 + lane * 16;
  u32x4 p0, p1;
  u32 b1v;
  if ((w >> 2) == 1) {
    const int u = w & 3;
    const int qoff = WS_QP + (b * 8 + kvh) * 8192 + u * 2048 + lane * 16;
    asm volatile("s_waitcnt vmcnt(0)" ::: "memory");
    __builtin_amdgcn_s_sleep(NAP_Q);
    u32x4 q0, q1;
#pragma unroll 1
    for (int spin = 0; spin < SPIN_CAP; ++spin) {
      q0 = ldws(ws, qoff); q1 = ldws(ws, qoff + 1024);
      if (__all(tagged(q0, tag) && tagged(q1, tag))) break;
      __builtin_amdgcn_s_sleep(NAP_RETRY);
      asm volatile("" ::: "memory");
    }
    const float qa = half16(half32(first2(q0[0], q0[1]), first2(q1[0], q1[1])), half32(first2(q0[2], q0[3]), first2(q1[2], q1[3])));
    const float qb = half16(half32(second2(q0[0], q0[1]), second2(q1[0], q1[1])), half32(second2(q0[2], q0[3]), second2(q1[2], q1[3])));
    *(LAS u32*)(lds + L_Q + u * 256 + ((g >> 1) * 32 + c * 2 + (g & 1)) * 4) = pk(qa, qb);
    set_flag(lds, F_Q + u);
    if (w == 7 && mlp2) {
      p0 = ldws(ws, paoff); p1 = ldws(ws, paoff + 1024);
      b1v = *(const GAS u32*)(b1_ + mhi * 64 + c * 4 + (g >> 1) * 2);
      PIN();
    }
    __builtin_amdgcn_s_setprio(0);
  }
  wait_flags(lds, F_Q, 3, lane);

  float sc[8];
  {
    const int x0 = (c >> 2) & 1;
    const u32x4 q0 = *(LAS u32x4*)(lds + L_Q + (c & 3) * 256 + (g * 2 + x0) * 16);
    const u32x4 q1 = *(LAS u32x4*)(lds + L_Q + (c & 3) * 256 + 128 + (g * 2 + x0) * 16);
    constexpr float C2 = 0.08838834764831845f * LOG2E;
#pragma unroll
    for (int o = 0; o < 4; ++o) {
      f32x4 s = mfma(kf[2 * o], q0, (f32x4){0.f, 0.f, 0.f, 0.f});
      s = mfma(kf[2 * o + 1], q1, s);
#pragma unroll
      for (int a = 0; a < 2; ++a) {
        float e1 = s[2 * a + 1];
        asm("" : "+v"(e1));
        const float t = x0 ? e1 : s[2 * a];
        sc[2 * o + a] = (t + dpp<ROR4>(t)) * C2;
      }
    }
  }
  PIN();

  float m = fmaxf(fmaxf(fmaxf(sc[0], sc[1]), fmaxf(sc[2], sc[3])), fmaxf(fmaxf(sc[4], sc[5]), fmaxf(sc[6], sc[7])));
  m = max32(max16(m));
  float p[8], l = 0.f;
#pragma unroll
  for (int e = 0; e < 8; ++e) { p[e] = __builtin_amdgcn_exp2f(sc[e] - m); l += p[e]; }
  l = half16(l, l); l = half32(l, l);
  if (lane < 4) { *(LAS float*)(lds + L_MW + (lane * 16 + w) * 4) = m; *(LAS float*)(lds + L_LW + (lane * 16 + w) * 4) = l; }
  PIN();
  if ((w >> 1) == 1) set_flag(lds, F_W2 + (w & 1));
  if (lane == 0) __hip_atomic_fetch_add((LAS u32*)(lds + L_CNT), 1u, __ATOMIC_RELAXED, __HIP_MEMORY_SCOPE_WORKGROUP);
  const u32x4 pf = {pk(p[0], p[1]), pk(p[2], p[3]), pk(p[4], p[5]), pk(p[6], p[7])};
  u32x4 am[4];
#pragma unroll
  for (int u = 0; u < 4; ++u) am[u] = sel4((c >> 2) == u, pf);
  PIN();
  u32x4 ve[4], vo[4];
  u32 slo, shi;
  asm volatile("s_mov_b32 %0, 0x05040100\n\ts_mov_b32 %1, 0x07060302" : "=s"(slo), "=s"(shi) : "v"(am[0][0]), "v"(am[1][1]), "v"(am[2][2]), "v"(am[3][3]));
#pragma unroll
  for (int r = 0; r < 4; ++r) {
#pragma unroll
    for (int u = 0; u < 4; ++u) {
      ve[u][r] = __builtin_amdgcn_perm(vf[2 * r + 1][u], vf[2 * r][u], slo);
      vo[u][r] = __builtin_amdgcn_perm(vf[2 * r + 1][u], vf[2 * r][u], shi);
    }
    if (r < 3)
      asm volatile("" : "+s"(slo), "+s"(shi) : "v"(ve[0][r]), "v"(ve[1][r]), "v"(ve[2][r]), "v"(ve[3][r]), "v"(vo[0][r]), "v"(vo[1][r]), "v"(vo[2][r]), "v"(vo[3][r]));
  }
  f32x4 oe = {0.f, 0.f, 0.f, 0.f}, oo = oe;
#pragma unroll
  for (int u = 0; u < 4; ++u) { oe = mfma(am[u], ve[u], oe); oo = mfma(am[u], vo[u], oo); }
#pragma unroll
  for (int h = 0; h < 4; ++h) *(LAS f32x2*)(lds + L_OW + w * OW_STRIDE + h * 512 + c * 32 + g * 8) = (f32x2){oe[h], oo[h]};
  PIN();

  const bool gath = (w & 4) == 0;
  const int v = (w & 3) + (w >> 3) * 4;
  auto wait_ml = [&]() __attribute__((always_inline)) {
#pragma unroll 1
    for (int spin = 0; spin < (SPIN_CAP << 4); ++spin) {
      if (__builtin_amdgcn_readfirstlane(*(volatile LAS u32*)(lds + L_CNT)) == 16u) break;
      __builtin_amdgcn_s_sleep(1);
    }
    PIN();
  };
  const int cf = bid & 3, bf = (bid >> 2) & 1, seg = bid >> 3;
  if (w >= 12) {
    LDS_BARRIER();
    if (bid < NFIN) {
      const int row = (w - 12) * 8 + (lane >> 3);
      const int moff = WS_MP + (cf * 28 + min(row, 27)) * 1024 + bf * 512 + seg * 128 + (lane & 7) * 16;
      u32x4 mv;
#pragma unroll 1
      for (int spin = 0; spin < SPIN_CAP; ++spin) {
        mv = ldws(ws, moff);
        if (__all(tagged(mv, tag))) break;
        __builtin_amdgcn_s_sleep(NAP_RETRY);
        asm volatile("" ::: "memory");
      }
      const float t = sum_records(sel4(row < 28, mv));
      if ((lane & 8) == 0) *(LAS float*)(lds + L_MF + (w - 12) * 128 + ((lane & 7) * 4 + g) * 4) = t;
      LDS_BARRIER();
    }
  } else if (gath) {
    {
      const int f = v * 16 + (lane >> 2), p4 = lane & 3, hd = v >> 1;
      wait_ml();
      const f32x4 mv = *(LAS f32x4*)(lds + L_MW + hd * 64 + p4 * 16), lv = *(LAS f32x4*)(lds + L_LW + hd * 64 + p4 * 16);
      float M = fmaxf(fmaxf(mv[0], mv[1]), fmaxf(mv[2], mv[3]));
      M = fmaxf(M, dpp<XOR1>(M)); M = fmaxf(M, dpp<XOR2>(M));
      float L = 0.f, fw[4];
#pragma unroll
      for (int i = 0; i < 4; ++i) { fw[i] = __builtin_amdgcn_exp2f(mv[i] - M); L = fmaf(fw[i], lv[i], L); }
      L += dpp<XOR1>(L); L += dpp<XOR2>(L);
      const float inv = __builtin_amdgcn_rcpf(L);
#pragma unroll
      for (int i = 0; i < 4; ++i) fw[i] *= inv;
      const u32 lam = f2u(M + __builtin_amdgcn_logf(L)) >> ((lane >> 5) * 16);
      u32 t0 = tag | (lam & 255u), t1 = tag | ((lam >> 8) & 255u);
      int ooff = WS_OP + ((kvh * 2 + b) * 16 + sp) * 2048 + f * 16;
      int laddr = L_OW + p4 * 4 * OW_STRIDE + f * 16;
      asm volatile("" : "+v"(fw[0]), "+v"(fw[1]), "+v"(fw[2]), "+v"(fw[3]), "+v"(t0), "+v"(t1), "+v"(ooff), "+v"(laddr));
      LDS_BARRIER();
      f32x4 s = fw[0] * *(LAS f32x4*)(lds + laddr);
#pragma unroll
      for (int i = 1; i < 4; ++i) s += fw[i] * *(LAS f32x4*)(lds + laddr + i * OW_STRIDE);
      float s0 = s[0], s1 = s[1], s2 = s[2], s3 = s[3];
      quad_sum4(s0, s1, s2, s3);
      if (p4 == 0) __builtin_amdgcn_raw_buffer_store_b128((u32x4){pk(s0, s1), t0, pk(s2, s3), t1}, ws, ooff, 0, SC1);
    }
    PIN();
    {
      u32 gaddr2 = (u32)(size_t)(lds + L_GP + v * GP_STRIDE + 256), gaddr = gaddr2 - 256 + (c * 4 + g) * 4;
      u32 lsel = (c & 8) ? 0x05040100u : 0x01000504u;
      asm volatile("" : "+v"(gaddr), "+v"(gaddr2), "+v"(lsel));
      __builtin_amdgcn_s_sleep(NAP_O);
      const u32x4 ov = poll16(ws, WS_OP + (kvh * 32 + v * 4 + g) * 2048 + rb * 256 + c * 16, tag);
      u32 a, lam, m1, x0, x1, x2, x3;
      f32x2 gl;
      asm volatile("v_perm_b32 %[a], %[t1], %[t0], %[sel]\n\t"
                   "v_lshlrev_b32 %[x0], 16, %[d0]\n\t"
                   "v_lshlrev_b32 %[x2], 16, %[d1]\n\t"
                   "v_mov_b32_dpp %[m1], %[a] row_ror:8 row_mask:0xf bank_mask:0xf\n\t"
                   "v_and_b32 %[x1], 0xffff0000, %[d0]\n\t"
                   "v_perm_b32 %[lam], %[a], %[m1], %[lsel]\n\t"
                   "v_mov_b32 %[m1], %[lam]\n\t"
                   "v_mov_b32 %[a], %[lam]\n\t"
                   "v_and_b32 %[x3], 0xffff0000, %[d1]\n\t"
                   "s_nop 0\n\t"
                   "v_permlane16_swap_b32 %[m1], %[a]\n\t"
                   "v_max_f32 %[m1], %[m1], %[a]\n\t"
                   "v_mov_b32 %[a], %[m1]\n\t"
                   "s_nop 1\n\t"
                   "v_permlane32_swap_b32 %[m1], %[a]\n\t"
                   "v_max_f32 v104, %[m1], %[a]\n\t"
                   "v_sub_f32 %[lam], %[lam], v104\n\t"
                   "v_exp_f32 %[lam], %[lam]\n\t"
                   "s_nop 0\n\t"
                   "v_mul_f32 %[x0], %[lam], %[x0]\n\t"
                   "v_mul_f32 %[x2], %[lam], %[x2]\n\t"
                   "v_mul_f32 %[x1], %[lam], %[x1]\n\t"
                   "v_mul_f32 %[x3], %[lam], %[x3]\n\t"
                   "v_mov_b32 %[a], %[lam]\n\t"
                   "v_permlane32_swap_b32 %[x0], %[x2]\n\t"
                   "v_permlane32_swap_b32 %[x1], %[x3]\n\t"
                   "v_permlane16_swap_b32 %[lam], %[a]\n\t"
                   "v_add_f32 %[x0], %[x0], %[x2]\n\t"
                   "v_add_f32 %[x1], %[x1], %[x3]\n\t"
                   "v_add_f32 %[lam], %[lam], %[a]\n\t"
                   "v_mov_b32 %[a], %[lam]\n\t"
                   "v_permlane16_swap_b32 %[x0], %[x1]\n\t"
                   "v_add_f32 %[x0], %[x0], %[x1]\n\t"
                   "v_permlane32_swap_b32 %[lam], %[a]\n\t"
                   "ds_write_b32 %[ga], %[x0]\n\t"
                   "v_add_f32 v105, %[lam], %[a]\n\t"
                   "ds_write_b64 %[gb], v[104:105]"
                   : [a] "=&v"(a), [lam] "=&v"(lam), [m1] "=&v"(m1), [x0] "=&v"(x0), [x1] "=&v"(x1), [x2] "=&v"(x2), [x3] "=&v"(x3), "=&{v[104:105]}"(gl)
                   : [d0] "v"(ov[0]), [t0] "v"(ov[1]), [d1] "v"(ov[2]), [t1] "v"(ov[3]), [sel] "s"(0x04000400u), [lsel] "v"(lsel), [ga] "v"(gaddr), [gb] "v"(gaddr2)
                   : "memory");
    }
    LDS_BARRIER();
    if (bid < NFIN) {
      int faddr = L_FP + ((lane & 7) * 4 + g) * 32 + v * 4;
      asm volatile("" : "+v"(faddr));
      __builtin_amdgcn_s_sleep(NAP_S);
      const u32x4 sv = poll16(ws, WS_S2 + (cf * 64 + v * 8 + (lane >> 3)) * 1024 + bf * 512 + seg * 128 + (lane & 7) * 16, tag);
      *(LAS float*)(lds + faddr) = sum_records(sv);
      LDS_BARRIER();
    }
  } else if (w < 6) {
    u32x4 wt[8], bw[8];
    wait_ml();
    const GAS u16* wp = (const GAS u16*)wo_ + (size_t)(kvh * 512 + rb * 64 + g * 4) * D + cb * 128 + (w - 4) * 64 + c * 4;
#pragma unroll
    for (int i = 0; i < 8; ++i) {
      const u32x2 t0 = ldnt((const GAS u32x2*)(wp + (size_t)(32 * (i >> 2) + (i & 3)) * D));
      const u32x2 t1 = ldnt((const GAS u32x2*)(wp + (size_t)(32 * (i >> 2) + 16 + (i & 3)) * D));
      wt[i] = (u32x4){t0[0], t0[1], t1[0], t1[1]};
    }
    PIN();
    LDS_BARRIER();
#pragma unroll
    for (int i = 0; i < 8; ++i) bw[i] = colfrag(wt, i);
    int gp = L_GP + (c & 7) * GP_STRIDE, go = gp + ((c >> 3) * 16 + g * 4) * 4;
    int soff = WS_S2 + (cb * 64 + kvh * 8 + rb) * 1024 + (g & 1) * 512 + ((w - 4) * 32 + c * 2 + (g >> 1)) * 8;
    asm volatile("" : "+v"(gp), "+v"(go), "+v"(soff) : "v"(bw[0]), "v"(bw[1]), "v"(bw[2]), "v"(bw[3]), "v"(bw[4]), "v"(bw[5]), "v"(bw[6]), "v"(bw[7]));
    LDS_BARRIER();
    f32x2 gl = *(LAS f32x2*)(lds + gp + 256);
    u32x4 of = *(LAS u32x4*)(lds + go), o1 = *(LAS u32x4*)(lds + go + 128);
#define QP1(r) "v_add_f32_dpp " r ", " r ", " r " quad_perm:[1,0,3,2] row_mask:0xf bank_mask:0xf\n\t"
#define QP2(r) "v_add_f32_dpp " r ", " r ", " r " quad_perm:[2,3,0,1] row_mask:0xf bank_mask:0xf\n\t"
#define PKM(r) "v_pk_mul_f32 " r ", v[118:119], " r " op_sel_hi:[0,1]\n\t"
    asm volatile("s_nop 1\n\t"
                 "v_max_f32_dpp v118, v116, v116 quad_perm:[1,0,3,2] row_mask:0xf bank_mask:0xf\n\t"
                 "s_nop 1\n\t"
                 "v_max_f32_dpp v118, v118, v118 quad_perm:[2,3,0,1] row_mask:0xf bank_mask:0xf\n\t"
                 "v_sub_f32 v118, v116, v118\n\t"
                 "v_exp_f32 v118, v118\n\t"
                 "s_nop 0\n\t"
                 "v_mul_f32 v117, v118, v117\n\t"
                 PKM("v[108:109]") PKM("v[110:111]") PKM("v[112:113]") PKM("v[114:115]")
                 QP1("v117") QP1("v108") QP1("v109") QP1("v110") QP1("v111") QP1("v112") QP1("v113") QP1("v114") QP1("v115")
                 QP2("v117") QP2("v108") QP2("v109")
                 "v_rcp_f32 v118, v117\n\t"
                 QP2("v110") QP2("v111") QP2("v112") QP2("v113") QP2("v114") QP2("v115")
                 PKM("v[108:109]") PKM("v[110:111]") PKM("v[112:113]") PKM("v[114:115]")
                 "v_cvt_pk_bf16_f32 v108, v108, v109\n\t"
                 "v_cvt_pk_bf16_f32 v109, v110, v111\n\t"
                 "v_cvt_pk_bf16_f32 v110, v112, v113\n\t"
                 "v_cvt_pk_bf16_f32 v111, v114, v115\n\t"
                 "s_nop 1"
                 : "+{v[108:111]}"(of), "+{v[112:115]}"(o1), "+{v[116:117]}"(gl)
                 :
                 : "v118", "v119");
    f32x4 acc[8];
#pragma unroll
    for (int i = 0; i < 8; ++i) { const int k = (i & 3) * 2 + (i >> 2); acc[k] = mfma(of, bw[k], (f32x4){0.f, 0.f, 0.f, 0.f}); }
    __builtin_amdgcn_sched_barrier(0);
    float r[4];
#pragma unroll
    for (int d = 0; d < 4; ++d) r[d] = (g >> 1) ? acc[4 + d][0] : acc[d][0];
    stws1(ws, soff, pack2(half32(r[0], r[2]), half32(r[1], r[3]), tag));
  } else if (w == 7 && mlp2) {
#pragma unroll 1
    for (int spin = 0; spin < SPIN_CAP; ++spin) {
      if (__all(tagged(p0, tag) && tagged(p1, tag))) break;
      __builtin_amdgcn_s_sleep(NAP_RETRY);
      asm volatile("" ::: "memory");
      p0 = ldws(ws, paoff); p1 = ldws(ws, paoff + 1024);
    }
    const float t2[2] = {half32(first2(p0[0], p0[1]) + first2(p1[0], p1[1]), first2(p0[2], p0[3]) + first2(p1[2], p1[3])),
                         half32(second2(p0[0], p0[1]) + second2(p1[0], p1[1]), second2(p0[2], p0[3]) + second2(p1[2], p1[3]))};
    float hv[2];
#pragma unroll
    for (int e = 0; e < 2; ++e) {
      const float t = rbf(rbf(t2[e]) + (e ? bfhi(b1v) : bflo(b1v)));
      const float uu = 0.7978845608028654f * (t + 0.044715f * t * t * t);
      hv[e] = t / (1.f + __builtin_amdgcn_exp2f(-2.f * LOG2E * uu));
    }
    *(LAS u32*)(lds + L_HS + (g & 1) * 128 + (c * 2 + (g >> 1)) * 4) = pk(hv[0], hv[1]);
    wait_flags(lds, F_W2, 1, lane);
    f32x4 r = {0.f, 0.f, 0.f, 0.f};
#pragma unroll
    for (int s = 0; s < 2; ++s) {
      const u32x4 ha = *(LAS u32x4*)(lds + L_HS + ((c >> 2) & 1) * 128 + s * 64 + g * 16);
      u32x4 w2t[8];
#pragma unroll
      for (int i = 0; i < 8; ++i) w2t[i] = *(LAS u32x4*)(lds + L_W2 + s * 8192 + i * 1024 + lane * 16);
#pragma unroll
      for (int d = 0; d < 8; ++d) r = mfma(sel4((c >> 3) == (d >> 2) && (c & 3) == (d & 3), ha), colfrag(w2t, d), r);
    }
    stws(ws, WS_MP + (mlo * 28 + mhi) * 1024 + (g & 1) * 512 + (c * 2 + (g >> 1)) * 16, pack2(r[0], r[1], tag), pack2(r[2], r[3], tag));
    LDS_BARRIER();
  } else if (w == 6 && bid < NFIN) {
    const int col = cf * 128 + seg * 32 + (lane & 31);
    const u32 xv = ((const GAS u16*)x_)[bf * D + col], b2v = ((const GAS u16*)b2_)[col];
    LDS_BARRIER();
    LDS_BARRIER();
    LAS float* mf = (LAS float*)(lds + L_MF + (lane >> 5) * 256 + (lane & 31) * 4);
    float t = mf[0] + mf[32];
    t = half32(t, t);
    t = rbf(rbf(t) + bflo(b2v));
    float gate = rbf(t / (1.f + __builtin_amdgcn_exp2f(-LOG2E * t))), xf = bflo(xv);
    int faddr = L_FP + (lane & 31) * 32 + (lane >> 5) * 16;
    GAS u16* op = (GAS u16*)out_ + bf * D + col;
    asm volatile("" : "+v"(gate), "+v"(xf), "+v"(faddr), "+v"(op));
    LDS_BARRIER();
    const f32x4 fp = *(LAS f32x4*)(lds + faddr);
    float s = (fp[0] + fp[1]) + (fp[2] + fp[3]);
    s = half32(s, s);
    const float res = xf + rbf(rbf(s) * gate);
    if (lane < 32) __hip_atomic_store(op, (u16)pk(res, res), RLX_AGENT);
    if (bid == 0 && lane == 0) __hip_atomic_store((GAS u32*)(ws_ + WS_EPOCH), tag >> 8, RLX_AGENT);
  } else {
    LDS_BARRIER();
  }
}

extern "C" void kernel_launch(void* const* d_in, const int* in_sizes, int n_in, void* d_out, int out_size, void* d_ws, size_t ws_size,
                              hipStream_t stream) {
  fused_decode<<<256, 1024, 0, stream>>>((const u16*)d_in[0], (const u16*)d_in[1], (const u16*)d_in[2], (const u16*)d_in[3],
                                         (const u16*)d_in[4], (const u16*)d_in[6], (const u16*)d_in[7], (const u16*)d_in[8],
                                         (const u16*)d_in[9], (u16*)d_out, (unsigned char*)d_ws);
}
```

```cpp
#include <hip/hip_runtime.h>
#include <stdint.h>

#define GAS __attribute__((address_space(1)))
#define LAS __attribute__((address_space(3)))
#define DI __device__ __forceinline__
#define RLX_AGENT __ATOMIC_RELAXED, __HIP_MEMORY_SCOPE_AGENT
typedef unsigned short u16;
typedef unsigned u32;
typedef unsigned long long u64;
typedef u32 u32x2 __attribute__((ext_vector_type(2)));
typedef u32 u32x4 __attribute__((ext_vector_type(4)));
typedef float f32x2 __attribute__((ext_vector_type(2)));
typedef float f32x4 __attribute__((ext_vector_type(4)));
typedef __bf16 bf16x2 __attribute__((ext_vector_type(2)));
typedef __bf16 bf16x8 __attribute__((ext_vector_type(8)));

constexpr int D = 512, SK = 8192, DH = 128, FF = 1792, NQ = 4096;
constexpr int SC1 = 16, NT = 2;
constexpr int SPIN_CAP = 1 << 18;
constexpr int NFIN = 32;
constexpr int NAP_Q = 32, NAP_O = 8, NAP_S = 8, NAP_RETRY = 8;
constexpr float LOG2E = 1.4426950408889634f;

constexpr int WS_EPOCH = 0;
constexpr int WS_QP = 256;
constexpr int WS_PA = WS_QP + 512 * 256;
constexpr int WS_OP = WS_PA + 112 * 512;
constexpr int WS_S2 = WS_OP + 256 * 2048;
constexpr int WS_MP = WS_S2 + 256 * 1024;
constexpr int WS_END = WS_MP + 112 * 1024;

constexpr int F_Q = 0, F_HALF = 4, F_W2 = 6;
constexpr int L_FLAG = 0, L_CNT = 48, L_XN = 64, L_HALF = L_XN + 8192, L_Q = L_HALF + 1024, L_MW = L_Q + 1024, L_LW = L_MW + 256,
              L_HS = L_LW + 256, L_GP = L_HS + 256, GP_STRIDE = 272, L_FP = L_GP + 8 * GP_STRIDE, L_MF = L_FP + 1024,
              L_W2 = L_MF + 512, L_OW = L_W2 + 16384, OW_STRIDE = 2064, L_END = L_OW + 16 * OW_STRIDE;

DI float bflo(u32 v) { return __builtin_bit_cast(float, v << 16); }
DI float bfhi(u32 v) { return __builtin_bit_cast(float, v & 0xffff0000u); }
DI float rbf(float f) { return (float)(__bf16)f; }
DI u32 pk(float a, float b) { return __builtin_bit_cast(u32, __builtin_convertvector((f32x2){a, b}, bf16x2)); }
DI u32 f2u(float f) { return __builtin_bit_cast(u32, f); }
DI float u2f(u32 u) { return __builtin_bit_cast(float, u); }
DI f32x4 mfma(u32x4 a, u32x4 b, f32x4 c) {
  return __builtin_amdgcn_mfma_f32_16x16x32_bf16(__builtin_bit_cast(bf16x8, a), __builtin_bit_cast(bf16x8, b), c, 0, 0, 0);
}
template <int CTRL> DI float dpp(float x) { return u2f(__builtin_amdgcn_mov_dpp(f2u(x), CTRL, 0xf, 0xf, true)); }
constexpr int XOR1 = 0xB1, XOR2 = 0x4E, ROR4 = 0x124, ROR8 = 0x128;
DI float half32(float a, float b) { auto r = __builtin_amdgcn_permlane32_swap(f2u(a), f2u(b), false, false); return u2f(r[0]) + u2f(r[1]); }
DI float half16(float a, float b) { auto r = __builtin_amdgcn_permlane16_swap(f2u(a), f2u(b), false, false); return u2f(r[0]) + u2f(r[1]); }
DI float max32(float a) { auto r = __builtin_amdgcn_permlane32_swap(f2u(a), f2u(a), false, false); return fmaxf(u2f(r[0]), u2f(r[1])); }
DI float max16(float a) { auto r = __builtin_amdgcn_permlane16_swap(f2u(a), f2u(a), false, false); return fmaxf(u2f(r[0]), u2f(r[1])); }
DI float half_sum(float v) {
  v += dpp<XOR1>(v); v += dpp<XOR2>(v); v += dpp<ROR4>(v); v += dpp<ROR8>(v);
  return half16(v, v);
}
DI u32 plo(u32 k1, u32 k0) { return __builtin_amdgcn_perm(k1, k0, 0x05040100u); }
DI u32 phi(u32 k1, u32 k0) { return __builtin_amdgcn_perm(k1, k0, 0x07060302u); }
DI u32x4 colfrag(const u32x4 (&r)[8], int d) {
  const int q = d >> 1;
  return (d & 1) ? (u32x4){phi(r[1][q], r[0][q]), phi(r[3][q], r[2][q]), phi(r[5][q], r[4][q]), phi(r[7][q], r[6][q])}
                 : (u32x4){plo(r[1][q], r[0][q]), plo(r[3][q], r[2][q]), plo(r[5][q], r[4][q]), plo(r[7][q], r[6][q])};
}
DI u32x4 sel4(bool c, u32x4 v) { return (u32x4){c ? v[0] : 0u, c ? v[1] : 0u, c ? v[2] : 0u, c ? v[3] : 0u}; }
template <typename T> DI T ldnt(const GAS T* p) { return __builtin_nontemporal_load(p); }
DI u32x4 ldws(__amdgpu_buffer_rsrc_t ws, int off) { return __builtin_amdgcn_raw_buffer_load_b128(ws, off, 0, SC1); }
DI void stws(__amdgpu_buffer_rsrc_t ws, int off, u32x2 a, u32x2 b) { __builtin_amdgcn_raw_buffer_store_b128((u32x4){a[0], a[1], b[0], b[1]}, ws, off, 0, SC1); }
DI void stws1(__amdgpu_buffer_rsrc_t ws, int off, u32x2 a) { __builtin_amdgcn_raw_buffer_store_b64(a, ws, off, 0, SC1); }
DI u32x2 pack2(float a, float b, u32 tag8) {
  const u32 ar = f2u(a) + 0x800u;
  const u32 u = (f2u(b) & 0xf000u) | ((ar >> 4) & ~0xf000u);
  return (u32x2){phi(f2u(b), ar), __builtin_amdgcn_perm(tag8, u, 0x07060501u)};
}
DI float first2(u32 w0, u32 w1) { return u2f(__builtin_amdgcn_perm(w0, w1 << 4, 0x0504000cu)); }
DI float second2(u32 w0, u32 w1) { return u2f(__builtin_amdgcn_perm(w0, w1, 0x0706000cu)); }
DI bool tagged(u32x4 v, u32 tag8) { return ((v[1] ^ tag8) | (v[3] ^ tag8)) < 256u; }
#define PIN() do { asm volatile("" ::: "memory"); __builtin_amdgcn_sched_barrier(0); } while (0)
#define LDS_BARRIER() do { asm volatile("s_waitcnt lgkmcnt(0)\n\ts_barrier" ::: "memory"); __builtin_amdgcn_sched_barrier(0); } while (0)
#define RAW_BARRIER() do { __builtin_amdgcn_sched_barrier(0); asm volatile("s_barrier" ::: "memory"); __builtin_amdgcn_sched_barrier(0); } while (0)
DI void dma16(const u16* src, u32 dst) {
  u32 keep;
  asm volatile("s_mov_b32 %0, m0\n\ts_mov_b32 m0, %2\n\ts_nop 0\n\tglobal_load_lds_dwordx4 %1, off nt\n\ts_mov_b32 m0, %0"
               : "=&s"(keep) : "v"(src), "s"(dst) : "memory");
}
DI void set_flag(LAS unsigned char* lds, int f) {
  asm volatile("s_waitcnt lgkmcnt(0)" ::: "memory");
  *(volatile LAS u32*)(lds + L_FLAG + f * 4) = 1u;
}
DI void wait_flags(LAS unsigned char* lds, int first, int mask, int lane) {
#pragma unroll 1
  for (int spin = 0; spin < (SPIN_CAP << 4); ++spin) {
    if (__all(*(volatile LAS u32*)(lds + L_FLAG + (first + (lane & mask)) * 4) != 0u)) break;
    __builtin_amdgcn_s_sleep(1);
  }
  PIN();
}
DI u32 read_tag(const unsigned char* ws) {
  u32 e;
  asm volatile("s_load_dword %0, %1, 0x0\n\ts_waitcnt lgkmcnt(0)" : "=s"(e) : "s"(ws) : "memory");
  return (e + 1u) << 8;
}
DI u32x4 poll16(__amdgpu_buffer_rsrc_t ws, int off, u32 want) {
  u32x4 v;
  u64 keep;
  u32 left = SPIN_CAP, t;
  asm volatile("s_mov_b64 %[keep], exec\n\t"
               "s_branch 2f\n"
               "1:\n\t"
               "s_sub_u32 %[left], %[left], 1\n\t"
               "s_cbranch_scc1 3f\n"
               "2:\n\t"
               "buffer_load_dwordx4 v[100:103], %[off], %[ws], 0 offen sc1\n\t"
               "s_waitcnt vmcnt(0)\n\t"
               "v_bitop3_b32 %[t], v101, v103, %[want] bitop3:0x7e\n\t"
               "v_cmp_lt_u32 vcc, 0xff, %[t]\n\t"
               "s_and_b64 exec, exec, vcc\n\t"
               "s_cbranch_scc1 1b\n"
               "3:\n\t"
               "s_mov_b64 exec, %[keep]"
               : "=&{v[100:103]}"(v), [keep] "=&s"(keep), [left] "+s"(left), [t] "=&v"(t)
               : [off] "v"(off), [ws] "s"(ws), [want] "s"(want)
               : "vcc", "scc", "memory");
  return v;
}
DI void quad_sum4(float& a, float& b, float& c, float& d) {
  asm volatile("s_nop 1\n\t"
      "v_add_f32_dpp %0, %0, %0 quad_perm:[1,0,3,2] row_mask:0xf bank_mask:0xf\n\t"
      "v_add_f32_dpp %1, %1, %1 quad_perm:[1,0,3,2] row_mask:0xf bank_mask:0xf\n\t"
      "v_add_f32_dpp %2, %2, %2 quad_perm:[1,0,3,2] row_mask:0xf bank_mask:0xf\n\t"
      "v_add_f32_dpp %3, %3, %3 quad_perm:[1,0,3,2] row_mask:0xf bank_mask:0xf\n\t"
      "v_add_f32_dpp %0, %0, %0 quad_perm:[2,3,0,1] row_mask:0xf bank_mask:0xf\n\t"
      "v_add_f32_dpp %1, %1, %1 quad_perm:[2,3,0,1] row_mask:0xf bank_mask:0xf\n\t"
      "v_add_f32_dpp %2, %2, %2 quad_perm:[2,3,0,1] row_mask:0xf bank_mask:0xf\n\t"
      "v_add_f32_dpp %3, %3, %3 quad_perm:[2,3,0,1] row_mask:0xf bank_mask:0xf"
      : "+v"(a), "+v"(b), "+v"(c), "+v"(d));
}
DI float sum_records(u32x4 v) {
  const float t = half16(half32(first2(v[0], v[1]), first2(v[2], v[3])), half32(second2(v[0], v[1]), second2(v[2], v[3])));
  return t + dpp<ROR8>(t);
}

DI void layernorm(u32 (&x)[8]) {
  float v[16], s = 0.f;
#pragma unroll
  for (int e = 0; e < 8; ++e) { v[2 * e] = bflo(x[e]); v[2 * e + 1] = bfhi(x[e]); s += v[2 * e] + v[2 * e + 1]; }
  const float mean = half_sum(s) * (1.f / 512.f);
  float q = 0.f;
#pragma unroll
  for (int e = 0; e < 16; ++e) { const float d = v[e] - mean; q = fmaf(d, d, q); }
  const float var = half_sum(q) * (1.f / 512.f);
  const float mb = rbf(mean), inv = __builtin_amdgcn_rcpf(rbf(__builtin_amdgcn_sqrtf(rbf(rbf(var) + 1e-5f))));
#pragma unroll
  for (int e = 0; e < 8; ++e) { const u32 d = pk(v[2 * e] - mb, v[2 * e + 1] - mb); x[e] = pk(bflo(d) * inv, bfhi(d) * inv); }
}

DI f32x2 tile_matvec(const u32x4 (&wv)[8], LAS unsigned char* xin, int g, int c) {
  const u32x4 xf = *(LAS u32x4*)(xin + ((c >> 2) & 1) * 1024 + ((c >> 3) * 32 + g * 8) * 2);
  f32x4 lo = {0.f, 0.f, 0.f, 0.f}, hi = lo;
#pragma unroll
  for (int d = 0; d < 4; ++d) {
    const u32x4 am = sel4((c & 3) == d, xf);
    lo = mfma(am, colfrag(wv, d), lo);
    hi = mfma(am, colfrag(wv, 4 + d), hi);
  }
  const bool valid = (g >> 1) == (c >> 3);
  float t[4];
#pragma unroll
  for (int e = 0; e < 4; ++e) { t[e] = half32(valid ? lo[e] : 0.f, valid ? hi[e] : 0.f); t[e] += dpp<ROR8>(t[e]); }
  return (c & 8) ? (f32x2){t[2], t[3]} : (f32x2){t[0], t[1]};
}

__global__ void __launch_bounds__(1024, 4)
fused_decode(const u16* x_, const u16* k_, const u16* v_, const u16* wq_, const u16* wo_, const u16* w1_, const u16* b1_,
             const u16* w2_, const u16* b2_, u16* out_, unsigned char* ws_) {
  __shared__ __attribute__((aligned(16))) unsigned char lds_raw[L_END];
  LAS unsigned char* lds = (LAS unsigned char*)lds_raw;
  asm volatile("" :: "s"(x_), "s"(k_), "s"(v_), "s"(wq_), "s"(wo_), "s"(w1_), "s"(b1_), "s"(w2_), "s"(b2_), "s"(out_), "s"(ws_));
  const int tid = threadIdx.x, lane = tid & 63, w = __builtin_amdgcn_readfirstlane(tid >> 6);
  const int g = lane >> 4, c = lane & 15;
  const int bid = blockIdx.x, kvh = bid & 7, j = bid >> 3, b = j >> 4, sp = j & 15, rb = j >> 2, cb = j & 3;
  const bool mlp1 = j < 14, mlp2 = j >= 14 && j < 28;
  const __amdgpu_buffer_rsrc_t ws = __builtin_amdgcn_make_buffer_rsrc(ws_, 0, WS_END, 0x00020000);
  u32 tag;
  const size_t kvseq = (size_t)(b * 8 + kvh) * SK * DH;
  const __amdgpu_buffer_rsrc_t kr = __builtin_amdgcn_make_buffer_rsrc((void*)(k_ + kvseq), 0, SK * DH * 2, 0x00020000);
  const __amdgpu_buffer_rsrc_t vr = __builtin_amdgcn_make_buffer_rsrc((void*)(v_ + kvseq), 0, SK * DH * 2, 0x00020000);
  const int kvoff = (sp * 16 + ((w + j) & 15)) * 8192;
  const int klane = (((c >> 1) & 1) * 4 + (c >> 2)) * 256 + (g * 2 + (c & 1)) * 16;
  u32x4 kf[8], vf[8];

  const int cq = j & 7, kq = j >> 3;
  const int mt = (mlp1 ? j : j - 14) * 8 + kvh;
  const int mhi = mt >> 2, mlo = mt & 3;
  if (w < 2 || (w < 4 && mlp1)) {
    __builtin_amdgcn_s_setprio(3);
    if (w == 0 && lane < 16) *(LAS u32*)(lds + L_FLAG + lane * 4) = 0u;
    const GAS u32x4* xp = (const GAS u32x4*)x_ + lane * 2;
    const u32x4 x0 = xp[0], x1 = xp[1];
    PIN();
    const int rs = w < 2 ? NQ / 8 : FF / 8, row0 = (w < 2 ? kq : mlo) * 128 + (w & 1) * 64;
    const u16* tile = w < 2 ? wq_ + kvh * 512 + cq * 64 : w1_ + mhi * 64;
    const GAS u32x4* wp = (const GAS u32x4*)tile + (row0 + (c >> 3) * 32 + g * 8) * rs + (c & 7);
    u32x4 wv[8];
#pragma unroll
    for (int i = 0; i < 8; ++i) wv[i] = ldnt(wp + i * rs);
    PIN();
    LDS_BARRIER();
    tag = read_tag(ws_);
    PIN();
    u32 xs[8] = {x0[0], x0[1], x0[2], x0[3], x1[0], x1[1], x1[2], x1[3]};
    layernorm(xs);
    if (w >= 2) layernorm(xs);
    LAS unsigned char* xl = lds + L_XN + w * 2048;
    *(LAS u32x4*)(xl + lane * 32) = (u32x4){xs[0], xs[1], xs[2], xs[3]};
    *(LAS u32x4*)(xl + lane * 32 + 16) = (u32x4){xs[4], xs[5], xs[6], xs[7]};
    f32x2 r = tile_matvec(wv, xl + row0 * 2, g, c);
    LAS f32x2* half = (LAS f32x2*)(lds + L_HALF + (w >> 1) * 512 + lane * 8);
    if (w & 1) {
      *half = r;
      set_flag(lds, F_HALF + (w >> 1));
    } else {
      wait_flags(lds, F_HALF + (w >> 1), 0, lane);
      r += *half;
      const int rec = w == 0 ? WS_QP + ((((g & 1) * 8 + kvh) * 8 + cq) * 4 + kq) * 256 : WS_PA + mt * 512 + (g & 1) * 256;
      stws1(ws, rec + ((c & 7) * 4 + (g >> 1) * 2 + (c >> 3)) * 8, pack2(r[0], r[1], tag));
    }
    __builtin_amdgcn_s_setprio(1);
  } else {
    if (w < 4 && mlp2) {
      const u16* p = w2_ + (mhi * 64 + (w & 1) * 32 + g * 8) * D + mlo * 128 + c * 8;
#pragma unroll
      for (int i = 0; i < 8; ++i) dma16(p + i * D, (u32)(size_t)(lds + L_W2 + (w & 1) * 8192 + i * 1024));
    }
    RAW_BARRIER();
    if ((w >> 2) == 1) __builtin_amdgcn_s_setprio(2); else __builtin_amdgcn_s_setprio(1);
  }
  PIN();

#pragma unroll
  for (int i = 0; i < 8; ++i) {
    kf[i] = __builtin_amdgcn_raw_buffer_load_b128(kr, klane + (i & 1) * 128, kvoff + (i >> 1) * 2048, NT);
    __builtin_amdgcn_sched_barrier(0);
  }
  if ((w >> 2) != 1) __builtin_amdgcn_s_setprio(0);
  PIN();
#pragma unroll
  for (int i = 0; i < 8; ++i) {
    vf[i] = __builtin_amdgcn_raw_buffer_load_b128(vr, lane * 16, kvoff + i * 1024, NT);
    __builtin_amdgcn_sched_barrier(0);
  }
  PIN();
  tag = read_tag(ws_);
  const int paoff = WS_PA + mhi * 2048 + lane * 16;
  u32x4 p0, p1;
  u32 b1v;
  if ((w >> 2) == 1) {
    const int u = w & 3;
    const int qoff = WS_QP + (b * 8 + kvh) * 8192 + u * 2048 + lane * 16;
    asm volatile("s_waitcnt vmcnt(0)" ::: "memory");
    __builtin_amdgcn_s_sleep(NAP_Q);
    u32x4 q0, q1;
#pragma unroll 1
    for (int spin = 0; spin < SPIN_CAP; ++spin) {
      q0 = ldws(ws, qoff); q1 = ldws(ws, qoff + 1024);
      if (__all(tagged(q0, tag) && tagged(q1, tag))) break;
      __builtin_amdgcn_s_sleep(NAP_RETRY);
      asm volatile("" ::: "memory");
    }
    const float qa = half16(half32(first2(q0[0], q0[1]), first2(q1[0], q1[1])), half32(first2(q0[2], q0[3]), first2(q1[2], q1[3])));
    const float qb = half16(half32(second2(q0[0], q0[1]), second2(q1[0], q1[1])), half32(second2(q0[2], q0[3]), second2(q1[2], q1[3])));
    *(LAS u32*)(lds + L_Q + u * 256 + ((g >> 1) * 32 + c * 2 + (g & 1)) * 4) = pk(qa, qb);
    set_flag(lds, F_Q + u);
    if (w == 7 && mlp2) {
      p0 = ldws(ws, paoff); p1 = ldws(ws, paoff + 1024);
      b1v = *(const GAS u32*)(b1_ + mhi * 64 + c * 4 + (g >> 1) * 2);
      PIN();
    }
    __builtin_amdgcn_s_setprio(0);
  }
  wait_flags(lds, F_Q, 3, lane);

  float sc[8];
  {
    const int x0 = (c >> 2) & 1;
    const u32x4 q0 = *(LAS u32x4*)(lds + L_Q + (c & 3) * 256 + (g * 2 + x0) * 16);
    const u32x4 q1 = *(LAS u32x4*)(lds + L_Q + (c & 3) * 256 + 128 + (g * 2 + x0) * 16);
#pragma unroll
    for (int o = 0; o < 4; ++o) {
      f32x4 s = mfma(kf[2 * o], q0, (f32x4){0.f, 0.f, 0.f, 0.f});
      s = mfma(kf[2 * o + 1], q1, s);
#pragma unroll
      for (int a = 0; a < 2; ++a) {
        float e1 = s[2 * a + 1];
        asm("" : "+v"(e1));
        const float t = x0 ? e1 : s[2 * a];
        sc[2 * o + a] = t + dpp<ROR4>(t);
      }
      const u32 d = pk(sc[2 * o], sc[2 * o + 1]), e = pk(bflo(d) * 0.08838834764831845f, bfhi(d) * 0.08838834764831845f);
      sc[2 * o] = bflo(e); sc[2 * o + 1] = bfhi(e);
    }
  }
  PIN();

  float m = fmaxf(fmaxf(fmaxf(sc[0], sc[1]), fmaxf(sc[2], sc[3])), fmaxf(fmaxf(sc[4], sc[5]), fmaxf(sc[6], sc[7])));
  m = max32(max16(m)) * LOG2E;
  float p[8], l = 0.f;
#pragma unroll
  for (int e = 0; e < 8; ++e) { p[e] = __builtin_amdgcn_exp2f(fmaf(sc[e], LOG2E, -m)); l += p[e]; }
  l = half16(l, l); l = half32(l, l);
  if (lane < 4) { *(LAS float*)(lds + L_MW + (lane * 16 + w) * 4) = m; *(LAS float*)(lds + L_LW + (lane * 16 + w) * 4) = l; }
  PIN();
  if ((w >> 1) == 1) set_flag(lds, F_W2 + (w & 1));
  if (lane == 0) __hip_atomic_fetch_add((LAS u32*)(lds + L_CNT), 1u, __ATOMIC_RELAXED, __HIP_MEMORY_SCOPE_WORKGROUP);
  const u32x4 pf = {pk(p[0], p[1]), pk(p[2], p[3]), pk(p[4], p[5]), pk(p[6], p[7])};
  u32x4 am[4];
#pragma unroll
  for (int u = 0; u < 4; ++u) am[u] = sel4((c >> 2) == u, pf);
  PIN();
  u32x4 ve[4], vo[4];
  u32 slo, shi;
  asm volatile("s_mov_b32 %0, 0x05040100\n\ts_mov_b32 %1, 0x07060302" : "=s"(slo), "=s"(shi) : "v"(am[0][0]), "v"(am[1][1]), "v"(am[2][2]), "v"(am[3][3]));
#pragma unroll
  for (int r = 0; r < 4; ++r) {
#pragma unroll
    for (int u = 0; u < 4; ++u) {
      ve[u][r] = __builtin_amdgcn_perm(vf[2 * r + 1][u], vf[2 * r][u], slo);
      vo[u][r] = __builtin_amdgcn_perm(vf[2 * r + 1][u], vf[2 * r][u], shi);
    }
    if (r < 3)
      asm volatile("" : "+s"(slo), "+s"(shi) : "v"(ve[0][r]), "v"(ve[1][r]), "v"(ve[2][r]), "v"(ve[3][r]), "v"(vo[0][r]), "v"(vo[1][r]), "v"(vo[2][r]), "v"(vo[3][r]));
  }
  f32x4 oe = {0.f, 0.f, 0.f, 0.f}, oo = oe;
#pragma unroll
  for (int u = 0; u < 4; ++u) { oe = mfma(am[u], ve[u], oe); oo = mfma(am[u], vo[u], oo); }
#pragma unroll
  for (int h = 0; h < 4; ++h) *(LAS f32x2*)(lds + L_OW + w * OW_STRIDE + h * 512 + c * 32 + g * 8) = (f32x2){oe[h], oo[h]};
  PIN();

  const bool gath = (w & 4) == 0;
  const int v = (w & 3) + (w >> 3) * 4;
  auto wait_ml = [&]() __attribute__((always_inline)) {
#pragma unroll 1
    for (int spin = 0; spin < (SPIN_CAP << 4); ++spin) {
      if (__builtin_amdgcn_readfirstlane(*(volatile LAS u32*)(lds + L_CNT)) == 16u) break;
      __builtin_amdgcn_s_sleep(1);
    }
    PIN();
  };
  const int cf = bid & 3, bf = (bid >> 2) & 1, seg = bid >> 3;
  if (w >= 12) {
    LDS_BARRIER();
    if (bid < NFIN) {
      const int row = (w - 12) * 8 + (lane >> 3);
      const int moff = WS_MP + (cf * 28 + min(row, 27)) * 1024 + bf * 512 + seg * 128 + (lane & 7) * 16;
      u32x4 mv;
#pragma unroll 1
      for (int spin = 0; spin < SPIN_CAP; ++spin) {
        mv = ldws(ws, moff);
        if (__all(tagged(mv, tag))) break;
        __builtin_amdgcn_s_sleep(NAP_RETRY);
        asm volatile("" ::: "memory");
      }
      const float t = sum_records(sel4(row < 28, mv));
      if ((lane & 8) == 0) *(LAS float*)(lds + L_MF + (w - 12) * 128 + ((lane & 7) * 4 + g) * 4) = t;
      LDS_BARRIER();
    }
  } else if (gath) {
    {
      const int f = v * 16 + (lane >> 2), p4 = lane & 3, hd = v >> 1;
      wait_ml();
      const f32x4 mv = *(LAS f32x4*)(lds + L_MW + hd * 64 + p4 * 16), lv = *(LAS f32x4*)(lds + L_LW + hd * 64 + p4 * 16);
      float M = fmaxf(fmaxf(mv[0], mv[1]), fmaxf(mv[2], mv[3]));
      M = fmaxf(M, dpp<XOR1>(M)); M = fmaxf(M, dpp<XOR2>(M));
      float L = 0.f, fw[4];
#pragma unroll
      for (int i = 0; i < 4; ++i) { fw[i] = __builtin_amdgcn_exp2f(mv[i] - M); L = fmaf(fw[i], lv[i], L); }
      L += dpp<XOR1>(L); L += dpp<XOR2>(L);
      const float inv = __builtin_amdgcn_rcpf(L);
#pragma unroll
      for (int i = 0; i < 4; ++i) fw[i] *= inv;
      const u32 lam = f2u(M + __builtin_amdgcn_logf(L)) >> ((lane >> 5) * 16);
      u32 t0 = tag | (lam & 255u), t1 = tag | ((lam >> 8) & 255u);
      int ooff = WS_OP + ((kvh * 2 + b) * 16 + sp) * 2048 + f * 16;
      int laddr = L_OW + p4 * 4 * OW_STRIDE + f * 16;
      asm volatile("" : "+v"(fw[0]), "+v"(fw[1]), "+v"(fw[2]), "+v"(fw[3]), "+v"(t0), "+v"(t1), "+v"(ooff), "+v"(laddr));
      LDS_BARRIER();
      f32x4 s = fw[0] * *(LAS f32x4*)(lds + laddr);
#pragma unroll
      for (int i = 1; i < 4; ++i) s += fw[i] * *(LAS f32x4*)(lds + laddr + i * OW_STRIDE);
      float s0 = s[0], s1 = s[1], s2 = s[2], s3 = s[3];
      quad_sum4(s0, s1, s2, s3);
      if (p4 == 0) __builtin_amdgcn_raw_buffer_store_b128((u32x4){pk(s0, s1), t0, pk(s2, s3), t1}, ws, ooff, 0, SC1);
    }
    PIN();
    {
      u32 gaddr2 = (u32)(size_t)(lds + L_GP + v * GP_STRIDE + 256), gaddr = gaddr2 - 256 + (c * 4 + g) * 4;
      u32 lsel = (c & 8) ? 0x05040100u : 0x01000504u;
      asm volatile("" : "+v"(gaddr), "+v"(gaddr2), "+v"(lsel));
      __builtin_amdgcn_s_sleep(NAP_O);
      const u32x4 ov = poll16(ws, WS_OP + (kvh * 32 + v * 4 + g) * 2048 + rb * 256 + c * 16, tag);
      u32 a, lam, m1, x0, x1, x2, x3;
      f32x2 gl;
      asm volatile("v_perm_b32 %[a], %[t1], %[t0], %[sel]\n\t"
                   "v_lshlrev_b32 %[x0], 16, %[d0]\n\t"
                   "v_lshlrev_b32 %[x2], 16, %[d1]\n\t"
                   "v_mov_b32_dpp %[m1], %[a] row_ror:8 row_mask:0xf bank_mask:0xf\n\t"
                   "v_and_b32 %[x1], 0xffff0000, %[d0]\n\t"
                   "v_perm_b32 %[lam], %[a], %[m1], %[lsel]\n\t"
                   "v_mov_b32 %[m1], %[lam]\n\t"
                   "v_mov_b32 %[a], %[lam]\n\t"
                   "v_and_b32 %[x3], 0xffff0000, %[d1]\n\t"
                   "s_nop 0\n\t"
                   "v_permlane16_swap_b32 %[m1], %[a]\n\t"
                   "v_max_f32 %[m1], %[m1], %[a]\n\t"
                   "v_mov_b32 %[a], %[m1]\n\t"
                   "s_nop 1\n\t"
                   "v_permlane32_swap_b32 %[m1], %[a]\n\t"
                   "v_max_f32 v104, %[m1], %[a]\n\t"
                   "v_sub_f32 %[lam], %[lam], v104\n\t"
                   "v_exp_f32 %[lam], %[lam]\n\t"
                   "s_nop 0\n\t"
                   "v_mul_f32 %[x0], %[lam], %[x0]\n\t"
                   "v_mul_f32 %[x2], %[lam], %[x2]\n\t"
                   "v_mul_f32 %[x1], %[lam], %[x1]\n\t"
                   "v_mul_f32 %[x3], %[lam], %[x3]\n\t"
                   "v_mov_b32 %[a], %[lam]\n\t"
                   "v_permlane32_swap_b32 %[x0], %[x2]\n\t"
                   "v_permlane32_swap_b32 %[x1], %[x3]\n\t"
                   "v_permlane16_swap_b32 %[lam], %[a]\n\t"
                   "v_add_f32 %[x0], %[x0], %[x2]\n\t"
                   "v_add_f32 %[x1], %[x1], %[x3]\n\t"
                   "v_add_f32 %[lam], %[lam], %[a]\n\t"
                   "v_mov_b32 %[a], %[lam]\n\t"
                   "v_permlane16_swap_b32 %[x0], %[x1]\n\t"
                   "v_add_f32 %[x0], %[x0], %[x1]\n\t"
                   "v_permlane32_swap_b32 %[lam], %[a]\n\t"
                   "ds_write_b32 %[ga], %[x0]\n\t"
                   "v_add_f32 v105, %[lam], %[a]\n\t"
                   "ds_write_b64 %[gb], v[104:105]"
                   : [a] "=&v"(a), [lam] "=&v"(lam), [m1] "=&v"(m1), [x0] "=&v"(x0), [x1] "=&v"(x1), [x2] "=&v"(x2), [x3] "=&v"(x3), "=&{v[104:105]}"(gl)
                   : [d0] "v"(ov[0]), [t0] "v"(ov[1]), [d1] "v"(ov[2]), [t1] "v"(ov[3]), [sel] "s"(0x04000400u), [lsel] "v"(lsel), [ga] "v"(gaddr), [gb] "v"(gaddr2)
                   : "memory");
    }
    LDS_BARRIER();
    if (bid < NFIN) {
      int faddr = L_FP + ((lane & 7) * 4 + g) * 32 + v * 4;
      asm volatile("" : "+v"(faddr));
      __builtin_amdgcn_s_sleep(NAP_S);
      const u32x4 sv = poll16(ws, WS_S2 + (cf * 64 + v * 8 + (lane >> 3)) * 1024 + bf * 512 + seg * 128 + (lane & 7) * 16, tag);
      *(LAS float*)(lds + faddr) = sum_records(sv);
      LDS_BARRIER();
    }
  } else if (w < 6) {
    u32x4 wt[8], bw[8];
    wait_ml();
    const GAS u16* wp = (const GAS u16*)wo_ + (size_t)(kvh * 512 + rb * 64 + g * 4) * D + cb * 128 + (w - 4) * 64 + c * 4;
#pragma unroll
    for (int i = 0; i < 8; ++i) {
      const u32x2 t0 = ldnt((const GAS u32x2*)(wp + (size_t)(32 * (i >> 2) + (i & 3)) * D));
      const u32x2 t1 = ldnt((const GAS u32x2*)(wp + (size_t)(32 * (i >> 2) + 16 + (i & 3)) * D));
      wt[i] = (u32x4){t0[0], t0[1], t1[0], t1[1]};
    }
    PIN();
    LDS_BARRIER();
#pragma unroll
    for (int i = 0; i < 8; ++i) bw[i] = colfrag(wt, i);
    int gp = L_GP + (c & 7) * GP_STRIDE, go = gp + ((c >> 3) * 16 + g * 4) * 4;
    int soff = WS_S2 + (cb * 64 + kvh * 8 + rb) * 1024 + (g & 1) * 512 + ((w - 4) * 32 + c * 2 + (g >> 1)) * 8;
    asm volatile("" : "+v"(gp), "+v"(go), "+v"(soff) : "v"(bw[0]), "v"(bw[1]), "v"(bw[2]), "v"(bw[3]), "v"(bw[4]), "v"(bw[5]), "v"(bw[6]), "v"(bw[7]));
    LDS_BARRIER();
    f32x2 gl = *(LAS f32x2*)(lds + gp + 256);
    u32x4 of = *(LAS u32x4*)(lds + go), o1 = *(LAS u32x4*)(lds + go + 128);
#define QP1(r) "v_add_f32_dpp " r ", " r ", " r " quad_perm:[1,0,3,2] row_mask:0xf bank_mask:0xf\n\t"
#define QP2(r) "v_add_f32_dpp " r ", " r ", " r " quad_perm:[2,3,0,1] row_mask:0xf bank_mask:0xf\n\t"
#define PKM(r) "v_pk_mul_f32 " r ", v[118:119], " r " op_sel_hi:[0,1]\n\t"
    asm volatile("s_nop 1\n\t"
                 "v_max_f32_dpp v118, v116, v116 quad_perm:[1,0,3,2] row_mask:0xf bank_mask:0xf\n\t"
                 "s_nop 1\n\t"
                 "v_max_f32_dpp v118, v118, v118 quad_perm:[2,3,0,1] row_mask:0xf bank_mask:0xf\n\t"
                 "v_sub_f32 v118, v116, v118\n\t"
                 "v_exp_f32 v118, v118\n\t"
                 "s_nop 0\n\t"
                 "v_mul_f32 v117, v118, v117\n\t"
                 PKM("v[108:109]") PKM("v[110:111]") PKM("v[112:113]") PKM("v[114:115]")
                 QP1("v117") QP1("v108") QP1("v109") QP1("v110") QP1("v111") QP1("v112") QP1("v113") QP1("v114") QP1("v115")
                 QP2("v117") QP2("v108") QP2("v109")
                 "v_rcp_f32 v118, v117\n\t"
                 QP2("v110") QP2("v111") QP2("v112") QP2("v113") QP2("v114") QP2("v115")
                 PKM("v[108:109]") PKM("v[110:111]") PKM("v[112:113]") PKM("v[114:115]")
                 "v_cvt_pk_bf16_f32 v108, v108, v109\n\t"
                 "v_cvt_pk_bf16_f32 v109, v110, v111\n\t"
                 "v_cvt_pk_bf16_f32 v110, v112, v113\n\t"
                 "v_cvt_pk_bf16_f32 v111, v114, v115\n\t"
                 "s_nop 1"
                 : "+{v[108:111]}"(of), "+{v[112:115]}"(o1), "+{v[116:117]}"(gl)
                 :
                 : "v118", "v119");
    f32x4 acc[8];
#pragma unroll
    for (int i = 0; i < 8; ++i) { const int k = (i & 3) * 2 + (i >> 2); acc[k] = mfma(of, bw[k], (f32x4){0.f, 0.f, 0.f, 0.f}); }
    __builtin_amdgcn_sched_barrier(0);
    float r[4];
#pragma unroll
    for (int d = 0; d < 4; ++d) r[d] = (g >> 1) ? acc[4 + d][0] : acc[d][0];
    stws1(ws, soff, pack2(half32(r[0], r[2]), half32(r[1], r[3]), tag));
  } else if (w == 7 && mlp2) {
#pragma unroll 1
    for (int spin = 0; spin < SPIN_CAP; ++spin) {
      if (__all(tagged(p0, tag) && tagged(p1, tag))) break;
      __builtin_amdgcn_s_sleep(NAP_RETRY);
      asm volatile("" ::: "memory");
      p0 = ldws(ws, paoff); p1 = ldws(ws, paoff + 1024);
    }
    const float t2[2] = {half32(first2(p0[0], p0[1]) + first2(p1[0], p1[1]), first2(p0[2], p0[3]) + first2(p1[2], p1[3])),
                         half32(second2(p0[0], p0[1]) + second2(p1[0], p1[1]), second2(p0[2], p0[3]) + second2(p1[2], p1[3]))};
    float hv[2];
#pragma unroll
    for (int e = 0; e < 2; ++e) {
      const float t = rbf(rbf(t2[e]) + (e ? bfhi(b1v) : bflo(b1v)));
      const float uu = 0.7978845608028654f * (t + 0.044715f * t * t * t);
      hv[e] = t / (1.f + __builtin_amdgcn_exp2f(-2.f * LOG2E * uu));
    }
    *(LAS u32*)(lds + L_HS + (g & 1) * 128 + (c * 2 + (g >> 1)) * 4) = pk(hv[0], hv[1]);
    wait_flags(lds, F_W2, 1, lane);
    f32x4 r = {0.f, 0.f, 0.f, 0.f};
#pragma unroll
    for (int s = 0; s < 2; ++s) {
      const u32x4 ha = *(LAS u32x4*)(lds + L_HS + ((c >> 2) & 1) * 128 + s * 64 + g * 16);
      u32x4 w2t[8];
#pragma unroll
      for (int i = 0; i < 8; ++i) w2t[i] = *(LAS u32x4*)(lds + L_W2 + s * 8192 + i * 1024 + lane * 16);
#pragma unroll
      for (int d = 0; d < 8; ++d) r = mfma(sel4((c >> 3) == (d >> 2) && (c & 3) == (d & 3), ha), colfrag(w2t, d), r);
    }
    stws(ws, WS_MP + (mlo * 28 + mhi) * 1024 + (g & 1) * 512 + (c * 2 + (g >> 1)) * 16, pack2(r[0], r[1], tag), pack2(r[2], r[3], tag));
    LDS_BARRIER();
  } else if (w == 6 && bid < NFIN) {
    const int col = cf * 128 + seg * 32 + (lane & 31);
    const u32 xv = ((const GAS u16*)x_)[bf * D + col], b2v = ((const GAS u16*)b2_)[col];
    LDS_BARRIER();
    LDS_BARRIER();
    LAS float* mf = (LAS float*)(lds + L_MF + (lane >> 5) * 256 + (lane & 31) * 4);
    float t = mf[0] + mf[32];
    t = half32(t, t);
    t = rbf(rbf(t) + bflo(b2v));
    float gate = rbf(t / (1.f + __builtin_amdgcn_exp2f(-LOG2E * t))), xf = bflo(xv);
    int faddr = L_FP + (lane & 31) * 32 + (lane >> 5) * 16;
    GAS u16* op = (GAS u16*)out_ + bf * D + col;
    asm volatile("" : "+v"(gate), "+v"(xf), "+v"(faddr), "+v"(op));
    LDS_BARRIER();
    const f32x4 fp = *(LAS f32x4*)(lds + faddr);
    float s = (fp[0] + fp[1]) + (fp[2] + fp[3]);
    s = half32(s, s);
    const float res = xf + rbf(rbf(s) * gate);
    if (lane < 32) __hip_atomic_store(op, (u16)pk(res, res), RLX_AGENT);
    if (bid == 0 && lane == 0) __hip_atomic_store((GAS u32*)(ws_ + WS_EPOCH), tag >> 8, RLX_AGENT);
  } else {
    LDS_BARRIER();
  }
}

extern "C" void kernel_launch(void* const* d_in, const int* in_sizes, int n_in, void* d_out, int out_size, void* d_ws, size_t ws_size,
                              hipStream_t stream) {
  fused_decode<<<256, 1024, 0, stream>>>((const u16*)d_in[0], (const u16*)d_in[1], (const u16*)d_in[2], (const u16*)d_in[3],
                                         (const u16*)d_in[4], (const u16*)d_in[6], (const u16*)d_in[7], (const u16*)d_in[8],
                                         (const u16*)d_in[9], (u16*)d_out, (unsigned char*)d_ws);
}
```

```cpp
#define STAMP 1
#define PROBE 0
#define STMASK (1 | (1 << 0))
#define FINLAST 0
#include <hip/hip_runtime.h>
#include <stdint.h>

#define WS_AT_END 0
#define SLEEP_Q 8
#define SLEEP_O 8
#define SLEEP_S 8
#define WO_MODE 1
#define PRIO_K 1
#define ROT 1
#define CODE_LINES 0
#define RND 1
#define BUF 1
#define Q_DELAY 32
#define MP_LATE 0
#define SLEEP_M 4
#define WO_AT 1150
#define WO_SLEEP 0
#define ABL 0

#define GAS __attribute__((address_space(1)))
#define LAS __attribute__((address_space(3)))
#define DI __device__ __forceinline__
#define RLX_AGENT __ATOMIC_RELAXED, __HIP_MEMORY_SCOPE_AGENT
typedef unsigned short u16;
typedef unsigned u32;
typedef unsigned long long u64;
typedef u32 u32x2 __attribute__((ext_vector_type(2)));
typedef u32 u32x4 __attribute__((ext_vector_type(4)));
typedef float f32x2 __attribute__((ext_vector_type(2)));
typedef float f32x4 __attribute__((ext_vector_type(4)));
typedef __bf16 bf16x2 __attribute__((ext_vector_type(2)));
typedef __bf16 bf16x8 __attribute__((ext_vector_type(8)));

constexpr int D = 512, SK = 8192, DH = 128, FF = 1792, NQ = 4096;
constexpr int SC1 = 16, NT = 2;
constexpr int SPIN_CAP = 1 << 18;
constexpr int NFIN = 32;
constexpr float LOG2E = 1.4426950408889634f;

constexpr int WS_EPOCH = 0;
constexpr int WS_QP = 256;
constexpr int WS_PA = WS_QP + 512 * 256;
constexpr int WS_OP = WS_PA + 112 * 512;
constexpr int WS_S2 = WS_OP + 256 * 2048;
constexpr int WS_MP = WS_S2 + 256 * 1024;
constexpr int WS_END = WS_MP + 112 * 1024;
constexpr int WS_SPAN = 4 << 20;
constexpr int WS_STAMP = 8 << 20;

constexpr int F_Q = 0, F_HALF = 4, F_W2 = 6;
constexpr int L_FLAG = 0, L_CNT = 48, L_XN = 64, L_HALF = L_XN + 8192, L_Q = L_HALF + 1024, L_MW = L_Q + 1024, L_LW = L_MW + 256,
              L_HS = L_LW + 256, L_GP = L_HS + 256, GP_STRIDE = 272, L_FP = L_GP + 8 * GP_STRIDE, L_MF = L_FP + 1024,
              L_TOUCH = L_MF + 512, L_W2 = L_TOUCH + 256, L_OW = L_W2 + 16384, OW_STRIDE = 2064, L_END = L_OW + 16 * OW_STRIDE;

DI float bflo(u32 v) { return __builtin_bit_cast(float, v << 16); }
DI float bfhi(u32 v) { return __builtin_bit_cast(float, v & 0xffff0000u); }
DI float rbf(float f) { return (float)(__bf16)f; }
DI u32 pk(float a, float b) { return __builtin_bit_cast(u32, __builtin_convertvector((f32x2){a, b}, bf16x2)); }
DI u32 f2u(float f) { return __builtin_bit_cast(u32, f); }
DI float u2f(u32 u) { return __builtin_bit_cast(float, u); }
DI f32x4 mfma(u32x4 a, u32x4 b, f32x4 c) {
  return __builtin_amdgcn_mfma_f32_16x16x32_bf16(__builtin_bit_cast(bf16x8, a), __builtin_bit_cast(bf16x8, b), c, 0, 0, 0);
}
template <int CTRL> DI float dpp(float x) { return u2f(__builtin_amdgcn_mov_dpp(f2u(x), CTRL, 0xf, 0xf, true)); }
constexpr int XOR1 = 0xB1, XOR2 = 0x4E, ROR4 = 0x124, ROR8 = 0x128;
DI float half32(float a, float b) { auto r = __builtin_amdgcn_permlane32_swap(f2u(a), f2u(b), false, false); return u2f(r[0]) + u2f(r[1]); }
DI float half16(float a, float b) { auto r = __builtin_amdgcn_permlane16_swap(f2u(a), f2u(b), false, false); return u2f(r[0]) + u2f(r[1]); }
DI float max32(float a) { auto r = __builtin_amdgcn_permlane32_swap(f2u(a), f2u(a), false, false); return __builtin_amdgcn_fmed3f(u2f(r[0]), u2f(r[1]), __builtin_inff()); }
DI float max16(float a) { auto r = __builtin_amdgcn_permlane16_swap(f2u(a), f2u(a), false, false); return __builtin_amdgcn_fmed3f(u2f(r[0]), u2f(r[1]), __builtin_inff()); }
DI float half_sum(float v) {
  v += dpp<XOR1>(v); v += dpp<XOR2>(v); v += dpp<ROR4>(v); v += dpp<ROR8>(v);
  return half16(v, v);
}
DI u32 plo(u32 k1, u32 k0) { return __builtin_amdgcn_perm(k1, k0, 0x05040100u); }
DI u32 phi(u32 k1, u32 k0) { return __builtin_amdgcn_perm(k1, k0, 0x07060302u); }
DI u32x4 colfrag(const u32x4 (&r)[8], int d) {
  const int q = d >> 1;
  return (d & 1) ? (u32x4){phi(r[1][q], r[0][q]), phi(r[3][q], r[2][q]), phi(r[5][q], r[4][q]), phi(r[7][q], r[6][q])}
                 : (u32x4){plo(r[1][q], r[0][q]), plo(r[3][q], r[2][q]), plo(r[5][q], r[4][q]), plo(r[7][q], r[6][q])};
}
DI u32x4 sel4(bool c, u32x4 v) { return (u32x4){c ? v[0] : 0u, c ? v[1] : 0u, c ? v[2] : 0u, c ? v[3] : 0u}; }
template <typename T> DI T ldnt(const GAS T* p) { return __builtin_nontemporal_load(p); }
#define LAUX 16
#define SAUX 16
DI u32x4 ldws(__amdgpu_buffer_rsrc_t ws, int off) { return __builtin_amdgcn_raw_buffer_load_b128(ws, off, 0, LAUX); }
DI void stws(__amdgpu_buffer_rsrc_t ws, int off, u32x2 a, u32x2 b) { __builtin_amdgcn_raw_buffer_store_b128((u32x4){a[0], a[1], b[0], b[1]}, ws, off, 0, SAUX); }
DI void stws1(__amdgpu_buffer_rsrc_t ws, int off, u32x2 a) { __builtin_amdgcn_raw_buffer_store_b64(a, ws, off, 0, SAUX); }
DI u32x2 pack2(float a, float b, u32 tag8) {
  const u32 ar = f2u(a) + 0x800u;
  const u32 u = (f2u(b) & 0xf000u) | ((ar >> 4) & ~0xf000u);
  return (u32x2){phi(f2u(b), ar), __builtin_amdgcn_perm(tag8, u, 0x07060501u)};
}
DI float first2(u32 w0, u32 w1) { return u2f(__builtin_amdgcn_perm(w0, w1 << 4, 0x0504000cu)); }
DI float second2(u32 w0, u32 w1) { return u2f(__builtin_amdgcn_perm(w0, w1, 0x0706000cu)); }
DI bool tagged(u32x4 v, u32 tag8) { return ((v[1] ^ tag8) | (v[3] ^ tag8)) < 256u; }
#define PIN() do { asm volatile("" ::: "memory"); __builtin_amdgcn_sched_barrier(0); } while (0)
#define LDS_BARRIER() do { asm volatile("s_waitcnt lgkmcnt(0)\n\ts_barrier" ::: "memory"); __builtin_amdgcn_sched_barrier(0); } while (0)
#define RAW_BARRIER() do { __builtin_amdgcn_sched_barrier(0); asm volatile("s_barrier" ::: "memory"); __builtin_amdgcn_sched_barrier(0); } while (0)
DI void dma16(const u16* src, u32 dst) {
  u32 keep;
  asm volatile("s_mov_b32 %0, m0\n\ts_mov_b32 m0, %2\n\ts_nop 0\n\tglobal_load_lds_dwordx4 %1, off nt\n\ts_mov_b32 m0, %0"
               : "=&s"(keep) : "v"(src), "s"(dst) : "memory");
}
DI void touch(const void* src, u32 dst) {
  u32 keep;
  asm volatile("s_mov_b32 %0, m0\n\ts_mov_b32 m0, %2\n\ts_nop 0\n\tglobal_load_lds_dword %1, off\n\ts_mov_b32 m0, %0"
               : "=&s"(keep) : "v"(src), "s"(dst) : "memory");
}
DI void set_flag(LAS unsigned char* lds, int f) {
  asm volatile("s_waitcnt lgkmcnt(0)" ::: "memory");
  *(volatile LAS u32*)(lds + L_FLAG + f * 4) = 1u;
}
DI void wait_flags(LAS unsigned char* lds, int first, int mask, int lane) {
#pragma unroll 1
  for (int spin = 0; spin < (SPIN_CAP << 4); ++spin) {
    if (__all(*(volatile LAS u32*)(lds + L_FLAG + (first + (lane & mask)) * 4) != 0u)) break;
    __builtin_amdgcn_s_sleep(1);
  }
  PIN();
}
DI u32 read_tag(const unsigned char* ws) {
  u32 e;
  asm volatile("s_load_dword %0, %1, 0x0\n\ts_waitcnt lgkmcnt(0)" : "=s"(e) : "s"(ws) : "memory");
  return (e + 1u) << 8;
}
template <int LOW> DI u32x4 poll16(__amdgpu_buffer_rsrc_t ws, int off, u32 want) {
  u32x4 v;
  u64 keep;
  u32 left = SPIN_CAP, t;
  asm volatile("s_mov_b64 %[keep], exec\n\t"
               "s_branch 2f\n"
               "1:\n\t"
               "s_sub_u32 %[left], %[left], 1\n\t"
               "s_cbranch_scc1 3f\n"
               "2:\n\t"
               "buffer_load_dwordx4 v[100:103], %[off], %[ws], 0 offen sc1\n\t"
               "s_waitcnt vmcnt(0)\n\t"
               "v_bitop3_b32 %[t], v101, v103, %[want] bitop3:0x7e\n\t"
               "v_cmp_lt_u32 vcc, %[low], %[t]\n\t"
               "s_and_b64 exec, exec, vcc\n\t"
               "s_cbranch_scc1 1b\n"
               "3:\n\t"
               "s_mov_b64 exec, %[keep]"
               : "={v[100:103]}"(v), [keep] "=&s"(keep), [left] "+s"(left), [t] "=&v"(t)
               : [off] "v"(off), [ws] "s"(ws), [want] "s"(want), [low] "i"(LOW)
               : "vcc", "scc", "memory");
  return v;
}
DI void quad_sum4(float& a, float& b, float& c, float& d) {
  asm("s_nop 1\n\t"
      "v_add_f32_dpp %0, %0, %0 quad_perm:[1,0,3,2] row_mask:0xf bank_mask:0xf\n\t"
      "v_add_f32_dpp %1, %1, %1 quad_perm:[1,0,3,2] row_mask:0xf bank_mask:0xf\n\t"
      "v_add_f32_dpp %2, %2, %2 quad_perm:[1,0,3,2] row_mask:0xf bank_mask:0xf\n\t"
      "v_add_f32_dpp %3, %3, %3 quad_perm:[1,0,3,2] row_mask:0xf bank_mask:0xf\n\t"
      "v_add_f32_dpp %0, %0, %0 quad_perm:[2,3,0,1] row_mask:0xf bank_mask:0xf\n\t"
      "v_add_f32_dpp %1, %1, %1 quad_perm:[2,3,0,1] row_mask:0xf bank_mask:0xf\n\t"
      "v_add_f32_dpp %2, %2, %2 quad_perm:[2,3,0,1] row_mask:0xf bank_mask:0xf\n\t"
      "v_add_f32_dpp %3, %3, %3 quad_perm:[2,3,0,1] row_mask:0xf bank_mask:0xf"
      : "+v"(a), "+v"(b), "+v"(c), "+v"(d));
}
DI float sum_records(u32x4 v) {
  const float t = half16(half32(first2(v[0], v[1]), first2(v[2], v[3])), half32(second2(v[0], v[1]), second2(v[2], v[3])));
  return t + dpp<ROR8>(t);
}

DI void layernorm(u32 (&x)[8]) {
  float v[16], s = 0.f;
#pragma unroll
  for (int e = 0; e < 8; ++e) { v[2 * e] = bflo(x[e]); v[2 * e + 1] = bfhi(x[e]); s += v[2 * e] + v[2 * e + 1]; }
  const float mean = half_sum(s) * (1.f / 512.f);
  float q = 0.f;
#pragma unroll
  for (int e = 0; e < 16; ++e) { const float d = v[e] - mean; q = fmaf(d, d, q); }
  const float var = half_sum(q) * (1.f / 512.f);
  const float mb = rbf(mean), inv = __builtin_amdgcn_rcpf(rbf(__builtin_amdgcn_sqrtf(rbf(rbf(var) + 1e-5f))));
#pragma unroll
  for (int e = 0; e < 8; ++e) { const u32 d = pk(v[2 * e] - mb, v[2 * e + 1] - mb); x[e] = pk(bflo(d) * inv, bfhi(d) * inv); }
}

DI f32x2 tile_matvec(const u32x4 (&wv)[8], LAS unsigned char* xin, int g, int c) {
  const u32x4 xf = *(LAS u32x4*)(xin + ((c >> 2) & 1) * 1024 + ((c >> 3) * 32 + g * 8) * 2);
  f32x4 lo = {0.f, 0.f, 0.f, 0.f}, hi = lo;
#pragma unroll
  for (int d = 0; d < 4; ++d) {
    const u32x4 am = sel4((c & 3) == d, xf);
    lo = mfma(am, colfrag(wv, d), lo);
    hi = mfma(am, colfrag(wv, 4 + d), hi);
  }
  const bool valid = (g >> 1) == (c >> 3);
  float t[4];
#pragma unroll
  for (int e = 0; e < 4; ++e) { t[e] = half32(valid ? lo[e] : 0.f, valid ? hi[e] : 0.f); t[e] += dpp<ROR8>(t[e]); }
  return (c & 8) ? (f32x2){t[2], t[3]} : (f32x2){t[0], t[1]};
}

#define ST(i) do { if (!((STMASK >> (i)) & 1)) break; __builtin_amdgcn_sched_barrier(0); ts[i] = __builtin_amdgcn_s_memrealtime(); __builtin_amdgcn_s_waitcnt(0xC07F); __builtin_amdgcn_sched_barrier(0); } while (0)

__global__ void __launch_bounds__(1024, 4)
fused_decode(const u16* x_, const u16* k_, const u16* v_, const u16* wq_, const u16* wo_, const u16* w1_, const u16* b1_,
             const u16* w2_, const u16* b2_, u16* out_, unsigned char* ws_
             ) {
  __shared__ __attribute__((aligned(16))) unsigned char lds_raw[L_END];
  LAS unsigned char* lds = (LAS unsigned char*)lds_raw;
  u64 ts[12];
  for (int i = 0; i < 12; ++i) ts[i] = 0;
  ST(0);
  const u64 t_start = WO_MODE == 3 ? __builtin_amdgcn_s_memrealtime() : 0;
  asm volatile("" :: "s"(x_), "s"(k_), "s"(v_), "s"(wq_), "s"(wo_), "s"(w1_), "s"(b1_), "s"(w2_), "s"(b2_), "s"(out_), "s"(ws_));
  const int tid = threadIdx.x, lane = tid & 63, w = __builtin_amdgcn_readfirstlane(tid >> 6);
  const int g = lane >> 4, c = lane & 15;
  const int bid = blockIdx.x, kvh = bid & 7, j = bid >> 3, b = j >> 4, sp = j & 15, rb = j >> 2, cb = j & 3;
  const bool mlp1 = j < 14, mlp2 = j >= 14 && j < 28;
  const __amdgpu_buffer_rsrc_t ws = __builtin_amdgcn_make_buffer_rsrc(ws_, 0, WS_END, 0x00020000);
  u32 tag;
  const u32 hsh = ((u32)(bid * 16 + w) * 2654435761u) >> 20, hb = ((u32)bid * 2654435761u) >> 16;
  const int wu = (w + (ROT == 0 ? 0 : ROT == 1 || ROT == 2 || ROT == 3 ? j : (int)hb)) & 15;
  const int ro = ROT == 2 ? kvh : ROT == 3 || ROT == 4 ? (int)(hsh & 7) : 0;
  const size_t kvseq = (size_t)(b * 8 + kvh) * SK * DH;
  const __amdgpu_buffer_rsrc_t kr = __builtin_amdgcn_make_buffer_rsrc((void*)(k_ + kvseq), 0, SK * DH * 2, 0x00020000);
  const __amdgpu_buffer_rsrc_t vr = __builtin_amdgcn_make_buffer_rsrc((void*)(v_ + kvseq), 0, SK * DH * 2, 0x00020000);
  const int kvoff = (sp * 16 + wu) * 8192;
  const int klane = ((((c >> 1) ^ ro) & 1) * 4 + (c >> 2)) * 256 + (g * 2 + (c & 1)) * 16;
  u32x4 kf[8], vf[8];

  const int cq = j & 7, kq = j >> 3;
  const int mt = (mlp1 ? j : j - 14) * 8 + kvh;
  const int mhi = mt >> 2, mlo = mt & 3;
  if (w < 2 || (w < 4 && mlp1)) {
    __builtin_amdgcn_s_setprio(3);
    if (w == 0 && lane < 16) *(LAS u32*)(lds + L_FLAG + lane * 4) = 0u;
    const GAS u32x4* xp = (const GAS u32x4*)x_ + lane * 2;
    const u32x4 x0 = xp[0], x1 = xp[1];
    PIN();
    const int rs = w < 2 ? NQ / 8 : FF / 8, row0 = (w < 2 ? kq : mlo) * 128 + (w & 1) * 64;
    const u16* tile = w < 2 ? wq_ + kvh * 512 + cq * 64 : w1_ + mhi * 64;
    const GAS u32x4* wp = (const GAS u32x4*)tile + (row0 + (c >> 3) * 32 + g * 8) * rs + (c & 7);
    u32x4 wv[8];
#pragma unroll
    for (int i = 0; i < 8; ++i) wv[i] = ldnt(wp + i * rs);
    PIN();
    LDS_BARRIER();
    ST(1);
    tag = read_tag(ws_);
    PIN();
    u32 xs[8] = {x0[0], x0[1], x0[2], x0[3], x1[0], x1[1], x1[2], x1[3]};
    layernorm(xs);
    if (w >= 2) layernorm(xs);
    LAS unsigned char* xl = lds + L_XN + w * 2048;
    *(LAS u32x4*)(xl + lane * 32) = (u32x4){xs[0], xs[1], xs[2], xs[3]};
    *(LAS u32x4*)(xl + lane * 32 + 16) = (u32x4){xs[4], xs[5], xs[6], xs[7]};
    f32x2 r = tile_matvec(wv, xl + row0 * 2, g, c);
    LAS f32x2* half = (LAS f32x2*)(lds + L_HALF + (w >> 1) * 512 + lane * 8);
    if (w & 1) {
      *half = r;
      set_flag(lds, F_HALF + (w >> 1));
    } else {
      wait_flags(lds, F_HALF + (w >> 1), 0, lane);
      r += *half;
      const int rec = w == 0 ? WS_QP + ((((g & 1) * 8 + kvh) * 8 + cq) * 4 + kq) * 256 : WS_PA + mt * 512 + (g & 1) * 256;
      stws1(ws, rec + ((c & 7) * 4 + (g >> 1) * 2 + (c >> 3)) * 8, pack2(r[0], r[1], tag));
    }
    ST(2);
    __builtin_amdgcn_s_setprio(PRIO_K);
  } else {
    if (w < 4 && mlp2) {
      const u16* p = w2_ + (mhi * 64 + (w & 1) * 32 + g * 8) * D + mlo * 128 + c * 8;
#pragma unroll
      for (int i = 0; i < 8; ++i) dma16(p + i * D, (u32)(size_t)(lds + L_W2 + (w & 1) * 8192 + i * 1024));
    }
    if (CODE_LINES && w == 4) {
      const unsigned char* pc;
      asm volatile("s_getpc_b64 %0" : "=s"(pc));
      if (lane < 4 && j * 4 + lane < CODE_LINES) touch(pc + (j * 4 + lane) * 128, (u32)(size_t)(lds + L_TOUCH));
    }
    RAW_BARRIER();
    if ((w >> 2) == 1) __builtin_amdgcn_s_setprio(2); else __builtin_amdgcn_s_setprio(PRIO_K);
  }
  PIN();

#pragma unroll
  for (int i = 0; i < 8; ++i) { kf[i] = BUF ? __builtin_amdgcn_raw_buffer_load_b128(kr, klane + (i & 1) * 128, kvoff + ((i >> 1) ^ (ro >> 1)) * 2048, NT) : ldnt((const GAS u32x4*)((const GAS char*)(k_ + kvseq) + kvoff + klane + (i & 1) * 128 + (i >> 1) * 2048)); __builtin_amdgcn_sched_barrier(0); }
  if ((w >> 2) != 1) __builtin_amdgcn_s_setprio(0);
  PIN();
#pragma unroll
  for (int i = 0; i < 8; ++i) { vf[i] = BUF ? __builtin_amdgcn_raw_buffer_load_b128(vr, lane * 16, kvoff + (i ^ ro) * 1024, NT) : ldnt((const GAS u32x4*)((const GAS char*)(v_ + kvseq) + kvoff + lane * 16 + i * 1024)); __builtin_amdgcn_sched_barrier(0); }
  PIN();
  ST(4);
  tag = read_tag(ws_);
  const int paoff = WS_PA + mhi * 2048 + lane * 16;
  u32x4 p0, p1;
  u32 b1v;
  if ((w >> 2) == 1) {
    const int u = w & 3;
    const int qoff = WS_QP + (b * 8 + kvh) * 8192 + u * 2048 + lane * 16;
    asm volatile("s_waitcnt vmcnt(0)" ::: "memory");
    if (Q_DELAY) __builtin_amdgcn_s_sleep(Q_DELAY);
    u32x4 q0, q1;
#pragma unroll 1
    for (int spin = 0; spin < SPIN_CAP; ++spin) {
      q0 = ldws(ws, qoff); q1 = ldws(ws, qoff + 1024);
      if (__all(tagged(q0, tag) && tagged(q1, tag))) break;
      __builtin_amdgcn_s_sleep(SLEEP_Q);
      asm volatile("" ::: "memory");
    }
    const float qa = half16(half32(first2(q0[0], q0[1]), first2(q1[0], q1[1])), half32(first2(q0[2], q0[3]), first2(q1[2], q1[3])));
    const float qb = half16(half32(second2(q0[0], q0[1]), second2(q1[0], q1[1])), half32(second2(q0[2], q0[3]), second2(q1[2], q1[3])));
    *(LAS u32*)(lds + L_Q + u * 256 + ((g >> 1) * 32 + c * 2 + (g & 1)) * 4) = pk(qa, qb);
    set_flag(lds, F_Q + u);
    ST(3);
    if (w == 7 && mlp2) {
      p0 = ldws(ws, paoff); p1 = ldws(ws, paoff + 1024);
      b1v = *(const GAS u32*)(b1_ + mhi * 64 + c * 4 + (g >> 1) * 2);
      PIN();
    }
    __builtin_amdgcn_s_setprio(0);
  }
  wait_flags(lds, F_Q, 3, lane);

  float sc[8];
  {
    const int x0 = (c >> 2) & 1;
    const u32x4 q0 = *(LAS u32x4*)(lds + L_Q + (c & 3) * 256 + (g * 2 + x0) * 16);
    const u32x4 q1 = *(LAS u32x4*)(lds + L_Q + (c & 3) * 256 + 128 + (g * 2 + x0) * 16);
#pragma unroll
    for (int o = 0; o < 4; ++o) {
      f32x4 s = mfma(kf[2 * o], q0, (f32x4){0.f, 0.f, 0.f, 0.f});
      s = mfma(kf[2 * o + 1], q1, s);
#pragma unroll
      for (int a = 0; a < 2; ++a) {
        float e1 = s[2 * a + 1];
        asm("" : "+v"(e1));
        const float t = x0 ? e1 : s[2 * a];
        sc[2 * o + a] = t + dpp<ROR4>(t);
      }
      const u32 d = pk(sc[2 * o], sc[2 * o + 1]), e = pk(bflo(d) * 0.08838834764831845f, bfhi(d) * 0.08838834764831845f);
      sc[2 * o] = bflo(e); sc[2 * o + 1] = bfhi(e);
    }
  }
  PIN();

    float m = fmaxf(fmaxf(fmaxf(sc[0], sc[1]), fmaxf(sc[2], sc[3])), fmaxf(fmaxf(sc[4], sc[5]), fmaxf(sc[6], sc[7])));
    m = max32(max16(m)) * LOG2E;
    float p[8], l = 0.f;
#pragma unroll
    for (int e = 0; e < 8; ++e) { p[e] = __builtin_amdgcn_exp2f(fmaf(sc[e], LOG2E, -m)); l += p[e]; }
    l = half16(l, l); l = half32(l, l);
    if (lane < 4) { *(LAS float*)(lds + L_MW + (lane * 16 + w) * 4) = m; *(LAS float*)(lds + L_LW + (lane * 16 + w) * 4) = l; }
    PIN();
    if ((w >> 1) == 1) set_flag(lds, F_W2 + (w & 1));
    if (lane == 0) __hip_atomic_fetch_add((LAS u32*)(lds + L_CNT), 1u, __ATOMIC_RELAXED, __HIP_MEMORY_SCOPE_WORKGROUP);
    const u32x4 pf = {pk(p[0], p[1]), pk(p[2], p[3]), pk(p[4], p[5]), pk(p[6], p[7])};
    u32x4 am[4];
#pragma unroll
    for (int u = 0; u < 4; ++u) am[u] = sel4((c >> 2) == u, pf);
    PIN();
    u32x4 ve[4], vo[4];
    u32 slo, shi;
    asm volatile("s_mov_b32 %0, 0x05040100\n\ts_mov_b32 %1, 0x07060302" : "=s"(slo), "=s"(shi) : "v"(am[0][0]), "v"(am[1][1]), "v"(am[2][2]), "v"(am[3][3]));
#pragma unroll
    for (int r = 0; r < 4; ++r) {
#pragma unroll
      for (int u = 0; u < 4; ++u) {
        ve[u][r] = __builtin_amdgcn_perm(vf[2 * r + 1][u], vf[2 * r][u], slo);
        vo[u][r] = __builtin_amdgcn_perm(vf[2 * r + 1][u], vf[2 * r][u], shi);
      }
      if (r < 3)
        asm volatile("" : "+s"(slo), "+s"(shi) : "v"(ve[0][r]), "v"(ve[1][r]), "v"(ve[2][r]), "v"(ve[3][r]), "v"(vo[0][r]), "v"(vo[1][r]), "v"(vo[2][r]), "v"(vo[3][r]));
    }
    f32x4 oe = {0.f, 0.f, 0.f, 0.f}, oo = oe;
#pragma unroll
    for (int u = 0; u < 4; ++u) { oe = mfma(am[u], ve[u], oe); oo = mfma(am[u], vo[u], oo); }
#pragma unroll
    for (int h = 0; h < 4; ++h) *(LAS f32x2*)(lds + L_OW + w * OW_STRIDE + h * 512 + c * 32 + g * 8) = (f32x2){oe[h], oo[h]};
    PIN();
    ST(5);

  const bool gath = (w & 4) == 0;
  const int v = (w & 3) + (w >> 3) * 4;
  auto wait_ml = [&]() __attribute__((always_inline)) {
#pragma unroll 1
    for (int spin = 0; spin < (SPIN_CAP << 4); ++spin) {
      if (__builtin_amdgcn_readfirstlane(*(volatile LAS u32*)(lds + L_CNT)) == 16u) break;
      __builtin_amdgcn_s_sleep(1);
    }
    PIN();
  };
  auto load_wo = [&](u32x4 (&wt)[8]) __attribute__((always_inline)) {
    const GAS u16* p = (const GAS u16*)wo_ + (size_t)(kvh * 512 + rb * 64 + g * 4) * D + cb * 128 + (w - 4) * 64 + c * 4;
#pragma unroll
    for (int i = 0; i < 8; ++i) {
      const u32x2 t0 = ldnt((const GAS u32x2*)(p + (size_t)(32 * (i >> 2) + (i & 3)) * D));
      const u32x2 t1 = ldnt((const GAS u32x2*)(p + (size_t)(32 * (i >> 2) + 16 + (i & 3)) * D));
      wt[i] = (u32x4){t0[0], t0[1], t1[0], t1[1]};
    }
    PIN();
  };
  const bool fin = FINLAST ? bid >= 256 - NFIN : bid < NFIN;
  const int cf = bid & 3, bf = (bid >> 2) & 1, seg = (bid >> 3) & 3;
  if (w >= 12) {
    LDS_BARRIER();
    ST(6);
    if (fin) {
      const int row = (w - 12) * 8 + (lane >> 3);
      const int moff = WS_MP + (cf * 28 + min(row, 27)) * 1024 + bf * 512 + seg * 128 + (lane & 7) * 16;
      u32x4 mv;
#pragma unroll 1
      for (int spin = 0; spin < SPIN_CAP; ++spin) {
        mv = ldws(ws, moff);
        if (__all(tagged(mv, tag))) break;
        __builtin_amdgcn_s_sleep(SLEEP_M);
        asm volatile("" ::: "memory");
      }
      const float t = sum_records(sel4(row < 28, mv));
      if ((lane & 8) == 0) *(LAS float*)(lds + L_MF + (w - 12) * 128 + ((lane & 7) * 4 + g) * 4) = t;
      LDS_BARRIER();
    }
  } else if (gath) {
    {
      const int f = v * 16 + (lane >> 2), p4 = lane & 3, hd = v >> 1;
      wait_ml();
      const f32x4 mv = *(LAS f32x4*)(lds + L_MW + hd * 64 + p4 * 16), lv = *(LAS f32x4*)(lds + L_LW + hd * 64 + p4 * 16);
      float M = fmaxf(fmaxf(mv[0], mv[1]), fmaxf(mv[2], mv[3]));
      M = fmaxf(M, dpp<XOR1>(M)); M = fmaxf(M, dpp<XOR2>(M));
      float L = 0.f, fw[4];
#pragma unroll
      for (int i = 0; i < 4; ++i) { fw[i] = __builtin_amdgcn_exp2f(mv[i] - M); L = fmaf(fw[i], lv[i], L); }
      L += dpp<XOR1>(L); L += dpp<XOR2>(L);
      const float inv = __builtin_amdgcn_rcpf(L);
#pragma unroll
      for (int i = 0; i < 4; ++i) fw[i] *= inv;
      const u32 lam = f2u(M + __builtin_amdgcn_logf(L)) >> ((lane >> 5) * 16);
      u32 t0 = tag | (lam & 255u), t1 = tag | ((lam >> 8) & 255u);
      int ooff = WS_OP + ((kvh * 2 + b) * 16 + sp) * 2048 + f * 16;
      int laddr = L_OW + p4 * 4 * OW_STRIDE + f * 16;
      asm volatile("" : "+v"(fw[0]), "+v"(fw[1]), "+v"(fw[2]), "+v"(fw[3]), "+v"(t0), "+v"(t1), "+v"(ooff), "+v"(laddr));
      ST(3);
      LDS_BARRIER();
      ST(6);
      f32x4 s = fw[0] * *(LAS f32x4*)(lds + laddr);
#pragma unroll
      for (int i = 1; i < 4; ++i) s += fw[i] * *(LAS f32x4*)(lds + laddr + i * OW_STRIDE);
      float s0 = s[0], s1 = s[1], s2 = s[2], s3 = s[3];
      quad_sum4(s0, s1, s2, s3);
      if (p4 == 0) __builtin_amdgcn_raw_buffer_store_b128((u32x4){pk(s0, s1), t0, pk(s2, s3), t1}, ws, ooff, 0, SAUX);
    }
    PIN();
    ST(7);
    {
      u32 gaddr2 = (u32)(size_t)(lds + L_GP + v * GP_STRIDE + 256), gaddr = gaddr2 - 256 + (c * 4 + g) * 4;
      u32 lsel = (c & 8) ? 0x05040100u : 0x01000504u;
      asm volatile("" : "+v"(gaddr), "+v"(gaddr2), "+v"(lsel));
      if (SLEEP_O) __builtin_amdgcn_s_sleep(SLEEP_O);
      const u32x4 ov = poll16<255>(ws, WS_OP + (kvh * 32 + v * 4 + g) * 2048 + rb * 256 + c * 16, tag);
      ST(8);
      u32 a, lam, m1, x0, x1, x2, x3;
      f32x2 gl;
      asm volatile("v_perm_b32 %[a], %[t1], %[t0], %[sel]\n\t"
                   "v_lshlrev_b32 %[x0], 16, %[d0]\n\t"
                   "v_lshlrev_b32 %[x2], 16, %[d1]\n\t"
                   "v_mov_b32_dpp %[m1], %[a] row_ror:8 row_mask:0xf bank_mask:0xf\n\t"
                   "v_and_b32 %[x1], 0xffff0000, %[d0]\n\t"
                   "v_perm_b32 %[lam], %[a], %[m1], %[lsel]\n\t"
                   "v_mov_b32 %[m1], %[lam]\n\t"
                   "v_mov_b32 %[a], %[lam]\n\t"
                   "v_and_b32 %[x3], 0xffff0000, %[d1]\n\t"
                   "s_nop 0\n\t"
                   "v_permlane16_swap_b32 %[m1], %[a]\n\t"
                   "v_max_f32 %[m1], %[m1], %[a]\n\t"
                   "v_mov_b32 %[a], %[m1]\n\t"
                   "s_nop 1\n\t"
                   "v_permlane32_swap_b32 %[m1], %[a]\n\t"
                   "v_max_f32 v104, %[m1], %[a]\n\t"
                   "v_sub_f32 %[lam], %[lam], v104\n\t"
                   "v_exp_f32 %[lam], %[lam]\n\t"
                   "s_nop 0\n\t"
                   "v_mul_f32 %[x0], %[lam], %[x0]\n\t"
                   "v_mul_f32 %[x2], %[lam], %[x2]\n\t"
                   "v_mul_f32 %[x1], %[lam], %[x1]\n\t"
                   "v_mul_f32 %[x3], %[lam], %[x3]\n\t"
                   "v_mov_b32 %[a], %[lam]\n\t"
                   "v_permlane32_swap_b32 %[x0], %[x2]\n\t"
                   "v_permlane32_swap_b32 %[x1], %[x3]\n\t"
                   "v_permlane16_swap_b32 %[lam], %[a]\n\t"
                   "v_add_f32 %[x0], %[x0], %[x2]\n\t"
                   "v_add_f32 %[x1], %[x1], %[x3]\n\t"
                   "v_add_f32 %[lam], %[lam], %[a]\n\t"
                   "v_mov_b32 %[a], %[lam]\n\t"
                   "v_permlane16_swap_b32 %[x0], %[x1]\n\t"
                   "v_add_f32 %[x0], %[x0], %[x1]\n\t"
                   "v_permlane32_swap_b32 %[lam], %[a]\n\t"
                   "ds_write_b32 %[ga], %[x0]\n\t"
                   "v_add_f32 v105, %[lam], %[a]\n\t"
                   "ds_write_b64 %[gb], v[104:105]"
                   : [a] "=&v"(a), [lam] "=&v"(lam), [m1] "=&v"(m1), [x0] "=&v"(x0), [x1] "=&v"(x1), [x2] "=&v"(x2), [x3] "=&v"(x3), "=&{v[104:105]}"(gl)
                   : [d0] "v"(ov[0]), [t0] "v"(ov[1]), [d1] "v"(ov[2]), [t1] "v"(ov[3]), [sel] "s"(0x04000400u), [lsel] "v"(lsel), [ga] "v"(gaddr), [gb] "v"(gaddr2)
                   : "memory");
    }
    LDS_BARRIER();
    ST(9);
    if (fin) {
      int faddr = L_FP + ((lane & 7) * 4 + g) * 32 + v * 4;
      asm volatile("" : "+v"(faddr));
      if (SLEEP_S) __builtin_amdgcn_s_sleep(SLEEP_S);
      const u32x4 sv = poll16<255>(ws, WS_S2 + (cf * 64 + v * 8 + (lane >> 3)) * 1024 + bf * 512 + seg * 128 + (lane & 7) * 16, tag);
      ST(10);
      *(LAS float*)(lds + faddr) = sum_records(sv);
      LDS_BARRIER();
    }
  } else if (w < 6) {
    u32x4 wt[8], bw[8];
    if (WO_MODE == 3) {
      wait_ml();
      const bool now = __builtin_amdgcn_s_memrealtime() >= t_start + WO_AT;
      if (!now) {
        LDS_BARRIER();
        ST(6);
        while (__builtin_amdgcn_s_memrealtime() < t_start + WO_AT) __builtin_amdgcn_s_sleep(1);
      }
      load_wo(wt);
      if (now) { LDS_BARRIER(); ST(6); }
    } else {
    if (WO_MODE == 1) wait_ml();
    if (WO_MODE < 2) load_wo(wt);
    ST(7);
    LDS_BARRIER();
    ST(6);
    if (WO_MODE == 2) { if (WO_SLEEP) __builtin_amdgcn_s_sleep(WO_SLEEP); load_wo(wt); }
    }
#pragma unroll
    for (int i = 0; i < 8; ++i) bw[i] = colfrag(wt, i);
    int gp = L_GP + (c & 7) * GP_STRIDE, go = gp + ((c >> 3) * 16 + g * 4) * 4;
    int soff = WS_S2 + (cb * 64 + kvh * 8 + rb) * 1024 + (g & 1) * 512 + ((w - 4) * 32 + c * 2 + (g >> 1)) * 8;
    asm volatile("" : "+v"(gp), "+v"(go), "+v"(soff) : "v"(bw[0]), "v"(bw[1]), "v"(bw[2]), "v"(bw[3]), "v"(bw[4]), "v"(bw[5]), "v"(bw[6]), "v"(bw[7]));
    LDS_BARRIER();
    ST(9);
    f32x2 gl = *(LAS f32x2*)(lds + gp + 256);
    u32x4 of = *(LAS u32x4*)(lds + go), o1 = *(LAS u32x4*)(lds + go + 128);
#define QP1(r) "v_add_f32_dpp " r ", " r ", " r " quad_perm:[1,0,3,2] row_mask:0xf bank_mask:0xf\n\t"
#define QP2(r) "v_add_f32_dpp " r ", " r ", " r " quad_perm:[2,3,0,1] row_mask:0xf bank_mask:0xf\n\t"
#define PKM(r) "v_pk_mul_f32 " r ", v[118:119], " r " op_sel_hi:[0,1]\n\t"
    asm volatile("v_max_f32_dpp v118, v116, v116 quad_perm:[1,0,3,2] row_mask:0xf bank_mask:0xf\n\t"
                 "s_nop 1\n\t"
                 "v_max_f32_dpp v118, v118, v118 quad_perm:[2,3,0,1] row_mask:0xf bank_mask:0xf\n\t"
                 "v_sub_f32 v118, v116, v118\n\t"
                 "v_exp_f32 v118, v118\n\t"
                 "s_nop 0\n\t"
                 "v_mul_f32 v117, v118, v117\n\t"
                 PKM("v[108:109]") PKM("v[110:111]") PKM("v[112:113]") PKM("v[114:115]")
                 QP1("v117") QP1("v108") QP1("v109") QP1("v110") QP1("v111") QP1("v112") QP1("v113") QP1("v114") QP1("v115")
                 QP2("v117") QP2("v108") QP2("v109")
                 "v_rcp_f32 v118, v117\n\t"
                 QP2("v110") QP2("v111") QP2("v112") QP2("v113") QP2("v114") QP2("v115")
                 PKM("v[108:109]") PKM("v[110:111]") PKM("v[112:113]") PKM("v[114:115]")
                 "v_cvt_pk_bf16_f32 v108, v108, v109\n\t"
                 "v_cvt_pk_bf16_f32 v109, v110, v111\n\t"
                 "v_cvt_pk_bf16_f32 v110, v112, v113\n\t"
                 "v_cvt_pk_bf16_f32 v111, v114, v115\n\t"
                 "s_nop 1"
                 : "+{v[108:111]}"(of), "+{v[112:115]}"(o1), "+{v[116:117]}"(gl)
                 :
                 : "v118", "v119");
    f32x4 acc[8];
#pragma unroll
    for (int i = 0; i < 8; ++i) { const int k = (i & 3) * 2 + (i >> 2); acc[k] = mfma(of, bw[k], (f32x4){0.f, 0.f, 0.f, 0.f}); }
    __builtin_amdgcn_sched_barrier(0);
    float r[4];
#pragma unroll
    for (int d = 0; d < 4; ++d) r[d] = (g >> 1) ? acc[4 + d][0] : acc[d][0];
    stws1(ws, soff, pack2(half32(r[0], r[2]), half32(r[1], r[3]), tag));
    ST(10);
  } else if (w == 7 && mlp2) {
#pragma unroll 1
    for (int spin = 0; spin < SPIN_CAP; ++spin) {
      if (__all(tagged(p0, tag) && tagged(p1, tag))) break;
      __builtin_amdgcn_s_sleep(SLEEP_Q);
      asm volatile("" ::: "memory");
      p0 = ldws(ws, paoff); p1 = ldws(ws, paoff + 1024);
    }
    const float t2[2] = {half32(first2(p0[0], p0[1]) + first2(p1[0], p1[1]), first2(p0[2], p0[3]) + first2(p1[2], p1[3])),
                         half32(second2(p0[0], p0[1]) + second2(p1[0], p1[1]), second2(p0[2], p0[3]) + second2(p1[2], p1[3]))};
    float hv[2];
#pragma unroll
    for (int e = 0; e < 2; ++e) {
      const float t = rbf(rbf(t2[e]) + (e ? bfhi(b1v) : bflo(b1v)));
      const float uu = 0.7978845608028654f * (t + 0.044715f * t * t * t);
      hv[e] = t / (1.f + __builtin_amdgcn_exp2f(-2.f * LOG2E * uu));
    }
    *(LAS u32*)(lds + L_HS + (g & 1) * 128 + (c * 2 + (g >> 1)) * 4) = pk(hv[0], hv[1]);
    wait_flags(lds, F_W2, 1, lane);
    f32x4 r = {0.f, 0.f, 0.f, 0.f};
#pragma unroll
    for (int s = 0; s < 2; ++s) {
      const u32x4 ha = *(LAS u32x4*)(lds + L_HS + ((c >> 2) & 1) * 128 + s * 64 + g * 16);
      u32x4 w2t[8];
#pragma unroll
      for (int i = 0; i < 8; ++i) w2t[i] = *(LAS u32x4*)(lds + L_W2 + s * 8192 + i * 1024 + lane * 16);
#pragma unroll
      for (int d = 0; d < 8; ++d) r = mfma(sel4((c >> 3) == (d >> 2) && (c & 3) == (d & 3), ha), colfrag(w2t, d), r);
    }
    u32x2 m0 = pack2(r[0], r[1], tag), m1 = pack2(r[2], r[3], tag);
    asm volatile("" : "+v"(m0), "+v"(m1));
    ST(2);
    if (MP_LATE) LDS_BARRIER();
    stws(ws, WS_MP + (mlo * 28 + mhi) * 1024 + (g & 1) * 512 + (c * 2 + (g >> 1)) * 16, m0, m1);
    if (!MP_LATE) LDS_BARRIER();
    ST(6);
  } else if (w == 6 && fin) {
    const int col = cf * 128 + seg * 32 + (lane & 31);
    const u32 xv = ((const GAS u16*)x_)[bf * D + col], b2v = ((const GAS u16*)b2_)[col];
    LDS_BARRIER();
    ST(6);
    LDS_BARRIER();
    LAS float* mf = (LAS float*)(lds + L_MF + (lane >> 5) * 256 + (lane & 31) * 4);
    float t = mf[0] + mf[32];
    t = half32(t, t);
    t = rbf(rbf(t) + bflo(b2v));
    float gate = rbf(t / (1.f + __builtin_amdgcn_exp2f(-LOG2E * t))), xf = bflo(xv);
    int faddr = L_FP + (lane & 31) * 32 + (lane >> 5) * 16;
    GAS u16* op = (GAS u16*)out_ + bf * D + col;
    asm volatile("" : "+v"(gate), "+v"(xf), "+v"(faddr), "+v"(op));
    LDS_BARRIER();
    ST(10);
    const f32x4 fp = *(LAS f32x4*)(lds + faddr);
    float s = (fp[0] + fp[1]) + (fp[2] + fp[3]);
    s = half32(s, s);
    const float res = xf + rbf(rbf(s) * gate);
    if (lane < 32) __hip_atomic_store(op, (u16)pk(res, res), RLX_AGENT);
    if ((bid & 31) == 0 && lane == 0) __hip_atomic_store((GAS u32*)(ws_ + WS_EPOCH), tag >> 8, RLX_AGENT);
  } else {
    LDS_BARRIER();
    ST(6);
  }
  ST(11);
  if (ts[PROBE] && (w & 4) == 0) {
    const u64 until = ts[0] + 4000 + (ts[PROBE] - ts[0]);
    while (__builtin_amdgcn_s_memrealtime() < until) __builtin_amdgcn_s_sleep(1);
  }
}

extern "C" void kernel_launch(void* const* d_in, const int* in_sizes, int n_in, void* d_out, int out_size, void* d_ws, size_t ws_size,
                              hipStream_t stream) {
  unsigned char* ws = (unsigned char*)d_ws + (WS_AT_END ? ((ws_size - WS_SPAN) & ~(size_t)4095) : 0);
  fused_decode<<<256, 1024, 0, stream>>>((const u16*)d_in[0], (const u16*)d_in[1], (const u16*)d_in[2], (const u16*)d_in[3],
                                         (const u16*)d_in[4], (const u16*)d_in[6], (const u16*)d_in[7], (const u16*)d_in[8],
                                         (const u16*)d_in[9], (u16*)d_out, ws
                                         );
}
```

```cpp
#define STAMP 1
#define PROBE 7
#define PROBE_B 6
#define STMASK (1 | (1 << 7) | (1 << 6))
#define FINLAST 0
#include <hip/hip_runtime.h>
#include <stdint.h>

#define WS_AT_END 0
#define SLEEP_Q 8
#define SLEEP_O 8
#define SLEEP_S 8
#define WO_MODE 1
#define PRIO_K 1
#define ROT 1
#define CODE_LINES 0
#define RND 1
#define BUF 1
#define Q_DELAY 32
#define MP_LATE 0
#define SLEEP_M 4
#define WO_AT 1150
#define WO_SLEEP 0
#define ABL 0

#define GAS __attribute__((address_space(1)))
#define LAS __attribute__((address_space(3)))
#define DI __device__ __forceinline__
#define RLX_AGENT __ATOMIC_RELAXED, __HIP_MEMORY_SCOPE_AGENT
typedef unsigned short u16;
typedef unsigned u32;
typedef unsigned long long u64;
typedef u32 u32x2 __attribute__((ext_vector_type(2)));
typedef u32 u32x4 __attribute__((ext_vector_type(4)));
typedef float f32x2 __attribute__((ext_vector_type(2)));
typedef float f32x4 __attribute__((ext_vector_type(4)));
typedef __bf16 bf16x2 __attribute__((ext_vector_type(2)));
typedef __bf16 bf16x8 __attribute__((ext_vector_type(8)));

constexpr int D = 512, SK = 8192, DH = 128, FF = 1792, NQ = 4096;
constexpr int SC1 = 16, NT = 2;
constexpr int SPIN_CAP = 1 << 18;
constexpr int NFIN = 32;
constexpr float LOG2E = 1.4426950408889634f;

constexpr int WS_EPOCH = 0;
constexpr int WS_QP = 256;
constexpr int WS_PA = WS_QP + 512 * 256;
constexpr int WS_OP = WS_PA + 112 * 512;
constexpr int WS_S2 = WS_OP + 256 * 2048;
constexpr int WS_MP = WS_S2 + 256 * 1024;
constexpr int WS_END = WS_MP + 112 * 1024;
constexpr int WS_SPAN = 4 << 20;
constexpr int WS_STAMP = 8 << 20;

constexpr int F_Q = 0, F_HALF = 4, F_W2 = 6;
constexpr int L_FLAG = 0, L_CNT = 48, L_XN = 64, L_HALF = L_XN + 8192, L_Q = L_HALF + 1024, L_MW = L_Q + 1024, L_LW = L_MW + 256,
              L_HS = L_LW + 256, L_GP = L_HS + 256, GP_STRIDE = 272, L_FP = L_GP + 8 * GP_STRIDE, L_MF = L_FP + 1024,
              L_TOUCH = L_MF + 512, L_W2 = L_TOUCH + 256, L_OW = L_W2 + 16384, OW_STRIDE = 2064, L_END = L_OW + 16 * OW_STRIDE;

DI float bflo(u32 v) { return __builtin_bit_cast(float, v << 16); }
DI float bfhi(u32 v) { return __builtin_bit_cast(float, v & 0xffff0000u); }
DI float rbf(float f) { return (float)(__bf16)f; }
DI u32 pk(float a, float b) { return __builtin_bit_cast(u32, __builtin_convertvector((f32x2){a, b}, bf16x2)); }
DI u32 f2u(float f) { return __builtin_bit_cast(u32, f); }
DI float u2f(u32 u) { return __builtin_bit_cast(float, u); }
DI f32x4 mfma(u32x4 a, u32x4 b, f32x4 c) {
  return __builtin_amdgcn_mfma_f32_16x16x32_bf16(__builtin_bit_cast(bf16x8, a), __builtin_bit_cast(bf16x8, b), c, 0, 0, 0);
}
template <int CTRL> DI float dpp(float x) { return u2f(__builtin_amdgcn_mov_dpp(f2u(x), CTRL, 0xf, 0xf, true)); }
constexpr int XOR1 = 0xB1, XOR2 = 0x4E, ROR4 = 0x124, ROR8 = 0x128;
DI float half32(float a, float b) { auto r = __builtin_amdgcn_permlane32_swap(f2u(a), f2u(b), false, false); return u2f(r[0]) + u2f(r[1]); }
DI float half16(float a, float b) { auto r = __builtin_amdgcn_permlane16_swap(f2u(a), f2u(b), false, false); return u2f(r[0]) + u2f(r[1]); }
DI float max32(float a) { auto r = __builtin_amdgcn_permlane32_swap(f2u(a), f2u(a), false, false); return __builtin_amdgcn_fmed3f(u2f(r[0]), u2f(r[1]), __builtin_inff()); }
DI float max16(float a) { auto r = __builtin_amdgcn_permlane16_swap(f2u(a), f2u(a), false, false); return __builtin_amdgcn_fmed3f(u2f(r[0]), u2f(r[1]), __builtin_inff()); }
DI float half_sum(float v) {
  v += dpp<XOR1>(v); v += dpp<XOR2>(v); v += dpp<ROR4>(v); v += dpp<ROR8>(v);
  return half16(v, v);
}
DI u32 plo(u32 k1, u32 k0) { return __builtin_amdgcn_perm(k1, k0, 0x05040100u); }
DI u32 phi(u32 k1, u32 k0) { return __builtin_amdgcn_perm(k1, k0, 0x07060302u); }
DI u32x4 colfrag(const u32x4 (&r)[8], int d) {
  const int q = d >> 1;
  return (d & 1) ? (u32x4){phi(r[1][q], r[0][q]), phi(r[3][q], r[2][q]), phi(r[5][q], r[4][q]), phi(r[7][q], r[6][q])}
                 : (u32x4){plo(r[1][q], r[0][q]), plo(r[3][q], r[2][q]), plo(r[5][q], r[4][q]), plo(r[7][q], r[6][q])};
}
DI u32x4 sel4(bool c, u32x4 v) { return (u32x4){c ? v[0] : 0u, c ? v[1] : 0u, c ? v[2] : 0u, c ? v[3] : 0u}; }
template <typename T> DI T ldnt(const GAS T* p) { return __builtin_nontemporal_load(p); }
#define LAUX 16
#define SAUX 16
DI u32x4 ldws(__amdgpu_buffer_rsrc_t ws, int off) { return __builtin_amdgcn_raw_buffer_load_b128(ws, off, 0, LAUX); }
DI void stws(__amdgpu_buffer_rsrc_t ws, int off, u32x2 a, u32x2 b) { __builtin_amdgcn_raw_buffer_store_b128((u32x4){a[0], a[1], b[0], b[1]}, ws, off, 0, SAUX); }
DI void stws1(__amdgpu_buffer_rsrc_t ws, int off, u32x2 a) { __builtin_amdgcn_raw_buffer_store_b64(a, ws, off, 0, SAUX); }
DI u32x2 pack2(float a, float b, u32 tag8) {
  const u32 ar = f2u(a) + 0x800u;
  const u32 u = (f2u(b) & 0xf000u) | ((ar >> 4) & ~0xf000u);
  return (u32x2){phi(f2u(b), ar), __builtin_amdgcn_perm(tag8, u, 0x07060501u)};
}
DI float first2(u32 w0, u32 w1) { return u2f(__builtin_amdgcn_perm(w0, w1 << 4, 0x0504000cu)); }
DI float second2(u32 w0, u32 w1) { return u2f(__builtin_amdgcn_perm(w0, w1, 0x0706000cu)); }
DI bool tagged(u32x4 v, u32 tag8) { return ((v[1] ^ tag8) | (v[3] ^ tag8)) < 256u; }
#define PIN() do { asm volatile("" ::: "memory"); __builtin_amdgcn_sched_barrier(0); } while (0)
#define LDS_BARRIER() do { asm volatile("s_waitcnt lgkmcnt(0)\n\ts_barrier" ::: "memory"); __builtin_amdgcn_sched_barrier(0); } while (0)
#define RAW_BARRIER() do { __builtin_amdgcn_sched_barrier(0); asm volatile("s_barrier" ::: "memory"); __builtin_amdgcn_sched_barrier(0); } while (0)
DI void dma16(const u16* src, u32 dst) {
  u32 keep;
  asm volatile("s_mov_b32 %0, m0\n\ts_mov_b32 m0, %2\n\ts_nop 0\n\tglobal_load_lds_dwordx4 %1, off nt\n\ts_mov_b32 m0, %0"
               : "=&s"(keep) : "v"(src), "s"(dst) : "memory");
}
DI void touch(const void* src, u32 dst) {
  u32 keep;
  asm volatile("s_mov_b32 %0, m0\n\ts_mov_b32 m0, %2\n\ts_nop 0\n\tglobal_load_lds_dword %1, off\n\ts_mov_b32 m0, %0"
               : "=&s"(keep) : "v"(src), "s"(dst) : "memory");
}
DI void set_flag(LAS unsigned char* lds, int f) {
  asm volatile("s_waitcnt lgkmcnt(0)" ::: "memory");
  *(volatile LAS u32*)(lds + L_FLAG + f * 4) = 1u;
}
DI void wait_flags(LAS unsigned char* lds, int first, int mask, int lane) {
#pragma unroll 1
  for (int spin = 0; spin < (SPIN_CAP << 4); ++spin) {
    if (__all(*(volatile LAS u32*)(lds + L_FLAG + (first + (lane & mask)) * 4) != 0u)) break;
    __builtin_amdgcn_s_sleep(1);
  }
  PIN();
}
DI u32 read_tag(const unsigned char* ws) {
  u32 e;
  asm volatile("s_load_dword %0, %1, 0x0\n\ts_waitcnt lgkmcnt(0)" : "=s"(e) : "s"(ws) : "memory");
  return (e + 1u) << 8;
}
template <int LOW> DI u32x4 poll16(__amdgpu_buffer_rsrc_t ws, int off, u32 want) {
  u32x4 v;
  u64 keep;
  u32 left = SPIN_CAP, t;
  asm volatile("s_mov_b64 %[keep], exec\n\t"
               "s_branch 2f\n"
               "1:\n\t"
               "s_sub_u32 %[left], %[left], 1\n\t"
               "s_cbranch_scc1 3f\n"
               "2:\n\t"
               "buffer_load_dwordx4 v[100:103], %[off], %[ws], 0 offen sc1\n\t"
               "s_waitcnt vmcnt(0)\n\t"
               "v_bitop3_b32 %[t], v101, v103, %[want] bitop3:0x7e\n\t"
               "v_cmp_lt_u32 vcc, %[low], %[t]\n\t"
               "s_and_b64 exec, exec, vcc\n\t"
               "s_cbranch_scc1 1b\n"
               "3:\n\t"
               "s_mov_b64 exec, %[keep]"
               : "={v[100:103]}"(v), [keep] "=&s"(keep), [left] "+s"(left), [t] "=&v"(t)
               : [off] "v"(off), [ws] "s"(ws), [want] "s"(want), [low] "i"(LOW)
               : "vcc", "scc", "memory");
  return v;
}
DI void quad_sum4(float& a, float& b, float& c, float& d) {
  asm("s_nop 1\n\t"
      "v_add_f32_dpp %0, %0, %0 quad_perm:[1,0,3,2] row_mask:0xf bank_mask:0xf\n\t"
      "v_add_f32_dpp %1, %1, %1 quad_perm:[1,0,3,2] row_mask:0xf bank_mask:0xf\n\t"
      "v_add_f32_dpp %2, %2, %2 quad_perm:[1,0,3,2] row_mask:0xf bank_mask:0xf\n\t"
      "v_add_f32_dpp %3, %3, %3 quad_perm:[1,0,3,2] row_mask:0xf bank_mask:0xf\n\t"
      "v_add_f32_dpp %0, %0, %0 quad_perm:[2,3,0,1] row_mask:0xf bank_mask:0xf\n\t"
      "v_add_f32_dpp %1, %1, %1 quad_perm:[2,3,0,1] row_mask:0xf bank_mask:0xf\n\t"
      "v_add_f32_dpp %2, %2, %2 quad_perm:[2,3,0,1] row_mask:0xf bank_mask:0xf\n\t"
      "v_add_f32_dpp %3, %3, %3 quad_perm:[2,3,0,1] row_mask:0xf bank_mask:0xf"
      : "+v"(a), "+v"(b), "+v"(c), "+v"(d));
}
DI float sum_records(u32x4 v) {
  const float t = half16(half32(first2(v[0], v[1]), first2(v[2], v[3])), half32(second2(v[0], v[1]), second2(v[2], v[3])));
  return t + dpp<ROR8>(t);
}

DI void layernorm(u32 (&x)[8]) {
  float v[16], s = 0.f;
#pragma unroll
  for (int e = 0; e < 8; ++e) { v[2 * e] = bflo(x[e]); v[2 * e + 1] = bfhi(x[e]); s += v[2 * e] + v[2 * e + 1]; }
  const float mean = half_sum(s) * (1.f / 512.f);
  float q = 0.f;
#pragma unroll
  for (int e = 0; e < 16; ++e) { const float d = v[e] - mean; q = fmaf(d, d, q); }
  const float var = half_sum(q) * (1.f / 512.f);
  const float mb = rbf(mean), inv = __builtin_amdgcn_rcpf(rbf(__builtin_amdgcn_sqrtf(rbf(rbf(var) + 1e-5f))));
#pragma unroll
  for (int e = 0; e < 8; ++e) { const u32 d = pk(v[2 * e] - mb, v[2 * e + 1] - mb); x[e] = pk(bflo(d) * inv, bfhi(d) * inv); }
}

DI f32x2 tile_matvec(const u32x4 (&wv)[8], LAS unsigned char* xin, int g, int c) {
  const u32x4 xf = *(LAS u32x4*)(xin + ((c >> 2) & 1) * 1024 + ((c >> 3) * 32 + g * 8) * 2);
  f32x4 lo = {0.f, 0.f, 0.f, 0.f}, hi = lo;
#pragma unroll
  for (int d = 0; d < 4; ++d) {
    const u32x4 am = sel4((c & 3) == d, xf);
    lo = mfma(am, colfrag(wv, d), lo);
    hi = mfma(am, colfrag(wv, 4 + d), hi);
  }
  const bool valid = (g >> 1) == (c >> 3);
  float t[4];
#pragma unroll
  for (int e = 0; e < 4; ++e) { t[e] = half32(valid ? lo[e] : 0.f, valid ? hi[e] : 0.f); t[e] += dpp<ROR8>(t[e]); }
  return (c & 8) ? (f32x2){t[2], t[3]} : (f32x2){t[0], t[1]};
}

#define ST(i) do { if (!((STMASK >> (i)) & 1)) break; __builtin_amdgcn_sched_barrier(0); ts[i] = __builtin_amdgcn_s_memrealtime(); __builtin_amdgcn_s_waitcnt(0xC07F); __builtin_amdgcn_sched_barrier(0); } while (0)

__global__ void __launch_bounds__(1024, 4)
fused_decode(const u16* x_, const u16* k_, const u16* v_, const u16* wq_, const u16* wo_, const u16* w1_, const u16* b1_,
             const u16* w2_, const u16* b2_, u16* out_, unsigned char* ws_
             ) {
  __shared__ __attribute__((aligned(16))) unsigned char lds_raw[L_END];
  LAS unsigned char* lds = (LAS unsigned char*)lds_raw;
  u64 ts[12];
  for (int i = 0; i < 12; ++i) ts[i] = 0;
  ST(0);
  const u64 t_start = WO_MODE == 3 ? __builtin_amdgcn_s_memrealtime() : 0;
  asm volatile("" :: "s"(x_), "s"(k_), "s"(v_), "s"(wq_), "s"(wo_), "s"(w1_), "s"(b1_), "s"(w2_), "s"(b2_), "s"(out_), "s"(ws_));
  const int tid = threadIdx.x, lane = tid & 63, w = __builtin_amdgcn_readfirstlane(tid >> 6);
  const int g = lane >> 4, c = lane & 15;
  const int bid = blockIdx.x, kvh = bid & 7, j = bid >> 3, b = j >> 4, sp = j & 15, rb = j >> 2, cb = j & 3;
  const bool mlp1 = j < 14, mlp2 = j >= 14 && j < 28;
  const __amdgpu_buffer_rsrc_t ws = __builtin_amdgcn_make_buffer_rsrc(ws_, 0, WS_END, 0x00020000);
  u32 tag;
  const u32 hsh = ((u32)(bid * 16 + w) * 2654435761u) >> 20, hb = ((u32)bid * 2654435761u) >> 16;
  const int wu = (w + (ROT == 0 ? 0 : ROT == 1 || ROT == 2 || ROT == 3 ? j : (int)hb)) & 15;
  const int ro = ROT == 2 ? kvh : ROT == 3 || ROT == 4 ? (int)(hsh & 7) : 0;
  const size_t kvseq = (size_t)(b * 8 + kvh) * SK * DH;
  const __amdgpu_buffer_rsrc_t kr = __builtin_amdgcn_make_buffer_rsrc((void*)(k_ + kvseq), 0, SK * DH * 2, 0x00020000);
  const __amdgpu_buffer_rsrc_t vr = __builtin_amdgcn_make_buffer_rsrc((void*)(v_ + kvseq), 0, SK * DH * 2, 0x00020000);
  const int kvoff = (sp * 16 + wu) * 8192;
  const int klane = ((((c >> 1) ^ ro) & 1) * 4 + (c >> 2)) * 256 + (g * 2 + (c & 1)) * 16;
  u32x4 kf[8], vf[8];

  const int cq = j & 7, kq = j >> 3;
  const int mt = (mlp1 ? j : j - 14) * 8 + kvh;
  const int mhi = mt >> 2, mlo = mt & 3;
  if (w < 2 || (w < 4 && mlp1)) {
    __builtin_amdgcn_s_setprio(3);
    if (w == 0 && lane < 16) *(LAS u32*)(lds + L_FLAG + lane * 4) = 0u;
    const GAS u32x4* xp = (const GAS u32x4*)x_ + lane * 2;
    const u32x4 x0 = xp[0], x1 = xp[1];
    PIN();
    const int rs = w < 2 ? NQ / 8 : FF / 8, row0 = (w < 2 ? kq : mlo) * 128 + (w & 1) * 64;
    const u16* tile = w < 2 ? wq_ + kvh * 512 + cq * 64 : w1_ + mhi * 64;
    const GAS u32x4* wp = (const GAS u32x4*)tile + (row0 + (c >> 3) * 32 + g * 8) * rs + (c & 7);
    u32x4 wv[8];
#pragma unroll
    for (int i = 0; i < 8; ++i) wv[i] = ldnt(wp + i * rs);
    PIN();
    LDS_BARRIER();
    tag = read_tag(ws_);
    PIN();
    u32 xs[8] = {x0[0], x0[1], x0[2], x0[3], x1[0], x1[1], x1[2], x1[3]};
    layernorm(xs);
    if (w >= 2) layernorm(xs);
    LAS unsigned char* xl = lds + L_XN + w * 2048;
    *(LAS u32x4*)(xl + lane * 32) = (u32x4){xs[0], xs[1], xs[2], xs[3]};
    *(LAS u32x4*)(xl + lane * 32 + 16) = (u32x4){xs[4], xs[5], xs[6], xs[7]};
    f32x2 r = tile_matvec(wv, xl + row0 * 2, g, c);
    LAS f32x2* half = (LAS f32x2*)(lds + L_HALF + (w >> 1) * 512 + lane * 8);
    if (w & 1) {
      *half = r;
      set_flag(lds, F_HALF + (w >> 1));
    } else {
      wait_flags(lds, F_HALF + (w >> 1), 0, lane);
      r += *half;
      const int rec = w == 0 ? WS_QP + ((((g & 1) * 8 + kvh) * 8 + cq) * 4 + kq) * 256 : WS_PA + mt * 512 + (g & 1) * 256;
      stws1(ws, rec + ((c & 7) * 4 + (g >> 1) * 2 + (c >> 3)) * 8, pack2(r[0], r[1], tag));
    }
    ST(2);
    __builtin_amdgcn_s_setprio(PRIO_K);
  } else {
    if (w < 4 && mlp2) {
      const u16* p = w2_ + (mhi * 64 + (w & 1) * 32 + g * 8) * D + mlo * 128 + c * 8;
#pragma unroll
      for (int i = 0; i < 8; ++i) dma16(p + i * D, (u32)(size_t)(lds + L_W2 + (w & 1) * 8192 + i * 1024));
    }
    if (CODE_LINES && w == 4) {
      const unsigned char* pc;
      asm volatile("s_getpc_b64 %0" : "=s"(pc));
      if (lane < 4 && j * 4 + lane < CODE_LINES) touch(pc + (j * 4 + lane) * 128, (u32)(size_t)(lds + L_TOUCH));
    }
    RAW_BARRIER();
    if ((w >> 2) == 1) __builtin_amdgcn_s_setprio(2); else __builtin_amdgcn_s_setprio(PRIO_K);
  }
  PIN();

#pragma unroll
  for (int i = 0; i < 8; ++i) { kf[i] = BUF ? __builtin_amdgcn_raw_buffer_load_b128(kr, klane + (i & 1) * 128, kvoff + ((i >> 1) ^ (ro >> 1)) * 2048, NT) : ldnt((const GAS u32x4*)((const GAS char*)(k_ + kvseq) + kvoff + klane + (i & 1) * 128 + (i >> 1) * 2048)); __builtin_amdgcn_sched_barrier(0); }
  if ((w >> 2) != 1) __builtin_amdgcn_s_setprio(0);
  PIN();
#pragma unroll
  for (int i = 0; i < 8; ++i) { vf[i] = BUF ? __builtin_amdgcn_raw_buffer_load_b128(vr, lane * 16, kvoff + (i ^ ro) * 1024, NT) : ldnt((const GAS u32x4*)((const GAS char*)(v_ + kvseq) + kvoff + lane * 16 + i * 1024)); __builtin_amdgcn_sched_barrier(0); }
  PIN();
  ST(4);
  tag = read_tag(ws_);
  const int paoff = WS_PA + mhi * 2048 + lane * 16;
  u32x4 p0, p1;
  u32 b1v;
  if ((w >> 2) == 1) {
    const int u = w & 3;
    const int qoff = WS_QP + (b * 8 + kvh) * 8192 + u * 2048 + lane * 16;
    asm volatile("s_waitcnt vmcnt(0)" ::: "memory");
    if (Q_DELAY) __builtin_amdgcn_s_sleep(Q_DELAY);
    u32x4 q0, q1;
#pragma unroll 1
    for (int spin = 0; spin < SPIN_CAP; ++spin) {
      q0 = ldws(ws, qoff); q1 = ldws(ws, qoff + 1024);
      if (__all(tagged(q0, tag) && tagged(q1, tag))) break;
      __builtin_amdgcn_s_sleep(SLEEP_Q);
      asm volatile("" ::: "memory");
    }
    const float qa = half16(half32(first2(q0[0], q0[1]), first2(q1[0], q1[1])), half32(first2(q0[2], q0[3]), first2(q1[2], q1[3])));
    const float qb = half16(half32(second2(q0[0], q0[1]), second2(q1[0], q1[1])), half32(second2(q0[2], q0[3]), second2(q1[2], q1[3])));
    *(LAS u32*)(lds + L_Q + u * 256 + ((g >> 1) * 32 + c * 2 + (g & 1)) * 4) = pk(qa, qb);
    set_flag(lds, F_Q + u);
    ST(3);
    if (w == 7 && mlp2) {
      p0 = ldws(ws, paoff); p1 = ldws(ws, paoff + 1024);
      b1v = *(const GAS u32*)(b1_ + mhi * 64 + c * 4 + (g >> 1) * 2);
      PIN();
    }
    __builtin_amdgcn_s_setprio(0);
  }
  wait_flags(lds, F_Q, 3, lane);

  float sc[8];
  {
    const int x0 = (c >> 2) & 1;
    const u32x4 q0 = *(LAS u32x4*)(lds + L_Q + (c & 3) * 256 + (g * 2 + x0) * 16);
    const u32x4 q1 = *(LAS u32x4*)(lds + L_Q + (c & 3) * 256 + 128 + (g * 2 + x0) * 16);
#pragma unroll
    for (int o = 0; o < 4; ++o) {
      f32x4 s = mfma(kf[2 * o], q0, (f32x4){0.f, 0.f, 0.f, 0.f});
      s = mfma(kf[2 * o + 1], q1, s);
#pragma unroll
      for (int a = 0; a < 2; ++a) {
        float e1 = s[2 * a + 1];
        asm("" : "+v"(e1));
        const float t = x0 ? e1 : s[2 * a];
        sc[2 * o + a] = t + dpp<ROR4>(t);
      }
      const u32 d = pk(sc[2 * o], sc[2 * o + 1]), e = pk(bflo(d) * 0.08838834764831845f, bfhi(d) * 0.08838834764831845f);
      sc[2 * o] = bflo(e); sc[2 * o + 1] = bfhi(e);
    }
  }
  PIN();

    float m = fmaxf(fmaxf(fmaxf(sc[0], sc[1]), fmaxf(sc[2], sc[3])), fmaxf(fmaxf(sc[4], sc[5]), fmaxf(sc[6], sc[7])));
    m = max32(max16(m)) * LOG2E;
    float p[8], l = 0.f;
#pragma unroll
    for (int e = 0; e < 8; ++e) { p[e] = __builtin_amdgcn_exp2f(fmaf(sc[e], LOG2E, -m)); l += p[e]; }
    l = half16(l, l); l = half32(l, l);
    if (lane < 4) { *(LAS float*)(lds + L_MW + (lane * 16 + w) * 4) = m; *(LAS float*)(lds + L_LW + (lane * 16 + w) * 4) = l; }
    PIN();
    if ((w >> 1) == 1) set_flag(lds, F_W2 + (w & 1));
    if (lane == 0) __hip_atomic_fetch_add((LAS u32*)(lds + L_CNT), 1u, __ATOMIC_RELAXED, __HIP_MEMORY_SCOPE_WORKGROUP);
    const u32x4 pf = {pk(p[0], p[1]), pk(p[2], p[3]), pk(p[4], p[5]), pk(p[6], p[7])};
    u32x4 am[4];
#pragma unroll
    for (int u = 0; u < 4; ++u) am[u] = sel4((c >> 2) == u, pf);
    PIN();
    u32x4 ve[4], vo[4];
    u32 slo, shi;
    asm volatile("s_mov_b32 %0, 0x05040100\n\ts_mov_b32 %1, 0x07060302" : "=s"(slo), "=s"(shi) : "v"(am[0][0]), "v"(am[1][1]), "v"(am[2][2]), "v"(am[3][3]));
#pragma unroll
    for (int r = 0; r < 4; ++r) {
#pragma unroll
      for (int u = 0; u < 4; ++u) {
        ve[u][r] = __builtin_amdgcn_perm(vf[2 * r + 1][u], vf[2 * r][u], slo);
        vo[u][r] = __builtin_amdgcn_perm(vf[2 * r + 1][u], vf[2 * r][u], shi);
      }
      if (r < 3)
        asm volatile("" : "+s"(slo), "+s"(shi) : "v"(ve[0][r]), "v"(ve[1][r]), "v"(ve[2][r]), "v"(ve[3][r]), "v"(vo[0][r]), "v"(vo[1][r]), "v"(vo[2][r]), "v"(vo[3][r]));
    }
    f32x4 oe = {0.f, 0.f, 0.f, 0.f}, oo = oe;
#pragma unroll
    for (int u = 0; u < 4; ++u) { oe = mfma(am[u], ve[u], oe); oo = mfma(am[u], vo[u], oo); }
#pragma unroll
    for (int h = 0; h < 4; ++h) *(LAS f32x2*)(lds + L_OW + w * OW_STRIDE + h * 512 + c * 32 + g * 8) = (f32x2){oe[h], oo[h]};
    PIN();
    ST(5);

  const bool gath = (w & 4) == 0;
  const int v = (w & 3) + (w >> 3) * 4;
  auto wait_ml = [&]() __attribute__((always_inline)) {
#pragma unroll 1
    for (int spin = 0; spin < (SPIN_CAP << 4); ++spin) {
      if (__builtin_amdgcn_readfirstlane(*(volatile LAS u32*)(lds + L_CNT)) == 16u) break;
      __builtin_amdgcn_s_sleep(1);
    }
    PIN();
  };
  auto load_wo = [&](u32x4 (&wt)[8]) __attribute__((always_inline)) {
    const GAS u16* p = (const GAS u16*)wo_ + (size_t)(kvh * 512 + rb * 64 + g * 4) * D + cb * 128 + (w - 4) * 64 + c * 4;
#pragma unroll
    for (int i = 0; i < 8; ++i) {
      const u32x2 t0 = ldnt((const GAS u32x2*)(p + (size_t)(32 * (i >> 2) + (i & 3)) * D));
      const u32x2 t1 = ldnt((const GAS u32x2*)(p + (size_t)(32 * (i >> 2) + 16 + (i & 3)) * D));
      wt[i] = (u32x4){t0[0], t0[1], t1[0], t1[1]};
    }
    PIN();
  };
  const bool fin = FINLAST ? bid >= 256 - NFIN : bid < NFIN;
  const int cf = bid & 3, bf = (bid >> 2) & 1, seg = (bid >> 3) & 3;
  if (w >= 12) {
    LDS_BARRIER();
    ST(6);
    if (fin) {
      const int row = (w - 12) * 8 + (lane >> 3);
      const int moff = WS_MP + (cf * 28 + min(row, 27)) * 1024 + bf * 512 + seg * 128 + (lane & 7) * 16;
      u32x4 mv;
#pragma unroll 1
      for (int spin = 0; spin < SPIN_CAP; ++spin) {
        mv = ldws(ws, moff);
        if (__all(tagged(mv, tag))) break;
        __builtin_amdgcn_s_sleep(SLEEP_M);
        asm volatile("" ::: "memory");
      }
      const float t = sum_records(sel4(row < 28, mv));
      if ((lane & 8) == 0) *(LAS float*)(lds + L_MF + (w - 12) * 128 + ((lane & 7) * 4 + g) * 4) = t;
      LDS_BARRIER();
    }
  } else if (gath) {
    {
      const int f = v * 16 + (lane >> 2), p4 = lane & 3, hd = v >> 1;
      wait_ml();
      const f32x4 mv = *(LAS f32x4*)(lds + L_MW + hd * 64 + p4 * 16), lv = *(LAS f32x4*)(lds + L_LW + hd * 64 + p4 * 16);
      float M = fmaxf(fmaxf(mv[0], mv[1]), fmaxf(mv[2], mv[3]));
      M = fmaxf(M, dpp<XOR1>(M)); M = fmaxf(M, dpp<XOR2>(M));
      float L = 0.f, fw[4];
#pragma unroll
      for (int i = 0; i < 4; ++i) { fw[i] = __builtin_amdgcn_exp2f(mv[i] - M); L = fmaf(fw[i], lv[i], L); }
      L += dpp<XOR1>(L); L += dpp<XOR2>(L);
      const float inv = __builtin_amdgcn_rcpf(L);
#pragma unroll
      for (int i = 0; i < 4; ++i) fw[i] *= inv;
      const u32 lam = f2u(M + __builtin_amdgcn_logf(L)) >> ((lane >> 5) * 16);
      u32 t0 = tag | (lam & 255u), t1 = tag | ((lam >> 8) & 255u);
      int ooff = WS_OP + ((kvh * 2 + b) * 16 + sp) * 2048 + f * 16;
      int laddr = L_OW + p4 * 4 * OW_STRIDE + f * 16;
      asm volatile("" : "+v"(fw[0]), "+v"(fw[1]), "+v"(fw[2]), "+v"(fw[3]), "+v"(t0), "+v"(t1), "+v"(ooff), "+v"(laddr));
      ST(3);
      LDS_BARRIER();
      ST(6);
      f32x4 s = fw[0] * *(LAS f32x4*)(lds + laddr);
#pragma unroll
      for (int i = 1; i < 4; ++i) s += fw[i] * *(LAS f32x4*)(lds + laddr + i * OW_STRIDE);
      float s0 = s[0], s1 = s[1], s2 = s[2], s3 = s[3];
      quad_sum4(s0, s1, s2, s3);
      if (p4 == 0) __builtin_amdgcn_raw_buffer_store_b128((u32x4){pk(s0, s1), t0, pk(s2, s3), t1}, ws, ooff, 0, SAUX);
    }
    PIN();
    ST(7);
    {
      u32 gaddr2 = (u32)(size_t)(lds + L_GP + v * GP_STRIDE + 256), gaddr = gaddr2 - 256 + (c * 4 + g) * 4;
      u32 lsel = (c & 8) ? 0x05040100u : 0x01000504u;
      asm volatile("" : "+v"(gaddr), "+v"(gaddr2), "+v"(lsel));
      if (SLEEP_O) __builtin_amdgcn_s_sleep(SLEEP_O);
      const u32x4 ov = poll16<255>(ws, WS_OP + (kvh * 32 + v * 4 + g) * 2048 + rb * 256 + c * 16, tag);
      ST(8);
      u32 a, lam, m1, x0, x1, x2, x3;
      f32x2 gl;
      asm volatile("v_perm_b32 %[a], %[t1], %[t0], %[sel]\n\t"
                   "v_lshlrev_b32 %[x0], 16, %[d0]\n\t"
                   "v_lshlrev_b32 %[x2], 16, %[d1]\n\t"
                   "v_mov_b32_dpp %[m1], %[a] row_ror:8 row_mask:0xf bank_mask:0xf\n\t"
                   "v_and_b32 %[x1], 0xffff0000, %[d0]\n\t"
                   "v_perm_b32 %[lam], %[a], %[m1], %[lsel]\n\t"
                   "v_mov_b32 %[m1], %[lam]\n\t"
                   "v_mov_b32 %[a], %[lam]\n\t"
                   "v_and_b32 %[x3], 0xffff0000, %[d1]\n\t"
                   "s_nop 0\n\t"
                   "v_permlane16_swap_b32 %[m1], %[a]\n\t"
                   "v_max_f32 %[m1], %[m1], %[a]\n\t"
                   "v_mov_b32 %[a], %[m1]\n\t"
                   "s_nop 1\n\t"
                   "v_permlane32_swap_b32 %[m1], %[a]\n\t"
                   "v_max_f32 v104, %[m1], %[a]\n\t"
                   "v_sub_f32 %[lam], %[lam], v104\n\t"
                   "v_exp_f32 %[lam], %[lam]\n\t"
                   "s_nop 0\n\t"
                   "v_mul_f32 %[x0], %[lam], %[x0]\n\t"
                   "v_mul_f32 %[x2], %[lam], %[x2]\n\t"
                   "v_mul_f32 %[x1], %[lam], %[x1]\n\t"
                   "v_mul_f32 %[x3], %[lam], %[x3]\n\t"
                   "v_mov_b32 %[a], %[lam]\n\t"
                   "v_permlane32_swap_b32 %[x0], %[x2]\n\t"
                   "v_permlane32_swap_b32 %[x1], %[x3]\n\t"
                   "v_permlane16_swap_b32 %[lam], %[a]\n\t"
                   "v_add_f32 %[x0], %[x0], %[x2]\n\t"
                   "v_add_f32 %[x1], %[x1], %[x3]\n\t"
                   "v_add_f32 %[lam], %[lam], %[a]\n\t"
                   "v_mov_b32 %[a], %[lam]\n\t"
                   "v_permlane16_swap_b32 %[x0], %[x1]\n\t"
                   "v_add_f32 %[x0], %[x0], %[x1]\n\t"
                   "v_permlane32_swap_b32 %[lam], %[a]\n\t"
                   "ds_write_b32 %[ga], %[x0]\n\t"
                   "v_add_f32 v105, %[lam], %[a]\n\t"
                   "ds_write_b64 %[gb], v[104:105]"
                   : [a] "=&v"(a), [lam] "=&v"(lam), [m1] "=&v"(m1), [x0] "=&v"(x0), [x1] "=&v"(x1), [x2] "=&v"(x2), [x3] "=&v"(x3), "=&{v[104:105]}"(gl)
                   : [d0] "v"(ov[0]), [t0] "v"(ov[1]), [d1] "v"(ov[2]), [t1] "v"(ov[3]), [sel] "s"(0x04000400u), [lsel] "v"(lsel), [ga] "v"(gaddr), [gb] "v"(gaddr2)
                   : "memory");
    }
    LDS_BARRIER();
    ST(9);
    if (fin) {
      int faddr = L_FP + ((lane & 7) * 4 + g) * 32 + v * 4;
      asm volatile("" : "+v"(faddr));
      if (SLEEP_S) __builtin_amdgcn_s_sleep(SLEEP_S);
      const u32x4 sv = poll16<255>(ws, WS_S2 + (cf * 64 + v * 8 + (lane >> 3)) * 1024 + bf * 512 + seg * 128 + (lane & 7) * 16, tag);
      ST(10);
      *(LAS float*)(lds + faddr) = sum_records(sv);
      LDS_BARRIER();
    }
  } else if (w < 6) {
    u32x4 wt[8], bw[8];
    if (WO_MODE == 3) {
      wait_ml();
      const bool now = __builtin_amdgcn_s_memrealtime() >= t_start + WO_AT;
      if (!now) {
        LDS_BARRIER();
        ST(6);
        while (__builtin_amdgcn_s_memrealtime() < t_start + WO_AT) __builtin_amdgcn_s_sleep(1);
      }
      load_wo(wt);
      if (now) { LDS_BARRIER(); ST(6); }
    } else {
    if (WO_MODE == 1) wait_ml();
    if (WO_MODE < 2) load_wo(wt);
    LDS_BARRIER();
    ST(6);
    if (WO_MODE == 2) { if (WO_SLEEP) __builtin_amdgcn_s_sleep(WO_SLEEP); load_wo(wt); }
    }
#pragma unroll
    for (int i = 0; i < 8; ++i) bw[i] = colfrag(wt, i);
    int gp = L_GP + (c & 7) * GP_STRIDE, go = gp + ((c >> 3) * 16 + g * 4) * 4;
    int soff = WS_S2 + (cb * 64 + kvh * 8 + rb) * 1024 + (g & 1) * 512 + ((w - 4) * 32 + c * 2 + (g >> 1)) * 8;
    asm volatile("" : "+v"(gp), "+v"(go), "+v"(soff) : "v"(bw[0]), "v"(bw[1]), "v"(bw[2]), "v"(bw[3]), "v"(bw[4]), "v"(bw[5]), "v"(bw[6]), "v"(bw[7]));
    ST(1);
    LDS_BARRIER();
    ST(9);
    f32x2 gl = *(LAS f32x2*)(lds + gp + 256);
    u32x4 of = *(LAS u32x4*)(lds + go), o1 = *(LAS u32x4*)(lds + go + 128);
#define QP1(r) "v_add_f32_dpp " r ", " r ", " r " quad_perm:[1,0,3,2] row_mask:0xf bank_mask:0xf\n\t"
#define QP2(r) "v_add_f32_dpp " r ", " r ", " r " quad_perm:[2,3,0,1] row_mask:0xf bank_mask:0xf\n\t"
#define PKM(r) "v_pk_mul_f32 " r ", v[118:119], " r " op_sel_hi:[0,1]\n\t"
    asm volatile("v_max_f32_dpp v118, v116, v116 quad_perm:[1,0,3,2] row_mask:0xf bank_mask:0xf\n\t"
                 "s_nop 1\n\t"
                 "v_max_f32_dpp v118, v118, v118 quad_perm:[2,3,0,1] row_mask:0xf bank_mask:0xf\n\t"
                 "v_sub_f32 v118, v116, v118\n\t"
                 "v_exp_f32 v118, v118\n\t"
                 "s_nop 0\n\t"
                 "v_mul_f32 v117, v118, v117\n\t"
                 PKM("v[108:109]") PKM("v[110:111]") PKM("v[112:113]") PKM("v[114:115]")
                 QP1("v117") QP1("v108") QP1("v109") QP1("v110") QP1("v111") QP1("v112") QP1("v113") QP1("v114") QP1("v115")
                 QP2("v117") QP2("v108") QP2("v109")
                 "v_rcp_f32 v118, v117\n\t"
                 QP2("v110") QP2("v111") QP2("v112") QP2("v113") QP2("v114") QP2("v115")
                 PKM("v[108:109]") PKM("v[110:111]") PKM("v[112:113]") PKM("v[114:115]")
                 "v_cvt_pk_bf16_f32 v108, v108, v109\n\t"
                 "v_cvt_pk_bf16_f32 v109, v110, v111\n\t"
                 "v_cvt_pk_bf16_f32 v110, v112, v113\n\t"
                 "v_cvt_pk_bf16_f32 v111, v114, v115\n\t"
                 "s_nop 1"
                 : "+{v[108:111]}"(of), "+{v[112:115]}"(o1), "+{v[116:117]}"(gl)
                 :
                 : "v118", "v119");
    f32x4 acc[8];
#pragma unroll
    for (int i = 0; i < 8; ++i) { const int k = (i & 3) * 2 + (i >> 2); acc[k] = mfma(of, bw[k], (f32x4){0.f, 0.f, 0.f, 0.f}); }
    __builtin_amdgcn_sched_barrier(0);
    float r[4];
#pragma unroll
    for (int d = 0; d < 4; ++d) r[d] = (g >> 1) ? acc[4 + d][0] : acc[d][0];
    stws1(ws, soff, pack2(half32(r[0], r[2]), half32(r[1], r[3]), tag));
    ST(10);
  } else if (w == 7 && mlp2) {
#pragma unroll 1
    for (int spin = 0; spin < SPIN_CAP; ++spin) {
      if (__all(tagged(p0, tag) && tagged(p1, tag))) break;
      __builtin_amdgcn_s_sleep(SLEEP_Q);
      asm volatile("" ::: "memory");
      p0 = ldws(ws, paoff); p1 = ldws(ws, paoff + 1024);
    }
    const float t2[2] = {half32(first2(p0[0], p0[1]) + first2(p1[0], p1[1]), first2(p0[2], p0[3]) + first2(p1[2], p1[3])),
                         half32(second2(p0[0], p0[1]) + second2(p1[0], p1[1]), second2(p0[2], p0[3]) + second2(p1[2], p1[3]))};
    float hv[2];
#pragma unroll
    for (int e = 0; e < 2; ++e) {
      const float t = rbf(rbf(t2[e]) + (e ? bfhi(b1v) : bflo(b1v)));
      const float uu = 0.7978845608028654f * (t + 0.044715f * t * t * t);
      hv[e] = t / (1.f + __builtin_amdgcn_exp2f(-2.f * LOG2E * uu));
    }
    *(LAS u32*)(lds + L_HS + (g & 1) * 128 + (c * 2 + (g >> 1)) * 4) = pk(hv[0], hv[1]);
    wait_flags(lds, F_W2, 1, lane);
    f32x4 r = {0.f, 0.f, 0.f, 0.f};
#pragma unroll
    for (int s = 0; s < 2; ++s) {
      const u32x4 ha = *(LAS u32x4*)(lds + L_HS + ((c >> 2) & 1) * 128 + s * 64 + g * 16);
      u32x4 w2t[8];
#pragma unroll
      for (int i = 0; i < 8; ++i) w2t[i] = *(LAS u32x4*)(lds + L_W2 + s * 8192 + i * 1024 + lane * 16);
#pragma unroll
      for (int d = 0; d < 8; ++d) r = mfma(sel4((c >> 3) == (d >> 2) && (c & 3) == (d & 3), ha), colfrag(w2t, d), r);
    }
    u32x2 m0 = pack2(r[0], r[1], tag), m1 = pack2(r[2], r[3], tag);
    asm volatile("" : "+v"(m0), "+v"(m1));
    ST(2);
    if (MP_LATE) LDS_BARRIER();
    stws(ws, WS_MP + (mlo * 28 + mhi) * 1024 + (g & 1) * 512 + (c * 2 + (g >> 1)) * 16, m0, m1);
    if (!MP_LATE) LDS_BARRIER();
    ST(6);
  } else if (w == 6 && fin) {
    const int col = cf * 128 + seg * 32 + (lane & 31);
    const u32 xv = ((const GAS u16*)x_)[bf * D + col], b2v = ((const GAS u16*)b2_)[col];
    LDS_BARRIER();
    ST(6);
    LDS_BARRIER();
    LAS float* mf = (LAS float*)(lds + L_MF + (lane >> 5) * 256 + (lane & 31) * 4);
    float t = mf[0] + mf[32];
    t = half32(t, t);
    t = rbf(rbf(t) + bflo(b2v));
    float gate = rbf(t / (1.f + __builtin_amdgcn_exp2f(-LOG2E * t))), xf = bflo(xv);
    int faddr = L_FP + (lane & 31) * 32 + (lane >> 5) * 16;
    GAS u16* op = (GAS u16*)out_ + bf * D + col;
    asm volatile("" : "+v"(gate), "+v"(xf), "+v"(faddr), "+v"(op));
    LDS_BARRIER();
    ST(10);
    const f32x4 fp = *(LAS f32x4*)(lds + faddr);
    float s = (fp[0] + fp[1]) + (fp[2] + fp[3]);
    s = half32(s, s);
    const float res = xf + rbf(rbf(s) * gate);
    if (lane < 32) __hip_atomic_store(op, (u16)pk(res, res), RLX_AGENT);
    if ((bid & 31) == 0 && lane == 0) __hip_atomic_store((GAS u32*)(ws_ + WS_EPOCH), tag >> 8, RLX_AGENT);
  } else {
    LDS_BARRIER();
    ST(6);
  }
  ST(11);
  {
    GAS u64* st = (GAS u64*)(ws_ + (8 << 20));
    if (lane == 0) {
      __hip_atomic_store(st + (bid * 16 + w) * 2, ts[PROBE], RLX_AGENT);
      __hip_atomic_store(st + (bid * 16 + w) * 2 + 1, ts[PROBE_B], RLX_AGENT);
    }
    if (bid == 0 && w == 0) {
      while (__builtin_amdgcn_s_memrealtime() < ts[0] + 3000) __builtin_amdgcn_s_sleep(1);
      u64 ma = 0, mb = PROBE_B == 0 ? ~0ull : 0ull;
      for (int i = lane; i < 4096; i += 64) {
        const u64 x = __hip_atomic_load(st + i * 2, RLX_AGENT), y = __hip_atomic_load(st + i * 2 + 1, RLX_AGENT);
        ma = x > ma ? x : ma;
        if (PROBE_B == 0) mb = y < mb ? y : mb; else mb = y > mb ? y : mb;
      }
      for (int o = 32; o; o >>= 1) {
        const u64 x = __shfl_xor(ma, o), y = __shfl_xor(mb, o);
        ma = x > ma ? x : ma;
        if (PROBE_B == 0) mb = y < mb ? y : mb; else mb = y > mb ? y : mb;
      }
      const u64 until = ts[0] + 5000 + 10 * (ma - mb);
      while (__builtin_amdgcn_s_memrealtime() < until && __builtin_amdgcn_s_memrealtime() < ts[0] + 40000) __builtin_amdgcn_s_sleep(1);
    }
  }
}

extern "C" void kernel_launch(void* const* d_in, const int* in_sizes, int n_in, void* d_out, int out_size, void* d_ws, size_t ws_size,
                              hipStream_t stream) {
  unsigned char* ws = (unsigned char*)d_ws + (WS_AT_END ? ((ws_size - WS_SPAN) & ~(size_t)4095) : 0);
  fused_decode<<<256, 1024, 0, stream>>>((const u16*)d_in[0], (const u16*)d_in[1], (const u16*)d_in[2], (const u16*)d_in[3],
                                         (const u16*)d_in[4], (const u16*)d_in[6], (const u16*)d_in[7], (const u16*)d_in[8],
                                         (const u16*)d_in[9], (u16*)d_out, ws
                                         );
}
```

```cpp
#define STAMP 1
#define PROBE 8
#define PROBE_B 6
#define STMASK (1 | (1 << 8) | (1 << 6))
#define FINLAST 0
#define WO_MODE 0
#include <hip/hip_runtime.h>
#include <stdint.h>

#define WS_AT_END 0
#define SLEEP_Q 8
#define SLEEP_O 8
#define SLEEP_S 8
#define PRIO_K 1
#define ROT 1
#define CODE_LINES 0
#define RND 1
#define BUF 1
#define Q_DELAY 32
#define MP_LATE 0
#define SLEEP_M 4
#define WO_AT 1150
#define WO_SLEEP 0
#define ABL 0

#define GAS __attribute__((address_space(1)))
#define LAS __attribute__((address_space(3)))
#define DI __device__ __forceinline__
#define RLX_AGENT __ATOMIC_RELAXED, __HIP_MEMORY_SCOPE_AGENT
typedef unsigned short u16;
typedef unsigned u32;
typedef unsigned long long u64;
typedef u32 u32x2 __attribute__((ext_vector_type(2)));
typedef u32 u32x4 __attribute__((ext_vector_type(4)));
typedef float f32x2 __attribute__((ext_vector_type(2)));
typedef float f32x4 __attribute__((ext_vector_type(4)));
typedef __bf16 bf16x2 __attribute__((ext_vector_type(2)));
typedef __bf16 bf16x8 __attribute__((ext_vector_type(8)));

constexpr int D = 512, SK = 8192, DH = 128, FF = 1792, NQ = 4096;
constexpr int SC1 = 16, NT = 2;
constexpr int SPIN_CAP = 1 << 18;
constexpr int NFIN = 32;
constexpr float LOG2E = 1.4426950408889634f;

constexpr int WS_EPOCH = 0;
constexpr int WS_QP = 256;
constexpr int WS_PA = WS_QP + 512 * 256;
constexpr int WS_OP = WS_PA + 112 * 512;
constexpr int WS_S2 = WS_OP + 256 * 2048;
constexpr int WS_MP = WS_S2 + 256 * 1024;
constexpr int WS_END = WS_MP + 112 * 1024;
constexpr int WS_SPAN = 4 << 20;
constexpr int WS_STAMP = 8 << 20;

constexpr int F_Q = 0, F_HALF = 4, F_W2 = 6;
constexpr int L_FLAG = 0, L_CNT = 48, L_XN = 64, L_HALF = L_XN + 8192, L_Q = L_HALF + 1024, L_MW = L_Q + 1024, L_LW = L_MW + 256,
              L_HS = L_LW + 256, L_GP = L_HS + 256, GP_STRIDE = 272, L_FP = L_GP + 8 * GP_STRIDE, L_MF = L_FP + 1024,
              L_TOUCH = L_MF + 512, L_W2 = L_TOUCH + 256, L_OW = L_W2 + 16384, OW_STRIDE = 2064, L_END = L_OW + 16 * OW_STRIDE;

DI float bflo(u32 v) { return __builtin_bit_cast(float, v << 16); }
DI float bfhi(u32 v) { return __builtin_bit_cast(float, v & 0xffff0000u); }
DI float rbf(float f) { return (float)(__bf16)f; }
DI u32 pk(float a, float b) { return __builtin_bit_cast(u32, __builtin_convertvector((f32x2){a, b}, bf16x2)); }
DI u32 f2u(float f) { return __builtin_bit_cast(u32, f); }
DI float u2f(u32 u) { return __builtin_bit_cast(float, u); }
DI f32x4 mfma(u32x4 a, u32x4 b, f32x4 c) {
  return __builtin_amdgcn_mfma_f32_16x16x32_bf16(__builtin_bit_cast(bf16x8, a), __builtin_bit_cast(bf16x8, b), c, 0, 0, 0);
}
template <int CTRL> DI float dpp(float x) { return u2f(__builtin_amdgcn_mov_dpp(f2u(x), CTRL, 0xf, 0xf, true)); }
constexpr int XOR1 = 0xB1, XOR2 = 0x4E, ROR4 = 0x124, ROR8 = 0x128;
DI float half32(float a, float b) { auto r = __builtin_amdgcn_permlane32_swap(f2u(a), f2u(b), false, false); return u2f(r[0]) + u2f(r[1]); }
DI float half16(float a, float b) { auto r = __builtin_amdgcn_permlane16_swap(f2u(a), f2u(b), false, false); return u2f(r[0]) + u2f(r[1]); }
DI float max32(float a) { auto r = __builtin_amdgcn_permlane32_swap(f2u(a), f2u(a), false, false); return __builtin_amdgcn_fmed3f(u2f(r[0]), u2f(r[1]), __builtin_inff()); }
DI float max16(float a) { auto r = __builtin_amdgcn_permlane16_swap(f2u(a), f2u(a), false, false); return __builtin_amdgcn_fmed3f(u2f(r[0]), u2f(r[1]), __builtin_inff()); }
DI float half_sum(float v) {
  v += dpp<XOR1>(v); v += dpp<XOR2>(v); v += dpp<ROR4>(v); v += dpp<ROR8>(v);
  return half16(v, v);
}
DI u32 plo(u32 k1, u32 k0) { return __builtin_amdgcn_perm(k1, k0, 0x05040100u); }
DI u32 phi(u32 k1, u32 k0) { return __builtin_amdgcn_perm(k1, k0, 0x07060302u); }
DI u32x4 colfrag(const u32x4 (&r)[8], int d) {
  const int q = d >> 1;
  return (d & 1) ? (u32x4){phi(r[1][q], r[0][q]), phi(r[3][q], r[2][q]), phi(r[5][q], r[4][q]), phi(r[7][q], r[6][q])}
                 : (u32x4){plo(r[1][q], r[0][q]), plo(r[3][q], r[2][q]), plo(r[5][q], r[4][q]), plo(r[7][q], r[6][q])};
}
DI u32x4 sel4(bool c, u32x4 v) { return (u32x4){c ? v[0] : 0u, c ? v[1] : 0u, c ? v[2] : 0u, c ? v[3] : 0u}; }
template <typename T> DI T ldnt(const GAS T* p) { return __builtin_nontemporal_load(p); }
#define LAUX 16
#define SAUX 16
DI u32x4 ldws(__amdgpu_buffer_rsrc_t ws, int off) { return __builtin_amdgcn_raw_buffer_load_b128(ws, off, 0, LAUX); }
DI void stws(__amdgpu_buffer_rsrc_t ws, int off, u32x2 a, u32x2 b) { __builtin_amdgcn_raw_buffer_store_b128((u32x4){a[0], a[1], b[0], b[1]}, ws, off, 0, SAUX); }
DI void stws1(__amdgpu_buffer_rsrc_t ws, int off, u32x2 a) { __builtin_amdgcn_raw_buffer_store_b64(a, ws, off, 0, SAUX); }
DI u32x2 pack2(float a, float b, u32 tag8) {
  const u32 ar = f2u(a) + 0x800u;
  const u32 u = (f2u(b) & 0xf000u) | ((ar >> 4) & ~0xf000u);
  return (u32x2){phi(f2u(b), ar), __builtin_amdgcn_perm(tag8, u, 0x07060501u)};
}
DI float first2(u32 w0, u32 w1) { return u2f(__builtin_amdgcn_perm(w0, w1 << 4, 0x0504000cu)); }
DI float second2(u32 w0, u32 w1) { return u2f(__builtin_amdgcn_perm(w0, w1, 0x0706000cu)); }
DI bool tagged(u32x4 v, u32 tag8) { return ((v[1] ^ tag8) | (v[3] ^ tag8)) < 256u; }
#define PIN() do { asm volatile("" ::: "memory"); __builtin_amdgcn_sched_barrier(0); } while (0)
#define LDS_BARRIER() do { asm volatile("s_waitcnt lgkmcnt(0)\n\ts_barrier" ::: "memory"); __builtin_amdgcn_sched_barrier(0); } while (0)
#define RAW_BARRIER() do { __builtin_amdgcn_sched_barrier(0); asm volatile("s_barrier" ::: "memory"); __builtin_amdgcn_sched_barrier(0); } while (0)
DI void dma16(const u16* src, u32 dst) {
  u32 keep;
  asm volatile("s_mov_b32 %0, m0\n\ts_mov_b32 m0, %2\n\ts_nop 0\n\tglobal_load_lds_dwordx4 %1, off nt\n\ts_mov_b32 m0, %0"
               : "=&s"(keep) : "v"(src), "s"(dst) : "memory");
}
DI void touch(const void* src, u32 dst) {
  u32 keep;
  asm volatile("s_mov_b32 %0, m0\n\ts_mov_b32 m0, %2\n\ts_nop 0\n\tglobal_load_lds_dword %1, off\n\ts_mov_b32 m0, %0"
               : "=&s"(keep) : "v"(src), "s"(dst) : "memory");
}
DI void set_flag(LAS unsigned char* lds, int f) {
  asm volatile("s_waitcnt lgkmcnt(0)" ::: "memory");
  *(volatile LAS u32*)(lds + L_FLAG + f * 4) = 1u;
}
DI void wait_flags(LAS unsigned char* lds, int first, int mask, int lane) {
#pragma unroll 1
  for (int spin = 0; spin < (SPIN_CAP << 4); ++spin) {
    if (__all(*(volatile LAS u32*)(lds + L_FLAG + (first + (lane & mask)) * 4) != 0u)) break;
    __builtin_amdgcn_s_sleep(1);
  }
  PIN();
}
DI u32 read_tag(const unsigned char* ws) {
  u32 e;
  asm volatile("s_load_dword %0, %1, 0x0\n\ts_waitcnt lgkmcnt(0)" : "=s"(e) : "s"(ws) : "memory");
  return (e + 1u) << 8;
}
template <int LOW> DI u32x4 poll16(__amdgpu_buffer_rsrc_t ws, int off, u32 want) {
  u32x4 v;
  u64 keep;
  u32 left = SPIN_CAP, t;
  asm volatile("s_mov_b64 %[keep], exec\n\t"
               "s_branch 2f\n"
               "1:\n\t"
               "s_sub_u32 %[left], %[left], 1\n\t"
               "s_cbranch_scc1 3f\n"
               "2:\n\t"
               "buffer_load_dwordx4 v[100:103], %[off], %[ws], 0 offen sc1\n\t"
               "s_waitcnt vmcnt(0)\n\t"
               "v_bitop3_b32 %[t], v101, v103, %[want] bitop3:0x7e\n\t"
               "v_cmp_lt_u32 vcc, %[low], %[t]\n\t"
               "s_and_b64 exec, exec, vcc\n\t"
               "s_cbranch_scc1 1b\n"
               "3:\n\t"
               "s_mov_b64 exec, %[keep]"
               : "={v[100:103]}"(v), [keep] "=&s"(keep), [left] "+s"(left), [t] "=&v"(t)
               : [off] "v"(off), [ws] "s"(ws), [want] "s"(want), [low] "i"(LOW)
               : "vcc", "scc", "memory");
  return v;
}
DI void quad_sum4(float& a, float& b, float& c, float& d) {
  asm("s_nop 1\n\t"
      "v_add_f32_dpp %0, %0, %0 quad_perm:[1,0,3,2] row_mask:0xf bank_mask:0xf\n\t"
      "v_add_f32_dpp %1, %1, %1 quad_perm:[1,0,3,2] row_mask:0xf bank_mask:0xf\n\t"
      "v_add_f32_dpp %2, %2, %2 quad_perm:[1,0,3,2] row_mask:0xf bank_mask:0xf\n\t"
      "v_add_f32_dpp %3, %3, %3 quad_perm:[1,0,3,2] row_mask:0xf bank_mask:0xf\n\t"
      "v_add_f32_dpp %0, %0, %0 quad_perm:[2,3,0,1] row_mask:0xf bank_mask:0xf\n\t"
      "v_add_f32_dpp %1, %1, %1 quad_perm:[2,3,0,1] row_mask:0xf bank_mask:0xf\n\t"
      "v_add_f32_dpp %2, %2, %2 quad_perm:[2,3,0,1] row_mask:0xf bank_mask:0xf\n\t"
      "v_add_f32_dpp %3, %3, %3 quad_perm:[2,3,0,1] row_mask:0xf bank_mask:0xf"
      : "+v"(a), "+v"(b), "+v"(c), "+v"(d));
}
DI float sum_records(u32x4 v) {
  const float t = half16(half32(first2(v[0], v[1]), first2(v[2], v[3])), half32(second2(v[0], v[1]), second2(v[2], v[3])));
  return t + dpp<ROR8>(t);
}

DI void layernorm(u32 (&x)[8]) {
  float v[16], s = 0.f;
#pragma unroll
  for (int e = 0; e < 8; ++e) { v[2 * e] = bflo(x[e]); v[2 * e + 1] = bfhi(x[e]); s += v[2 * e] + v[2 * e + 1]; }
  const float mean = half_sum(s) * (1.f / 512.f);
  float q = 0.f;
#pragma unroll
  for (int e = 0; e < 16; ++e) { const float d = v[e] - mean; q = fmaf(d, d, q); }
  const float var = half_sum(q) * (1.f / 512.f);
  const float mb = rbf(mean), inv = __builtin_amdgcn_rcpf(rbf(__builtin_amdgcn_sqrtf(rbf(rbf(var) + 1e-5f))));
#pragma unroll
  for (int e = 0; e < 8; ++e) { const u32 d = pk(v[2 * e] - mb, v[2 * e + 1] - mb); x[e] = pk(bflo(d) * inv, bfhi(d) * inv); }
}

DI f32x2 tile_matvec(const u32x4 (&wv)[8], LAS unsigned char* xin, int g, int c) {
  const u32x4 xf = *(LAS u32x4*)(xin + ((c >> 2) & 1) * 1024 + ((c >> 3) * 32 + g * 8) * 2);
  f32x4 lo = {0.f, 0.f, 0.f, 0.f}, hi = lo;
#pragma unroll
  for (int d = 0; d < 4; ++d) {
    const u32x4 am = sel4((c & 3) == d, xf);
    lo = mfma(am, colfrag(wv, d), lo);
    hi = mfma(am, colfrag(wv, 4 + d), hi);
  }
  const bool valid = (g >> 1) == (c >> 3);
  float t[4];
#pragma unroll
  for (int e = 0; e < 4; ++e) { t[e] = half32(valid ? lo[e] : 0.f, valid ? hi[e] : 0.f); t[e] += dpp<ROR8>(t[e]); }
  return (c & 8) ? (f32x2){t[2], t[3]} : (f32x2){t[0], t[1]};
}

#define ST(i) do { if (!((STMASK >> (i)) & 1)) break; __builtin_amdgcn_sched_barrier(0); ts[i] = __builtin_amdgcn_s_memrealtime(); __builtin_amdgcn_s_waitcnt(0xC07F); __builtin_amdgcn_sched_barrier(0); } while (0)

__global__ void __launch_bounds__(1024, 4)
fused_decode(const u16* x_, const u16* k_, const u16* v_, const u16* wq_, const u16* wo_, const u16* w1_, const u16* b1_,
             const u16* w2_, const u16* b2_, u16* out_, unsigned char* ws_
             ) {
  __shared__ __attribute__((aligned(16))) unsigned char lds_raw[L_END];
  LAS unsigned char* lds = (LAS unsigned char*)lds_raw;
  u64 ts[12];
  for (int i = 0; i < 12; ++i) ts[i] = 0;
  ST(0);
  const u64 t_start = WO_MODE == 3 ? __builtin_amdgcn_s_memrealtime() : 0;
  asm volatile("" :: "s"(x_), "s"(k_), "s"(v_), "s"(wq_), "s"(wo_), "s"(w1_), "s"(b1_), "s"(w2_), "s"(b2_), "s"(out_), "s"(ws_));
  const int tid = threadIdx.x, lane = tid & 63, w = __builtin_amdgcn_readfirstlane(tid >> 6);
  const int g = lane >> 4, c = lane & 15;
  const int bid = blockIdx.x, kvh = bid & 7, j = bid >> 3, b = j >> 4, sp = j & 15, rb = j >> 2, cb = j & 3;
  const bool mlp1 = j < 14, mlp2 = j >= 14 && j < 28;
  const __amdgpu_buffer_rsrc_t ws = __builtin_amdgcn_make_buffer_rsrc(ws_, 0, WS_END, 0x00020000);
  u32 tag;
  const u32 hsh = ((u32)(bid * 16 + w) * 2654435761u) >> 20, hb = ((u32)bid * 2654435761u) >> 16;
  const int wu = (w + (ROT == 0 ? 0 : ROT == 1 || ROT == 2 || ROT == 3 ? j : (int)hb)) & 15;
  const int ro = ROT == 2 ? kvh : ROT == 3 || ROT == 4 ? (int)(hsh & 7) : 0;
  const size_t kvseq = (size_t)(b * 8 + kvh) * SK * DH;
  const __amdgpu_buffer_rsrc_t kr = __builtin_amdgcn_make_buffer_rsrc((void*)(k_ + kvseq), 0, SK * DH * 2, 0x00020000);
  const __amdgpu_buffer_rsrc_t vr = __builtin_amdgcn_make_buffer_rsrc((void*)(v_ + kvseq), 0, SK * DH * 2, 0x00020000);
  const int kvoff = (sp * 16 + wu) * 8192;
  const int klane = ((((c >> 1) ^ ro) & 1) * 4 + (c >> 2)) * 256 + (g * 2 + (c & 1)) * 16;
  u32x4 kf[8], vf[8];

  const int cq = j & 7, kq = j >> 3;
  const int mt = (mlp1 ? j : j - 14) * 8 + kvh;
  const int mhi = mt >> 2, mlo = mt & 3;
  if (w < 2 || (w < 4 && mlp1)) {
    __builtin_amdgcn_s_setprio(3);
    if (w == 0 && lane < 16) *(LAS u32*)(lds + L_FLAG + lane * 4) = 0u;
    const GAS u32x4* xp = (const GAS u32x4*)x_ + lane * 2;
    const u32x4 x0 = xp[0], x1 = xp[1];
    PIN();
    const int rs = w < 2 ? NQ / 8 : FF / 8, row0 = (w < 2 ? kq : mlo) * 128 + (w & 1) * 64;
    const u16* tile = w < 2 ? wq_ + kvh * 512 + cq * 64 : w1_ + mhi * 64;
    const GAS u32x4* wp = (const GAS u32x4*)tile + (row0 + (c >> 3) * 32 + g * 8) * rs + (c & 7);
    u32x4 wv[8];
#pragma unroll
    for (int i = 0; i < 8; ++i) wv[i] = ldnt(wp + i * rs);
    PIN();
    LDS_BARRIER();
    tag = read_tag(ws_);
    PIN();
    u32 xs[8] = {x0[0], x0[1], x0[2], x0[3], x1[0], x1[1], x1[2], x1[3]};
    layernorm(xs);
    if (w >= 2) layernorm(xs);
    LAS unsigned char* xl = lds + L_XN + w * 2048;
    *(LAS u32x4*)(xl + lane * 32) = (u32x4){xs[0], xs[1], xs[2], xs[3]};
    *(LAS u32x4*)(xl + lane * 32 + 16) = (u32x4){xs[4], xs[5], xs[6], xs[7]};
    f32x2 r = tile_matvec(wv, xl + row0 * 2, g, c);
    LAS f32x2* half = (LAS f32x2*)(lds + L_HALF + (w >> 1) * 512 + lane * 8);
    if (w & 1) {
      *half = r;
      set_flag(lds, F_HALF + (w >> 1));
    } else {
      wait_flags(lds, F_HALF + (w >> 1), 0, lane);
      r += *half;
      const int rec = w == 0 ? WS_QP + ((((g & 1) * 8 + kvh) * 8 + cq) * 4 + kq) * 256 : WS_PA + mt * 512 + (g & 1) * 256;
      stws1(ws, rec + ((c & 7) * 4 + (g >> 1) * 2 + (c >> 3)) * 8, pack2(r[0], r[1], tag));
    }
    ST(2);
    __builtin_amdgcn_s_setprio(PRIO_K);
  } else {
    if (w < 4 && mlp2) {
      const u16* p = w2_ + (mhi * 64 + (w & 1) * 32 + g * 8) * D + mlo * 128 + c * 8;
#pragma unroll
      for (int i = 0; i < 8; ++i) dma16(p + i * D, (u32)(size_t)(lds + L_W2 + (w & 1) * 8192 + i * 1024));
    }
    if (CODE_LINES && w == 4) {
      const unsigned char* pc;
      asm volatile("s_getpc_b64 %0" : "=s"(pc));
      if (lane < 4 && j * 4 + lane < CODE_LINES) touch(pc + (j * 4 + lane) * 128, (u32)(size_t)(lds + L_TOUCH));
    }
    RAW_BARRIER();
    if ((w >> 2) == 1) __builtin_amdgcn_s_setprio(2); else __builtin_amdgcn_s_setprio(PRIO_K);
  }
  PIN();

#pragma unroll
  for (int i = 0; i < 8; ++i) { kf[i] = BUF ? __builtin_amdgcn_raw_buffer_load_b128(kr, klane + (i & 1) * 128, kvoff + ((i >> 1) ^ (ro >> 1)) * 2048, NT) : ldnt((const GAS u32x4*)((const GAS char*)(k_ + kvseq) + kvoff + klane + (i & 1) * 128 + (i >> 1) * 2048)); __builtin_amdgcn_sched_barrier(0); }
  if ((w >> 2) != 1) __builtin_amdgcn_s_setprio(0);
  PIN();
#pragma unroll
  for (int i = 0; i < 8; ++i) { vf[i] = BUF ? __builtin_amdgcn_raw_buffer_load_b128(vr, lane * 16, kvoff + (i ^ ro) * 1024, NT) : ldnt((const GAS u32x4*)((const GAS char*)(v_ + kvseq) + kvoff + lane * 16 + i * 1024)); __builtin_amdgcn_sched_barrier(0); }
  PIN();
  ST(4);
  tag = read_tag(ws_);
  const int paoff = WS_PA + mhi * 2048 + lane * 16;
  u32x4 p0, p1;
  u32 b1v;
  if ((w >> 2) == 1) {
    const int u = w & 3;
    const int qoff = WS_QP + (b * 8 + kvh) * 8192 + u * 2048 + lane * 16;
    asm volatile("s_waitcnt vmcnt(0)" ::: "memory");
    if (Q_DELAY) __builtin_amdgcn_s_sleep(Q_DELAY);
    u32x4 q0, q1;
#pragma unroll 1
    for (int spin = 0; spin < SPIN_CAP; ++spin) {
      q0 = ldws(ws, qoff); q1 = ldws(ws, qoff + 1024);
      if (__all(tagged(q0, tag) && tagged(q1, tag))) break;
      __builtin_amdgcn_s_sleep(SLEEP_Q);
      asm volatile("" ::: "memory");
    }
    const float qa = half16(half32(first2(q0[0], q0[1]), first2(q1[0], q1[1])), half32(first2(q0[2], q0[3]), first2(q1[2], q1[3])));
    const float qb = half16(half32(second2(q0[0], q0[1]), second2(q1[0], q1[1])), half32(second2(q0[2], q0[3]), second2(q1[2], q1[3])));
    *(LAS u32*)(lds + L_Q + u * 256 + ((g >> 1) * 32 + c * 2 + (g & 1)) * 4) = pk(qa, qb);
    set_flag(lds, F_Q + u);
    ST(3);
    if (w == 7 && mlp2) {
      p0 = ldws(ws, paoff); p1 = ldws(ws, paoff + 1024);
      b1v = *(const GAS u32*)(b1_ + mhi * 64 + c * 4 + (g >> 1) * 2);
      PIN();
    }
    __builtin_amdgcn_s_setprio(0);
  }
  wait_flags(lds, F_Q, 3, lane);

  float sc[8];
  {
    const int x0 = (c >> 2) & 1;
    const u32x4 q0 = *(LAS u32x4*)(lds + L_Q + (c & 3) * 256 + (g * 2 + x0) * 16);
    const u32x4 q1 = *(LAS u32x4*)(lds + L_Q + (c & 3) * 256 + 128 + (g * 2 + x0) * 16);
#pragma unroll
    for (int o = 0; o < 4; ++o) {
      f32x4 s = mfma(kf[2 * o], q0, (f32x4){0.f, 0.f, 0.f, 0.f});
      s = mfma(kf[2 * o + 1], q1, s);
#pragma unroll
      for (int a = 0; a < 2; ++a) {
        float e1 = s[2 * a + 1];
        asm("" : "+v"(e1));
        const float t = x0 ? e1 : s[2 * a];
        sc[2 * o + a] = t + dpp<ROR4>(t);
      }
      const u32 d = pk(sc[2 * o], sc[2 * o + 1]), e = pk(bflo(d) * 0.08838834764831845f, bfhi(d) * 0.08838834764831845f);
      sc[2 * o] = bflo(e); sc[2 * o + 1] = bfhi(e);
    }
  }
  PIN();

    float m = fmaxf(fmaxf(fmaxf(sc[0], sc[1]), fmaxf(sc[2], sc[3])), fmaxf(fmaxf(sc[4], sc[5]), fmaxf(sc[6], sc[7])));
    m = max32(max16(m)) * LOG2E;
    float p[8], l = 0.f;
#pragma unroll
    for (int e = 0; e < 8; ++e) { p[e] = __builtin_amdgcn_exp2f(fmaf(sc[e], LOG2E, -m)); l += p[e]; }
    l = half16(l, l); l = half32(l, l);
    if (lane < 4) { *(LAS float*)(lds + L_MW + (lane * 16 + w) * 4) = m; *(LAS float*)(lds + L_LW + (lane * 16 + w) * 4) = l; }
    PIN();
    if ((w >> 1) == 1) set_flag(lds, F_W2 + (w & 1));
    if (lane == 0) __hip_atomic_fetch_add((LAS u32*)(lds + L_CNT), 1u, __ATOMIC_RELAXED, __HIP_MEMORY_SCOPE_WORKGROUP);
    const u32x4 pf = {pk(p[0], p[1]), pk(p[2], p[3]), pk(p[4], p[5]), pk(p[6], p[7])};
    u32x4 am[4];
#pragma unroll
    for (int u = 0; u < 4; ++u) am[u] = sel4((c >> 2) == u, pf);
    PIN();
    u32x4 ve[4], vo[4];
    u32 slo, shi;
    asm volatile("s_mov_b32 %0, 0x05040100\n\ts_mov_b32 %1, 0x07060302" : "=s"(slo), "=s"(shi) : "v"(am[0][0]), "v"(am[1][1]), "v"(am[2][2]), "v"(am[3][3]));
#pragma unroll
    for (int r = 0; r < 4; ++r) {
#pragma unroll
      for (int u = 0; u < 4; ++u) {
        ve[u][r] = __builtin_amdgcn_perm(vf[2 * r + 1][u], vf[2 * r][u], slo);
        vo[u][r] = __builtin_amdgcn_perm(vf[2 * r + 1][u], vf[2 * r][u], shi);
      }
      if (r < 3)
        asm volatile("" : "+s"(slo), "+s"(shi) : "v"(ve[0][r]), "v"(ve[1][r]), "v"(ve[2][r]), "v"(ve[3][r]), "v"(vo[0][r]), "v"(vo[1][r]), "v"(vo[2][r]), "v"(vo[3][r]));
    }
    f32x4 oe = {0.f, 0.f, 0.f, 0.f}, oo = oe;
#pragma unroll
    for (int u = 0; u < 4; ++u) { oe = mfma(am[u], ve[u], oe); oo = mfma(am[u], vo[u], oo); }
#pragma unroll
    for (int h = 0; h < 4; ++h) *(LAS f32x2*)(lds + L_OW + w * OW_STRIDE + h * 512 + c * 32 + g * 8) = (f32x2){oe[h], oo[h]};
    PIN();
    ST(5);

  const bool gath = (w & 4) == 0;
  const int v = (w & 3) + (w >> 3) * 4;
  auto wait_ml = [&]() __attribute__((always_inline)) {
#pragma unroll 1
    for (int spin = 0; spin < (SPIN_CAP << 4); ++spin) {
      if (__builtin_amdgcn_readfirstlane(*(volatile LAS u32*)(lds + L_CNT)) == 16u) break;
      __builtin_amdgcn_s_sleep(1);
    }
    PIN();
  };
  auto load_wo = [&](u32x4 (&wt)[8]) __attribute__((always_inline)) {
    const GAS u16* p = (const GAS u16*)wo_ + (size_t)(kvh * 512 + rb * 64 + g * 4) * D + cb * 128 + (w - 4) * 64 + c * 4;
#pragma unroll
    for (int i = 0; i < 8; ++i) {
      const u32x2 t0 = ldnt((const GAS u32x2*)(p + (size_t)(32 * (i >> 2) + (i & 3)) * D));
      const u32x2 t1 = ldnt((const GAS u32x2*)(p + (size_t)(32 * (i >> 2) + 16 + (i & 3)) * D));
      wt[i] = (u32x4){t0[0], t0[1], t1[0], t1[1]};
    }
    PIN();
  };
  const bool fin = FINLAST ? bid >= 256 - NFIN : bid < NFIN;
  const int cf = bid & 3, bf = (bid >> 2) & 1, seg = (bid >> 3) & 3;
  if (w >= 12) {
    LDS_BARRIER();
    ST(6);
    if (fin) {
      const int row = (w - 12) * 8 + (lane >> 3);
      const int moff = WS_MP + (cf * 28 + min(row, 27)) * 1024 + bf * 512 + seg * 128 + (lane & 7) * 16;
      u32x4 mv;
#pragma unroll 1
      for (int spin = 0; spin < SPIN_CAP; ++spin) {
        mv = ldws(ws, moff);
        if (__all(tagged(mv, tag))) break;
        __builtin_amdgcn_s_sleep(SLEEP_M);
        asm volatile("" ::: "memory");
      }
      const float t = sum_records(sel4(row < 28, mv));
      if ((lane & 8) == 0) *(LAS float*)(lds + L_MF + (w - 12) * 128 + ((lane & 7) * 4 + g) * 4) = t;
      LDS_BARRIER();
    }
  } else if (gath) {
    {
      const int f = v * 16 + (lane >> 2), p4 = lane & 3, hd = v >> 1;
      wait_ml();
      const f32x4 mv = *(LAS f32x4*)(lds + L_MW + hd * 64 + p4 * 16), lv = *(LAS f32x4*)(lds + L_LW + hd * 64 + p4 * 16);
      float M = fmaxf(fmaxf(mv[0], mv[1]), fmaxf(mv[2], mv[3]));
      M = fmaxf(M, dpp<XOR1>(M)); M = fmaxf(M, dpp<XOR2>(M));
      float L = 0.f, fw[4];
#pragma unroll
      for (int i = 0; i < 4; ++i) { fw[i] = __builtin_amdgcn_exp2f(mv[i] - M); L = fmaf(fw[i], lv[i], L); }
      L += dpp<XOR1>(L); L += dpp<XOR2>(L);
      const float inv = __builtin_amdgcn_rcpf(L);
#pragma unroll
      for (int i = 0; i < 4; ++i) fw[i] *= inv;
      const u32 lam = f2u(M + __builtin_amdgcn_logf(L)) >> ((lane >> 5) * 16);
      u32 t0 = tag | (lam & 255u), t1 = tag | ((lam >> 8) & 255u);
      int ooff = WS_OP + ((kvh * 2 + b) * 16 + sp) * 2048 + f * 16;
      int laddr = L_OW + p4 * 4 * OW_STRIDE + f * 16;
      asm volatile("" : "+v"(fw[0]), "+v"(fw[1]), "+v"(fw[2]), "+v"(fw[3]), "+v"(t0), "+v"(t1), "+v"(ooff), "+v"(laddr));
      ST(3);
      LDS_BARRIER();
      ST(6);
      f32x4 s = fw[0] * *(LAS f32x4*)(lds + laddr);
#pragma unroll
      for (int i = 1; i < 4; ++i) s += fw[i] * *(LAS f32x4*)(lds + laddr + i * OW_STRIDE);
      float s0 = s[0], s1 = s[1], s2 = s[2], s3 = s[3];
      quad_sum4(s0, s1, s2, s3);
      if (p4 == 0) __builtin_amdgcn_raw_buffer_store_b128((u32x4){pk(s0, s1), t0, pk(s2, s3), t1}, ws, ooff, 0, SAUX);
    }
    PIN();
    ST(7);
    {
      u32 gaddr2 = (u32)(size_t)(lds + L_GP + v * GP_STRIDE + 256), gaddr = gaddr2 - 256 + (c * 4 + g) * 4;
      u32 lsel = (c & 8) ? 0x05040100u : 0x01000504u;
      asm volatile("" : "+v"(gaddr), "+v"(gaddr2), "+v"(lsel));
      if (SLEEP_O) __builtin_amdgcn_s_sleep(SLEEP_O);
      const u32x4 ov = poll16<255>(ws, WS_OP + (kvh * 32 + v * 4 + g) * 2048 + rb * 256 + c * 16, tag);
      ST(8);
      u32 a, lam, m1, x0, x1, x2, x3;
      f32x2 gl;
      asm volatile("v_perm_b32 %[a], %[t1], %[t0], %[sel]\n\t"
                   "v_lshlrev_b32 %[x0], 16, %[d0]\n\t"
                   "v_lshlrev_b32 %[x2], 16, %[d1]\n\t"
                   "v_mov_b32_dpp %[m1], %[a] row_ror:8 row_mask:0xf bank_mask:0xf\n\t"
                   "v_and_b32 %[x1], 0xffff0000, %[d0]\n\t"
                   "v_perm_b32 %[lam], %[a], %[m1], %[lsel]\n\t"
                   "v_mov_b32 %[m1], %[lam]\n\t"
                   "v_mov_b32 %[a], %[lam]\n\t"
                   "v_and_b32 %[x3], 0xffff0000, %[d1]\n\t"
                   "s_nop 0\n\t"
                   "v_permlane16_swap_b32 %[m1], %[a]\n\t"
                   "v_max_f32 %[m1], %[m1], %[a]\n\t"
                   "v_mov_b32 %[a], %[m1]\n\t"
                   "s_nop 1\n\t"
                   "v_permlane32_swap_b32 %[m1], %[a]\n\t"
                   "v_max_f32 v104, %[m1], %[a]\n\t"
                   "v_sub_f32 %[lam], %[lam], v104\n\t"
                   "v_exp_f32 %[lam], %[lam]\n\t"
                   "s_nop 0\n\t"
                   "v_mul_f32 %[x0], %[lam], %[x0]\n\t"
                   "v_mul_f32 %[x2], %[lam], %[x2]\n\t"
                   "v_mul_f32 %[x1], %[lam], %[x1]\n\t"
                   "v_mul_f32 %[x3], %[lam], %[x3]\n\t"
                   "v_mov_b32 %[a], %[lam]\n\t"
                   "v_permlane32_swap_b32 %[x0], %[x2]\n\t"
                   "v_permlane32_swap_b32 %[x1], %[x3]\n\t"
                   "v_permlane16_swap_b32 %[lam], %[a]\n\t"
                   "v_add_f32 %[x0], %[x0], %[x2]\n\t"
                   "v_add_f32 %[x1], %[x1], %[x3]\n\t"
                   "v_add_f32 %[lam], %[lam], %[a]\n\t"
                   "v_mov_b32 %[a], %[lam]\n\t"
                   "v_permlane16_swap_b32 %[x0], %[x1]\n\t"
                   "v_add_f32 %[x0], %[x0], %[x1]\n\t"
                   "v_permlane32_swap_b32 %[lam], %[a]\n\t"
                   "ds_write_b32 %[ga], %[x0]\n\t"
                   "v_add_f32 v105, %[lam], %[a]\n\t"
                   "ds_write_b64 %[gb], v[104:105]"
                   : [a] "=&v"(a), [lam] "=&v"(lam), [m1] "=&v"(m1), [x0] "=&v"(x0), [x1] "=&v"(x1), [x2] "=&v"(x2), [x3] "=&v"(x3), "=&{v[104:105]}"(gl)
                   : [d0] "v"(ov[0]), [t0] "v"(ov[1]), [d1] "v"(ov[2]), [t1] "v"(ov[3]), [sel] "s"(0x04000400u), [lsel] "v"(lsel), [ga] "v"(gaddr), [gb] "v"(gaddr2)
                   : "memory");
    }
    LDS_BARRIER();
    ST(9);
    if (fin) {
      int faddr = L_FP + ((lane & 7) * 4 + g) * 32 + v * 4;
      asm volatile("" : "+v"(faddr));
      if (SLEEP_S) __builtin_amdgcn_s_sleep(SLEEP_S);
      const u32x4 sv = poll16<255>(ws, WS_S2 + (cf * 64 + v * 8 + (lane >> 3)) * 1024 + bf * 512 + seg * 128 + (lane & 7) * 16, tag);
      ST(10);
      *(LAS float*)(lds + faddr) = sum_records(sv);
      LDS_BARRIER();
    }
  } else if (w < 6) {
    u32x4 wt[8], bw[8];
    if (WO_MODE == 3) {
      wait_ml();
      const bool now = __builtin_amdgcn_s_memrealtime() >= t_start + WO_AT;
      if (!now) {
        LDS_BARRIER();
        ST(6);
        while (__builtin_amdgcn_s_memrealtime() < t_start + WO_AT) __builtin_amdgcn_s_sleep(1);
      }
      load_wo(wt);
      if (now) { LDS_BARRIER(); ST(6); }
    } else {
    if (WO_MODE == 1) wait_ml();
    if (WO_MODE < 2) load_wo(wt);
    LDS_BARRIER();
    ST(6);
    if (WO_MODE == 2) { if (WO_SLEEP) __builtin_amdgcn_s_sleep(WO_SLEEP); load_wo(wt); }
    }
#pragma unroll
    for (int i = 0; i < 8; ++i) bw[i] = colfrag(wt, i);
    int gp = L_GP + (c & 7) * GP_STRIDE, go = gp + ((c >> 3) * 16 + g * 4) * 4;
    int soff = WS_S2 + (cb * 64 + kvh * 8 + rb) * 1024 + (g & 1) * 512 + ((w - 4) * 32 + c * 2 + (g >> 1)) * 8;
    asm volatile("" : "+v"(gp), "+v"(go), "+v"(soff) : "v"(bw[0]), "v"(bw[1]), "v"(bw[2]), "v"(bw[3]), "v"(bw[4]), "v"(bw[5]), "v"(bw[6]), "v"(bw[7]));
    ST(1);
    LDS_BARRIER();
    ST(9);
    f32x2 gl = *(LAS f32x2*)(lds + gp + 256);
    u32x4 of = *(LAS u32x4*)(lds + go), o1 = *(LAS u32x4*)(lds + go + 128);
#define QP1(r) "v_add_f32_dpp " r ", " r ", " r " quad_perm:[1,0,3,2] row_mask:0xf bank_mask:0xf\n\t"
#define QP2(r) "v_add_f32_dpp " r ", " r ", " r " quad_perm:[2,3,0,1] row_mask:0xf bank_mask:0xf\n\t"
#define PKM(r) "v_pk_mul_f32 " r ", v[118:119], " r " op_sel_hi:[0,1]\n\t"
    asm volatile("v_max_f32_dpp v118, v116, v116 quad_perm:[1,0,3,2] row_mask:0xf bank_mask:0xf\n\t"
                 "s_nop 1\n\t"
                 "v_max_f32_dpp v118, v118, v118 quad_perm:[2,3,0,1] row_mask:0xf bank_mask:0xf\n\t"
                 "v_sub_f32 v118, v116, v118\n\t"
                 "v_exp_f32 v118, v118\n\t"
                 "s_nop 0\n\t"
                 "v_mul_f32 v117, v118, v117\n\t"
                 PKM("v[108:109]") PKM("v[110:111]") PKM("v[112:113]") PKM("v[114:115]")
                 QP1("v117") QP1("v108") QP1("v109") QP1("v110") QP1("v111") QP1("v112") QP1("v113") QP1("v114") QP1("v115")
                 QP2("v117") QP2("v108") QP2("v109")
                 "v_rcp_f32 v118, v117\n\t"
                 QP2("v110") QP2("v111") QP2("v112") QP2("v113") QP2("v114") QP2("v115")
                 PKM("v[108:109]") PKM("v[110:111]") PKM("v[112:113]") PKM("v[114:115]")
                 "v_cvt_pk_bf16_f32 v108, v108, v109\n\t"
                 "v_cvt_pk_bf16_f32 v109, v110, v111\n\t"
                 "v_cvt_pk_bf16_f32 v110, v112, v113\n\t"
                 "v_cvt_pk_bf16_f32 v111, v114, v115\n\t"
                 "s_nop 1"
                 : "+{v[108:111]}"(of), "+{v[112:115]}"(o1), "+{v[116:117]}"(gl)
                 :
                 : "v118", "v119");
    f32x4 acc[8];
#pragma unroll
    for (int i = 0; i < 8; ++i) { const int k = (i & 3) * 2 + (i >> 2); acc[k] = mfma(of, bw[k], (f32x4){0.f, 0.f, 0.f, 0.f}); }
    __builtin_amdgcn_sched_barrier(0);
    float r[4];
#pragma unroll
    for (int d = 0; d < 4; ++d) r[d] = (g >> 1) ? acc[4 + d][0] : acc[d][0];
    stws1(ws, soff, pack2(half32(r[0], r[2]), half32(r[1], r[3]), tag));
    ST(10);
  } else if (w == 7 && mlp2) {
#pragma unroll 1
    for (int spin = 0; spin < SPIN_CAP; ++spin) {
      if (__all(tagged(p0, tag) && tagged(p1, tag))) break;
      __builtin_amdgcn_s_sleep(SLEEP_Q);
      asm volatile("" ::: "memory");
      p0 = ldws(ws, paoff); p1 = ldws(ws, paoff + 1024);
    }
    const float t2[2] = {half32(first2(p0[0], p0[1]) + first2(p1[0], p1[1]), first2(p0[2], p0[3]) + first2(p1[2], p1[3])),
                         half32(second2(p0[0], p0[1]) + second2(p1[0], p1[1]), second2(p0[2], p0[3]) + second2(p1[2], p1[3]))};
    float hv[2];
#pragma unroll
    for (int e = 0; e < 2; ++e) {
      const float t = rbf(rbf(t2[e]) + (e ? bfhi(b1v) : bflo(b1v)));
      const float uu = 0.7978845608028654f * (t + 0.044715f * t * t * t);
      hv[e] = t / (1.f + __builtin_amdgcn_exp2f(-2.f * LOG2E * uu));
    }
    *(LAS u32*)(lds + L_HS + (g & 1) * 128 + (c * 2 + (g >> 1)) * 4) = pk(hv[0], hv[1]);
    wait_flags(lds, F_W2, 1, lane);
    f32x4 r = {0.f, 0.f, 0.f, 0.f};
#pragma unroll
    for (int s = 0; s < 2; ++s) {
      const u32x4 ha = *(LAS u32x4*)(lds + L_HS + ((c >> 2) & 1) * 128 + s * 64 + g * 16);
      u32x4 w2t[8];
#pragma unroll
      for (int i = 0; i < 8; ++i) w2t[i] = *(LAS u32x4*)(lds + L_W2 + s * 8192 + i * 1024 + lane * 16);
#pragma unroll
      for (int d = 0; d < 8; ++d) r = mfma(sel4((c >> 3) == (d >> 2) && (c & 3) == (d & 3), ha), colfrag(w2t, d), r);
    }
    u32x2 m0 = pack2(r[0], r[1], tag), m1 = pack2(r[2], r[3], tag);
    asm volatile("" : "+v"(m0), "+v"(m1));
    ST(2);
    if (MP_LATE) LDS_BARRIER();
    stws(ws, WS_MP + (mlo * 28 + mhi) * 1024 + (g & 1) * 512 + (c * 2 + (g >> 1)) * 16, m0, m1);
    if (!MP_LATE) LDS_BARRIER();
    ST(6);
  } else if (w == 6 && fin) {
    const int col = cf * 128 + seg * 32 + (lane & 31);
    const u32 xv = ((const GAS u16*)x_)[bf * D + col], b2v = ((const GAS u16*)b2_)[col];
    LDS_BARRIER();
    ST(6);
    LDS_BARRIER();
    LAS float* mf = (LAS float*)(lds + L_MF + (lane >> 5) * 256 + (lane & 31) * 4);
    float t = mf[0] + mf[32];
    t = half32(t, t);
    t = rbf(rbf(t) + bflo(b2v));
    float gate = rbf(t / (1.f + __builtin_amdgcn_exp2f(-LOG2E * t))), xf = bflo(xv);
    int faddr = L_FP + (lane & 31) * 32 + (lane >> 5) * 16;
    GAS u16* op = (GAS u16*)out_ + bf * D + col;
    asm volatile("" : "+v"(gate), "+v"(xf), "+v"(faddr), "+v"(op));
    LDS_BARRIER();
    ST(10);
    const f32x4 fp = *(LAS f32x4*)(lds + faddr);
    float s = (fp[0] + fp[1]) + (fp[2] + fp[3]);
    s = half32(s, s);
    const float res = xf + rbf(rbf(s) * gate);
    if (lane < 32) __hip_atomic_store(op, (u16)pk(res, res), RLX_AGENT);
    if ((bid & 31) == 0 && lane == 0) __hip_atomic_store((GAS u32*)(ws_ + WS_EPOCH), tag >> 8, RLX_AGENT);
  } else {
    LDS_BARRIER();
    ST(6);
  }
  ST(11);
  {
    GAS u64* st = (GAS u64*)(ws_ + (WS_AT_END ? -(8 << 20) : (8 << 20)));
    volatile LAS u64* ls = (volatile LAS u64*)(lds + L_TOUCH);
    if (lane == 0) { ls[w * 2] = ts[PROBE]; ls[w * 2 + 1] = ts[PROBE_B]; }
    if (w == 0) {
      while (__builtin_amdgcn_s_memrealtime() < ts[0] + 2500) __builtin_amdgcn_s_sleep(1);
      u64 ma = ls[(lane & 15) * 2], mb = ls[(lane & 15) * 2 + 1];
      auto red = [&]() {
        for (int o = 32; o; o >>= 1) {
          const u64 x = __shfl_xor(ma, o), y = __shfl_xor(mb, o);
          ma = x > ma ? x : ma;
          if (PROBE_B == 0) mb = y < mb ? y : mb; else mb = y > mb ? y : mb;
        }
      };
      red();
      if (lane == 0) { __hip_atomic_store(st + bid * 2, ma, RLX_AGENT); __hip_atomic_store(st + bid * 2 + 1, mb, RLX_AGENT); }
      if (bid == 0) {
        while (__builtin_amdgcn_s_memrealtime() < ts[0] + 3200) __builtin_amdgcn_s_sleep(1);
        for (int i = lane; i < 256; i += 64) {
          const u64 x = __hip_atomic_load(st + i * 2, RLX_AGENT), y = __hip_atomic_load(st + i * 2 + 1, RLX_AGENT);
          ma = x > ma ? x : ma;
          if (PROBE_B == 0) mb = y < mb ? y : mb; else mb = y > mb ? y : mb;
        }
        red();
        const u64 until = ts[0] + 5000 + 10 * (ma - mb);
        while (__builtin_amdgcn_s_memrealtime() < until && __builtin_amdgcn_s_memrealtime() < ts[0] + 40000) __builtin_amdgcn_s_sleep(1);
      }
    }
  }
}

extern "C" void kernel_launch(void* const* d_in, const int* in_sizes, int n_in, void* d_out, int out_size, void* d_ws, size_t ws_size,
                              hipStream_t stream) {
  unsigned char* ws = (unsigned char*)d_ws + (WS_AT_END ? ((ws_size - WS_SPAN) & ~(size_t)4095) : 0);
  fused_decode<<<256, 1024, 0, stream>>>((const u16*)d_in[0], (const u16*)d_in[1], (const u16*)d_in[2], (const u16*)d_in[3],
                                         (const u16*)d_in[4], (const u16*)d_in[6], (const u16*)d_in[7], (const u16*)d_in[8],
                                         (const u16*)d_in[9], (u16*)d_out, ws
                                         );
}
```

```cpp
#define STAMP 1
#define PROBE 8
#define PROBE_B 6
#define STMASK (1 | (1 << 8) | (1 << 6))
#define FINLAST 0
#define PRE 2
#include <hip/hip_runtime.h>
#include <stdint.h>

#define WS_AT_END 0
#define SLEEP_Q 8
#define SLEEP_O 8
#define SLEEP_S 8
#define WO_MODE 1
#define PRIO_K 1
#define ROT 1
#define CODE_LINES 0
#define RND 1
#define BUF 1
#define PREW 12
#define Q_DELAY 32
#define MP_LATE 0
#define SLEEP_M 4
#define WO_AT 1150
#define WO_SLEEP 0
#define ABL 0

#define GAS __attribute__((address_space(1)))
#define LAS __attribute__((address_space(3)))
#define DI __device__ __forceinline__
#define RLX_AGENT __ATOMIC_RELAXED, __HIP_MEMORY_SCOPE_AGENT
typedef unsigned short u16;
typedef unsigned u32;
typedef unsigned long long u64;
typedef u32 u32x2 __attribute__((ext_vector_type(2)));
typedef u32 u32x4 __attribute__((ext_vector_type(4)));
typedef float f32x2 __attribute__((ext_vector_type(2)));
typedef float f32x4 __attribute__((ext_vector_type(4)));
typedef __bf16 bf16x2 __attribute__((ext_vector_type(2)));
typedef __bf16 bf16x8 __attribute__((ext_vector_type(8)));

constexpr int D = 512, SK = 8192, DH = 128, FF = 1792, NQ = 4096;
constexpr int SC1 = 16, NT = 2;
constexpr int SPIN_CAP = 1 << 18;
constexpr int NFIN = 32;
constexpr float LOG2E = 1.4426950408889634f;

constexpr int WS_EPOCH = 0;
constexpr int WS_QP = 256;
constexpr int WS_PA = WS_QP + 512 * 256;
constexpr int WS_OP = WS_PA + 112 * 512;
constexpr int WS_S2 = WS_OP + 256 * 2048;
constexpr int WS_MP = WS_S2 + 256 * 1024;
constexpr int WS_END = WS_MP + 112 * 1024;
constexpr int WS_SPAN = 4 << 20;
constexpr int WS_STAMP = 8 << 20;

constexpr int F_Q = 0, F_HALF = 4, F_W2 = 6;
constexpr int L_FLAG = 0, L_CNT = 48, L_XN = 64, L_HALF = L_XN + 8192, L_Q = L_HALF + 1024, L_MW = L_Q + 1024, L_LW = L_MW + 256,
              L_HS = L_LW + 256, L_GP = L_HS + 256, GP_STRIDE = 272, L_FP = L_GP + 8 * GP_STRIDE, L_MF = L_FP + 1024,
              L_TOUCH = L_MF + 512, L_T2 = L_TOUCH + 256, L_W2 = L_T2 + 256, L_OW = L_W2 + 16384, OW_STRIDE = 2064, L_END = L_OW + 16 * OW_STRIDE;

DI float bflo(u32 v) { return __builtin_bit_cast(float, v << 16); }
DI float bfhi(u32 v) { return __builtin_bit_cast(float, v & 0xffff0000u); }
DI float rbf(float f) { return (float)(__bf16)f; }
DI u32 pk(float a, float b) { return __builtin_bit_cast(u32, __builtin_convertvector((f32x2){a, b}, bf16x2)); }
DI u32 f2u(float f) { return __builtin_bit_cast(u32, f); }
DI float u2f(u32 u) { return __builtin_bit_cast(float, u); }
DI f32x4 mfma(u32x4 a, u32x4 b, f32x4 c) {
  return __builtin_amdgcn_mfma_f32_16x16x32_bf16(__builtin_bit_cast(bf16x8, a), __builtin_bit_cast(bf16x8, b), c, 0, 0, 0);
}
template <int CTRL> DI float dpp(float x) { return u2f(__builtin_amdgcn_mov_dpp(f2u(x), CTRL, 0xf, 0xf, true)); }
constexpr int XOR1 = 0xB1, XOR2 = 0x4E, ROR4 = 0x124, ROR8 = 0x128;
DI float half32(float a, float b) { auto r = __builtin_amdgcn_permlane32_swap(f2u(a), f2u(b), false, false); return u2f(r[0]) + u2f(r[1]); }
DI float half16(float a, float b) { auto r = __builtin_amdgcn_permlane16_swap(f2u(a), f2u(b), false, false); return u2f(r[0]) + u2f(r[1]); }
DI float max32(float a) { auto r = __builtin_amdgcn_permlane32_swap(f2u(a), f2u(a), false, false); return __builtin_amdgcn_fmed3f(u2f(r[0]), u2f(r[1]), __builtin_inff()); }
DI float max16(float a) { auto r = __builtin_amdgcn_permlane16_swap(f2u(a), f2u(a), false, false); return __builtin_amdgcn_fmed3f(u2f(r[0]), u2f(r[1]), __builtin_inff()); }
DI float half_sum(float v) {
  v += dpp<XOR1>(v); v += dpp<XOR2>(v); v += dpp<ROR4>(v); v += dpp<ROR8>(v);
  return half16(v, v);
}
DI u32 plo(u32 k1, u32 k0) { return __builtin_amdgcn_perm(k1, k0, 0x05040100u); }
DI u32 phi(u32 k1, u32 k0) { return __builtin_amdgcn_perm(k1, k0, 0x07060302u); }
DI u32x4 colfrag(const u32x4 (&r)[8], int d) {
  const int q = d >> 1;
  return (d & 1) ? (u32x4){phi(r[1][q], r[0][q]), phi(r[3][q], r[2][q]), phi(r[5][q], r[4][q]), phi(r[7][q], r[6][q])}
                 : (u32x4){plo(r[1][q], r[0][q]), plo(r[3][q], r[2][q]), plo(r[5][q], r[4][q]), plo(r[7][q], r[6][q])};
}
DI u32x4 sel4(bool c, u32x4 v) { return (u32x4){c ? v[0] : 0u, c ? v[1] : 0u, c ? v[2] : 0u, c ? v[3] : 0u}; }
template <typename T> DI T ldnt(const GAS T* p) { return __builtin_nontemporal_load(p); }
#define LAUX 16
#define SAUX 16
DI u32x4 ldws(__amdgpu_buffer_rsrc_t ws, int off) { return __builtin_amdgcn_raw_buffer_load_b128(ws, off, 0, LAUX); }
DI void stws(__amdgpu_buffer_rsrc_t ws, int off, u32x2 a, u32x2 b) { __builtin_amdgcn_raw_buffer_store_b128((u32x4){a[0], a[1], b[0], b[1]}, ws, off, 0, SAUX); }
DI void stws1(__amdgpu_buffer_rsrc_t ws, int off, u32x2 a) { __builtin_amdgcn_raw_buffer_store_b64(a, ws, off, 0, SAUX); }
DI u32x2 pack2(float a, float b, u32 tag8) {
  const u32 ar = f2u(a) + 0x800u;
  const u32 u = (f2u(b) & 0xf000u) | ((ar >> 4) & ~0xf000u);
  return (u32x2){phi(f2u(b), ar), __builtin_amdgcn_perm(tag8, u, 0x07060501u)};
}
DI float first2(u32 w0, u32 w1) { return u2f(__builtin_amdgcn_perm(w0, w1 << 4, 0x0504000cu)); }
DI float second2(u32 w0, u32 w1) { return u2f(__builtin_amdgcn_perm(w0, w1, 0x0706000cu)); }
DI bool tagged(u32x4 v, u32 tag8) { return ((v[1] ^ tag8) | (v[3] ^ tag8)) < 256u; }
#define PIN() do { asm volatile("" ::: "memory"); __builtin_amdgcn_sched_barrier(0); } while (0)
#define LDS_BARRIER() do { asm volatile("s_waitcnt lgkmcnt(0)\n\ts_barrier" ::: "memory"); __builtin_amdgcn_sched_barrier(0); } while (0)
#define RAW_BARRIER() do { __builtin_amdgcn_sched_barrier(0); asm volatile("s_barrier" ::: "memory"); __builtin_amdgcn_sched_barrier(0); } while (0)
DI void dma16(const u16* src, u32 dst) {
  u32 keep;
  asm volatile("s_mov_b32 %0, m0\n\ts_mov_b32 m0, %2\n\ts_nop 0\n\tglobal_load_lds_dwordx4 %1, off nt\n\ts_mov_b32 m0, %0"
               : "=&s"(keep) : "v"(src), "s"(dst) : "memory");
}
DI void touch(const void* src, u32 dst) {
  u32 keep;
  asm volatile("s_mov_b32 %0, m0\n\ts_mov_b32 m0, %2\n\ts_nop 0\n\tglobal_load_lds_dword %1, off\n\ts_mov_b32 m0, %0"
               : "=&s"(keep) : "v"(src), "s"(dst) : "memory");
}
DI void set_flag(LAS unsigned char* lds, int f) {
  asm volatile("s_waitcnt lgkmcnt(0)" ::: "memory");
  *(volatile LAS u32*)(lds + L_FLAG + f * 4) = 1u;
}
DI void wait_flags(LAS unsigned char* lds, int first, int mask, int lane) {
#pragma unroll 1
  for (int spin = 0; spin < (SPIN_CAP << 4); ++spin) {
    if (__all(*(volatile LAS u32*)(lds + L_FLAG + (first + (lane & mask)) * 4) != 0u)) break;
    __builtin_amdgcn_s_sleep(1);
  }
  PIN();
}
DI u32 read_tag(const unsigned char* ws) {
  u32 e;
  asm volatile("s_load_dword %0, %1, 0x0\n\ts_waitcnt lgkmcnt(0)" : "=s"(e) : "s"(ws) : "memory");
  return (e + 1u) << 8;
}
template <int LOW> DI u32x4 poll16(__amdgpu_buffer_rsrc_t ws, int off, u32 want) {
  u32x4 v;
  u64 keep;
  u32 left = SPIN_CAP, t;
  asm volatile("s_mov_b64 %[keep], exec\n\t"
               "s_branch 2f\n"
               "1:\n\t"
               "s_sub_u32 %[left], %[left], 1\n\t"
               "s_cbranch_scc1 3f\n"
               "2:\n\t"
               "buffer_load_dwordx4 v[100:103], %[off], %[ws], 0 offen sc1\n\t"
               "s_waitcnt vmcnt(0)\n\t"
               "v_bitop3_b32 %[t], v101, v103, %[want] bitop3:0x7e\n\t"
               "v_cmp_lt_u32 vcc, %[low], %[t]\n\t"
               "s_and_b64 exec, exec, vcc\n\t"
               "s_cbranch_scc1 1b\n"
               "3:\n\t"
               "s_mov_b64 exec, %[keep]"
               : "={v[100:103]}"(v), [keep] "=&s"(keep), [left] "+s"(left), [t] "=&v"(t)
               : [off] "v"(off), [ws] "s"(ws), [want] "s"(want), [low] "i"(LOW)
               : "vcc", "scc", "memory");
  return v;
}
DI void quad_sum4(float& a, float& b, float& c, float& d) {
  asm("s_nop 1\n\t"
      "v_add_f32_dpp %0, %0, %0 quad_perm:[1,0,3,2] row_mask:0xf bank_mask:0xf\n\t"
      "v_add_f32_dpp %1, %1, %1 quad_perm:[1,0,3,2] row_mask:0xf bank_mask:0xf\n\t"
      "v_add_f32_dpp %2, %2, %2 quad_perm:[1,0,3,2] row_mask:0xf bank_mask:0xf\n\t"
      "v_add_f32_dpp %3, %3, %3 quad_perm:[1,0,3,2] row_mask:0xf bank_mask:0xf\n\t"
      "v_add_f32_dpp %0, %0, %0 quad_perm:[2,3,0,1] row_mask:0xf bank_mask:0xf\n\t"
      "v_add_f32_dpp %1, %1, %1 quad_perm:[2,3,0,1] row_mask:0xf bank_mask:0xf\n\t"
      "v_add_f32_dpp %2, %2, %2 quad_perm:[2,3,0,1] row_mask:0xf bank_mask:0xf\n\t"
      "v_add_f32_dpp %3, %3, %3 quad_perm:[2,3,0,1] row_mask:0xf bank_mask:0xf"
      : "+v"(a), "+v"(b), "+v"(c), "+v"(d));
}
DI float sum_records(u32x4 v) {
  const float t = half16(half32(first2(v[0], v[1]), first2(v[2], v[3])), half32(second2(v[0], v[1]), second2(v[2], v[3])));
  return t + dpp<ROR8>(t);
}

DI void layernorm(u32 (&x)[8]) {
  float v[16], s = 0.f;
#pragma unroll
  for (int e = 0; e < 8; ++e) { v[2 * e] = bflo(x[e]); v[2 * e + 1] = bfhi(x[e]); s += v[2 * e] + v[2 * e + 1]; }
  const float mean = half_sum(s) * (1.f / 512.f);
  float q = 0.f;
#pragma unroll
  for (int e = 0; e < 16; ++e) { const float d = v[e] - mean; q = fmaf(d, d, q); }
  const float var = half_sum(q) * (1.f / 512.f);
  const float mb = rbf(mean), inv = __builtin_amdgcn_rcpf(rbf(__builtin_amdgcn_sqrtf(rbf(rbf(var) + 1e-5f))));
#pragma unroll
  for (int e = 0; e < 8; ++e) { const u32 d = pk(v[2 * e] - mb, v[2 * e + 1] - mb); x[e] = pk(bflo(d) * inv, bfhi(d) * inv); }
}

DI f32x2 tile_matvec(const u32x4 (&wv)[8], LAS unsigned char* xin, int g, int c) {
  const u32x4 xf = *(LAS u32x4*)(xin + ((c >> 2) & 1) * 1024 + ((c >> 3) * 32 + g * 8) * 2);
  f32x4 lo = {0.f, 0.f, 0.f, 0.f}, hi = lo;
#pragma unroll
  for (int d = 0; d < 4; ++d) {
    const u32x4 am = sel4((c & 3) == d, xf);
    lo = mfma(am, colfrag(wv, d), lo);
    hi = mfma(am, colfrag(wv, 4 + d), hi);
  }
  const bool valid = (g >> 1) == (c >> 3);
  float t[4];
#pragma unroll
  for (int e = 0; e < 4; ++e) { t[e] = half32(valid ? lo[e] : 0.f, valid ? hi[e] : 0.f); t[e] += dpp<ROR8>(t[e]); }
  return (c & 8) ? (f32x2){t[2], t[3]} : (f32x2){t[0], t[1]};
}

#define ST(i) do { if (!((STMASK >> (i)) & 1)) break; __builtin_amdgcn_sched_barrier(0); ts[i] = __builtin_amdgcn_s_memrealtime(); __builtin_amdgcn_s_waitcnt(0xC07F); __builtin_amdgcn_sched_barrier(0); } while (0)

__global__ void __launch_bounds__(1024, 4)
fused_decode(const u16* x_, const u16* k_, const u16* v_, const u16* wq_, const u16* wo_, const u16* w1_, const u16* b1_,
             const u16* w2_, const u16* b2_, u16* out_, unsigned char* ws_
             ) {
  __shared__ __attribute__((aligned(16))) unsigned char lds_raw[L_END];
  LAS unsigned char* lds = (LAS unsigned char*)lds_raw;
  u64 ts[12];
  for (int i = 0; i < 12; ++i) ts[i] = 0;
  ST(0);
  const u64 t_start = WO_MODE == 3 ? __builtin_amdgcn_s_memrealtime() : 0;
  asm volatile("" :: "s"(x_), "s"(k_), "s"(v_), "s"(wq_), "s"(wo_), "s"(w1_), "s"(b1_), "s"(w2_), "s"(b2_), "s"(out_), "s"(ws_));
  const int tid = threadIdx.x, lane = tid & 63, w = __builtin_amdgcn_readfirstlane(tid >> 6);
  const int g = lane >> 4, c = lane & 15;
  const int bid = blockIdx.x, kvh = bid & 7, j = bid >> 3, b = j >> 4, sp = j & 15, rb = j >> 2, cb = j & 3;
  const bool mlp1 = j < 14, mlp2 = j >= 14 && j < 28;
  const __amdgpu_buffer_rsrc_t ws = __builtin_amdgcn_make_buffer_rsrc(ws_, 0, WS_END, 0x00020000);
  u32 tag;
  const u32 hsh = ((u32)(bid * 16 + w) * 2654435761u) >> 20, hb = ((u32)bid * 2654435761u) >> 16;
  const int wu = (w + (ROT == 0 ? 0 : ROT == 1 || ROT == 2 || ROT == 3 ? j : (int)hb)) & 15;
  const int ro = ROT == 2 ? kvh : ROT == 3 || ROT == 4 ? (int)(hsh & 7) : 0;
  const size_t kvseq = (size_t)(b * 8 + kvh) * SK * DH;
  const __amdgpu_buffer_rsrc_t kr = __builtin_amdgcn_make_buffer_rsrc((void*)(k_ + kvseq), 0, SK * DH * 2, 0x00020000);
  const __amdgpu_buffer_rsrc_t vr = __builtin_amdgcn_make_buffer_rsrc((void*)(v_ + kvseq), 0, SK * DH * 2, 0x00020000);
  const int kvoff = (sp * 16 + wu) * 8192;
  const int klane = ((((c >> 1) ^ ro) & 1) * 4 + (c >> 2)) * 256 + (g * 2 + (c & 1)) * 16;
  u32x4 kf[8], vf[8];

  const int cq = j & 7, kq = j >> 3;
  const int mt = (mlp1 ? j : j - 14) * 8 + kvh;
  const int mhi = mt >> 2, mlo = mt & 3;
  auto pretouch = [&]() __attribute__((always_inline)) {
    const int o = lane < 16 ? WS_OP + ((kvh * 2 + b) * 16 + sp) * 2048 + lane * 128 : WS_S2 + (cb * 64 + kvh * 8 + rb) * 1024 + (lane & 7) * 128;
    if (lane < 24) touch(ws_ + o, (u32)(size_t)(lds + L_T2));
  };
  if (w < 2 || (w < 4 && mlp1)) {
    __builtin_amdgcn_s_setprio(3);
    if (w == 0 && lane < 16) *(LAS u32*)(lds + L_FLAG + lane * 4) = 0u;
    const GAS u32x4* xp = (const GAS u32x4*)x_ + lane * 2;
    const u32x4 x0 = xp[0], x1 = xp[1];
    PIN();
    const int rs = w < 2 ? NQ / 8 : FF / 8, row0 = (w < 2 ? kq : mlo) * 128 + (w & 1) * 64;
    const u16* tile = w < 2 ? wq_ + kvh * 512 + cq * 64 : w1_ + mhi * 64;
    const GAS u32x4* wp = (const GAS u32x4*)tile + (row0 + (c >> 3) * 32 + g * 8) * rs + (c & 7);
    u32x4 wv[8];
#pragma unroll
    for (int i = 0; i < 8; ++i) wv[i] = ldnt(wp + i * rs);
    PIN();
    LDS_BARRIER();
    tag = read_tag(ws_);
    PIN();
    u32 xs[8] = {x0[0], x0[1], x0[2], x0[3], x1[0], x1[1], x1[2], x1[3]};
    layernorm(xs);
    if (w >= 2) layernorm(xs);
    LAS unsigned char* xl = lds + L_XN + w * 2048;
    *(LAS u32x4*)(xl + lane * 32) = (u32x4){xs[0], xs[1], xs[2], xs[3]};
    *(LAS u32x4*)(xl + lane * 32 + 16) = (u32x4){xs[4], xs[5], xs[6], xs[7]};
    f32x2 r = tile_matvec(wv, xl + row0 * 2, g, c);
    LAS f32x2* half = (LAS f32x2*)(lds + L_HALF + (w >> 1) * 512 + lane * 8);
    if (w & 1) {
      *half = r;
      set_flag(lds, F_HALF + (w >> 1));
    } else {
      wait_flags(lds, F_HALF + (w >> 1), 0, lane);
      r += *half;
      const int rec = w == 0 ? WS_QP + ((((g & 1) * 8 + kvh) * 8 + cq) * 4 + kq) * 256 : WS_PA + mt * 512 + (g & 1) * 256;
      stws1(ws, rec + ((c & 7) * 4 + (g >> 1) * 2 + (c >> 3)) * 8, pack2(r[0], r[1], tag));
    }
    ST(2);
    __builtin_amdgcn_s_setprio(PRIO_K);
  } else {
    if (w < 4 && mlp2) {
      const u16* p = w2_ + (mhi * 64 + (w & 1) * 32 + g * 8) * D + mlo * 128 + c * 8;
#pragma unroll
      for (int i = 0; i < 8; ++i) dma16(p + i * D, (u32)(size_t)(lds + L_W2 + (w & 1) * 8192 + i * 1024));
    }
    if (CODE_LINES && w == 4) {
      const unsigned char* pc;
      asm volatile("s_getpc_b64 %0" : "=s"(pc));
      if (lane < 4 && j * 4 + lane < CODE_LINES) touch(pc + (j * 4 + lane) * 128, (u32)(size_t)(lds + L_TOUCH));
    }
    if (PRE == 1 && w == PREW) pretouch();
    RAW_BARRIER();
    if (PRE == 2 && w == PREW) pretouch();
    if (PRE == 3 && w == PREW) {
      const u32 junk = read_tag(ws_) ^ 0x80000000u;
      const u32x4 z = {0u, junk, 0u, junk};
      const int o = WS_OP + ((kvh * 2 + b) * 16 + sp) * 2048 + lane * 16;
      __builtin_amdgcn_raw_buffer_store_b128(z, ws, o, 0, SC1);
      __builtin_amdgcn_raw_buffer_store_b128(z, ws, o + 1024, 0, SC1);
      __builtin_amdgcn_raw_buffer_store_b128(z, ws, WS_S2 + (cb * 64 + kvh * 8 + rb) * 1024 + lane * 16, 0, SC1);
    }
    if ((w >> 2) == 1) __builtin_amdgcn_s_setprio(2); else __builtin_amdgcn_s_setprio(PRIO_K);
  }
  PIN();

#pragma unroll
  for (int i = 0; i < 8; ++i) { kf[i] = BUF ? __builtin_amdgcn_raw_buffer_load_b128(kr, klane + (i & 1) * 128, kvoff + ((i >> 1) ^ (ro >> 1)) * 2048, NT) : ldnt((const GAS u32x4*)((const GAS char*)(k_ + kvseq) + kvoff + klane + (i & 1) * 128 + (i >> 1) * 2048)); __builtin_amdgcn_sched_barrier(0); }
  if ((w >> 2) != 1) __builtin_amdgcn_s_setprio(0);
  PIN();
#pragma unroll
  for (int i = 0; i < 8; ++i) { vf[i] = BUF ? __builtin_amdgcn_raw_buffer_load_b128(vr, lane * 16, kvoff + (i ^ ro) * 1024, NT) : ldnt((const GAS u32x4*)((const GAS char*)(v_ + kvseq) + kvoff + lane * 16 + i * 1024)); __builtin_amdgcn_sched_barrier(0); }
  PIN();
  ST(4);
  tag = read_tag(ws_);
  const int paoff = WS_PA + mhi * 2048 + lane * 16;
  u32x4 p0, p1;
  u32 b1v;
  if ((w >> 2) == 1) {
    const int u = w & 3;
    const int qoff = WS_QP + (b * 8 + kvh) * 8192 + u * 2048 + lane * 16;
    asm volatile("s_waitcnt vmcnt(0)" ::: "memory");
    if (Q_DELAY) __builtin_amdgcn_s_sleep(Q_DELAY);
    u32x4 q0, q1;
#pragma unroll 1
    for (int spin = 0; spin < SPIN_CAP; ++spin) {
      q0 = ldws(ws, qoff); q1 = ldws(ws, qoff + 1024);
      if (__all(tagged(q0, tag) && tagged(q1, tag))) break;
      __builtin_amdgcn_s_sleep(SLEEP_Q);
      asm volatile("" ::: "memory");
    }
    const float qa = half16(half32(first2(q0[0], q0[1]), first2(q1[0], q1[1])), half32(first2(q0[2], q0[3]), first2(q1[2], q1[3])));
    const float qb = half16(half32(second2(q0[0], q0[1]), second2(q1[0], q1[1])), half32(second2(q0[2], q0[3]), second2(q1[2], q1[3])));
    *(LAS u32*)(lds + L_Q + u * 256 + ((g >> 1) * 32 + c * 2 + (g & 1)) * 4) = pk(qa, qb);
    set_flag(lds, F_Q + u);
    ST(3);
    if (w == 7 && mlp2) {
      p0 = ldws(ws, paoff); p1 = ldws(ws, paoff + 1024);
      b1v = *(const GAS u32*)(b1_ + mhi * 64 + c * 4 + (g >> 1) * 2);
      PIN();
    }
    __builtin_amdgcn_s_setprio(0);
  }
  wait_flags(lds, F_Q, 3, lane);

  float sc[8];
  {
    const int x0 = (c >> 2) & 1;
    const u32x4 q0 = *(LAS u32x4*)(lds + L_Q + (c & 3) * 256 + (g * 2 + x0) * 16);
    const u32x4 q1 = *(LAS u32x4*)(lds + L_Q + (c & 3) * 256 + 128 + (g * 2 + x0) * 16);
#pragma unroll
    for (int o = 0; o < 4; ++o) {
      f32x4 s = mfma(kf[2 * o], q0, (f32x4){0.f, 0.f, 0.f, 0.f});
      s = mfma(kf[2 * o + 1], q1, s);
#pragma unroll
      for (int a = 0; a < 2; ++a) {
        float e1 = s[2 * a + 1];
        asm("" : "+v"(e1));
        const float t = x0 ? e1 : s[2 * a];
        sc[2 * o + a] = t + dpp<ROR4>(t);
      }
      const u32 d = pk(sc[2 * o], sc[2 * o + 1]), e = pk(bflo(d) * 0.08838834764831845f, bfhi(d) * 0.08838834764831845f);
      sc[2 * o] = bflo(e); sc[2 * o + 1] = bfhi(e);
    }
  }
  PIN();

    float m = fmaxf(fmaxf(fmaxf(sc[0], sc[1]), fmaxf(sc[2], sc[3])), fmaxf(fmaxf(sc[4], sc[5]), fmaxf(sc[6], sc[7])));
    m = max32(max16(m)) * LOG2E;
    float p[8], l = 0.f;
#pragma unroll
    for (int e = 0; e < 8; ++e) { p[e] = __builtin_amdgcn_exp2f(fmaf(sc[e], LOG2E, -m)); l += p[e]; }
    l = half16(l, l); l = half32(l, l);
    if (lane < 4) { *(LAS float*)(lds + L_MW + (lane * 16 + w) * 4) = m; *(LAS float*)(lds + L_LW + (lane * 16 + w) * 4) = l; }
    PIN();
    if ((w >> 1) == 1) set_flag(lds, F_W2 + (w & 1));
    if (lane == 0) __hip_atomic_fetch_add((LAS u32*)(lds + L_CNT), 1u, __ATOMIC_RELAXED, __HIP_MEMORY_SCOPE_WORKGROUP);
    const u32x4 pf = {pk(p[0], p[1]), pk(p[2], p[3]), pk(p[4], p[5]), pk(p[6], p[7])};
    u32x4 am[4];
#pragma unroll
    for (int u = 0; u < 4; ++u) am[u] = sel4((c >> 2) == u, pf);
    PIN();
    u32x4 ve[4], vo[4];
    u32 slo, shi;
    asm volatile("s_mov_b32 %0, 0x05040100\n\ts_mov_b32 %1, 0x07060302" : "=s"(slo), "=s"(shi) : "v"(am[0][0]), "v"(am[1][1]), "v"(am[2][2]), "v"(am[3][3]));
#pragma unroll
    for (int r = 0; r < 4; ++r) {
#pragma unroll
      for (int u = 0; u < 4; ++u) {
        ve[u][r] = __builtin_amdgcn_perm(vf[2 * r + 1][u], vf[2 * r][u], slo);
        vo[u][r] = __builtin_amdgcn_perm(vf[2 * r + 1][u], vf[2 * r][u], shi);
      }
      if (r < 3)
        asm volatile("" : "+s"(slo), "+s"(shi) : "v"(ve[0][r]), "v"(ve[1][r]), "v"(ve[2][r]), "v"(ve[3][r]), "v"(vo[0][r]), "v"(vo[1][r]), "v"(vo[2][r]), "v"(vo[3][r]));
    }
    f32x4 oe = {0.f, 0.f, 0.f, 0.f}, oo = oe;
#pragma unroll
    for (int u = 0; u < 4; ++u) { oe = mfma(am[u], ve[u], oe); oo = mfma(am[u], vo[u], oo); }
#pragma unroll
    for (int h = 0; h < 4; ++h) *(LAS f32x2*)(lds + L_OW + w * OW_STRIDE + h * 512 + c * 32 + g * 8) = (f32x2){oe[h], oo[h]};
    PIN();
    ST(5);

  const bool gath = (w & 4) == 0;
  const int v = (w & 3) + (w >> 3) * 4;
  auto wait_ml = [&]() __attribute__((always_inline)) {
#pragma unroll 1
    for (int spin = 0; spin < (SPIN_CAP << 4); ++spin) {
      if (__builtin_amdgcn_readfirstlane(*(volatile LAS u32*)(lds + L_CNT)) == 16u) break;
      __builtin_amdgcn_s_sleep(1);
    }
    PIN();
  };
  auto load_wo = [&](u32x4 (&wt)[8]) __attribute__((always_inline)) {
    const GAS u16* p = (const GAS u16*)wo_ + (size_t)(kvh * 512 + rb * 64 + g * 4) * D + cb * 128 + (w - 4) * 64 + c * 4;
#pragma unroll
    for (int i = 0; i < 8; ++i) {
      const u32x2 t0 = ldnt((const GAS u32x2*)(p + (size_t)(32 * (i >> 2) + (i & 3)) * D));
      const u32x2 t1 = ldnt((const GAS u32x2*)(p + (size_t)(32 * (i >> 2) + 16 + (i & 3)) * D));
      wt[i] = (u32x4){t0[0], t0[1], t1[0], t1[1]};
    }
    PIN();
  };
  const bool fin = FINLAST ? bid >= 256 - NFIN : bid < NFIN;
  const int cf = bid & 3, bf = (bid >> 2) & 1, seg = (bid >> 3) & 3;
  if (w >= 12) {
    LDS_BARRIER();
    ST(6);
    if (fin) {
      const int row = (w - 12) * 8 + (lane >> 3);
      const int moff = WS_MP + (cf * 28 + min(row, 27)) * 1024 + bf * 512 + seg * 128 + (lane & 7) * 16;
      u32x4 mv;
#pragma unroll 1
      for (int spin = 0; spin < SPIN_CAP; ++spin) {
        mv = ldws(ws, moff);
        if (__all(tagged(mv, tag))) break;
        __builtin_amdgcn_s_sleep(SLEEP_M);
        asm volatile("" ::: "memory");
      }
      const float t = sum_records(sel4(row < 28, mv));
      if ((lane & 8) == 0) *(LAS float*)(lds + L_MF + (w - 12) * 128 + ((lane & 7) * 4 + g) * 4) = t;
      LDS_BARRIER();
    }
  } else if (gath) {
    {
      const int f = v * 16 + (lane >> 2), p4 = lane & 3, hd = v >> 1;
      wait_ml();
      const f32x4 mv = *(LAS f32x4*)(lds + L_MW + hd * 64 + p4 * 16), lv = *(LAS f32x4*)(lds + L_LW + hd * 64 + p4 * 16);
      float M = fmaxf(fmaxf(mv[0], mv[1]), fmaxf(mv[2], mv[3]));
      M = fmaxf(M, dpp<XOR1>(M)); M = fmaxf(M, dpp<XOR2>(M));
      float L = 0.f, fw[4];
#pragma unroll
      for (int i = 0; i < 4; ++i) { fw[i] = __builtin_amdgcn_exp2f(mv[i] - M); L = fmaf(fw[i], lv[i], L); }
      L += dpp<XOR1>(L); L += dpp<XOR2>(L);
      const float inv = __builtin_amdgcn_rcpf(L);
#pragma unroll
      for (int i = 0; i < 4; ++i) fw[i] *= inv;
      const u32 lam = f2u(M + __builtin_amdgcn_logf(L)) >> ((lane >> 5) * 16);
      u32 t0 = tag | (lam & 255u), t1 = tag | ((lam >> 8) & 255u);
      int ooff = WS_OP + ((kvh * 2 + b) * 16 + sp) * 2048 + f * 16;
      int laddr = L_OW + p4 * 4 * OW_STRIDE + f * 16;
      asm volatile("" : "+v"(fw[0]), "+v"(fw[1]), "+v"(fw[2]), "+v"(fw[3]), "+v"(t0), "+v"(t1), "+v"(ooff), "+v"(laddr));
      ST(3);
      LDS_BARRIER();
      ST(6);
      f32x4 s = fw[0] * *(LAS f32x4*)(lds + laddr);
#pragma unroll
      for (int i = 1; i < 4; ++i) s += fw[i] * *(LAS f32x4*)(lds + laddr + i * OW_STRIDE);
      float s0 = s[0], s1 = s[1], s2 = s[2], s3 = s[3];
      quad_sum4(s0, s1, s2, s3);
      if (p4 == 0) __builtin_amdgcn_raw_buffer_store_b128((u32x4){pk(s0, s1), t0, pk(s2, s3), t1}, ws, ooff, 0, SAUX);
    }
    PIN();
    ST(7);
    {
      u32 gaddr2 = (u32)(size_t)(lds + L_GP + v * GP_STRIDE + 256), gaddr = gaddr2 - 256 + (c * 4 + g) * 4;
      u32 lsel = (c & 8) ? 0x05040100u : 0x01000504u;
      asm volatile("" : "+v"(gaddr), "+v"(gaddr2), "+v"(lsel));
      if (SLEEP_O) __builtin_amdgcn_s_sleep(SLEEP_O);
      const u32x4 ov = poll16<255>(ws, WS_OP + (kvh * 32 + v * 4 + g) * 2048 + rb * 256 + c * 16, tag);
      ST(8);
      u32 a, lam, m1, x0, x1, x2, x3;
      f32x2 gl;
      asm volatile("v_perm_b32 %[a], %[t1], %[t0], %[sel]\n\t"
                   "v_lshlrev_b32 %[x0], 16, %[d0]\n\t"
                   "v_lshlrev_b32 %[x2], 16, %[d1]\n\t"
                   "v_mov_b32_dpp %[m1], %[a] row_ror:8 row_mask:0xf bank_mask:0xf\n\t"
                   "v_and_b32 %[x1], 0xffff0000, %[d0]\n\t"
                   "v_perm_b32 %[lam], %[a], %[m1], %[lsel]\n\t"
                   "v_mov_b32 %[m1], %[lam]\n\t"
                   "v_mov_b32 %[a], %[lam]\n\t"
                   "v_and_b32 %[x3], 0xffff0000, %[d1]\n\t"
                   "s_nop 0\n\t"
                   "v_permlane16_swap_b32 %[m1], %[a]\n\t"
                   "v_max_f32 %[m1], %[m1], %[a]\n\t"
                   "v_mov_b32 %[a], %[m1]\n\t"
                   "s_nop 1\n\t"
                   "v_permlane32_swap_b32 %[m1], %[a]\n\t"
                   "v_max_f32 v104, %[m1], %[a]\n\t"
                   "v_sub_f32 %[lam], %[lam], v104\n\t"
                   "v_exp_f32 %[lam], %[lam]\n\t"
                   "s_nop 0\n\t"
                   "v_mul_f32 %[x0], %[lam], %[x0]\n\t"
                   "v_mul_f32 %[x2], %[lam], %[x2]\n\t"
                   "v_mul_f32 %[x1], %[lam], %[x1]\n\t"
                   "v_mul_f32 %[x3], %[lam], %[x3]\n\t"
                   "v_mov_b32 %[a], %[lam]\n\t"
                   "v_permlane32_swap_b32 %[x0], %[x2]\n\t"
                   "v_permlane32_swap_b32 %[x1], %[x3]\n\t"
                   "v_permlane16_swap_b32 %[lam], %[a]\n\t"
                   "v_add_f32 %[x0], %[x0], %[x2]\n\t"
                   "v_add_f32 %[x1], %[x1], %[x3]\n\t"
                   "v_add_f32 %[lam], %[lam], %[a]\n\t"
                   "v_mov_b32 %[a], %[lam]\n\t"
                   "v_permlane16_swap_b32 %[x0], %[x1]\n\t"
                   "v_add_f32 %[x0], %[x0], %[x1]\n\t"
                   "v_permlane32_swap_b32 %[lam], %[a]\n\t"
                   "ds_write_b32 %[ga], %[x0]\n\t"
                   "v_add_f32 v105, %[lam], %[a]\n\t"
                   "ds_write_b64 %[gb], v[104:105]"
                   : [a] "=&v"(a), [lam] "=&v"(lam), [m1] "=&v"(m1), [x0] "=&v"(x0), [x1] "=&v"(x1), [x2] "=&v"(x2), [x3] "=&v"(x3), "=&{v[104:105]}"(gl)
                   : [d0] "v"(ov[0]), [t0] "v"(ov[1]), [d1] "v"(ov[2]), [t1] "v"(ov[3]), [sel] "s"(0x04000400u), [lsel] "v"(lsel), [ga] "v"(gaddr), [gb] "v"(gaddr2)
                   : "memory");
    }
    LDS_BARRIER();
    ST(9);
    if (fin) {
      int faddr = L_FP + ((lane & 7) * 4 + g) * 32 + v * 4;
      asm volatile("" : "+v"(faddr));
      if (SLEEP_S) __builtin_amdgcn_s_sleep(SLEEP_S);
      const u32x4 sv = poll16<255>(ws, WS_S2 + (cf * 64 + v * 8 + (lane >> 3)) * 1024 + bf * 512 + seg * 128 + (lane & 7) * 16, tag);
      ST(10);
      *(LAS float*)(lds + faddr) = sum_records(sv);
      LDS_BARRIER();
    }
  } else if (w < 6) {
    u32x4 wt[8], bw[8];
    if (WO_MODE == 3) {
      wait_ml();
      const bool now = __builtin_amdgcn_s_memrealtime() >= t_start + WO_AT;
      if (!now) {
        LDS_BARRIER();
        ST(6);
        while (__builtin_amdgcn_s_memrealtime() < t_start + WO_AT) __builtin_amdgcn_s_sleep(1);
      }
      load_wo(wt);
      if (now) { LDS_BARRIER(); ST(6); }
    } else {
    if (WO_MODE == 1) wait_ml();
    if (WO_MODE < 2) load_wo(wt);
    LDS_BARRIER();
    ST(6);
    if (WO_MODE == 2) { if (WO_SLEEP) __builtin_amdgcn_s_sleep(WO_SLEEP); load_wo(wt); }
    }
#pragma unroll
    for (int i = 0; i < 8; ++i) bw[i] = colfrag(wt, i);
    int gp = L_GP + (c & 7) * GP_STRIDE, go = gp + ((c >> 3) * 16 + g * 4) * 4;
    int soff = WS_S2 + (cb * 64 + kvh * 8 + rb) * 1024 + (g & 1) * 512 + ((w - 4) * 32 + c * 2 + (g >> 1)) * 8;
    asm volatile("" : "+v"(gp), "+v"(go), "+v"(soff) : "v"(bw[0]), "v"(bw[1]), "v"(bw[2]), "v"(bw[3]), "v"(bw[4]), "v"(bw[5]), "v"(bw[6]), "v"(bw[7]));
    ST(1);
    LDS_BARRIER();
    ST(9);
    f32x2 gl = *(LAS f32x2*)(lds + gp + 256);
    u32x4 of = *(LAS u32x4*)(lds + go), o1 = *(LAS u32x4*)(lds + go + 128);
#define QP1(r) "v_add_f32_dpp " r ", " r ", " r " quad_perm:[1,0,3,2] row_mask:0xf bank_mask:0xf\n\t"
#define QP2(r) "v_add_f32_dpp " r ", " r ", " r " quad_perm:[2,3,0,1] row_mask:0xf bank_mask:0xf\n\t"
#define PKM(r) "v_pk_mul_f32 " r ", v[118:119], " r " op_sel_hi:[0,1]\n\t"
    asm volatile("v_max_f32_dpp v118, v116, v116 quad_perm:[1,0,3,2] row_mask:0xf bank_mask:0xf\n\t"
                 "s_nop 1\n\t"
                 "v_max_f32_dpp v118, v118, v118 quad_perm:[2,3,0,1] row_mask:0xf bank_mask:0xf\n\t"
                 "v_sub_f32 v118, v116, v118\n\t"
                 "v_exp_f32 v118, v118\n\t"
                 "s_nop 0\n\t"
                 "v_mul_f32 v117, v118, v117\n\t"
                 PKM("v[108:109]") PKM("v[110:111]") PKM("v[112:113]") PKM("v[114:115]")
                 QP1("v117") QP1("v108") QP1("v109") QP1("v110") QP1("v111") QP1("v112") QP1("v113") QP1("v114") QP1("v115")
                 QP2("v117") QP2("v108") QP2("v109")
                 "v_rcp_f32 v118, v117\n\t"
                 QP2("v110") QP2("v111") QP2("v112") QP2("v113") QP2("v114") QP2("v115")
                 PKM("v[108:109]") PKM("v[110:111]") PKM("v[112:113]") PKM("v[114:115]")
                 "v_cvt_pk_bf16_f32 v108, v108, v109\n\t"
                 "v_cvt_pk_bf16_f32 v109, v110, v111\n\t"
                 "v_cvt_pk_bf16_f32 v110, v112, v113\n\t"
                 "v_cvt_pk_bf16_f32 v111, v114, v115\n\t"
                 "s_nop 1"
                 : "+{v[108:111]}"(of), "+{v[112:115]}"(o1), "+{v[116:117]}"(gl)
                 :
                 : "v118", "v119");
    f32x4 acc[8];
#pragma unroll
    for (int i = 0; i < 8; ++i) { const int k = (i & 3) * 2 + (i >> 2); acc[k] = mfma(of, bw[k], (f32x4){0.f, 0.f, 0.f, 0.f}); }
    __builtin_amdgcn_sched_barrier(0);
    float r[4];
#pragma unroll
    for (int d = 0; d < 4; ++d) r[d] = (g >> 1) ? acc[4 + d][0] : acc[d][0];
    stws1(ws, soff, pack2(half32(r[0], r[2]), half32(r[1], r[3]), tag));
    ST(10);
  } else if (w == 7 && mlp2) {
#pragma unroll 1
    for (int spin = 0; spin < SPIN_CAP; ++spin) {
      if (__all(tagged(p0, tag) && tagged(p1, tag))) break;
      __builtin_amdgcn_s_sleep(SLEEP_Q);
      asm volatile("" ::: "memory");
      p0 = ldws(ws, paoff); p1 = ldws(ws, paoff + 1024);
    }
    const float t2[2] = {half32(first2(p0[0], p0[1]) + first2(p1[0], p1[1]), first2(p0[2], p0[3]) + first2(p1[2], p1[3])),
                         half32(second2(p0[0], p0[1]) + second2(p1[0], p1[1]), second2(p0[2], p0[3]) + second2(p1[2], p1[3]))};
    float hv[2];
#pragma unroll
    for (int e = 0; e < 2; ++e) {
      const float t = rbf(rbf(t2[e]) + (e ? bfhi(b1v) : bflo(b1v)));
      const float uu = 0.7978845608028654f * (t + 0.044715f * t * t * t);
      hv[e] = t / (1.f + __builtin_amdgcn_exp2f(-2.f * LOG2E * uu));
    }
    *(LAS u32*)(lds + L_HS + (g & 1) * 128 + (c * 2 + (g >> 1)) * 4) = pk(hv[0], hv[1]);
    wait_flags(lds, F_W2, 1, lane);
    f32x4 r = {0.f, 0.f, 0.f, 0.f};
#pragma unroll
    for (int s = 0; s < 2; ++s) {
      const u32x4 ha = *(LAS u32x4*)(lds + L_HS + ((c >> 2) & 1) * 128 + s * 64 + g * 16);
      u32x4 w2t[8];
#pragma unroll
      for (int i = 0; i < 8; ++i) w2t[i] = *(LAS u32x4*)(lds + L_W2 + s * 8192 + i * 1024 + lane * 16);
#pragma unroll
      for (int d = 0; d < 8; ++d) r = mfma(sel4((c >> 3) == (d >> 2) && (c & 3) == (d & 3), ha), colfrag(w2t, d), r);
    }
    u32x2 m0 = pack2(r[0], r[1], tag), m1 = pack2(r[2], r[3], tag);
    asm volatile("" : "+v"(m0), "+v"(m1));
    ST(2);
    if (MP_LATE) LDS_BARRIER();
    stws(ws, WS_MP + (mlo * 28 + mhi) * 1024 + (g & 1) * 512 + (c * 2 + (g >> 1)) * 16, m0, m1);
    if (!MP_LATE) LDS_BARRIER();
    ST(6);
  } else if (w == 6 && fin) {
    const int col = cf * 128 + seg * 32 + (lane & 31);
    const u32 xv = ((const GAS u16*)x_)[bf * D + col], b2v = ((const GAS u16*)b2_)[col];
    LDS_BARRIER();
    ST(6);
    LDS_BARRIER();
    LAS float* mf = (LAS float*)(lds + L_MF + (lane >> 5) * 256 + (lane & 31) * 4);
    float t = mf[0] + mf[32];
    t = half32(t, t);
    t = rbf(rbf(t) + bflo(b2v));
    float gate = rbf(t / (1.f + __builtin_amdgcn_exp2f(-LOG2E * t))), xf = bflo(xv);
    int faddr = L_FP + (lane & 31) * 32 + (lane >> 5) * 16;
    GAS u16* op = (GAS u16*)out_ + bf * D + col;
    asm volatile("" : "+v"(gate), "+v"(xf), "+v"(faddr), "+v"(op));
    LDS_BARRIER();
    ST(10);
    const f32x4 fp = *(LAS f32x4*)(lds + faddr);
    float s = (fp[0] + fp[1]) + (fp[2] + fp[3]);
    s = half32(s, s);
    const float res = xf + rbf(rbf(s) * gate);
    if (lane < 32) __hip_atomic_store(op, (u16)pk(res, res), RLX_AGENT);
    if ((bid & 31) == 0 && lane == 0) __hip_atomic_store((GAS u32*)(ws_ + WS_EPOCH), tag >> 8, RLX_AGENT);
  } else {
    LDS_BARRIER();
    ST(6);
  }
  ST(11);
  {
    GAS u64* st = (GAS u64*)(ws_ + (WS_AT_END ? -(8 << 20) : (8 << 20)));
    volatile LAS u64* ls = (volatile LAS u64*)(lds + L_TOUCH);
    if (lane == 0) { ls[w * 2] = ts[PROBE]; ls[w * 2 + 1] = ts[PROBE_B]; }
    if (w == 0) {
      while (__builtin_amdgcn_s_memrealtime() < ts[0] + 2500) __builtin_amdgcn_s_sleep(1);
      u64 ma = ls[(lane & 15) * 2], mb = ls[(lane & 15) * 2 + 1];
      auto red = [&]() {
        for (int o = 32; o; o >>= 1) {
          const u64 x = __shfl_xor(ma, o), y = __shfl_xor(mb, o);
          ma = x > ma ? x : ma;
          if (PROBE_B == 0) mb = y < mb ? y : mb; else mb = y > mb ? y : mb;
        }
      };
      red();
      if (lane == 0) { __hip_atomic_store(st + bid * 2, ma, RLX_AGENT); __hip_atomic_store(st + bid * 2 + 1, mb, RLX_AGENT); }
      if (bid == 0) {
        while (__builtin_amdgcn_s_memrealtime() < ts[0] + 3200) __builtin_amdgcn_s_sleep(1);
        for (int i = lane; i < 256; i += 64) {
          const u64 x = __hip_atomic_load(st + i * 2, RLX_AGENT), y = __hip_atomic_load(st + i * 2 + 1, RLX_AGENT);
          ma = x > ma ? x : ma;
          if (PROBE_B == 0) mb = y < mb ? y : mb; else mb = y > mb ? y : mb;
        }
        red();
        const u64 until = ts[0] + 5000 + 10 * (ma - mb);
        while (__builtin_amdgcn_s_memrealtime() < until && __builtin_amdgcn_s_memrealtime() < ts[0] + 40000) __builtin_amdgcn_s_sleep(1);
      }
    }
  }
}

extern "C" void kernel_launch(void* const* d_in, const int* in_sizes, int n_in, void* d_out, int out_size, void* d_ws, size_t ws_size,
                              hipStream_t stream) {
  unsigned char* ws = (unsigned char*)d_ws + (WS_AT_END ? ((ws_size - WS_SPAN) & ~(size_t)4095) : 0);
  fused_decode<<<256, 1024, 0, stream>>>((const u16*)d_in[0], (const u16*)d_in[1], (const u16*)d_in[2], (const u16*)d_in[3],
                                         (const u16*)d_in[4], (const u16*)d_in[6], (const u16*)d_in[7], (const u16*)d_in[8],
                                         (const u16*)d_in[9], (u16*)d_out, ws
                                         );
}
```

```cpp
#define FINLAST 0
#define PRE_A 4
#define PRE_B 11
#include <hip/hip_runtime.h>
#include <stdint.h>

#define WS_AT_END 0
#define SLEEP_Q 8
#define SLEEP_O 8
#define SLEEP_S 8
#define WO_MODE 1
#define PRIO_K 1
#define ROT 1
#define CODE_LINES 0
#define RND 1
#define BUF 1
#define PREW 12
#define Q_DELAY 32
#define MP_LATE 0
#define SLEEP_M 4
#define WO_AT 1150
#define WO_SLEEP 0
#define ABL 0
#define STAMP 0

#define GAS __attribute__((address_space(1)))
#define LAS __attribute__((address_space(3)))
#define DI __device__ __forceinline__
#define RLX_AGENT __ATOMIC_RELAXED, __HIP_MEMORY_SCOPE_AGENT
typedef unsigned short u16;
typedef unsigned u32;
typedef unsigned long long u64;
typedef u32 u32x2 __attribute__((ext_vector_type(2)));
typedef u32 u32x4 __attribute__((ext_vector_type(4)));
typedef float f32x2 __attribute__((ext_vector_type(2)));
typedef float f32x4 __attribute__((ext_vector_type(4)));
typedef __bf16 bf16x2 __attribute__((ext_vector_type(2)));
typedef __bf16 bf16x8 __attribute__((ext_vector_type(8)));

constexpr int D = 512, SK = 8192, DH = 128, FF = 1792, NQ = 4096;
constexpr int SC1 = 16, NT = 2;
constexpr int SPIN_CAP = 1 << 18;
constexpr int NFIN = 32;
constexpr float LOG2E = 1.4426950408889634f;

constexpr int WS_EPOCH = 0;
constexpr int WS_QP = 256;
constexpr int WS_PA = WS_QP + 512 * 256;
constexpr int WS_OP = WS_PA + 112 * 512;
constexpr int WS_S2 = WS_OP + 256 * 2048;
constexpr int WS_MP = WS_S2 + 256 * 1024;
constexpr int WS_END = WS_MP + 112 * 1024;
constexpr int WS_SPAN = 4 << 20;
constexpr int WS_STAMP = 8 << 20;

constexpr int F_Q = 0, F_HALF = 4, F_W2 = 6;
constexpr int L_FLAG = 0, L_CNT = 48, L_XN = 64, L_HALF = L_XN + 8192, L_Q = L_HALF + 1024, L_MW = L_Q + 1024, L_LW = L_MW + 256,
              L_HS = L_LW + 256, L_GP = L_HS + 256, GP_STRIDE = 272, L_FP = L_GP + 8 * GP_STRIDE, L_MF = L_FP + 1024,
              L_TOUCH = L_MF + 512, L_T2 = L_TOUCH + 256, L_W2 = L_T2 + 256, L_OW = L_W2 + 16384, OW_STRIDE = 2064, L_END = L_OW + 16 * OW_STRIDE;

DI float bflo(u32 v) { return __builtin_bit_cast(float, v << 16); }
DI float bfhi(u32 v) { return __builtin_bit_cast(float, v & 0xffff0000u); }
DI float rbf(float f) { return (float)(__bf16)f; }
DI u32 pk(float a, float b) { return __builtin_bit_cast(u32, __builtin_convertvector((f32x2){a, b}, bf16x2)); }
DI u32 f2u(float f) { return __builtin_bit_cast(u32, f); }
DI float u2f(u32 u) { return __builtin_bit_cast(float, u); }
DI f32x4 mfma(u32x4 a, u32x4 b, f32x4 c) {
  return __builtin_amdgcn_mfma_f32_16x16x32_bf16(__builtin_bit_cast(bf16x8, a), __builtin_bit_cast(bf16x8, b), c, 0, 0, 0);
}
template <int CTRL> DI float dpp(float x) { return u2f(__builtin_amdgcn_mov_dpp(f2u(x), CTRL, 0xf, 0xf, true)); }
constexpr int XOR1 = 0xB1, XOR2 = 0x4E, ROR4 = 0x124, ROR8 = 0x128;
DI float half32(float a, float b) { auto r = __builtin_amdgcn_permlane32_swap(f2u(a), f2u(b), false, false); return u2f(r[0]) + u2f(r[1]); }
DI float half16(float a, float b) { auto r = __builtin_amdgcn_permlane16_swap(f2u(a), f2u(b), false, false); return u2f(r[0]) + u2f(r[1]); }
DI float max32(float a) { auto r = __builtin_amdgcn_permlane32_swap(f2u(a), f2u(a), false, false); return __builtin_amdgcn_fmed3f(u2f(r[0]), u2f(r[1]), __builtin_inff()); }
DI float max16(float a) { auto r = __builtin_amdgcn_permlane16_swap(f2u(a), f2u(a), false, false); return __builtin_amdgcn_fmed3f(u2f(r[0]), u2f(r[1]), __builtin_inff()); }
DI float half_sum(float v) {
  v += dpp<XOR1>(v); v += dpp<XOR2>(v); v += dpp<ROR4>(v); v += dpp<ROR8>(v);
  return half16(v, v);
}
DI u32 plo(u32 k1, u32 k0) { return __builtin_amdgcn_perm(k1, k0, 0x05040100u); }
DI u32 phi(u32 k1, u32 k0) { return __builtin_amdgcn_perm(k1, k0, 0x07060302u); }
DI u32x4 colfrag(const u32x4 (&r)[8], int d) {
  const int q = d >> 1;
  return (d & 1) ? (u32x4){phi(r[1][q], r[0][q]), phi(r[3][q], r[2][q]), phi(r[5][q], r[4][q]), phi(r[7][q], r[6][q])}
                 : (u32x4){plo(r[1][q], r[0][q]), plo(r[3][q], r[2][q]), plo(r[5][q], r[4][q]), plo(r[7][q], r[6][q])};
}
DI u32x4 sel4(bool c, u32x4 v) { return (u32x4){c ? v[0] : 0u, c ? v[1] : 0u, c ? v[2] : 0u, c ? v[3] : 0u}; }
template <typename T> DI T ldnt(const GAS T* p) { return __builtin_nontemporal_load(p); }
#define LAUX 16
#define SAUX 16
DI u32x4 ldws(__amdgpu_buffer_rsrc_t ws, int off) { return __builtin_amdgcn_raw_buffer_load_b128(ws, off, 0, LAUX); }
DI void stws(__amdgpu_buffer_rsrc_t ws, int off, u32x2 a, u32x2 b) { __builtin_amdgcn_raw_buffer_store_b128((u32x4){a[0], a[1], b[0], b[1]}, ws, off, 0, SAUX); }
DI void stws1(__amdgpu_buffer_rsrc_t ws, int off, u32x2 a) { __builtin_amdgcn_raw_buffer_store_b64(a, ws, off, 0, SAUX); }
DI u32x2 pack2(float a, float b, u32 tag8) {
  const u32 ar = f2u(a) + 0x800u;
  const u32 u = (f2u(b) & 0xf000u) | ((ar >> 4) & ~0xf000u);
  return (u32x2){phi(f2u(b), ar), __builtin_amdgcn_perm(tag8, u, 0x07060501u)};
}
DI float first2(u32 w0, u32 w1) { return u2f(__builtin_amdgcn_perm(w0, w1 << 4, 0x0504000cu)); }
DI float second2(u32 w0, u32 w1) { return u2f(__builtin_amdgcn_perm(w0, w1, 0x0706000cu)); }
DI bool tagged(u32x4 v, u32 tag8) { return ((v[1] ^ tag8) | (v[3] ^ tag8)) < 256u; }
#define PIN() do { asm volatile("" ::: "memory"); __builtin_amdgcn_sched_barrier(0); } while (0)
#define LDS_BARRIER() do { asm volatile("s_waitcnt lgkmcnt(0)\n\ts_barrier" ::: "memory"); __builtin_amdgcn_sched_barrier(0); } while (0)
#define RAW_BARRIER() do { __builtin_amdgcn_sched_barrier(0); asm volatile("s_barrier" ::: "memory"); __builtin_amdgcn_sched_barrier(0); } while (0)
DI void dma16(const u16* src, u32 dst) {
  u32 keep;
  asm volatile("s_mov_b32 %0, m0\n\ts_mov_b32 m0, %2\n\ts_nop 0\n\tglobal_load_lds_dwordx4 %1, off nt\n\ts_mov_b32 m0, %0"
               : "=&s"(keep) : "v"(src), "s"(dst) : "memory");
}
DI void touch(const void* src, u32 dst) {
  u32 keep;
  asm volatile("s_mov_b32 %0, m0\n\ts_mov_b32 m0, %2\n\ts_nop 0\n\tglobal_load_lds_dword %1, off\n\ts_mov_b32 m0, %0"
               : "=&s"(keep) : "v"(src), "s"(dst) : "memory");
}
DI void set_flag(LAS unsigned char* lds, int f) {
  asm volatile("s_waitcnt lgkmcnt(0)" ::: "memory");
  *(volatile LAS u32*)(lds + L_FLAG + f * 4) = 1u;
}
DI void wait_flags(LAS unsigned char* lds, int first, int mask, int lane) {
#pragma unroll 1
  for (int spin = 0; spin < (SPIN_CAP << 4); ++spin) {
    if (__all(*(volatile LAS u32*)(lds + L_FLAG + (first + (lane & mask)) * 4) != 0u)) break;
    __builtin_amdgcn_s_sleep(1);
  }
  PIN();
}
DI u32 read_tag(const unsigned char* ws) {
  u32 e;
  asm volatile("s_load_dword %0, %1, 0x0\n\ts_waitcnt lgkmcnt(0)" : "=s"(e) : "s"(ws) : "memory");
  return (e + 1u) << 8;
}
template <int LOW> DI u32x4 poll16(__amdgpu_buffer_rsrc_t ws, int off, u32 want) {
  u32x4 v;
  u64 keep;
  u32 left = SPIN_CAP, t;
  asm volatile("s_mov_b64 %[keep], exec\n\t"
               "s_branch 2f\n"
               "1:\n\t"
               "s_sub_u32 %[left], %[left], 1\n\t"
               "s_cbranch_scc1 3f\n"
               "2:\n\t"
               "buffer_load_dwordx4 v[100:103], %[off], %[ws], 0 offen sc1\n\t"
               "s_waitcnt vmcnt(0)\n\t"
               "v_bitop3_b32 %[t], v101, v103, %[want] bitop3:0x7e\n\t"
               "v_cmp_lt_u32 vcc, %[low], %[t]\n\t"
               "s_and_b64 exec, exec, vcc\n\t"
               "s_cbranch_scc1 1b\n"
               "3:\n\t"
               "s_mov_b64 exec, %[keep]"
               : "={v[100:103]}"(v), [keep] "=&s"(keep), [left] "+s"(left), [t] "=&v"(t)
               : [off] "v"(off), [ws] "s"(ws), [want] "s"(want), [low] "i"(LOW)
               : "vcc", "scc", "memory");
  return v;
}
DI void quad_sum4(float& a, float& b, float& c, float& d) {
  asm("s_nop 1\n\t"
      "v_add_f32_dpp %0, %0, %0 quad_perm:[1,0,3,2] row_mask:0xf bank_mask:0xf\n\t"
      "v_add_f32_dpp %1, %1, %1 quad_perm:[1,0,3,2] row_mask:0xf bank_mask:0xf\n\t"
      "v_add_f32_dpp %2, %2, %2 quad_perm:[1,0,3,2] row_mask:0xf bank_mask:0xf\n\t"
      "v_add_f32_dpp %3, %3, %3 quad_perm:[1,0,3,2] row_mask:0xf bank_mask:0xf\n\t"
      "v_add_f32_dpp %0, %0, %0 quad_perm:[2,3,0,1] row_mask:0xf bank_mask:0xf\n\t"
      "v_add_f32_dpp %1, %1, %1 quad_perm:[2,3,0,1] row_mask:0xf bank_mask:0xf\n\t"
      "v_add_f32_dpp %2, %2, %2 quad_perm:[2,3,0,1] row_mask:0xf bank_mask:0xf\n\t"
      "v_add_f32_dpp %3, %3, %3 quad_perm:[2,3,0,1] row_mask:0xf bank_mask:0xf"
      : "+v"(a), "+v"(b), "+v"(c), "+v"(d));
}
DI float sum_records(u32x4 v) {
  const float t = half16(half32(first2(v[0], v[1]), first2(v[2], v[3])), half32(second2(v[0], v[1]), second2(v[2], v[3])));
  return t + dpp<ROR8>(t);
}

DI void layernorm(u32 (&x)[8]) {
  float v[16], s = 0.f;
#pragma unroll
  for (int e = 0; e < 8; ++e) { v[2 * e] = bflo(x[e]); v[2 * e + 1] = bfhi(x[e]); s += v[2 * e] + v[2 * e + 1]; }
  const float mean = half_sum(s) * (1.f / 512.f);
  float q = 0.f;
#pragma unroll
  for (int e = 0; e < 16; ++e) { const float d = v[e] - mean; q = fmaf(d, d, q); }
  const float var = half_sum(q) * (1.f / 512.f);
  const float mb = rbf(mean), inv = __builtin_amdgcn_rcpf(rbf(__builtin_amdgcn_sqrtf(rbf(rbf(var) + 1e-5f))));
#pragma unroll
  for (int e = 0; e < 8; ++e) { const u32 d = pk(v[2 * e] - mb, v[2 * e + 1] - mb); x[e] = pk(bflo(d) * inv, bfhi(d) * inv); }
}

DI f32x2 tile_matvec(const u32x4 (&wv)[8], LAS unsigned char* xin, int g, int c) {
  const u32x4 xf = *(LAS u32x4*)(xin + ((c >> 2) & 1) * 1024 + ((c >> 3) * 32 + g * 8) * 2);
  f32x4 lo = {0.f, 0.f, 0.f, 0.f}, hi = lo;
#pragma unroll
  for (int d = 0; d < 4; ++d) {
    const u32x4 am = sel4((c & 3) == d, xf);
    lo = mfma(am, colfrag(wv, d), lo);
    hi = mfma(am, colfrag(wv, 4 + d), hi);
  }
  const bool valid = (g >> 1) == (c >> 3);
  float t[4];
#pragma unroll
  for (int e = 0; e < 4; ++e) { t[e] = half32(valid ? lo[e] : 0.f, valid ? hi[e] : 0.f); t[e] += dpp<ROR8>(t[e]); }
  return (c & 8) ? (f32x2){t[2], t[3]} : (f32x2){t[0], t[1]};
}

#define ST(i)

__global__ void __launch_bounds__(1024, 4)
fused_decode(const u16* x_, const u16* k_, const u16* v_, const u16* wq_, const u16* wo_, const u16* w1_, const u16* b1_,
             const u16* w2_, const u16* b2_, u16* out_, unsigned char* ws_
             ) {
  __shared__ __attribute__((aligned(16))) unsigned char lds_raw[L_END];
  LAS unsigned char* lds = (LAS unsigned char*)lds_raw;
  ST(0);
  const u64 t_start = WO_MODE == 3 ? __builtin_amdgcn_s_memrealtime() : 0;
  asm volatile("" :: "s"(x_), "s"(k_), "s"(v_), "s"(wq_), "s"(wo_), "s"(w1_), "s"(b1_), "s"(w2_), "s"(b2_), "s"(out_), "s"(ws_));
  const int tid = threadIdx.x, lane = tid & 63, w = __builtin_amdgcn_readfirstlane(tid >> 6);
  const int g = lane >> 4, c = lane & 15;
  const int bid = blockIdx.x, kvh = bid & 7, j = bid >> 3, b = j >> 4, sp = j & 15, rb = j >> 2, cb = j & 3;
  const bool mlp1 = j < 14, mlp2 = j >= 14 && j < 28;
  const __amdgpu_buffer_rsrc_t ws = __builtin_amdgcn_make_buffer_rsrc(ws_, 0, WS_END, 0x00020000);
  u32 tag;
  const u32 hsh = ((u32)(bid * 16 + w) * 2654435761u) >> 20, hb = ((u32)bid * 2654435761u) >> 16;
  const int wu = (w + (ROT == 0 ? 0 : ROT == 1 || ROT == 2 || ROT == 3 ? j : (int)hb)) & 15;
  const int ro = ROT == 2 ? kvh : ROT == 3 || ROT == 4 ? (int)(hsh & 7) : 0;
  const size_t kvseq = (size_t)(b * 8 + kvh) * SK * DH;
  const __amdgpu_buffer_rsrc_t kr = __builtin_amdgcn_make_buffer_rsrc((void*)(k_ + kvseq), 0, SK * DH * 2, 0x00020000);
  const __amdgpu_buffer_rsrc_t vr = __builtin_amdgcn_make_buffer_rsrc((void*)(v_ + kvseq), 0, SK * DH * 2, 0x00020000);
  const int kvoff = (sp * 16 + wu) * 8192;
  const int klane = ((((c >> 1) ^ ro) & 1) * 4 + (c >> 2)) * 256 + (g * 2 + (c & 1)) * 16;
  u32x4 kf[8], vf[8];

  const int cq = j & 7, kq = j >> 3;
  const int mt = (mlp1 ? j : j - 14) * 8 + kvh;
  const int mhi = mt >> 2, mlo = mt & 3;
  auto pretouch = [&](int what) __attribute__((always_inline)) {
    const unsigned char* p = ws_ + WS_OP + ((kvh * 2 + b) * 16 + sp) * 2048 + c * 128;
    bool on = lane < 16 && (what & 1);
    if ((lane >> 3) == 2) { p = ws_ + WS_S2 + (cb * 64 + kvh * 8 + rb) * 1024 + (lane & 7) * 128; on = what & 1; }
    if ((lane >> 3) == 3) { p = ws_ + WS_MP + (mlo * 28 + mhi) * 1024 + (lane & 7) * 128; on = mlp2 && (what & 2); }
    if ((lane >> 2) == 8) { p = ws_ + WS_QP + ((((lane >> 1) & 1) * 8 + kvh) * 32 + cq * 4 + kq) * 256 + (lane & 1) * 128; on = what & 4; }
    if ((lane >> 2) == 9) { p = ws_ + WS_PA + mt * 512 + (lane & 3) * 128; on = mlp1 && (what & 4); }
    if (lane == 40) { p = (const unsigned char*)(out_ + ((bid >> 2) & 1) * D + (bid & 3) * 128 + ((bid >> 3) & 3) * 32); on = bid < NFIN && (what & 8); }
    if (on) touch(p, (u32)(size_t)(lds + L_T2));
  };
  if (w < 2 || (w < 4 && mlp1)) {
    __builtin_amdgcn_s_setprio(3);
    if (w == 0 && lane < 16) *(LAS u32*)(lds + L_FLAG + lane * 4) = 0u;
    const GAS u32x4* xp = (const GAS u32x4*)x_ + lane * 2;
    const u32x4 x0 = xp[0], x1 = xp[1];
    PIN();
    const int rs = w < 2 ? NQ / 8 : FF / 8, row0 = (w < 2 ? kq : mlo) * 128 + (w & 1) * 64;
    const u16* tile = w < 2 ? wq_ + kvh * 512 + cq * 64 : w1_ + mhi * 64;
    const GAS u32x4* wp = (const GAS u32x4*)tile + (row0 + (c >> 3) * 32 + g * 8) * rs + (c & 7);
    u32x4 wv[8];
#pragma unroll
    for (int i = 0; i < 8; ++i) wv[i] = ldnt(wp + i * rs);
    PIN();
    LDS_BARRIER();
    tag = read_tag(ws_);
    PIN();
    u32 xs[8] = {x0[0], x0[1], x0[2], x0[3], x1[0], x1[1], x1[2], x1[3]};
    layernorm(xs);
    if (w >= 2) layernorm(xs);
    LAS unsigned char* xl = lds + L_XN + w * 2048;
    *(LAS u32x4*)(xl + lane * 32) = (u32x4){xs[0], xs[1], xs[2], xs[3]};
    *(LAS u32x4*)(xl + lane * 32 + 16) = (u32x4){xs[4], xs[5], xs[6], xs[7]};
    f32x2 r = tile_matvec(wv, xl + row0 * 2, g, c);
    LAS f32x2* half = (LAS f32x2*)(lds + L_HALF + (w >> 1) * 512 + lane * 8);
    if (w & 1) {
      *half = r;
      set_flag(lds, F_HALF + (w >> 1));
    } else {
      wait_flags(lds, F_HALF + (w >> 1), 0, lane);
      r += *half;
      const int rec = w == 0 ? WS_QP + ((((g & 1) * 8 + kvh) * 8 + cq) * 4 + kq) * 256 : WS_PA + mt * 512 + (g & 1) * 256;
      stws1(ws, rec + ((c & 7) * 4 + (g >> 1) * 2 + (c >> 3)) * 8, pack2(r[0], r[1], tag));
    }
    ST(2);
    __builtin_amdgcn_s_setprio(PRIO_K);
  } else {
    if (w < 4 && mlp2) {
      const u16* p = w2_ + (mhi * 64 + (w & 1) * 32 + g * 8) * D + mlo * 128 + c * 8;
#pragma unroll
      for (int i = 0; i < 8; ++i) dma16(p + i * D, (u32)(size_t)(lds + L_W2 + (w & 1) * 8192 + i * 1024));
    }
    if (CODE_LINES && w == 4) {
      const unsigned char* pc;
      asm volatile("s_getpc_b64 %0" : "=s"(pc));
      if (lane < 4 && j * 4 + lane < CODE_LINES) touch(pc + (j * 4 + lane) * 128, (u32)(size_t)(lds + L_TOUCH));
    }
    if (PRE_A && w == PREW) pretouch(PRE_A);
    RAW_BARRIER();
    if (PRE_B && w == PREW + 1) pretouch(PRE_B);
    if ((w >> 2) == 1) __builtin_amdgcn_s_setprio(2); else __builtin_amdgcn_s_setprio(PRIO_K);
  }
  PIN();

#pragma unroll
  for (int i = 0; i < 8; ++i) { kf[i] = BUF ? __builtin_amdgcn_raw_buffer_load_b128(kr, klane + (i & 1) * 128, kvoff + ((i >> 1) ^ (ro >> 1)) * 2048, NT) : ldnt((const GAS u32x4*)((const GAS char*)(k_ + kvseq) + kvoff + klane + (i & 1) * 128 + (i >> 1) * 2048)); __builtin_amdgcn_sched_barrier(0); }
  if ((w >> 2) != 1) __builtin_amdgcn_s_setprio(0);
  PIN();
#pragma unroll
  for (int i = 0; i < 8; ++i) { vf[i] = BUF ? __builtin_amdgcn_raw_buffer_load_b128(vr, lane * 16, kvoff + (i ^ ro) * 1024, NT) : ldnt((const GAS u32x4*)((const GAS char*)(v_ + kvseq) + kvoff + lane * 16 + i * 1024)); __builtin_amdgcn_sched_barrier(0); }
  PIN();
  ST(4);
  tag = read_tag(ws_);
  const int paoff = WS_PA + mhi * 2048 + lane * 16;
  u32x4 p0, p1;
  u32 b1v;
  if ((w >> 2) == 1) {
    const int u = w & 3;
    const int qoff = WS_QP + (b * 8 + kvh) * 8192 + u * 2048 + lane * 16;
    asm volatile("s_waitcnt vmcnt(0)" ::: "memory");
    if (Q_DELAY) __builtin_amdgcn_s_sleep(Q_DELAY);
    u32x4 q0, q1;
#pragma unroll 1
    for (int spin = 0; spin < SPIN_CAP; ++spin) {
      q0 = ldws(ws, qoff); q1 = ldws(ws, qoff + 1024);
      if (__all(tagged(q0, tag) && tagged(q1, tag))) break;
      __builtin_amdgcn_s_sleep(SLEEP_Q);
      asm volatile("" ::: "memory");
    }
    const float qa = half16(half32(first2(q0[0], q0[1]), first2(q1[0], q1[1])), half32(first2(q0[2], q0[3]), first2(q1[2], q1[3])));
    const float qb = half16(half32(second2(q0[0], q0[1]), second2(q1[0], q1[1])), half32(second2(q0[2], q0[3]), second2(q1[2], q1[3])));
    *(LAS u32*)(lds + L_Q + u * 256 + ((g >> 1) * 32 + c * 2 + (g & 1)) * 4) = pk(qa, qb);
    set_flag(lds, F_Q + u);
    ST(3);
    if (w == 7 && mlp2) {
      p0 = ldws(ws, paoff); p1 = ldws(ws, paoff + 1024);
      b1v = *(const GAS u32*)(b1_ + mhi * 64 + c * 4 + (g >> 1) * 2);
      PIN();
    }
    __builtin_amdgcn_s_setprio(0);
  }
  wait_flags(lds, F_Q, 3, lane);

  float sc[8];
  {
    const int x0 = (c >> 2) & 1;
    const u32x4 q0 = *(LAS u32x4*)(lds + L_Q + (c & 3) * 256 + (g * 2 + x0) * 16);
    const u32x4 q1 = *(LAS u32x4*)(lds + L_Q + (c & 3) * 256 + 128 + (g * 2 + x0) * 16);
#pragma unroll
    for (int o = 0; o < 4; ++o) {
      f32x4 s = mfma(kf[2 * o], q0, (f32x4){0.f, 0.f, 0.f, 0.f});
      s = mfma(kf[2 * o + 1], q1, s);
#pragma unroll
      for (int a = 0; a < 2; ++a) {
        float e1 = s[2 * a + 1];
        asm("" : "+v"(e1));
        const float t = x0 ? e1 : s[2 * a];
        sc[2 * o + a] = t + dpp<ROR4>(t);
      }
      const u32 d = pk(sc[2 * o], sc[2 * o + 1]), e = pk(bflo(d) * 0.08838834764831845f, bfhi(d) * 0.08838834764831845f);
      sc[2 * o] = bflo(e); sc[2 * o + 1] = bfhi(e);
    }
  }
  PIN();

    float m = fmaxf(fmaxf(fmaxf(sc[0], sc[1]), fmaxf(sc[2], sc[3])), fmaxf(fmaxf(sc[4], sc[5]), fmaxf(sc[6], sc[7])));
    m = max32(max16(m)) * LOG2E;
    float p[8], l = 0.f;
#pragma unroll
    for (int e = 0; e < 8; ++e) { p[e] = __builtin_amdgcn_exp2f(fmaf(sc[e], LOG2E, -m)); l += p[e]; }
    l = half16(l, l); l = half32(l, l);
    if (lane < 4) { *(LAS float*)(lds + L_MW + (lane * 16 + w) * 4) = m; *(LAS float*)(lds + L_LW + (lane * 16 + w) * 4) = l; }
    PIN();
    if ((w >> 1) == 1) set_flag(lds, F_W2 + (w & 1));
    if (lane == 0) __hip_atomic_fetch_add((LAS u32*)(lds + L_CNT), 1u, __ATOMIC_RELAXED, __HIP_MEMORY_SCOPE_WORKGROUP);
    const u32x4 pf = {pk(p[0], p[1]), pk(p[2], p[3]), pk(p[4], p[5]), pk(p[6], p[7])};
    u32x4 am[4];
#pragma unroll
    for (int u = 0; u < 4; ++u) am[u] = sel4((c >> 2) == u, pf);
    PIN();
    u32x4 ve[4], vo[4];
    u32 slo, shi;
    asm volatile("s_mov_b32 %0, 0x05040100\n\ts_mov_b32 %1, 0x07060302" : "=s"(slo), "=s"(shi) : "v"(am[0][0]), "v"(am[1][1]), "v"(am[2][2]), "v"(am[3][3]));
#pragma unroll
    for (int r = 0; r < 4; ++r) {
#pragma unroll
      for (int u = 0; u < 4; ++u) {
        ve[u][r] = __builtin_amdgcn_perm(vf[2 * r + 1][u], vf[2 * r][u], slo);
        vo[u][r] = __builtin_amdgcn_perm(vf[2 * r + 1][u], vf[2 * r][u], shi);
      }
      if (r < 3)
        asm volatile("" : "+s"(slo), "+s"(shi) : "v"(ve[0][r]), "v"(ve[1][r]), "v"(ve[2][r]), "v"(ve[3][r]), "v"(vo[0][r]), "v"(vo[1][r]), "v"(vo[2][r]), "v"(vo[3][r]));
    }
    f32x4 oe = {0.f, 0.f, 0.f, 0.f}, oo = oe;
#pragma unroll
    for (int u = 0; u < 4; ++u) { oe = mfma(am[u], ve[u], oe); oo = mfma(am[u], vo[u], oo); }
#pragma unroll
    for (int h = 0; h < 4; ++h) *(LAS f32x2*)(lds + L_OW + w * OW_STRIDE + h * 512 + c * 32 + g * 8) = (f32x2){oe[h], oo[h]};
    PIN();
    ST(5);

  const bool gath = (w & 4) == 0;
  const int v = (w & 3) + (w >> 3) * 4;
  auto wait_ml = [&]() __attribute__((always_inline)) {
#pragma unroll 1
    for (int spin = 0; spin < (SPIN_CAP << 4); ++spin) {
      if (__builtin_amdgcn_readfirstlane(*(volatile LAS u32*)(lds + L_CNT)) == 16u) break;
      __builtin_amdgcn_s_sleep(1);
    }
    PIN();
  };
  auto load_wo = [&](u32x4 (&wt)[8]) __attribute__((always_inline)) {
    const GAS u16* p = (const GAS u16*)wo_ + (size_t)(kvh * 512 + rb * 64 + g * 4) * D + cb * 128 + (w - 4) * 64 + c * 4;
#pragma unroll
    for (int i = 0; i < 8; ++i) {
      const u32x2 t0 = ldnt((const GAS u32x2*)(p + (size_t)(32 * (i >> 2) + (i & 3)) * D));
      const u32x2 t1 = ldnt((const GAS u32x2*)(p + (size_t)(32 * (i >> 2) + 16 + (i & 3)) * D));
      wt[i] = (u32x4){t0[0], t0[1], t1[0], t1[1]};
    }
    PIN();
  };
  const bool fin = FINLAST ? bid >= 256 - NFIN : bid < NFIN;
  const int cf = bid & 3, bf = (bid >> 2) & 1, seg = (bid >> 3) & 3;
  if (w >= 12) {
    LDS_BARRIER();
    ST(6);
    if (fin) {
      const int row = (w - 12) * 8 + (lane >> 3);
      const int moff = WS_MP + (cf * 28 + min(row, 27)) * 1024 + bf * 512 + seg * 128 + (lane & 7) * 16;
      u32x4 mv;
#pragma unroll 1
      for (int spin = 0; spin < SPIN_CAP; ++spin) {
        mv = ldws(ws, moff);
        if (__all(tagged(mv, tag))) break;
        __builtin_amdgcn_s_sleep(SLEEP_M);
        asm volatile("" ::: "memory");
      }
      const float t = sum_records(sel4(row < 28, mv));
      if ((lane & 8) == 0) *(LAS float*)(lds + L_MF + (w - 12) * 128 + ((lane & 7) * 4 + g) * 4) = t;
      LDS_BARRIER();
    }
  } else if (gath) {
    {
      const int f = v * 16 + (lane >> 2), p4 = lane & 3, hd = v >> 1;
      wait_ml();
      const f32x4 mv = *(LAS f32x4*)(lds + L_MW + hd * 64 + p4 * 16), lv = *(LAS f32x4*)(lds + L_LW + hd * 64 + p4 * 16);
      float M = fmaxf(fmaxf(mv[0], mv[1]), fmaxf(mv[2], mv[3]));
      M = fmaxf(M, dpp<XOR1>(M)); M = fmaxf(M, dpp<XOR2>(M));
      float L = 0.f, fw[4];
#pragma unroll
      for (int i = 0; i < 4; ++i) { fw[i] = __builtin_amdgcn_exp2f(mv[i] - M); L = fmaf(fw[i], lv[i], L); }
      L += dpp<XOR1>(L); L += dpp<XOR2>(L);
      const float inv = __builtin_amdgcn_rcpf(L);
#pragma unroll
      for (int i = 0; i < 4; ++i) fw[i] *= inv;
      const u32 lam = f2u(M + __builtin_amdgcn_logf(L)) >> ((lane >> 5) * 16);
      u32 t0 = tag | (lam & 255u), t1 = tag | ((lam >> 8) & 255u);
      int ooff = WS_OP + ((kvh * 2 + b) * 16 + sp) * 2048 + f * 16;
      int laddr = L_OW + p4 * 4 * OW_STRIDE + f * 16;
      asm volatile("" : "+v"(fw[0]), "+v"(fw[1]), "+v"(fw[2]), "+v"(fw[3]), "+v"(t0), "+v"(t1), "+v"(ooff), "+v"(laddr));
      ST(3);
      LDS_BARRIER();
      ST(6);
      f32x4 s = fw[0] * *(LAS f32x4*)(lds + laddr);
#pragma unroll
      for (int i = 1; i < 4; ++i) s += fw[i] * *(LAS f32x4*)(lds + laddr + i * OW_STRIDE);
      float s0 = s[0], s1 = s[1], s2 = s[2], s3 = s[3];
      quad_sum4(s0, s1, s2, s3);
      if (p4 == 0) __builtin_amdgcn_raw_buffer_store_b128((u32x4){pk(s0, s1), t0, pk(s2, s3), t1}, ws, ooff, 0, SAUX);
    }
    PIN();
    ST(7);
    {
      u32 gaddr2 = (u32)(size_t)(lds + L_GP + v * GP_STRIDE + 256), gaddr = gaddr2 - 256 + (c * 4 + g) * 4;
      u32 lsel = (c & 8) ? 0x05040100u : 0x01000504u;
      asm volatile("" : "+v"(gaddr), "+v"(gaddr2), "+v"(lsel));
      if (SLEEP_O) __builtin_amdgcn_s_sleep(SLEEP_O);
      const u32x4 ov = poll16<255>(ws, WS_OP + (kvh * 32 + v * 4 + g) * 2048 + rb * 256 + c * 16, tag);
      ST(8);
      u32 a, lam, m1, x0, x1, x2, x3;
      f32x2 gl;
      asm volatile("v_perm_b32 %[a], %[t1], %[t0], %[sel]\n\t"
                   "v_lshlrev_b32 %[x0], 16, %[d0]\n\t"
                   "v_lshlrev_b32 %[x2], 16, %[d1]\n\t"
                   "v_mov_b32_dpp %[m1], %[a] row_ror:8 row_mask:0xf bank_mask:0xf\n\t"
                   "v_and_b32 %[x1], 0xffff0000, %[d0]\n\t"
                   "v_perm_b32 %[lam], %[a], %[m1], %[lsel]\n\t"
                   "v_mov_b32 %[m1], %[lam]\n\t"
                   "v_mov_b32 %[a], %[lam]\n\t"
                   "v_and_b32 %[x3], 0xffff0000, %[d1]\n\t"
                   "s_nop 0\n\t"
                   "v_permlane16_swap_b32 %[m1], %[a]\n\t"
                   "v_max_f32 %[m1], %[m1], %[a]\n\t"
                   "v_mov_b32 %[a], %[m1]\n\t"
                   "s_nop 1\n\t"
                   "v_permlane32_swap_b32 %[m1], %[a]\n\t"
                   "v_max_f32 v104, %[m1], %[a]\n\t"
                   "v_sub_f32 %[lam], %[lam], v104\n\t"
                   "v_exp_f32 %[lam], %[lam]\n\t"
                   "s_nop 0\n\t"
                   "v_mul_f32 %[x0], %[lam], %[x0]\n\t"
                   "v_mul_f32 %[x2], %[lam], %[x2]\n\t"
                   "v_mul_f32 %[x1], %[lam], %[x1]\n\t"
                   "v_mul_f32 %[x3], %[lam], %[x3]\n\t"
                   "v_mov_b32 %[a], %[lam]\n\t"
                   "v_permlane32_swap_b32 %[x0], %[x2]\n\t"
                   "v_permlane32_swap_b32 %[x1], %[x3]\n\t"
                   "v_permlane16_swap_b32 %[lam], %[a]\n\t"
                   "v_add_f32 %[x0], %[x0], %[x2]\n\t"
                   "v_add_f32 %[x1], %[x1], %[x3]\n\t"
                   "v_add_f32 %[lam], %[lam], %[a]\n\t"
                   "v_mov_b32 %[a], %[lam]\n\t"
                   "v_permlane16_swap_b32 %[x0], %[x1]\n\t"
                   "v_add_f32 %[x0], %[x0], %[x1]\n\t"
                   "v_permlane32_swap_b32 %[lam], %[a]\n\t"
                   "ds_write_b32 %[ga], %[x0]\n\t"
                   "v_add_f32 v105, %[lam], %[a]\n\t"
                   "ds_write_b64 %[gb], v[104:105]"
                   : [a] "=&v"(a), [lam] "=&v"(lam), [m1] "=&v"(m1), [x0] "=&v"(x0), [x1] "=&v"(x1), [x2] "=&v"(x2), [x3] "=&v"(x3), "=&{v[104:105]}"(gl)
                   : [d0] "v"(ov[0]), [t0] "v"(ov[1]), [d1] "v"(ov[2]), [t1] "v"(ov[3]), [sel] "s"(0x04000400u), [lsel] "v"(lsel), [ga] "v"(gaddr), [gb] "v"(gaddr2)
                   : "memory");
    }
    LDS_BARRIER();
    ST(9);
    if (fin) {
      int faddr = L_FP + ((lane & 7) * 4 + g) * 32 + v * 4;
      asm volatile("" : "+v"(faddr));
      if (SLEEP_S) __builtin_amdgcn_s_sleep(SLEEP_S);
      const u32x4 sv = poll16<255>(ws, WS_S2 + (cf * 64 + v * 8 + (lane >> 3)) * 1024 + bf * 512 + seg * 128 + (lane & 7) * 16, tag);
      ST(10);
      *(LAS float*)(lds + faddr) = sum_records(sv);
      LDS_BARRIER();
    }
  } else if (w < 6) {
    u32x4 wt[8], bw[8];
    if (WO_MODE == 3) {
      wait_ml();
      const bool now = __builtin_amdgcn_s_memrealtime() >= t_start + WO_AT;
      if (!now) {
        LDS_BARRIER();
        ST(6);
        while (__builtin_amdgcn_s_memrealtime() < t_start + WO_AT) __builtin_amdgcn_s_sleep(1);
      }
      load_wo(wt);
      if (now) { LDS_BARRIER(); ST(6); }
    } else {
    if (WO_MODE == 1) wait_ml();
    if (WO_MODE < 2) load_wo(wt);
    LDS_BARRIER();
    ST(6);
    if (WO_MODE == 2) { if (WO_SLEEP) __builtin_amdgcn_s_sleep(WO_SLEEP); load_wo(wt); }
    }
#pragma unroll
    for (int i = 0; i < 8; ++i) bw[i] = colfrag(wt, i);
    int gp = L_GP + (c & 7) * GP_STRIDE, go = gp + ((c >> 3) * 16 + g * 4) * 4;
    int soff = WS_S2 + (cb * 64 + kvh * 8 + rb) * 1024 + (g & 1) * 512 + ((w - 4) * 32 + c * 2 + (g >> 1)) * 8;
    asm volatile("" : "+v"(gp), "+v"(go), "+v"(soff) : "v"(bw[0]), "v"(bw[1]), "v"(bw[2]), "v"(bw[3]), "v"(bw[4]), "v"(bw[5]), "v"(bw[6]), "v"(bw[7]));
    ST(1);
    LDS_BARRIER();
    ST(9);
    f32x2 gl = *(LAS f32x2*)(lds + gp + 256);
    u32x4 of = *(LAS u32x4*)(lds + go), o1 = *(LAS u32x4*)(lds + go + 128);
#define QP1(r) "v_add_f32_dpp " r ", " r ", " r " quad_perm:[1,0,3,2] row_mask:0xf bank_mask:0xf\n\t"
#define QP2(r) "v_add_f32_dpp " r ", " r ", " r " quad_perm:[2,3,0,1] row_mask:0xf bank_mask:0xf\n\t"
#define PKM(r) "v_pk_mul_f32 " r ", v[118:119], " r " op_sel_hi:[0,1]\n\t"
    asm volatile("v_max_f32_dpp v118, v116, v116 quad_perm:[1,0,3,2] row_mask:0xf bank_mask:0xf\n\t"
                 "s_nop 1\n\t"
                 "v_max_f32_dpp v118, v118, v118 quad_perm:[2,3,0,1] row_mask:0xf bank_mask:0xf\n\t"
                 "v_sub_f32 v118, v116, v118\n\t"
                 "v_exp_f32 v118, v118\n\t"
                 "s_nop 0\n\t"
                 "v_mul_f32 v117, v118, v117\n\t"
                 PKM("v[108:109]") PKM("v[110:111]") PKM("v[112:113]") PKM("v[114:115]")
                 QP1("v117") QP1("v108") QP1("v109") QP1("v110") QP1("v111") QP1("v112") QP1("v113") QP1("v114") QP1("v115")
                 QP2("v117") QP2("v108") QP2("v109")
                 "v_rcp_f32 v118, v117\n\t"
                 QP2("v110") QP2("v111") QP2("v112") QP2("v113") QP2("v114") QP2("v115")
                 PKM("v[108:109]") PKM("v[110:111]") PKM("v[112:113]") PKM("v[114:115]")
                 "v_cvt_pk_bf16_f32 v108, v108, v109\n\t"
                 "v_cvt_pk_bf16_f32 v109, v110, v111\n\t"
                 "v_cvt_pk_bf16_f32 v110, v112, v113\n\t"
                 "v_cvt_pk_bf16_f32 v111, v114, v115\n\t"
                 "s_nop 1"
                 : "+{v[108:111]}"(of), "+{v[112:115]}"(o1), "+{v[116:117]}"(gl)
                 :
                 : "v118", "v119");
    f32x4 acc[8];
#pragma unroll
    for (int i = 0; i < 8; ++i) { const int k = (i & 3) * 2 + (i >> 2); acc[k] = mfma(of, bw[k], (f32x4){0.f, 0.f, 0.f, 0.f}); }
    __builtin_amdgcn_sched_barrier(0);
    float r[4];
#pragma unroll
    for (int d = 0; d < 4; ++d) r[d] = (g >> 1) ? acc[4 + d][0] : acc[d][0];
    stws1(ws, soff, pack2(half32(r[0], r[2]), half32(r[1], r[3]), tag));
    ST(10);
  } else if (w == 7 && mlp2) {
#pragma unroll 1
    for (int spin = 0; spin < SPIN_CAP; ++spin) {
      if (__all(tagged(p0, tag) && tagged(p1, tag))) break;
      __builtin_amdgcn_s_sleep(SLEEP_Q);
      asm volatile("" ::: "memory");
      p0 = ldws(ws, paoff); p1 = ldws(ws, paoff + 1024);
    }
    const float t2[2] = {half32(first2(p0[0], p0[1]) + first2(p1[0], p1[1]), first2(p0[2], p0[3]) + first2(p1[2], p1[3])),
                         half32(second2(p0[0], p0[1]) + second2(p1[0], p1[1]), second2(p0[2], p0[3]) + second2(p1[2], p1[3]))};
    float hv[2];
#pragma unroll
    for (int e = 0; e < 2; ++e) {
      const float t = rbf(rbf(t2[e]) + (e ? bfhi(b1v) : bflo(b1v)));
      const float uu = 0.7978845608028654f * (t + 0.044715f * t * t * t);
      hv[e] = t / (1.f + __builtin_amdgcn_exp2f(-2.f * LOG2E * uu));
    }
    *(LAS u32*)(lds + L_HS + (g & 1) * 128 + (c * 2 + (g >> 1)) * 4) = pk(hv[0], hv[1]);
    wait_flags(lds, F_W2, 1, lane);
    f32x4 r = {0.f, 0.f, 0.f, 0.f};
#pragma unroll
    for (int s = 0; s < 2; ++s) {
      const u32x4 ha = *(LAS u32x4*)(lds + L_HS + ((c >> 2) & 1) * 128 + s * 64 + g * 16);
      u32x4 w2t[8];
#pragma unroll
      for (int i = 0; i < 8; ++i) w2t[i] = *(LAS u32x4*)(lds + L_W2 + s * 8192 + i * 1024 + lane * 16);
#pragma unroll
      for (int d = 0; d < 8; ++d) r = mfma(sel4((c >> 3) == (d >> 2) && (c & 3) == (d & 3), ha), colfrag(w2t, d), r);
    }
    u32x2 m0 = pack2(r[0], r[1], tag), m1 = pack2(r[2], r[3], tag);
    asm volatile("" : "+v"(m0), "+v"(m1));
    ST(2);
    if (MP_LATE) LDS_BARRIER();
    stws(ws, WS_MP + (mlo * 28 + mhi) * 1024 + (g & 1) * 512 + (c * 2 + (g >> 1)) * 16, m0, m1);
    if (!MP_LATE) LDS_BARRIER();
    ST(6);
  } else if (w == 6 && fin) {
    const int col = cf * 128 + seg * 32 + (lane & 31);
    const u32 xv = ((const GAS u16*)x_)[bf * D + col], b2v = ((const GAS u16*)b2_)[col];
    LDS_BARRIER();
    ST(6);
    LDS_BARRIER();
    LAS float* mf = (LAS float*)(lds + L_MF + (lane >> 5) * 256 + (lane & 31) * 4);
    float t = mf[0] + mf[32];
    t = half32(t, t);
    t = rbf(rbf(t) + bflo(b2v));
    float gate = rbf(t / (1.f + __builtin_amdgcn_exp2f(-LOG2E * t))), xf = bflo(xv);
    int faddr = L_FP + (lane & 31) * 32 + (lane >> 5) * 16;
    GAS u16* op = (GAS u16*)out_ + bf * D + col;
    asm volatile("" : "+v"(gate), "+v"(xf), "+v"(faddr), "+v"(op));
    LDS_BARRIER();
    ST(10);
    const f32x4 fp = *(LAS f32x4*)(lds + faddr);
    float s = (fp[0] + fp[1]) + (fp[2] + fp[3]);
    s = half32(s, s);
    const float res = xf + rbf(rbf(s) * gate);
    if (lane < 32) __hip_atomic_store(op, (u16)pk(res, res), RLX_AGENT);
    if ((bid & 31) == 0 && lane == 0) __hip_atomic_store((GAS u32*)(ws_ + WS_EPOCH), tag >> 8, RLX_AGENT);
  } else {
    LDS_BARRIER();
    ST(6);
  }
  ST(11);
}

extern "C" void kernel_launch(void* const* d_in, const int* in_sizes, int n_in, void* d_out, int out_size, void* d_ws, size_t ws_size,
                              hipStream_t stream) {
  unsigned char* ws = (unsigned char*)d_ws + (WS_AT_END ? ((ws_size - WS_SPAN) & ~(size_t)4095) : 0);
  fused_decode<<<256, 1024, 0, stream>>>((const u16*)d_in[0], (const u16*)d_in[1], (const u16*)d_in[2], (const u16*)d_in[3],
                                         (const u16*)d_in[4], (const u16*)d_in[6], (const u16*)d_in[7], (const u16*)d_in[8],
                                         (const u16*)d_in[9], (u16*)d_out, ws
                                         );
}
```

```cpp
#define STAMP 1
#define PROBE 11
#define PROBE_B 0
#define STMASK (1 | (1 << 11) | (1 << 0))
#define FINLAST 0
#define PRE_D 25
#define WO_V 1
#include <hip/hip_runtime.h>
#include <stdint.h>

#define WS_AT_END 0
#define SLEEP_Q 8
#define SLEEP_O 8
#define SLEEP_S 8
#define WO_MODE 1
#define PRIO_K 1
#define ROT 1
#define CODE_LINES 0
#define RND 1
#define BUF 1
#define SPRE 0
#define TV 0
#define PRE_C 0
#define SLOTS 4
#define SLOT0 0
#define OSAUX 16
#define OPNT 0
#define PRE_E 0
#define PRE_AT 16u
#define PRE_A 0
#define PRE_B 0
#define PREW 12
#define Q_DELAY 32
#define MP_LATE 0
#define SLEEP_M 4
#define WO_AT 1150
#define WO_SLEEP 0
#define ABL 0

#define GAS __attribute__((address_space(1)))
#define LAS __attribute__((address_space(3)))
#define DI __device__ __forceinline__
#define RLX_AGENT __ATOMIC_RELAXED, __HIP_MEMORY_SCOPE_AGENT
typedef unsigned short u16;
typedef unsigned u32;
typedef unsigned long long u64;
typedef u32 u32x2 __attribute__((ext_vector_type(2)));
typedef u32 u32x4 __attribute__((ext_vector_type(4)));
typedef float f32x2 __attribute__((ext_vector_type(2)));
typedef float f32x4 __attribute__((ext_vector_type(4)));
typedef __bf16 bf16x2 __attribute__((ext_vector_type(2)));
typedef __bf16 bf16x8 __attribute__((ext_vector_type(8)));

constexpr int D = 512, SK = 8192, DH = 128, FF = 1792, NQ = 4096;
constexpr int SC1 = 16, NT = 2;
constexpr int SPIN_CAP = 1 << 18;
constexpr int NFIN = 32;
constexpr float LOG2E = 1.4426950408889634f;

constexpr int WS_EPOCH = 0;
constexpr int WS_QP = 256;
constexpr int WS_PA = WS_QP + 512 * 256;
constexpr int WS_OP = WS_PA + 112 * 512;
constexpr int WS_S2 = WS_OP + 256 * 2048;
constexpr int WS_MP = WS_S2 + 256 * 1024;
constexpr int WS_END = WS_MP + 112 * 1024;
constexpr int WS_SPAN = 4 << 20;
constexpr int WS_STAMP = 8 << 20;

constexpr int F_Q = 0, F_HALF = 4, F_W2 = 6;
constexpr int L_FLAG = 0, L_CNT = 48, L_XN = 64, L_HALF = L_XN + 8192, L_Q = L_HALF + 1024, L_MW = L_Q + 1024, L_LW = L_MW + 256,
              L_HS = L_LW + 256, L_GP = L_HS + 256, GP_STRIDE = 272, L_FP = L_GP + 8 * GP_STRIDE, L_MF = L_FP + 1024,
              L_TOUCH = L_MF + 512, L_T2 = L_TOUCH + 256, L_W2 = L_T2 + 256, L_OW = L_W2 + 16384, OW_STRIDE = 2064, L_END = L_OW + 16 * OW_STRIDE;

DI float bflo(u32 v) { return __builtin_bit_cast(float, v << 16); }
DI float bfhi(u32 v) { return __builtin_bit_cast(float, v & 0xffff0000u); }
DI float rbf(float f) { return (float)(__bf16)f; }
DI u32 pk(float a, float b) { return __builtin_bit_cast(u32, __builtin_convertvector((f32x2){a, b}, bf16x2)); }
DI u32 f2u(float f) { return __builtin_bit_cast(u32, f); }
DI float u2f(u32 u) { return __builtin_bit_cast(float, u); }
DI f32x4 mfma(u32x4 a, u32x4 b, f32x4 c) {
  return __builtin_amdgcn_mfma_f32_16x16x32_bf16(__builtin_bit_cast(bf16x8, a), __builtin_bit_cast(bf16x8, b), c, 0, 0, 0);
}
template <int CTRL> DI float dpp(float x) { return u2f(__builtin_amdgcn_mov_dpp(f2u(x), CTRL, 0xf, 0xf, true)); }
constexpr int XOR1 = 0xB1, XOR2 = 0x4E, ROR4 = 0x124, ROR8 = 0x128;
DI float half32(float a, float b) { auto r = __builtin_amdgcn_permlane32_swap(f2u(a), f2u(b), false, false); return u2f(r[0]) + u2f(r[1]); }
DI float half16(float a, float b) { auto r = __builtin_amdgcn_permlane16_swap(f2u(a), f2u(b), false, false); return u2f(r[0]) + u2f(r[1]); }
DI float max32(float a) { auto r = __builtin_amdgcn_permlane32_swap(f2u(a), f2u(a), false, false); return __builtin_amdgcn_fmed3f(u2f(r[0]), u2f(r[1]), __builtin_inff()); }
DI float max16(float a) { auto r = __builtin_amdgcn_permlane16_swap(f2u(a), f2u(a), false, false); return __builtin_amdgcn_fmed3f(u2f(r[0]), u2f(r[1]), __builtin_inff()); }
DI float half_sum(float v) {
  v += dpp<XOR1>(v); v += dpp<XOR2>(v); v += dpp<ROR4>(v); v += dpp<ROR8>(v);
  return half16(v, v);
}
DI u32 plo(u32 k1, u32 k0) { return __builtin_amdgcn_perm(k1, k0, 0x05040100u); }
DI u32 phi(u32 k1, u32 k0) { return __builtin_amdgcn_perm(k1, k0, 0x07060302u); }
DI u32x4 colfrag(const u32x4 (&r)[8], int d) {
  const int q = d >> 1;
  return (d & 1) ? (u32x4){phi(r[1][q], r[0][q]), phi(r[3][q], r[2][q]), phi(r[5][q], r[4][q]), phi(r[7][q], r[6][q])}
                 : (u32x4){plo(r[1][q], r[0][q]), plo(r[3][q], r[2][q]), plo(r[5][q], r[4][q]), plo(r[7][q], r[6][q])};
}
DI u32x4 sel4(bool c, u32x4 v) { return (u32x4){c ? v[0] : 0u, c ? v[1] : 0u, c ? v[2] : 0u, c ? v[3] : 0u}; }
template <typename T> DI T ldnt(const GAS T* p) { return __builtin_nontemporal_load(p); }
#define LAUX 16
#define SAUX 16
DI u32x4 ldws(__amdgpu_buffer_rsrc_t ws, int off) { return __builtin_amdgcn_raw_buffer_load_b128(ws, off, 0, LAUX); }
DI void stws(__amdgpu_buffer_rsrc_t ws, int off, u32x2 a, u32x2 b) { __builtin_amdgcn_raw_buffer_store_b128((u32x4){a[0], a[1], b[0], b[1]}, ws, off, 0, SAUX); }
DI void stws1(__amdgpu_buffer_rsrc_t ws, int off, u32x2 a) { __builtin_amdgcn_raw_buffer_store_b64(a, ws, off, 0, SAUX); }
DI u32x2 pack2(float a, float b, u32 tag8) {
  const u32 ar = f2u(a) + 0x800u;
  const u32 u = (f2u(b) & 0xf000u) | ((ar >> 4) & ~0xf000u);
  return (u32x2){phi(f2u(b), ar), __builtin_amdgcn_perm(tag8, u, 0x07060501u)};
}
DI float first2(u32 w0, u32 w1) { return u2f(__builtin_amdgcn_perm(w0, w1 << 4, 0x0504000cu)); }
DI float second2(u32 w0, u32 w1) { return u2f(__builtin_amdgcn_perm(w0, w1, 0x0706000cu)); }
DI bool tagged(u32x4 v, u32 tag8) { return ((v[1] ^ tag8) | (v[3] ^ tag8)) < 256u; }
#define PIN() do { asm volatile("" ::: "memory"); __builtin_amdgcn_sched_barrier(0); } while (0)
#define LDS_BARRIER() do { asm volatile("s_waitcnt lgkmcnt(0)\n\ts_barrier" ::: "memory"); __builtin_amdgcn_sched_barrier(0); } while (0)
#define RAW_BARRIER() do { __builtin_amdgcn_sched_barrier(0); asm volatile("s_barrier" ::: "memory"); __builtin_amdgcn_sched_barrier(0); } while (0)
DI void dma16(const u16* src, u32 dst) {
  u32 keep;
  asm volatile("s_mov_b32 %0, m0\n\ts_mov_b32 m0, %2\n\ts_nop 0\n\tglobal_load_lds_dwordx4 %1, off nt\n\ts_mov_b32 m0, %0"
               : "=&s"(keep) : "v"(src), "s"(dst) : "memory");
}
DI void touch(const void* src, u32 dst) {
  u32 keep;
  asm volatile("s_mov_b32 %0, m0\n\ts_mov_b32 m0, %2\n\ts_nop 0\n\tglobal_load_lds_dword %1, off\n\ts_mov_b32 m0, %0"
               : "=&s"(keep) : "v"(src), "s"(dst) : "memory");
}
DI void touch_nt(const void* src, u32 dst) {
  u32 keep;
  asm volatile("s_mov_b32 %0, m0\n\ts_mov_b32 m0, %2\n\ts_nop 0\n\tglobal_load_lds_dword %1, off nt\n\ts_mov_b32 m0, %0"
               : "=&s"(keep) : "v"(src), "s"(dst) : "memory");
}
DI void stouch8(const void* p) {
  u32 t;
  asm volatile("s_load_dword %0, %1, 0x0\n\ts_load_dword %0, %1, 0x80\n\ts_load_dword %0, %1, 0x100\n\ts_load_dword %0, %1, 0x180\n\t"
               "s_load_dword %0, %1, 0x200\n\ts_load_dword %0, %1, 0x280\n\ts_load_dword %0, %1, 0x300\n\ts_load_dword %0, %1, 0x380\n\t"
               "s_waitcnt lgkmcnt(0)" : "=&s"(t) : "s"(p) : "memory");
}
DI void stouch2x2(const void* p, const void* q) {
  u32 t;
  asm volatile("s_load_dword %0, %1, 0x0\n\ts_load_dword %0, %1, 0x80\n\ts_load_dword %0, %2, 0x0\n\ts_load_dword %0, %2, 0x80\n\t"
               "s_waitcnt lgkmcnt(0)" : "=&s"(t) : "s"(p), "s"(q) : "memory");
}
DI void set_flag(LAS unsigned char* lds, int f) {
  asm volatile("s_waitcnt lgkmcnt(0)" ::: "memory");
  *(volatile LAS u32*)(lds + L_FLAG + f * 4) = 1u;
}
DI void wait_flags(LAS unsigned char* lds, int first, int mask, int lane) {
#pragma unroll 1
  for (int spin = 0; spin < (SPIN_CAP << 4); ++spin) {
    if (__all(*(volatile LAS u32*)(lds + L_FLAG + (first + (lane & mask)) * 4) != 0u)) break;
    __builtin_amdgcn_s_sleep(1);
  }
  PIN();
}
DI u32 read_tag(const unsigned char* ws) {
  u32 e;
  asm volatile("s_load_dword %0, %1, 0x0\n\ts_waitcnt lgkmcnt(0)" : "=s"(e) : "s"(ws) : "memory");
  return (e + 1u) << 8;
}
template <int LOW, int PNT = 0> DI u32x4 poll16(__amdgpu_buffer_rsrc_t ws, int off, u32 want) {
  u32x4 v;
  u64 keep;
  u32 left = SPIN_CAP, t;
  asm volatile("s_mov_b64 %[keep], exec\n\t"
               "s_branch 2f\n"
               "1:\n\t"
               "s_sub_u32 %[left], %[left], 1\n\t"
               "s_cbranch_scc1 3f\n"
               "2:\n\t"
               ".if %[pnt]\n\tbuffer_load_dwordx4 v[100:103], %[off], %[ws], 0 offen sc1 nt\n\t.else\n\t"
               "buffer_load_dwordx4 v[100:103], %[off], %[ws], 0 offen sc1\n\t.endif\n\t"
               "s_waitcnt vmcnt(0)\n\t"
               "v_bitop3_b32 %[t], v101, v103, %[want] bitop3:0x7e\n\t"
               "v_cmp_lt_u32 vcc, %[low], %[t]\n\t"
               "s_and_b64 exec, exec, vcc\n\t"
               "s_cbranch_scc1 1b\n"
               "3:\n\t"
               "s_mov_b64 exec, %[keep]"
               : "={v[100:103]}"(v), [keep] "=&s"(keep), [left] "+s"(left), [t] "=&v"(t)
               : [off] "v"(off), [ws] "s"(ws), [want] "s"(want), [low] "i"(LOW), [pnt] "i"(PNT)
               : "vcc", "scc", "memory");
  return v;
}
DI void quad_sum4(float& a, float& b, float& c, float& d) {
  asm("s_nop 1\n\t"
      "v_add_f32_dpp %0, %0, %0 quad_perm:[1,0,3,2] row_mask:0xf bank_mask:0xf\n\t"
      "v_add_f32_dpp %1, %1, %1 quad_perm:[1,0,3,2] row_mask:0xf bank_mask:0xf\n\t"
      "v_add_f32_dpp %2, %2, %2 quad_perm:[1,0,3,2] row_mask:0xf bank_mask:0xf\n\t"
      "v_add_f32_dpp %3, %3, %3 quad_perm:[1,0,3,2] row_mask:0xf bank_mask:0xf\n\t"
      "v_add_f32_dpp %0, %0, %0 quad_perm:[2,3,0,1] row_mask:0xf bank_mask:0xf\n\t"
      "v_add_f32_dpp %1, %1, %1 quad_perm:[2,3,0,1] row_mask:0xf bank_mask:0xf\n\t"
      "v_add_f32_dpp %2, %2, %2 quad_perm:[2,3,0,1] row_mask:0xf bank_mask:0xf\n\t"
      "v_add_f32_dpp %3, %3, %3 quad_perm:[2,3,0,1] row_mask:0xf bank_mask:0xf"
      : "+v"(a), "+v"(b), "+v"(c), "+v"(d));
}
DI float sum_records(u32x4 v) {
  const float t = half16(half32(first2(v[0], v[1]), first2(v[2], v[3])), half32(second2(v[0], v[1]), second2(v[2], v[3])));
  return t + dpp<ROR8>(t);
}

DI void layernorm(u32 (&x)[8]) {
  float v[16], s = 0.f;
#pragma unroll
  for (int e = 0; e < 8; ++e) { v[2 * e] = bflo(x[e]); v[2 * e + 1] = bfhi(x[e]); s += v[2 * e] + v[2 * e + 1]; }
  const float mean = half_sum(s) * (1.f / 512.f);
  float q = 0.f;
#pragma unroll
  for (int e = 0; e < 16; ++e) { const float d = v[e] - mean; q = fmaf(d, d, q); }
  const float var = half_sum(q) * (1.f / 512.f);
  const float mb = rbf(mean), inv = __builtin_amdgcn_rcpf(rbf(__builtin_amdgcn_sqrtf(rbf(rbf(var) + 1e-5f))));
#pragma unroll
  for (int e = 0; e < 8; ++e) { const u32 d = pk(v[2 * e] - mb, v[2 * e + 1] - mb); x[e] = pk(bflo(d) * inv, bfhi(d) * inv); }
}

DI f32x2 tile_matvec(const u32x4 (&wv)[8], LAS unsigned char* xin, int g, int c) {
  const u32x4 xf = *(LAS u32x4*)(xin + ((c >> 2) & 1) * 1024 + ((c >> 3) * 32 + g * 8) * 2);
  f32x4 lo = {0.f, 0.f, 0.f, 0.f}, hi = lo;
#pragma unroll
  for (int d = 0; d < 4; ++d) {
    const u32x4 am = sel4((c & 3) == d, xf);
    lo = mfma(am, colfrag(wv, d), lo);
    hi = mfma(am, colfrag(wv, 4 + d), hi);
  }
  const bool valid = (g >> 1) == (c >> 3);
  float t[4];
#pragma unroll
  for (int e = 0; e < 4; ++e) { t[e] = half32(valid ? lo[e] : 0.f, valid ? hi[e] : 0.f); t[e] += dpp<ROR8>(t[e]); }
  return (c & 8) ? (f32x2){t[2], t[3]} : (f32x2){t[0], t[1]};
}

#define ST(i) do { if (!((STMASK >> (i)) & 1)) break; __builtin_amdgcn_sched_barrier(0); ts[i] = __builtin_amdgcn_s_memrealtime(); __builtin_amdgcn_s_waitcnt(0xC07F); __builtin_amdgcn_sched_barrier(0); } while (0)

__global__ void __launch_bounds__(1024, 4)
fused_decode(const u16* x_, const u16* k_, const u16* v_, const u16* wq_, const u16* wo_, const u16* w1_, const u16* b1_,
             const u16* w2_, const u16* b2_, u16* out_, unsigned char* ws_
             ) {
  __shared__ __attribute__((aligned(16))) unsigned char lds_raw[L_END];
  LAS unsigned char* lds = (LAS unsigned char*)lds_raw;
  u64 ts[12];
  for (int i = 0; i < 12; ++i) ts[i] = 0;
  ST(0);
  const u64 t_start = WO_MODE == 3 ? __builtin_amdgcn_s_memrealtime() : 0;
  asm volatile("" :: "s"(x_), "s"(k_), "s"(v_), "s"(wq_), "s"(wo_), "s"(w1_), "s"(b1_), "s"(w2_), "s"(b2_), "s"(out_), "s"(ws_));
  const int tid = threadIdx.x, lane = tid & 63, w = __builtin_amdgcn_readfirstlane(tid >> 6);
  const int g = lane >> 4, c = lane & 15;
  const int bid = blockIdx.x, kvh = bid & 7, j = bid >> 3, b = j >> 4, sp = j & 15, rb = j >> 2, cb = j & 3;
  const bool mlp1 = j < 14, mlp2 = j >= 14 && j < 28;
  const __amdgpu_buffer_rsrc_t ws = __builtin_amdgcn_make_buffer_rsrc(ws_, 0, WS_END, 0x00020000);
  u32 tag;
  const u32 hsh = ((u32)(bid * 16 + w) * 2654435761u) >> 20, hb = ((u32)bid * 2654435761u) >> 16;
  const int wu = (w + (ROT == 0 ? 0 : ROT == 1 || ROT == 2 || ROT == 3 ? j : (int)hb)) & 15;
  const int ro = ROT == 2 ? kvh : ROT == 3 || ROT == 4 ? (int)(hsh & 7) : 0;
  const size_t kvseq = (size_t)(b * 8 + kvh) * SK * DH;
  const __amdgpu_buffer_rsrc_t kr = __builtin_amdgcn_make_buffer_rsrc((void*)(k_ + kvseq), 0, SK * DH * 2, 0x00020000);
  const __amdgpu_buffer_rsrc_t vr = __builtin_amdgcn_make_buffer_rsrc((void*)(v_ + kvseq), 0, SK * DH * 2, 0x00020000);
  const int kvoff = (sp * 16 + wu) * 8192;
  const int klane = ((((c >> 1) ^ ro) & 1) * 4 + (c >> 2)) * 256 + (g * 2 + (c & 1)) * 16;
  u32x4 kf[8], vf[8];

  const int cq = j & 7, kq = j >> 3;
  const int mt = (mlp1 ? j : j - 14) * 8 + kvh;
  const int mhi = mt >> 2, mlo = mt & 3;
  auto pretouch = [&](int what) __attribute__((always_inline)) {
    const unsigned char* p = ws_ + WS_OP + ((kvh * 2 + b) * 16 + sp) * 2048 + c * 128;
    bool on = lane < 16 && (what & 1);
    if ((lane >> 3) == 2) { p = ws_ + WS_S2 + (cb * 64 + kvh * 8 + rb) * 1024 + (lane & 7) * 128; on = what & 16; }
    if ((lane >> 3) == 3) { p = ws_ + WS_MP + (mlo * 28 + mhi) * 1024 + (lane & 7) * 128; on = mlp2 && (what & 2); }
    if ((lane >> 2) == 8) { p = ws_ + WS_QP + ((((lane >> 1) & 1) * 8 + kvh) * 32 + cq * 4 + kq) * 256 + (lane & 1) * 128; on = what & 4; }
    if ((lane >> 2) == 9) { p = ws_ + WS_PA + mt * 512 + (lane & 3) * 128; on = mlp1 && (what & 4); }
    if (lane == 40) { p = (const unsigned char*)(out_ + ((bid >> 2) & 1) * D + (bid & 3) * 128 + ((bid >> 3) & 3) * 32); on = bid < NFIN && (what & 8); }
    if (TV == 1 || TV == 3) on = on && (lane & 15) == 0;
    if (TV >= 2) { if (on) touch_nt(p, (u32)(size_t)(lds + L_T2)); } else
    if (on) touch(p, (u32)(size_t)(lds + L_T2));
  };
  if (w < 2 || (w < 4 && mlp1)) {
    __builtin_amdgcn_s_setprio(3);
    if (w == 0 && lane < 16) *(LAS u32*)(lds + L_FLAG + lane * 4) = 0u;
    const GAS u32x4* xp = (const GAS u32x4*)x_ + lane * 2;
    const u32x4 x0 = xp[0], x1 = xp[1];
    PIN();
    const int rs = w < 2 ? NQ / 8 : FF / 8, row0 = (w < 2 ? kq : mlo) * 128 + (w & 1) * 64;
    const u16* tile = w < 2 ? wq_ + kvh * 512 + cq * 64 : w1_ + mhi * 64;
    const GAS u32x4* wp = (const GAS u32x4*)tile + (row0 + (c >> 3) * 32 + g * 8) * rs + (c & 7);
    u32x4 wv[8];
#pragma unroll
    for (int i = 0; i < 8; ++i) wv[i] = ldnt(wp + i * rs);
    PIN();
    LDS_BARRIER();
    tag = read_tag(ws_);
    PIN();
    u32 xs[8] = {x0[0], x0[1], x0[2], x0[3], x1[0], x1[1], x1[2], x1[3]};
    layernorm(xs);
    if (w >= 2) layernorm(xs);
    LAS unsigned char* xl = lds + L_XN + w * 2048;
    *(LAS u32x4*)(xl + lane * 32) = (u32x4){xs[0], xs[1], xs[2], xs[3]};
    *(LAS u32x4*)(xl + lane * 32 + 16) = (u32x4){xs[4], xs[5], xs[6], xs[7]};
    f32x2 r = tile_matvec(wv, xl + row0 * 2, g, c);
    LAS f32x2* half = (LAS f32x2*)(lds + L_HALF + (w >> 1) * 512 + lane * 8);
    if (w & 1) {
      *half = r;
      set_flag(lds, F_HALF + (w >> 1));
    } else {
      wait_flags(lds, F_HALF + (w >> 1), 0, lane);
      r += *half;
      const int rec = w == 0 ? WS_QP + ((((g & 1) * 8 + kvh) * 8 + cq) * 4 + kq) * 256 : WS_PA + mt * 512 + (g & 1) * 256;
      stws1(ws, rec + ((c & 7) * 4 + (g >> 1) * 2 + (c >> 3)) * 8, pack2(r[0], r[1], tag));
    }
    ST(2);
    __builtin_amdgcn_s_setprio(PRIO_K);
  } else {
    if (w < 4 && mlp2) {
      const u16* p = w2_ + (mhi * 64 + (w & 1) * 32 + g * 8) * D + mlo * 128 + c * 8;
#pragma unroll
      for (int i = 0; i < 8; ++i) dma16(p + i * D, (u32)(size_t)(lds + L_W2 + (w & 1) * 8192 + i * 1024));
    }
    if (CODE_LINES && w == 4) {
      const unsigned char* pc;
      asm volatile("s_getpc_b64 %0" : "=s"(pc));
      if (lane < 4 && j * 4 + lane < CODE_LINES) touch(pc + (j * 4 + lane) * 128, (u32)(size_t)(lds + L_TOUCH));
    }
    if (PRE_A && w == PREW) pretouch(PRE_A);
    if (SPRE & 16) RAW_BARRIER();
    if ((SPRE & 1) && (w >> 1) == 6) stouch8(ws_ + WS_OP + ((kvh * 2 + b) * 16 + sp) * 2048 + (w & 1) * 1024);
    if ((SPRE & 1) && w == 14) stouch8(ws_ + WS_S2 + (cb * 64 + kvh * 8 + rb) * 1024);
    if ((SPRE & 2) && w == 15 && mlp2) stouch8(ws_ + WS_MP + (mlo * 28 + mhi) * 1024);
    if ((SPRE & 4) && w == 11) { const unsigned char* q = ws_ + WS_QP + (kvh * 32 + cq * 4 + kq) * 256; stouch2x2(q, q + 65536); }
    if ((SPRE & 4) && w == 10 && mlp1) { const unsigned char* q = ws_ + WS_PA + mt * 512; stouch2x2(q, q + 256); }
    if ((SPRE & 8) && w == 9 && bid < NFIN) { const u16* q = out_ + ((bid >> 2) & 1) * D + (bid & 3) * 128; stouch2x2(q, q); }
    if (!(SPRE & 16)) RAW_BARRIER();
    if (PRE_B && w == PREW + 1) pretouch(PRE_B);
    if ((w >> 2) == 1) __builtin_amdgcn_s_setprio(2); else __builtin_amdgcn_s_setprio(PRIO_K);
  }
  PIN();

  const int slot = SLOTS == 1 ? SLOT0 : SLOTS == 2 ? SLOT0 + (j & 1) : j & 3;
  if (PRE_C && slot < 2 && w == 9 + slot * 4) pretouch(PRE_C);
#pragma unroll
  for (int i = 0; i < 8; ++i) { kf[i] = BUF ? __builtin_amdgcn_raw_buffer_load_b128(kr, klane + (i & 1) * 128, kvoff + ((i >> 1) ^ (ro >> 1)) * 2048, NT) : ldnt((const GAS u32x4*)((const GAS char*)(k_ + kvseq) + kvoff + klane + (i & 1) * 128 + (i >> 1) * 2048)); __builtin_amdgcn_sched_barrier(0); }
  if ((w >> 2) != 1) __builtin_amdgcn_s_setprio(0);
  if (PRE_C && slot >= 2 && w == 1 + (slot - 2) * 8) pretouch(PRE_C);
  PIN();
#pragma unroll
  for (int i = 0; i < 8; ++i) { vf[i] = BUF ? __builtin_amdgcn_raw_buffer_load_b128(vr, lane * 16, kvoff + (i ^ ro) * 1024, NT) : ldnt((const GAS u32x4*)((const GAS char*)(v_ + kvseq) + kvoff + lane * 16 + i * 1024)); __builtin_amdgcn_sched_barrier(0); }
  PIN();
  if ((PRE_D || WO_V) && lane == 0) __hip_atomic_fetch_add((LAS u32*)(lds + L_CNT + 4), 1u, __ATOMIC_RELAXED, __HIP_MEMORY_SCOPE_WORKGROUP);
  if (PRE_E && w == 13) pretouch(PRE_E);
  ST(4);
  tag = read_tag(ws_);
  const int paoff = WS_PA + mhi * 2048 + lane * 16;
  u32x4 p0, p1;
  u32 b1v;
  if ((w >> 2) == 1) {
    const int u = w & 3;
    const int qoff = WS_QP + (b * 8 + kvh) * 8192 + u * 2048 + lane * 16;
    asm volatile("s_waitcnt vmcnt(0)" ::: "memory");
    if (Q_DELAY) __builtin_amdgcn_s_sleep(Q_DELAY);
    u32x4 q0, q1;
#pragma unroll 1
    for (int spin = 0; spin < SPIN_CAP; ++spin) {
      q0 = ldws(ws, qoff); q1 = ldws(ws, qoff + 1024);
      if (__all(tagged(q0, tag) && tagged(q1, tag))) break;
      __builtin_amdgcn_s_sleep(SLEEP_Q);
      asm volatile("" ::: "memory");
    }
    const float qa = half16(half32(first2(q0[0], q0[1]), first2(q1[0], q1[1])), half32(first2(q0[2], q0[3]), first2(q1[2], q1[3])));
    const float qb = half16(half32(second2(q0[0], q0[1]), second2(q1[0], q1[1])), half32(second2(q0[2], q0[3]), second2(q1[2], q1[3])));
    *(LAS u32*)(lds + L_Q + u * 256 + ((g >> 1) * 32 + c * 2 + (g & 1)) * 4) = pk(qa, qb);
    set_flag(lds, F_Q + u);
    ST(3);
    if (w == 7 && mlp2) {
      p0 = ldws(ws, paoff); p1 = ldws(ws, paoff + 1024);
      b1v = *(const GAS u32*)(b1_ + mhi * 64 + c * 4 + (g >> 1) * 2);
      PIN();
    }
    __builtin_amdgcn_s_setprio(0);
  }
  wait_flags(lds, F_Q, 3, lane);

  float sc[8];
  {
    const int x0 = (c >> 2) & 1;
    const u32x4 q0 = *(LAS u32x4*)(lds + L_Q + (c & 3) * 256 + (g * 2 + x0) * 16);
    const u32x4 q1 = *(LAS u32x4*)(lds + L_Q + (c & 3) * 256 + 128 + (g * 2 + x0) * 16);
#pragma unroll
    for (int o = 0; o < 4; ++o) {
      f32x4 s = mfma(kf[2 * o], q0, (f32x4){0.f, 0.f, 0.f, 0.f});
      s = mfma(kf[2 * o + 1], q1, s);
#pragma unroll
      for (int a = 0; a < 2; ++a) {
        float e1 = s[2 * a + 1];
        asm("" : "+v"(e1));
        const float t = x0 ? e1 : s[2 * a];
        sc[2 * o + a] = t + dpp<ROR4>(t);
      }
      const u32 d = pk(sc[2 * o], sc[2 * o + 1]), e = pk(bflo(d) * 0.08838834764831845f, bfhi(d) * 0.08838834764831845f);
      sc[2 * o] = bflo(e); sc[2 * o + 1] = bfhi(e);
    }
  }
  PIN();

    float m = fmaxf(fmaxf(fmaxf(sc[0], sc[1]), fmaxf(sc[2], sc[3])), fmaxf(fmaxf(sc[4], sc[5]), fmaxf(sc[6], sc[7])));
    m = max32(max16(m)) * LOG2E;
    float p[8], l = 0.f;
#pragma unroll
    for (int e = 0; e < 8; ++e) { p[e] = __builtin_amdgcn_exp2f(fmaf(sc[e], LOG2E, -m)); l += p[e]; }
    l = half16(l, l); l = half32(l, l);
    if (lane < 4) { *(LAS float*)(lds + L_MW + (lane * 16 + w) * 4) = m; *(LAS float*)(lds + L_LW + (lane * 16 + w) * 4) = l; }
    PIN();
    if ((w >> 1) == 1) set_flag(lds, F_W2 + (w & 1));
    if (lane == 0) __hip_atomic_fetch_add((LAS u32*)(lds + L_CNT), 1u, __ATOMIC_RELAXED, __HIP_MEMORY_SCOPE_WORKGROUP);
    const u32x4 pf = {pk(p[0], p[1]), pk(p[2], p[3]), pk(p[4], p[5]), pk(p[6], p[7])};
    u32x4 am[4];
#pragma unroll
    for (int u = 0; u < 4; ++u) am[u] = sel4((c >> 2) == u, pf);
    PIN();
    u32x4 ve[4], vo[4];
    u32 slo, shi;
    asm volatile("s_mov_b32 %0, 0x05040100\n\ts_mov_b32 %1, 0x07060302" : "=s"(slo), "=s"(shi) : "v"(am[0][0]), "v"(am[1][1]), "v"(am[2][2]), "v"(am[3][3]));
#pragma unroll
    for (int r = 0; r < 4; ++r) {
#pragma unroll
      for (int u = 0; u < 4; ++u) {
        ve[u][r] = __builtin_amdgcn_perm(vf[2 * r + 1][u], vf[2 * r][u], slo);
        vo[u][r] = __builtin_amdgcn_perm(vf[2 * r + 1][u], vf[2 * r][u], shi);
      }
      if (r < 3)
        asm volatile("" : "+s"(slo), "+s"(shi) : "v"(ve[0][r]), "v"(ve[1][r]), "v"(ve[2][r]), "v"(ve[3][r]), "v"(vo[0][r]), "v"(vo[1][r]), "v"(vo[2][r]), "v"(vo[3][r]));
    }
    f32x4 oe = {0.f, 0.f, 0.f, 0.f}, oo = oe;
#pragma unroll
    for (int u = 0; u < 4; ++u) { oe = mfma(am[u], ve[u], oe); oo = mfma(am[u], vo[u], oo); }
#pragma unroll
    for (int h = 0; h < 4; ++h) *(LAS f32x2*)(lds + L_OW + w * OW_STRIDE + h * 512 + c * 32 + g * 8) = (f32x2){oe[h], oo[h]};
    PIN();
    ST(5);

  const bool gath = (w & 4) == 0;
  const int v = (w & 3) + (w >> 3) * 4;
  auto wait_ml = [&]() __attribute__((always_inline)) {
#pragma unroll 1
    for (int spin = 0; spin < (SPIN_CAP << 4); ++spin) {
      if (__builtin_amdgcn_readfirstlane(*(volatile LAS u32*)(lds + L_CNT)) == 16u) break;
      __builtin_amdgcn_s_sleep(1);
    }
    PIN();
  };
  auto load_wo = [&](u32x4 (&wt)[8]) __attribute__((always_inline)) {
    const GAS u16* p = (const GAS u16*)wo_ + (size_t)(kvh * 512 + rb * 64 + g * 4) * D + cb * 128 + (w - 4) * 64 + c * 4;
#pragma unroll
    for (int i = 0; i < 8; ++i) {
      const u32x2 t0 = ldnt((const GAS u32x2*)(p + (size_t)(32 * (i >> 2) + (i & 3)) * D));
      const u32x2 t1 = ldnt((const GAS u32x2*)(p + (size_t)(32 * (i >> 2) + 16 + (i & 3)) * D));
      wt[i] = (u32x4){t0[0], t0[1], t1[0], t1[1]};
    }
    PIN();
  };
  auto wait_v = [&](u32 n) __attribute__((always_inline)) {
#pragma unroll 1
    for (int spin = 0; spin < (SPIN_CAP << 4); ++spin) {
      if (__builtin_amdgcn_readfirstlane(*(volatile LAS u32*)(lds + L_CNT + 4)) >= n) break;
      __builtin_amdgcn_s_sleep(1);
    }
    PIN();
  };
  if (PRE_D && w == 6) { wait_v(PRE_AT); pretouch(PRE_D); }
  const bool fin = FINLAST ? bid >= 256 - NFIN : bid < NFIN;
  const int cf = bid & 3, bf = (bid >> 2) & 1, seg = (bid >> 3) & 3;
  if (w >= 12) {
    LDS_BARRIER();
    ST(6);
    if (fin) {
      const int row = (w - 12) * 8 + (lane >> 3);
      const int moff = WS_MP + (cf * 28 + min(row, 27)) * 1024 + bf * 512 + seg * 128 + (lane & 7) * 16;
      u32x4 mv;
#pragma unroll 1
      for (int spin = 0; spin < SPIN_CAP; ++spin) {
        mv = ldws(ws, moff);
        if (__all(tagged(mv, tag))) break;
        __builtin_amdgcn_s_sleep(SLEEP_M);
        asm volatile("" ::: "memory");
      }
      const float t = sum_records(sel4(row < 28, mv));
      if ((lane & 8) == 0) *(LAS float*)(lds + L_MF + (w - 12) * 128 + ((lane & 7) * 4 + g) * 4) = t;
      LDS_BARRIER();
    }
  } else if (gath) {
    {
      const int f = v * 16 + (lane >> 2), p4 = lane & 3, hd = v >> 1;
      wait_ml();
      const f32x4 mv = *(LAS f32x4*)(lds + L_MW + hd * 64 + p4 * 16), lv = *(LAS f32x4*)(lds + L_LW + hd * 64 + p4 * 16);
      float M = fmaxf(fmaxf(mv[0], mv[1]), fmaxf(mv[2], mv[3]));
      M = fmaxf(M, dpp<XOR1>(M)); M = fmaxf(M, dpp<XOR2>(M));
      float L = 0.f, fw[4];
#pragma unroll
      for (int i = 0; i < 4; ++i) { fw[i] = __builtin_amdgcn_exp2f(mv[i] - M); L = fmaf(fw[i], lv[i], L); }
      L += dpp<XOR1>(L); L += dpp<XOR2>(L);
      const float inv = __builtin_amdgcn_rcpf(L);
#pragma unroll
      for (int i = 0; i < 4; ++i) fw[i] *= inv;
      const u32 lam = f2u(M + __builtin_amdgcn_logf(L)) >> ((lane >> 5) * 16);
      u32 t0 = tag | (lam & 255u), t1 = tag | ((lam >> 8) & 255u);
      int ooff = WS_OP + ((kvh * 2 + b) * 16 + sp) * 2048 + f * 16;
      int laddr = L_OW + p4 * 4 * OW_STRIDE + f * 16;
      asm volatile("" : "+v"(fw[0]), "+v"(fw[1]), "+v"(fw[2]), "+v"(fw[3]), "+v"(t0), "+v"(t1), "+v"(ooff), "+v"(laddr));
      ST(3);
      LDS_BARRIER();
      ST(6);
      f32x4 s = fw[0] * *(LAS f32x4*)(lds + laddr);
#pragma unroll
      for (int i = 1; i < 4; ++i) s += fw[i] * *(LAS f32x4*)(lds + laddr + i * OW_STRIDE);
      float s0 = s[0], s1 = s[1], s2 = s[2], s3 = s[3];
      quad_sum4(s0, s1, s2, s3);
      if (p4 == 0) __builtin_amdgcn_raw_buffer_store_b128((u32x4){pk(s0, s1), t0, pk(s2, s3), t1}, ws, ooff, 0, OSAUX);
    }
    PIN();
    ST(7);
    {
      u32 gaddr2 = (u32)(size_t)(lds + L_GP + v * GP_STRIDE + 256), gaddr = gaddr2 - 256 + (c * 4 + g) * 4;
      u32 lsel = (c & 8) ? 0x05040100u : 0x01000504u;
      asm volatile("" : "+v"(gaddr), "+v"(gaddr2), "+v"(lsel));
      if (SLEEP_O) __builtin_amdgcn_s_sleep(SLEEP_O);
      const u32x4 ov = poll16<255, OPNT>(ws, WS_OP + (kvh * 32 + v * 4 + g) * 2048 + rb * 256 + c * 16, tag);
      ST(8);
      u32 a, lam, m1, x0, x1, x2, x3;
      f32x2 gl;
      asm volatile("v_perm_b32 %[a], %[t1], %[t0], %[sel]\n\t"
                   "v_lshlrev_b32 %[x0], 16, %[d0]\n\t"
                   "v_lshlrev_b32 %[x2], 16, %[d1]\n\t"
                   "v_mov_b32_dpp %[m1], %[a] row_ror:8 row_mask:0xf bank_mask:0xf\n\t"
                   "v_and_b32 %[x1], 0xffff0000, %[d0]\n\t"
                   "v_perm_b32 %[lam], %[a], %[m1], %[lsel]\n\t"
                   "v_mov_b32 %[m1], %[lam]\n\t"
                   "v_mov_b32 %[a], %[lam]\n\t"
                   "v_and_b32 %[x3], 0xffff0000, %[d1]\n\t"
                   "s_nop 0\n\t"
                   "v_permlane16_swap_b32 %[m1], %[a]\n\t"
                   "v_max_f32 %[m1], %[m1], %[a]\n\t"
                   "v_mov_b32 %[a], %[m1]\n\t"
                   "s_nop 1\n\t"
                   "v_permlane32_swap_b32 %[m1], %[a]\n\t"
                   "v_max_f32 v104, %[m1], %[a]\n\t"
                   "v_sub_f32 %[lam], %[lam], v104\n\t"
                   "v_exp_f32 %[lam], %[lam]\n\t"
                   "s_nop 0\n\t"
                   "v_mul_f32 %[x0], %[lam], %[x0]\n\t"
                   "v_mul_f32 %[x2], %[lam], %[x2]\n\t"
                   "v_mul_f32 %[x1], %[lam], %[x1]\n\t"
                   "v_mul_f32 %[x3], %[lam], %[x3]\n\t"
                   "v_mov_b32 %[a], %[lam]\n\t"
                   "v_permlane32_swap_b32 %[x0], %[x2]\n\t"
                   "v_permlane32_swap_b32 %[x1], %[x3]\n\t"
                   "v_permlane16_swap_b32 %[lam], %[a]\n\t"
                   "v_add_f32 %[x0], %[x0], %[x2]\n\t"
                   "v_add_f32 %[x1], %[x1], %[x3]\n\t"
                   "v_add_f32 %[lam], %[lam], %[a]\n\t"
                   "v_mov_b32 %[a], %[lam]\n\t"
                   "v_permlane16_swap_b32 %[x0], %[x1]\n\t"
                   "v_add_f32 %[x0], %[x0], %[x1]\n\t"
                   "v_permlane32_swap_b32 %[lam], %[a]\n\t"
                   "ds_write_b32 %[ga], %[x0]\n\t"
                   "v_add_f32 v105, %[lam], %[a]\n\t"
                   "ds_write_b64 %[gb], v[104:105]"
                   : [a] "=&v"(a), [lam] "=&v"(lam), [m1] "=&v"(m1), [x0] "=&v"(x0), [x1] "=&v"(x1), [x2] "=&v"(x2), [x3] "=&v"(x3), "=&{v[104:105]}"(gl)
                   : [d0] "v"(ov[0]), [t0] "v"(ov[1]), [d1] "v"(ov[2]), [t1] "v"(ov[3]), [sel] "s"(0x04000400u), [lsel] "v"(lsel), [ga] "v"(gaddr), [gb] "v"(gaddr2)
                   : "memory");
    }
    LDS_BARRIER();
    ST(9);
    if (fin) {
      int faddr = L_FP + ((lane & 7) * 4 + g) * 32 + v * 4;
      asm volatile("" : "+v"(faddr));
      if (SLEEP_S) __builtin_amdgcn_s_sleep(SLEEP_S);
      const u32x4 sv = poll16<255>(ws, WS_S2 + (cf * 64 + v * 8 + (lane >> 3)) * 1024 + bf * 512 + seg * 128 + (lane & 7) * 16, tag);
      ST(10);
      *(LAS float*)(lds + faddr) = sum_records(sv);
      LDS_BARRIER();
    }
  } else if (w < 6) {
    u32x4 wt[8], bw[8];
    if (WO_MODE == 3) {
      wait_ml();
      const bool now = __builtin_amdgcn_s_memrealtime() >= t_start + WO_AT;
      if (!now) {
        LDS_BARRIER();
        ST(6);
        while (__builtin_amdgcn_s_memrealtime() < t_start + WO_AT) __builtin_amdgcn_s_sleep(1);
      }
      load_wo(wt);
      if (now) { LDS_BARRIER(); ST(6); }
    } else {
    if (WO_MODE == 1) { if (WO_V) wait_v(16u); else wait_ml(); }
    if (WO_MODE < 2) load_wo(wt);
    LDS_BARRIER();
    ST(6);
    if (WO_MODE == 2) { if (WO_SLEEP) __builtin_amdgcn_s_sleep(WO_SLEEP); load_wo(wt); }
    }
#pragma unroll
    for (int i = 0; i < 8; ++i) bw[i] = colfrag(wt, i);
    int gp = L_GP + (c & 7) * GP_STRIDE, go = gp + ((c >> 3) * 16 + g * 4) * 4;
    int soff = WS_S2 + (cb * 64 + kvh * 8 + rb) * 1024 + (g & 1) * 512 + ((w - 4) * 32 + c * 2 + (g >> 1)) * 8;
    asm volatile("" : "+v"(gp), "+v"(go), "+v"(soff) : "v"(bw[0]), "v"(bw[1]), "v"(bw[2]), "v"(bw[3]), "v"(bw[4]), "v"(bw[5]), "v"(bw[6]), "v"(bw[7]));
    ST(1);
    LDS_BARRIER();
    ST(9);
    f32x2 gl = *(LAS f32x2*)(lds + gp + 256);
    u32x4 of = *(LAS u32x4*)(lds + go), o1 = *(LAS u32x4*)(lds + go + 128);
#define QP1(r) "v_add_f32_dpp " r ", " r ", " r " quad_perm:[1,0,3,2] row_mask:0xf bank_mask:0xf\n\t"
#define QP2(r) "v_add_f32_dpp " r ", " r ", " r " quad_perm:[2,3,0,1] row_mask:0xf bank_mask:0xf\n\t"
#define PKM(r) "v_pk_mul_f32 " r ", v[118:119], " r " op_sel_hi:[0,1]\n\t"
    asm volatile("v_max_f32_dpp v118, v116, v116 quad_perm:[1,0,3,2] row_mask:0xf bank_mask:0xf\n\t"
                 "s_nop 1\n\t"
                 "v_max_f32_dpp v118, v118, v118 quad_perm:[2,3,0,1] row_mask:0xf bank_mask:0xf\n\t"
                 "v_sub_f32 v118, v116, v118\n\t"
                 "v_exp_f32 v118, v118\n\t"
                 "s_nop 0\n\t"
                 "v_mul_f32 v117, v118, v117\n\t"
                 PKM("v[108:109]") PKM("v[110:111]") PKM("v[112:113]") PKM("v[114:115]")
                 QP1("v117") QP1("v108") QP1("v109") QP1("v110") QP1("v111") QP1("v112") QP1("v113") QP1("v114") QP1("v115")
                 QP2("v117") QP2("v108") QP2("v109")
                 "v_rcp_f32 v118, v117\n\t"
                 QP2("v110") QP2("v111") QP2("v112") QP2("v113") QP2("v114") QP2("v115")
                 PKM("v[108:109]") PKM("v[110:111]") PKM("v[112:113]") PKM("v[114:115]")
                 "v_cvt_pk_bf16_f32 v108, v108, v109\n\t"
                 "v_cvt_pk_bf16_f32 v109, v110, v111\n\t"
                 "v_cvt_pk_bf16_f32 v110, v112, v113\n\t"
                 "v_cvt_pk_bf16_f32 v111, v114, v115\n\t"
                 "s_nop 1"
                 : "+{v[108:111]}"(of), "+{v[112:115]}"(o1), "+{v[116:117]}"(gl)
                 :
                 : "v118", "v119");
    f32x4 acc[8];
#pragma unroll
    for (int i = 0; i < 8; ++i) { const int k = (i & 3) * 2 + (i >> 2); acc[k] = mfma(of, bw[k], (f32x4){0.f, 0.f, 0.f, 0.f}); }
    __builtin_amdgcn_sched_barrier(0);
    float r[4];
#pragma unroll
    for (int d = 0; d < 4; ++d) r[d] = (g >> 1) ? acc[4 + d][0] : acc[d][0];
    stws1(ws, soff, pack2(half32(r[0], r[2]), half32(r[1], r[3]), tag));
    ST(10);
  } else if (w == 7 && mlp2) {
#pragma unroll 1
    for (int spin = 0; spin < SPIN_CAP; ++spin) {
      if (__all(tagged(p0, tag) && tagged(p1, tag))) break;
      __builtin_amdgcn_s_sleep(SLEEP_Q);
      asm volatile("" ::: "memory");
      p0 = ldws(ws, paoff); p1 = ldws(ws, paoff + 1024);
    }
    const float t2[2] = {half32(first2(p0[0], p0[1]) + first2(p1[0], p1[1]), first2(p0[2], p0[3]) + first2(p1[2], p1[3])),
                         half32(second2(p0[0], p0[1]) + second2(p1[0], p1[1]), second2(p0[2], p0[3]) + second2(p1[2], p1[3]))};
    float hv[2];
#pragma unroll
    for (int e = 0; e < 2; ++e) {
      const float t = rbf(rbf(t2[e]) + (e ? bfhi(b1v) : bflo(b1v)));
      const float uu = 0.7978845608028654f * (t + 0.044715f * t * t * t);
      hv[e] = t / (1.f + __builtin_amdgcn_exp2f(-2.f * LOG2E * uu));
    }
    *(LAS u32*)(lds + L_HS + (g & 1) * 128 + (c * 2 + (g >> 1)) * 4) = pk(hv[0], hv[1]);
    wait_flags(lds, F_W2, 1, lane);
    f32x4 r = {0.f, 0.f, 0.f, 0.f};
#pragma unroll
    for (int s = 0; s < 2; ++s) {
      const u32x4 ha = *(LAS u32x4*)(lds + L_HS + ((c >> 2) & 1) * 128 + s * 64 + g * 16);
      u32x4 w2t[8];
#pragma unroll
      for (int i = 0; i < 8; ++i) w2t[i] = *(LAS u32x4*)(lds + L_W2 + s * 8192 + i * 1024 + lane * 16);
#pragma unroll
      for (int d = 0; d < 8; ++d) r = mfma(sel4((c >> 3) == (d >> 2) && (c & 3) == (d & 3), ha), colfrag(w2t, d), r);
    }
    u32x2 m0 = pack2(r[0], r[1], tag), m1 = pack2(r[2], r[3], tag);
    asm volatile("" : "+v"(m0), "+v"(m1));
    ST(2);
    if (MP_LATE) LDS_BARRIER();
    stws(ws, WS_MP + (mlo * 28 + mhi) * 1024 + (g & 1) * 512 + (c * 2 + (g >> 1)) * 16, m0, m1);
    if (!MP_LATE) LDS_BARRIER();
    ST(6);
  } else if (w == 6 && fin) {
    const int col = cf * 128 + seg * 32 + (lane & 31);
    const u32 xv = ((const GAS u16*)x_)[bf * D + col], b2v = ((const GAS u16*)b2_)[col];
    LDS_BARRIER();
    ST(6);
    LDS_BARRIER();
    LAS float* mf = (LAS float*)(lds + L_MF + (lane >> 5) * 256 + (lane & 31) * 4);
    float t = mf[0] + mf[32];
    t = half32(t, t);
    t = rbf(rbf(t) + bflo(b2v));
    float gate = rbf(t / (1.f + __builtin_amdgcn_exp2f(-LOG2E * t))), xf = bflo(xv);
    int faddr = L_FP + (lane & 31) * 32 + (lane >> 5) * 16;
    GAS u16* op = (GAS u16*)out_ + bf * D + col;
    asm volatile("" : "+v"(gate), "+v"(xf), "+v"(faddr), "+v"(op));
    LDS_BARRIER();
    ST(10);
    const f32x4 fp = *(LAS f32x4*)(lds + faddr);
    float s = (fp[0] + fp[1]) + (fp[2] + fp[3]);
    s = half32(s, s);
    const float res = xf + rbf(rbf(s) * gate);
    if (lane < 32) __hip_atomic_store(op, (u16)pk(res, res), RLX_AGENT);
    if ((bid & 31) == 0 && lane == 0) __hip_atomic_store((GAS u32*)(ws_ + WS_EPOCH), tag >> 8, RLX_AGENT);
  } else {
    LDS_BARRIER();
    ST(6);
  }
  ST(11);
  {
    GAS u64* st = (GAS u64*)(ws_ + (WS_AT_END ? -(8 << 20) : (8 << 20)));
    volatile LAS u64* ls = (volatile LAS u64*)(lds + L_TOUCH);
    if (lane == 0) { ls[w * 2] = ts[PROBE]; ls[w * 2 + 1] = ts[PROBE_B]; }
    if (w == 0) {
      while (__builtin_amdgcn_s_memrealtime() < ts[0] + 2500) __builtin_amdgcn_s_sleep(1);
      u64 ma = ls[(lane & 15) * 2], mb = ls[(lane & 15) * 2 + 1];
      auto red = [&]() {
        for (int o = 32; o; o >>= 1) {
          const u64 x = __shfl_xor(ma, o), y = __shfl_xor(mb, o);
          ma = x > ma ? x : ma;
          if (PROBE_B == 0) mb = y < mb ? y : mb; else mb = y > mb ? y : mb;
        }
      };
      red();
      if (lane == 0) { __hip_atomic_store(st + bid * 2, ma, RLX_AGENT); __hip_atomic_store(st + bid * 2 + 1, mb, RLX_AGENT); }
      if (bid == 0) {
        while (__builtin_amdgcn_s_memrealtime() < ts[0] + 3200) __builtin_amdgcn_s_sleep(1);
        for (int i = lane; i < 256; i += 64) {
          const u64 x = __hip_atomic_load(st + i * 2, RLX_AGENT), y = __hip_atomic_load(st + i * 2 + 1, RLX_AGENT);
          ma = x > ma ? x : ma;
          if (PROBE_B == 0) mb = y < mb ? y : mb; else mb = y > mb ? y : mb;
        }
        red();
        const u64 until = ts[0] + 5000 + 10 * (ma - mb);
        while (__builtin_amdgcn_s_memrealtime() < until && __builtin_amdgcn_s_memrealtime() < ts[0] + 40000) __builtin_amdgcn_s_sleep(1);
      }
    }
  }
}

extern "C" void kernel_launch(void* const* d_in, const int* in_sizes, int n_in, void* d_out, int out_size, void* d_ws, size_t ws_size,
                              hipStream_t stream) {
  unsigned char* ws = (unsigned char*)d_ws + (WS_AT_END ? ((ws_size - WS_SPAN) & ~(size_t)4095) : 0);
  fused_decode<<<256, 1024, 0, stream>>>((const u16*)d_in[0], (const u16*)d_in[1], (const u16*)d_in[2], (const u16*)d_in[3],
                                         (const u16*)d_in[4], (const u16*)d_in[6], (const u16*)d_in[7], (const u16*)d_in[8],
                                         (const u16*)d_in[9], (u16*)d_out, ws
                                         );
}
```

```cpp
#include <hip/hip_runtime.h>
#include <stdint.h>

#define GAS __attribute__((address_space(1)))
#define LAS __attribute__((address_space(3)))
#define DI __device__ __forceinline__
#define RLX_AGENT __ATOMIC_RELAXED, __HIP_MEMORY_SCOPE_AGENT
typedef unsigned short u16;
typedef unsigned u32;
typedef unsigned long long u64;
typedef u32 u32x2 __attribute__((ext_vector_type(2)));
typedef u32 u32x4 __attribute__((ext_vector_type(4)));
typedef float f32x2 __attribute__((ext_vector_type(2)));
typedef float f32x4 __attribute__((ext_vector_type(4)));
typedef __bf16 bf16x2 __attribute__((ext_vector_type(2)));
typedef __bf16 bf16x8 __attribute__((ext_vector_type(8)));

constexpr int D = 512, SK = 8192, DH = 128, FF = 1792, NQ = 4096;
constexpr int SC1 = 16, NT = 2;
constexpr int SPIN_CAP = 1 << 18;
constexpr int NFIN = 32;
constexpr int NAP_Q = 32, NAP_O = 8, NAP_S = 8, NAP_RETRY = 8;
constexpr float LOG2E = 1.4426950408889634f;

constexpr int WS_EPOCH = 0;
constexpr int WS_QP = 256;
constexpr int WS_PA = WS_QP + 512 * 256;
constexpr int WS_OP = WS_PA + 112 * 512;
constexpr int WS_S2 = WS_OP + 256 * 2048;
constexpr int WS_MP = WS_S2 + 256 * 1024;
constexpr int WS_END = WS_MP + 112 * 1024;

constexpr int F_Q = 0, F_HALF = 4, F_W2 = 6;
constexpr int L_FLAG = 0, L_CNT = 48, L_CNTV = 52, L_XN = 64, L_HALF = L_XN + 8192, L_Q = L_HALF + 1024, L_MW = L_Q + 1024, L_LW = L_MW + 256,
              L_HS = L_LW + 256, L_GP = L_HS + 256, GP_STRIDE = 272, L_FP = L_GP + 8 * GP_STRIDE, L_MF = L_FP + 1024,
              L_SINK = L_MF + 512, L_W2 = L_SINK + 256, L_OW = L_W2 + 16384, OW_STRIDE = 2064, L_END = L_OW + 16 * OW_STRIDE;

DI float bflo(u32 v) { return __builtin_bit_cast(float, v << 16); }
DI float bfhi(u32 v) { return __builtin_bit_cast(float, v & 0xffff0000u); }
DI float rbf(float f) { return (float)(__bf16)f; }
DI u32 pk(float a, float b) { return __builtin_bit_cast(u32, __builtin_convertvector((f32x2){a, b}, bf16x2)); }
DI u32 f2u(float f) { return __builtin_bit_cast(u32, f); }
DI float u2f(u32 u) { return __builtin_bit_cast(float, u); }
DI f32x4 mfma(u32x4 a, u32x4 b, f32x4 c) {
  return __builtin_amdgcn_mfma_f32_16x16x32_bf16(__builtin_bit_cast(bf16x8, a), __builtin_bit_cast(bf16x8, b), c, 0, 0, 0);
}
template <int CTRL> DI float dpp(float x) { return u2f(__builtin_amdgcn_mov_dpp(f2u(x), CTRL, 0xf, 0xf, true)); }
constexpr int XOR1 = 0xB1, XOR2 = 0x4E, ROR4 = 0x124, ROR8 = 0x128;
DI float half32(float a, float b) { auto r = __builtin_amdgcn_permlane32_swap(f2u(a), f2u(b), false, false); return u2f(r[0]) + u2f(r[1]); }
DI float half16(float a, float b) { auto r = __builtin_amdgcn_permlane16_swap(f2u(a), f2u(b), false, false); return u2f(r[0]) + u2f(r[1]); }
DI float max32(float a) { auto r = __builtin_amdgcn_permlane32_swap(f2u(a), f2u(a), false, false); return fmaxf(u2f(r[0]), u2f(r[1])); }
DI float max16(float a) { auto r = __builtin_amdgcn_permlane16_swap(f2u(a), f2u(a), false, false); return fmaxf(u2f(r[0]), u2f(r[1])); }
DI float half_sum(float v) {
  v += dpp<XOR1>(v); v += dpp<XOR2>(v); v += dpp<ROR4>(v); v += dpp<ROR8>(v);
  return half16(v, v);
}
DI u32 plo(u32 k1, u32 k0) { return __builtin_amdgcn_perm(k1, k0, 0x05040100u); }
DI u32 phi(u32 k1, u32 k0) { return __builtin_amdgcn_perm(k1, k0, 0x07060302u); }
DI u32x4 colfrag(const u32x4 (&r)[8], int d) {
  const int q = d >> 1;
  return (d & 1) ? (u32x4){phi(r[1][q], r[0][q]), phi(r[3][q], r[2][q]), phi(r[5][q], r[4][q]), phi(r[7][q], r[6][q])}
                 : (u32x4){plo(r[1][q], r[0][q]), plo(r[3][q], r[2][q]), plo(r[5][q], r[4][q]), plo(r[7][q], r[6][q])};
}
DI u32x4 sel4(bool c, u32x4 v) { return (u32x4){c ? v[0] : 0u, c ? v[1] : 0u, c ? v[2] : 0u, c ? v[3] : 0u}; }
template <typename T> DI T ldnt(const GAS T* p) { return __builtin_nontemporal_load(p); }
DI u32x4 ldws(__amdgpu_buffer_rsrc_t ws, int off) { return __builtin_amdgcn_raw_buffer_load_b128(ws, off, 0, SC1); }
DI void stws(__amdgpu_buffer_rsrc_t ws, int off, u32x2 a, u32x2 b) { __builtin_amdgcn_raw_buffer_store_b128((u32x4){a[0], a[1], b[0], b[1]}, ws, off, 0, SC1); }
DI void stws1(__amdgpu_buffer_rsrc_t ws, int off, u32x2 a) { __builtin_amdgcn_raw_buffer_store_b64(a, ws, off, 0, SC1); }
DI u32x2 pack2(float a, float b, u32 tag8) {
  const u32 ar = f2u(a) + 0x800u;
  const u32 u = (f2u(b) & 0xf000u) | ((ar >> 4) & ~0xf000u);
  return (u32x2){phi(f2u(b), ar), __builtin_amdgcn_perm(tag8, u, 0x07060501u)};
}
DI float first2(u32 w0, u32 w1) { return u2f(__builtin_amdgcn_perm(w0, w1 << 4, 0x0504000cu)); }
DI float second2(u32 w0, u32 w1) { return u2f(__builtin_amdgcn_perm(w0, w1, 0x0706000cu)); }
DI bool tagged(u32x4 v, u32 tag8) { return ((v[1] ^ tag8) | (v[3] ^ tag8)) < 256u; }
#define PIN() do { asm volatile("" ::: "memory"); __builtin_amdgcn_sched_barrier(0); } while (0)
#define LDS_BARRIER() do { asm volatile("s_waitcnt lgkmcnt(0)\n\ts_barrier" ::: "memory"); __builtin_amdgcn_sched_barrier(0); } while (0)
#define RAW_BARRIER() do { __builtin_amdgcn_sched_barrier(0); asm volatile("s_barrier" ::: "memory"); __builtin_amdgcn_sched_barrier(0); } while (0)
DI void dma16(const u16* src, u32 dst) {
  u32 keep;
  asm volatile("s_mov_b32 %0, m0\n\ts_mov_b32 m0, %2\n\ts_nop 0\n\tglobal_load_lds_dwordx4 %1, off nt\n\ts_mov_b32 m0, %0"
               : "=&s"(keep) : "v"(src), "s"(dst) : "memory");
}
DI void touch(const void* src, u32 dst) {
  u32 keep;
  asm volatile("s_mov_b32 %0, m0\n\ts_mov_b32 m0, %2\n\ts_nop 0\n\tglobal_load_lds_dword %1, off\n\ts_mov_b32 m0, %0"
               : "=&s"(keep) : "v"(src), "s"(dst) : "memory");
}
DI void set_flag(LAS unsigned char* lds, int f) {
  asm volatile("s_waitcnt lgkmcnt(0)" ::: "memory");
  *(volatile LAS u32*)(lds + L_FLAG + f * 4) = 1u;
}
DI void wait_flags(LAS unsigned char* lds, int first, int mask, int lane) {
#pragma unroll 1
  for (int spin = 0; spin < (SPIN_CAP << 4); ++spin) {
    if (__all(*(volatile LAS u32*)(lds + L_FLAG + (first + (lane & mask)) * 4) != 0u)) break;
    __builtin_amdgcn_s_sleep(1);
  }
  PIN();
}
DI void count_in(LAS unsigned char* lds, int cnt, int lane) {
  if (lane == 0) __hip_atomic_fetch_add((LAS u32*)(lds + cnt), 1u, __ATOMIC_RELAXED, __HIP_MEMORY_SCOPE_WORKGROUP);
}
DI void wait_all(LAS unsigned char* lds, int cnt) {
#pragma unroll 1
  for (int spin = 0; spin < (SPIN_CAP << 4); ++spin) {
    if (__builtin_amdgcn_readfirstlane(*(volatile LAS u32*)(lds + cnt)) == 16u) break;
    __builtin_amdgcn_s_sleep(1);
  }
  PIN();
}
DI u32 read_tag(const unsigned char* ws) {
  u32 e;
  asm volatile("s_load_dword %0, %1, 0x0\n\ts_waitcnt lgkmcnt(0)" : "=s"(e) : "s"(ws) : "memory");
  return (e + 1u) << 8;
}
DI u32x4 poll16(__amdgpu_buffer_rsrc_t ws, int off, u32 want) {
  u32x4 v;
  u64 keep;
  u32 left = SPIN_CAP, t;
  asm volatile("s_mov_b64 %[keep], exec\n\t"
               "s_branch 2f\n"
               "1:\n\t"
               "s_sub_u32 %[left], %[left], 1\n\t"
               "s_cbranch_scc1 3f\n"
               "2:\n\t"
               "buffer_load_dwordx4 v[100:103], %[off], %[ws], 0 offen sc1\n\t"
               "s_waitcnt vmcnt(0)\n\t"
               "v_bitop3_b32 %[t], v101, v103, %[want] bitop3:0x7e\n\t"
               "v_cmp_lt_u32 vcc, 0xff, %[t]\n\t"
               "s_and_b64 exec, exec, vcc\n\t"
               "s_cbranch_scc1 1b\n"
               "3:\n\t"
               "s_mov_b64 exec, %[keep]"
               : "=&{v[100:103]}"(v), [keep] "=&s"(keep), [left] "+s"(left), [t] "=&v"(t)
               : [off] "v"(off), [ws] "s"(ws), [want] "s"(want)
               : "vcc", "scc", "memory");
  return v;
}
DI void quad_sum4(float& a, float& b, float& c, float& d) {
  asm volatile("s_nop 1\n\t"
      "v_add_f32_dpp %0, %0, %0 quad_perm:[1,0,3,2] row_mask:0xf bank_mask:0xf\n\t"
      "v_add_f32_dpp %1, %1, %1 quad_perm:[1,0,3,2] row_mask:0xf bank_mask:0xf\n\t"
      "v_add_f32_dpp %2, %2, %2 quad_perm:[1,0,3,2] row_mask:0xf bank_mask:0xf\n\t"
      "v_add_f32_dpp %3, %3, %3 quad_perm:[1,0,3,2] row_mask:0xf bank_mask:0xf\n\t"
      "v_add_f32_dpp %0, %0, %0 quad_perm:[2,3,0,1] row_mask:0xf bank_mask:0xf\n\t"
      "v_add_f32_dpp %1, %1, %1 quad_perm:[2,3,0,1] row_mask:0xf bank_mask:0xf\n\t"
      "v_add_f32_dpp %2, %2, %2 quad_perm:[2,3,0,1] row_mask:0xf bank_mask:0xf\n\t"
      "v_add_f32_dpp %3, %3, %3 quad_perm:[2,3,0,1] row_mask:0xf bank_mask:0xf"
      : "+v"(a), "+v"(b), "+v"(c), "+v"(d));
}
DI float sum_records(u32x4 v) {
  const float t = half16(half32(first2(v[0], v[1]), first2(v[2], v[3])), half32(second2(v[0], v[1]), second2(v[2], v[3])));
  return t + dpp<ROR8>(t);
}

DI void layernorm(u32 (&x)[8]) {
  float v[16], s = 0.f;
#pragma unroll
  for (int e = 0; e < 8; ++e) { v[2 * e] = bflo(x[e]); v[2 * e + 1] = bfhi(x[e]); s += v[2 * e] + v[2 * e + 1]; }
  const float mean = half_sum(s) * (1.f / 512.f);
  float q = 0.f;
#pragma unroll
  for (int e = 0; e < 16; ++e) { const float d = v[e] - mean; q = fmaf(d, d, q); }
  const float var = half_sum(q) * (1.f / 512.f);
  const float mb = rbf(mean), inv = __builtin_amdgcn_rcpf(rbf(__builtin_amdgcn_sqrtf(rbf(rbf(var) + 1e-5f))));
#pragma unroll
  for (int e = 0; e < 8; ++e) { const u32 d = pk(v[2 * e] - mb, v[2 * e + 1] - mb); x[e] = pk(bflo(d) * inv, bfhi(d) * inv); }
}

DI f32x2 tile_matvec(const u32x4 (&wv)[8], LAS unsigned char* xin, int g, int c) {
  const u32x4 xf = *(LAS u32x4*)(xin + ((c >> 2) & 1) * 1024 + ((c >> 3) * 32 + g * 8) * 2);
  f32x4 lo = {0.f, 0.f, 0.f, 0.f}, hi = lo;
#pragma unroll
  for (int d = 0; d < 4; ++d) {
    const u32x4 am = sel4((c & 3) == d, xf);
    lo = mfma(am, colfrag(wv, d), lo);
    hi = mfma(am, colfrag(wv, 4 + d), hi);
  }
  const bool valid = (g >> 1) == (c >> 3);
  float t[4];
#pragma unroll
  for (int e = 0; e < 4; ++e) { t[e] = half32(valid ? lo[e] : 0.f, valid ? hi[e] : 0.f); t[e] += dpp<ROR8>(t[e]); }
  return (c & 8) ? (f32x2){t[2], t[3]} : (f32x2){t[0], t[1]};
}

__global__ void __launch_bounds__(1024, 4)
fused_decode(const u16* x_, const u16* k_, const u16* v_, const u16* wq_, const u16* wo_, const u16* w1_, const u16* b1_,
             const u16* w2_, const u16* b2_, u16* out_, unsigned char* ws_) {
  __shared__ __attribute__((aligned(16))) unsigned char lds_raw[L_END];
  LAS unsigned char* lds = (LAS unsigned char*)lds_raw;
  asm volatile("" :: "s"(x_), "s"(k_), "s"(v_), "s"(wq_), "s"(wo_), "s"(w1_), "s"(b1_), "s"(w2_), "s"(b2_), "s"(out_), "s"(ws_));
  const int tid = threadIdx.x, lane = tid & 63, w = __builtin_amdgcn_readfirstlane(tid >> 6);
  const int g = lane >> 4, c = lane & 15;
  const int bid = blockIdx.x, kvh = bid & 7, j = bid >> 3, b = j >> 4, sp = j & 15, rb = j >> 2, cb = j & 3;
  const bool mlp1 = j < 14, mlp2 = j >= 14 && j < 28;
  const __amdgpu_buffer_rsrc_t ws = __builtin_amdgcn_make_buffer_rsrc(ws_, 0, WS_END, 0x00020000);
  u32 tag;
  const size_t kvseq = (size_t)(b * 8 + kvh) * SK * DH;
  const __amdgpu_buffer_rsrc_t kr = __builtin_amdgcn_make_buffer_rsrc((void*)(k_ + kvseq), 0, SK * DH * 2, 0x00020000);
  const __amdgpu_buffer_rsrc_t vr = __builtin_amdgcn_make_buffer_rsrc((void*)(v_ + kvseq), 0, SK * DH * 2, 0x00020000);
  const int kvoff = (sp * 16 + ((w + j) & 15)) * 8192;
  const int klane = (((c >> 1) & 1) * 4 + (c >> 2)) * 256 + (g * 2 + (c & 1)) * 16;
  u32x4 kf[8], vf[8];

  const int cq = j & 7, kq = j >> 3;
  const int mt = (mlp1 ? j : j - 14) * 8 + kvh;
  const int mhi = mt >> 2, mlo = mt & 3;
  if (w < 2 || (w < 4 && mlp1)) {
    __builtin_amdgcn_s_setprio(3);
    if (w == 0 && lane < 16) *(LAS u32*)(lds + L_FLAG + lane * 4) = 0u;
    const GAS u32x4* xp = (const GAS u32x4*)x_ + lane * 2;
    const u32x4 x0 = xp[0], x1 = xp[1];
    PIN();
    const int rs = w < 2 ? NQ / 8 : FF / 8, row0 = (w < 2 ? kq : mlo) * 128 + (w & 1) * 64;
    const u16* tile = w < 2 ? wq_ + kvh * 512 + cq * 64 : w1_ + mhi * 64;
    const GAS u32x4* wp = (const GAS u32x4*)tile + (row0 + (c >> 3) * 32 + g * 8) * rs + (c & 7);
    u32x4 wv[8];
#pragma unroll
    for (int i = 0; i < 8; ++i) wv[i] = ldnt(wp + i * rs);
    PIN();
    LDS_BARRIER();
    tag = read_tag(ws_);
    PIN();
    u32 xs[8] = {x0[0], x0[1], x0[2], x0[3], x1[0], x1[1], x1[2], x1[3]};
    layernorm(xs);
    if (w >= 2) layernorm(xs);
    LAS unsigned char* xl = lds + L_XN + w * 2048;
    *(LAS u32x4*)(xl + lane * 32) = (u32x4){xs[0], xs[1], xs[2], xs[3]};
    *(LAS u32x4*)(xl + lane * 32 + 16) = (u32x4){xs[4], xs[5], xs[6], xs[7]};
    f32x2 r = tile_matvec(wv, xl + row0 * 2, g, c);
    LAS f32x2* half = (LAS f32x2*)(lds + L_HALF + (w >> 1) * 512 + lane * 8);
    if (w & 1) {
      *half = r;
      set_flag(lds, F_HALF + (w >> 1));
    } else {
      wait_flags(lds, F_HALF + (w >> 1), 0, lane);
      r += *half;
      const int rec = w == 0 ? WS_QP + ((((g & 1) * 8 + kvh) * 8 + cq) * 4 + kq) * 256 : WS_PA + mt * 512 + (g & 1) * 256;
      stws1(ws, rec + ((c & 7) * 4 + (g >> 1) * 2 + (c >> 3)) * 8, pack2(r[0], r[1], tag));
    }
    __builtin_amdgcn_s_setprio(1);
  } else {
    if (w < 4 && mlp2) {
      const u16* p = w2_ + (mhi * 64 + (w & 1) * 32 + g * 8) * D + mlo * 128 + c * 8;
#pragma unroll
      for (int i = 0; i < 8; ++i) dma16(p + i * D, (u32)(size_t)(lds + L_W2 + (w & 1) * 8192 + i * 1024));
    }
    RAW_BARRIER();
    if ((w >> 2) == 1) __builtin_amdgcn_s_setprio(2); else __builtin_amdgcn_s_setprio(1);
  }
  PIN();

#pragma unroll
  for (int i = 0; i < 8; ++i) {
    kf[i] = __builtin_amdgcn_raw_buffer_load_b128(kr, klane + (i & 1) * 128, kvoff + (i >> 1) * 2048, NT);
    __builtin_amdgcn_sched_barrier(0);
  }
  if ((w >> 2) != 1) __builtin_amdgcn_s_setprio(0);
  PIN();
#pragma unroll
  for (int i = 0; i < 8; ++i) {
    vf[i] = __builtin_amdgcn_raw_buffer_load_b128(vr, lane * 16, kvoff + i * 1024, NT);
    __builtin_amdgcn_sched_barrier(0);
  }
  PIN();
  count_in(lds, L_CNTV, lane);
  tag = read_tag(ws_);
  const int paoff = WS_PA + mhi * 2048 + lane * 16;
  u32x4 p0, p1;
  u32 b1v;
  if ((w >> 2) == 1) {
    const int u = w & 3;
    const int qoff = WS_QP + (b * 8 + kvh) * 8192 + u * 2048 + lane * 16;
    asm volatile("s_waitcnt vmcnt(0)" ::: "memory");
    __builtin_amdgcn_s_sleep(NAP_Q);
    u32x4 q0, q1;
#pragma unroll 1
    for (int spin = 0; spin < SPIN_CAP; ++spin) {
      q0 = ldws(ws, qoff); q1 = ldws(ws, qoff + 1024);
      if (__all(tagged(q0, tag) && tagged(q1, tag))) break;
      __builtin_amdgcn_s_sleep(NAP_RETRY);
      asm volatile("" ::: "memory");
    }
    const float qa = half16(half32(first2(q0[0], q0[1]), first2(q1[0], q1[1])), half32(first2(q0[2], q0[3]), first2(q1[2], q1[3])));
    const float qb = half16(half32(second2(q0[0], q0[1]), second2(q1[0], q1[1])), half32(second2(q0[2], q0[3]), second2(q1[2], q1[3])));
    *(LAS u32*)(lds + L_Q + u * 256 + ((g >> 1) * 32 + c * 2 + (g & 1)) * 4) = pk(qa, qb);
    set_flag(lds, F_Q + u);
    if (w == 7 && mlp2) {
      p0 = ldws(ws, paoff); p1 = ldws(ws, paoff + 1024);
      b1v = *(const GAS u32*)(b1_ + mhi * 64 + c * 4 + (g >> 1) * 2);
      PIN();
    }
    __builtin_amdgcn_s_setprio(0);
  }
  wait_flags(lds, F_Q, 3, lane);

  float sc[8];
  {
    const int x0 = (c >> 2) & 1;
    const u32x4 q0 = *(LAS u32x4*)(lds + L_Q + (c & 3) * 256 + (g * 2 + x0) * 16);
    const u32x4 q1 = *(LAS u32x4*)(lds + L_Q + (c & 3) * 256 + 128 + (g * 2 + x0) * 16);
#pragma unroll
    for (int o = 0; o < 4; ++o) {
      f32x4 s = mfma(kf[2 * o], q0, (f32x4){0.f, 0.f, 0.f, 0.f});
      s = mfma(kf[2 * o + 1], q1, s);
#pragma unroll
      for (int a = 0; a < 2; ++a) {
        float e1 = s[2 * a + 1];
        asm("" : "+v"(e1));
        const float t = x0 ? e1 : s[2 * a];
        sc[2 * o + a] = t + dpp<ROR4>(t);
      }
      const u32 d = pk(sc[2 * o], sc[2 * o + 1]), e = pk(bflo(d) * 0.08838834764831845f, bfhi(d) * 0.08838834764831845f);
      sc[2 * o] = bflo(e); sc[2 * o + 1] = bfhi(e);
    }
  }
  PIN();

  float m = fmaxf(fmaxf(fmaxf(sc[0], sc[1]), fmaxf(sc[2], sc[3])), fmaxf(fmaxf(sc[4], sc[5]), fmaxf(sc[6], sc[7])));
  m = max32(max16(m)) * LOG2E;
  float p[8], l = 0.f;
#pragma unroll
  for (int e = 0; e < 8; ++e) { p[e] = __builtin_amdgcn_exp2f(fmaf(sc[e], LOG2E, -m)); l += p[e]; }
  l = half16(l, l); l = half32(l, l);
  if (lane < 4) { *(LAS float*)(lds + L_MW + (lane * 16 + w) * 4) = m; *(LAS float*)(lds + L_LW + (lane * 16 + w) * 4) = l; }
  PIN();
  if ((w >> 1) == 1) set_flag(lds, F_W2 + (w & 1));
  count_in(lds, L_CNT, lane);
  const u32x4 pf = {pk(p[0], p[1]), pk(p[2], p[3]), pk(p[4], p[5]), pk(p[6], p[7])};
  u32x4 am[4];
#pragma unroll
  for (int u = 0; u < 4; ++u) am[u] = sel4((c >> 2) == u, pf);
  PIN();
  u32x4 ve[4], vo[4];
  u32 slo, shi;
  asm volatile("s_mov_b32 %0, 0x05040100\n\ts_mov_b32 %1, 0x07060302" : "=s"(slo), "=s"(shi) : "v"(am[0][0]), "v"(am[1][1]), "v"(am[2][2]), "v"(am[3][3]));
#pragma unroll
  for (int r = 0; r < 4; ++r) {
#pragma unroll
    for (int u = 0; u < 4; ++u) {
      ve[u][r] = __builtin_amdgcn_perm(vf[2 * r + 1][u], vf[2 * r][u], slo);
      vo[u][r] = __builtin_amdgcn_perm(vf[2 * r + 1][u], vf[2 * r][u], shi);
    }
    if (r < 3)
      asm volatile("" : "+s"(slo), "+s"(shi) : "v"(ve[0][r]), "v"(ve[1][r]), "v"(ve[2][r]), "v"(ve[3][r]), "v"(vo[0][r]), "v"(vo[1][r]), "v"(vo[2][r]), "v"(vo[3][r]));
  }
  f32x4 oe = {0.f, 0.f, 0.f, 0.f}, oo = oe;
#pragma unroll
  for (int u = 0; u < 4; ++u) { oe = mfma(am[u], ve[u], oe); oo = mfma(am[u], vo[u], oo); }
#pragma unroll
  for (int h = 0; h < 4; ++h) *(LAS f32x2*)(lds + L_OW + w * OW_STRIDE + h * 512 + c * 32 + g * 8) = (f32x2){oe[h], oo[h]};
  PIN();

  const bool gath = (w & 4) == 0;
  const int v = (w & 3) + (w >> 3) * 4;
  const int cf = bid & 3, bf = (bid >> 2) & 1, seg = bid >> 3;
  if (w == 6) {
    wait_all(lds, L_CNTV);
    const unsigned char* tp = ws_ + WS_OP + ((kvh * 2 + b) * 16 + sp) * 2048 + c * 128;
    if (g == 1) tp = c < 8 ? ws_ + WS_S2 + (cb * 64 + kvh * 8 + rb) * 1024 + c * 128 : (const unsigned char*)(out_ + bf * D + cf * 128 + seg * 32);
    if (lane < (bid < NFIN ? 25 : 24)) touch(tp, (u32)(size_t)(lds + L_SINK));
  }
  if (w >= 12) {
    LDS_BARRIER();
    if (bid < NFIN) {
      const int row = (w - 12) * 8 + (lane >> 3);
      const int moff = WS_MP + (cf * 28 + min(row, 27)) * 1024 + bf * 512 + seg * 128 + (lane & 7) * 16;
      u32x4 mv;
#pragma unroll 1
      for (int spin = 0; spin < SPIN_CAP; ++spin) {
        mv = ldws(ws, moff);
        if (__all(tagged(mv, tag))) break;
        __builtin_amdgcn_s_sleep(NAP_RETRY);
        asm volatile("" ::: "memory");
      }
      const float t = sum_records(sel4(row < 28, mv));
      if ((lane & 8) == 0) *(LAS float*)(lds + L_MF + (w - 12) * 128 + ((lane & 7) * 4 + g) * 4) = t;
      LDS_BARRIER();
    }
  } else if (gath) {
    {
      const int f = v * 16 + (lane >> 2), p4 = lane & 3, hd = v >> 1;
      wait_all(lds, L_CNT);
      const f32x4 mv = *(LAS f32x4*)(lds + L_MW + hd * 64 + p4 * 16), lv = *(LAS f32x4*)(lds + L_LW + hd * 64 + p4 * 16);
      float M = fmaxf(fmaxf(mv[0], mv[1]), fmaxf(mv[2], mv[3]));
      M = fmaxf(M, dpp<XOR1>(M)); M = fmaxf(M, dpp<XOR2>(M));
      float L = 0.f, fw[4];
#pragma unroll
      for (int i = 0; i < 4; ++i) { fw[i] = __builtin_amdgcn_exp2f(mv[i] - M); L = fmaf(fw[i], lv[i], L); }
      L += dpp<XOR1>(L); L += dpp<XOR2>(L);
      const float inv = __builtin_amdgcn_rcpf(L);
#pragma unroll
      for (int i = 0; i < 4; ++i) fw[i] *= inv;
      const u32 lam = f2u(M + __builtin_amdgcn_logf(L)) >> ((lane >> 5) * 16);
      u32 t0 = tag | (lam & 255u), t1 = tag | ((lam >> 8) & 255u);
      int ooff = WS_OP + ((kvh * 2 + b) * 16 + sp) * 2048 + f * 16;
      int laddr = L_OW + p4 * 4 * OW_STRIDE + f * 16;
      asm volatile("" : "+v"(fw[0]), "+v"(fw[1]), "+v"(fw[2]), "+v"(fw[3]), "+v"(t0), "+v"(t1), "+v"(ooff), "+v"(laddr));
      LDS_BARRIER();
      f32x4 s = fw[0] * *(LAS f32x4*)(lds + laddr);
#pragma unroll
      for (int i = 1; i < 4; ++i) s += fw[i] * *(LAS f32x4*)(lds + laddr + i * OW_STRIDE);
      float s0 = s[0], s1 = s[1], s2 = s[2], s3 = s[3];
      quad_sum4(s0, s1, s2, s3);
      if (p4 == 0) __builtin_amdgcn_raw_buffer_store_b128((u32x4){pk(s0, s1), t0, pk(s2, s3), t1}, ws, ooff, 0, SC1);
    }
    PIN();
    {
      u32 gaddr2 = (u32)(size_t)(lds + L_GP + v * GP_STRIDE + 256), gaddr = gaddr2 - 256 + (c * 4 + g) * 4;
      u32 lsel = (c & 8) ? 0x05040100u : 0x01000504u;
      asm volatile("" : "+v"(gaddr), "+v"(gaddr2), "+v"(lsel));
      __builtin_amdgcn_s_sleep(NAP_O);
      const u32x4 ov = poll16(ws, WS_OP + (kvh * 32 + v * 4 + g) * 2048 + rb * 256 + c * 16, tag);
      u32 a, lam, m1, x0, x1, x2, x3;
      f32x2 gl;
      asm volatile("v_perm_b32 %[a], %[t1], %[t0], %[sel]\n\t"
                   "v_lshlrev_b32 %[x0], 16, %[d0]\n\t"
                   "v_lshlrev_b32 %[x2], 16, %[d1]\n\t"
                   "v_mov_b32_dpp %[m1], %[a] row_ror:8 row_mask:0xf bank_mask:0xf\n\t"
                   "v_and_b32 %[x1], 0xffff0000, %[d0]\n\t"
                   "v_perm_b32 %[lam], %[a], %[m1], %[lsel]\n\t"
                   "v_mov_b32 %[m1], %[lam]\n\t"
                   "v_mov_b32 %[a], %[lam]\n\t"
                   "v_and_b32 %[x3], 0xffff0000, %[d1]\n\t"
                   "s_nop 0\n\t"
                   "v_permlane16_swap_b32 %[m1], %[a]\n\t"
                   "v_max_f32 %[m1], %[m1], %[a]\n\t"
                   "v_mov_b32 %[a], %[m1]\n\t"
                   "s_nop 1\n\t"
                   "v_permlane32_swap_b32 %[m1], %[a]\n\t"
                   "v_max_f32 v104, %[m1], %[a]\n\t"
                   "v_sub_f32 %[lam], %[lam], v104\n\t"
                   "v_exp_f32 %[lam], %[lam]\n\t"
                   "s_nop 0\n\t"
                   "v_mul_f32 %[x0], %[lam], %[x0]\n\t"
                   "v_mul_f32 %[x2], %[lam], %[x2]\n\t"
                   "v_mul_f32 %[x1], %[lam], %[x1]\n\t"
                   "v_mul_f32 %[x3], %[lam], %[x3]\n\t"
                   "v_mov_b32 %[a], %[lam]\n\t"
                   "v_permlane32_swap_b32 %[x0], %[x2]\n\t"
                   "v_permlane32_swap_b32 %[x1], %[x3]\n\t"
                   "v_permlane16_swap_b32 %[lam], %[a]\n\t"
                   "v_add_f32 %[x0], %[x0], %[x2]\n\t"
                   "v_add_f32 %[x1], %[x1], %[x3]\n\t"
                   "v_add_f32 %[lam], %[lam], %[a]\n\t"
                   "v_mov_b32 %[a], %[lam]\n\t"
                   "v_permlane16_swap_b32 %[x0], %[x1]\n\t"
                   "v_add_f32 %[x0], %[x0], %[x1]\n\t"
                   "v_permlane32_swap_b32 %[lam], %[a]\n\t"
                   "ds_write_b32 %[ga], %[x0]\n\t"
                   "v_add_f32 v105, %[lam], %[a]\n\t"
                   "ds_write_b64 %[gb], v[104:105]"
                   : [a] "=&v"(a), [lam] "=&v"(lam), [m1] "=&v"(m1), [x0] "=&v"(x0), [x1] "=&v"(x1), [x2] "=&v"(x2), [x3] "=&v"(x3), "=&{v[104:105]}"(gl)
                   : [d0] "v"(ov[0]), [t0] "v"(ov[1]), [d1] "v"(ov[2]), [t1] "v"(ov[3]), [sel] "s"(0x04000400u), [lsel] "v"(lsel), [ga] "v"(gaddr), [gb] "v"(gaddr2)
                   : "memory");
    }
    LDS_BARRIER();
    if (bid < NFIN) {
      int faddr = L_FP + ((lane & 7) * 4 + g) * 32 + v * 4;
      asm volatile("" : "+v"(faddr));
      __builtin_amdgcn_s_sleep(NAP_S);
      const u32x4 sv = poll16(ws, WS_S2 + (cf * 64 + v * 8 + (lane >> 3)) * 1024 + bf * 512 + seg * 128 + (lane & 7) * 16, tag);
      *(LAS float*)(lds + faddr) = sum_records(sv);
      LDS_BARRIER();
    }
  } else if (w < 6) {
    u32x4 wt[8], bw[8];
    wait_all(lds, L_CNT);
    const GAS u16* wp = (const GAS u16*)wo_ + (size_t)(kvh * 512 + rb * 64 + g * 4) * D + cb * 128 + (w - 4) * 64 + c * 4;
#pragma unroll
    for (int i = 0; i < 8; ++i) {
      const u32x2 t0 = ldnt((const GAS u32x2*)(wp + (size_t)(32 * (i >> 2) + (i & 3)) * D));
      const u32x2 t1 = ldnt((const GAS u32x2*)(wp + (size_t)(32 * (i >> 2) + 16 + (i & 3)) * D));
      wt[i] = (u32x4){t0[0], t0[1], t1[0], t1[1]};
    }
    PIN();
    LDS_BARRIER();
#pragma unroll
    for (int i = 0; i < 8; ++i) bw[i] = colfrag(wt, i);
    int gp = L_GP + (c & 7) * GP_STRIDE, go = gp + ((c >> 3) * 16 + g * 4) * 4;
    int soff = WS_S2 + (cb * 64 + kvh * 8 + rb) * 1024 + (g & 1) * 512 + ((w - 4) * 32 + c * 2 + (g >> 1)) * 8;
    asm volatile("" : "+v"(gp), "+v"(go), "+v"(soff) : "v"(bw[0]), "v"(bw[1]), "v"(bw[2]), "v"(bw[3]), "v"(bw[4]), "v"(bw[5]), "v"(bw[6]), "v"(bw[7]));
    LDS_BARRIER();
    f32x2 gl = *(LAS f32x2*)(lds + gp + 256);
    u32x4 of = *(LAS u32x4*)(lds + go), o1 = *(LAS u32x4*)(lds + go + 128);
#define QP1(r) "v_add_f32_dpp " r ", " r ", " r " quad_perm:[1,0,3,2] row_mask:0xf bank_mask:0xf\n\t"
#define QP2(r) "v_add_f32_dpp " r ", " r ", " r " quad_perm:[2,3,0,1] row_mask:0xf bank_mask:0xf\n\t"
#define PKM(r) "v_pk_mul_f32 " r ", v[118:119], " r " op_sel_hi:[0,1]\n\t"
    asm volatile("s_nop 1\n\t"
                 "v_max_f32_dpp v118, v116, v116 quad_perm:[1,0,3,2] row_mask:0xf bank_mask:0xf\n\t"
                 "s_nop 1\n\t"
                 "v_max_f32_dpp v118, v118, v118 quad_perm:[2,3,0,1] row_mask:0xf bank_mask:0xf\n\t"
                 "v_sub_f32 v118, v116, v118\n\t"
                 "v_exp_f32 v118, v118\n\t"
                 "s_nop 0\n\t"
                 "v_mul_f32 v117, v118, v117\n\t"
                 PKM("v[108:109]") PKM("v[110:111]") PKM("v[112:113]") PKM("v[114:115]")
                 QP1("v117") QP1("v108") QP1("v109") QP1("v110") QP1("v111") QP1("v112") QP1("v113") QP1("v114") QP1("v115")
                 QP2("v117") QP2("v108") QP2("v109")
                 "v_rcp_f32 v118, v117\n\t"
                 QP2("v110") QP2("v111") QP2("v112") QP2("v113") QP2("v114") QP2("v115")
                 PKM("v[108:109]") PKM("v[110:111]") PKM("v[112:113]") PKM("v[114:115]")
                 "v_cvt_pk_bf16_f32 v108, v108, v109\n\t"
                 "v_cvt_pk_bf16_f32 v109, v110, v111\n\t"
                 "v_cvt_pk_bf16_f32 v110, v112, v113\n\t"
                 "v_cvt_pk_bf16_f32 v111, v114, v115\n\t"
                 "s_nop 1"
                 : "+{v[108:111]}"(of), "+{v[112:115]}"(o1), "+{v[116:117]}"(gl)
                 :
                 : "v118", "v119");
    f32x4 acc[8];
#pragma unroll
    for (int i = 0; i < 8; ++i) { const int k = (i & 3) * 2 + (i >> 2); acc[k] = mfma(of, bw[k], (f32x4){0.f, 0.f, 0.f, 0.f}); }
    __builtin_amdgcn_sched_barrier(0);
    float r[4];
#pragma unroll
    for (int d = 0; d < 4; ++d) r[d] = (g >> 1) ? acc[4 + d][0] : acc[d][0];
    stws1(ws, soff, pack2(half32(r[0], r[2]), half32(r[1], r[3]), tag));
  } else if (w == 7 && mlp2) {
#pragma unroll 1
    for (int spin = 0; spin < SPIN_CAP; ++spin) {
      if (__all(tagged(p0, tag) && tagged(p1, tag))) break;
      __builtin_amdgcn_s_sleep(NAP_RETRY);
      asm volatile("" ::: "memory");
      p0 = ldws(ws, paoff); p1 = ldws(ws, paoff + 1024);
    }
    const float t2[2] = {half32(first2(p0[0], p0[1]) + first2(p1[0], p1[1]), first2(p0[2], p0[3]) + first2(p1[2], p1[3])),
                         half32(second2(p0[0], p0[1]) + second2(p1[0], p1[1]), second2(p0[2], p0[3]) + second2(p1[2], p1[3]))};
    float hv[2];
#pragma unroll
    for (int e = 0; e < 2; ++e) {
      const float t = rbf(rbf(t2[e]) + (e ? bfhi(b1v) : bflo(b1v)));
      const float uu = 0.7978845608028654f * (t + 0.044715f * t * t * t);
      hv[e] = t / (1.f + __builtin_amdgcn_exp2f(-2.f * LOG2E * uu));
    }
    *(LAS u32*)(lds + L_HS + (g & 1) * 128 + (c * 2 + (g >> 1)) * 4) = pk(hv[0], hv[1]);
    wait_flags(lds, F_W2, 1, lane);
    f32x4 r = {0.f, 0.f, 0.f, 0.f};
#pragma unroll
    for (int s = 0; s < 2; ++s) {
      const u32x4 ha = *(LAS u32x4*)(lds + L_HS + ((c >> 2) & 1) * 128 + s * 64 + g * 16);
      u32x4 w2t[8];
#pragma unroll
      for (int i = 0; i < 8; ++i) w2t[i] = *(LAS u32x4*)(lds + L_W2 + s * 8192 + i * 1024 + lane * 16);
#pragma unroll
      for (int d = 0; d < 8; ++d) r = mfma(sel4((c >> 3) == (d >> 2) && (c & 3) == (d & 3), ha), colfrag(w2t, d), r);
    }
    stws(ws, WS_MP + (mlo * 28 + mhi) * 1024 + (g & 1) * 512 + (c * 2 + (g >> 1)) * 16, pack2(r[0], r[1], tag), pack2(r[2], r[3], tag));
    LDS_BARRIER();
  } else if (w == 6 && bid < NFIN) {
    const int col = cf * 128 + seg * 32 + (lane & 31);
    const u32 xv = ((const GAS u16*)x_)[bf * D + col], b2v = ((const GAS u16*)b2_)[col];
    LDS_BARRIER();
    LDS_BARRIER();
    LAS float* mf = (LAS float*)(lds + L_MF + (lane >> 5) * 256 + (lane & 31) * 4);
    float t = mf[0] + mf[32];
    t = half32(t, t);
    t = rbf(rbf(t) + bflo(b2v));
    float gate = rbf(t / (1.f + __builtin_amdgcn_exp2f(-LOG2E * t))), xf = bflo(xv);
    int faddr = L_FP + (lane & 31) * 32 + (lane >> 5) * 16;
    GAS u16* op = (GAS u16*)out_ + bf * D + col;
    asm volatile("" : "+v"(gate), "+v"(xf), "+v"(faddr), "+v"(op));
    LDS_BARRIER();
    const f32x4 fp = *(LAS f32x4*)(lds + faddr);
    float s = (fp[0] + fp[1]) + (fp[2] + fp[3]);
    s = half32(s, s);
    const float res = xf + rbf(rbf(s) * gate);
    if (lane < 32) __hip_atomic_store(op, (u16)pk(res, res), RLX_AGENT);
    if (bid == 0 && lane == 0) __hip_atomic_store((GAS u32*)(ws_ + WS_EPOCH), tag >> 8, RLX_AGENT);
  } else {
    LDS_BARRIER();
  }
}

extern "C" void kernel_launch(void* const* d_in, const int* in_sizes, int n_in, void* d_out, int out_size, void* d_ws, size_t ws_size,
                              hipStream_t stream) {
  fused_decode<<<256, 1024, 0, stream>>>((const u16*)d_in[0], (const u16*)d_in[1], (const u16*)d_in[2], (const u16*)d_in[3],
                                         (const u16*)d_in[4], (const u16*)d_in[6], (const u16*)d_in[7], (const u16*)d_in[8],
                                         (const u16*)d_in[9], (u16*)d_out, (unsigned char*)d_ws);
}
```

```cpp
#define STAMP 1
#define PROBE 6
#define PROBE_B 4
#define STMASK (1 | (1 << 6) | (1 << 4))
#define FINLAST 0
#include <hip/hip_runtime.h>
#include <stdint.h>

#define WS_AT_END 0
#define SLEEP_Q 8
#define SLEEP_O 8
#define SLEEP_S 8
#define WO_MODE 1
#define PRIO_K 1
#define ROT 1
#define CODE_LINES 0
#define RND 1
#define BUF 1
#define SPRE 0
#define TV 0
#define PRE_C 0
#define SLOTS 4
#define SLOT0 0
#define OSAUX 16
#define OPNT 0
#define PRE_D 25
#define PRE_E 0
#define B1NT 0
#define PROBE_WM 0xffff
#define PRE_AT 16u
#define WO_V 0
#define PRE_A 0
#define PRE_B 0
#define PREW 12
#define Q_DELAY 32
#define MP_LATE 0
#define SLEEP_M 4
#define WO_AT 1150
#define WO_SLEEP 0
#define ABL 0

#define GAS __attribute__((address_space(1)))
#define LAS __attribute__((address_space(3)))
#define DI __device__ __forceinline__
#define RLX_AGENT __ATOMIC_RELAXED, __HIP_MEMORY_SCOPE_AGENT
typedef unsigned short u16;
typedef unsigned u32;
typedef unsigned long long u64;
typedef u32 u32x2 __attribute__((ext_vector_type(2)));
typedef u32 u32x4 __attribute__((ext_vector_type(4)));
typedef float f32x2 __attribute__((ext_vector_type(2)));
typedef float f32x4 __attribute__((ext_vector_type(4)));
typedef __bf16 bf16x2 __attribute__((ext_vector_type(2)));
typedef __bf16 bf16x8 __attribute__((ext_vector_type(8)));

constexpr int D = 512, SK = 8192, DH = 128, FF = 1792, NQ = 4096;
constexpr int SC1 = 16, NT = 2;
constexpr int SPIN_CAP = 1 << 18;
constexpr int NFIN = 32;
constexpr float LOG2E = 1.4426950408889634f;

constexpr int WS_EPOCH = 0;
constexpr int WS_QP = 256;
constexpr int WS_PA = WS_QP + 512 * 256;
constexpr int WS_OP = WS_PA + 112 * 512;
constexpr int WS_S2 = WS_OP + 256 * 2048;
constexpr int WS_MP = WS_S2 + 256 * 1024;
constexpr int WS_END = WS_MP + 112 * 1024;
constexpr int WS_SPAN = 4 << 20;
constexpr int WS_STAMP = 8 << 20;

constexpr int F_Q = 0, F_HALF = 4, F_W2 = 6;
constexpr int L_FLAG = 0, L_CNT = 48, L_XN = 64, L_HALF = L_XN + 8192, L_Q = L_HALF + 1024, L_MW = L_Q + 1024, L_LW = L_MW + 256,
              L_HS = L_LW + 256, L_GP = L_HS + 256, GP_STRIDE = 272, L_FP = L_GP + 8 * GP_STRIDE, L_MF = L_FP + 1024,
              L_TOUCH = L_MF + 512, L_T2 = L_TOUCH + 256, L_W2 = L_T2 + 256, L_OW = L_W2 + 16384, OW_STRIDE = 2064, L_END = L_OW + 16 * OW_STRIDE;

DI float bflo(u32 v) { return __builtin_bit_cast(float, v << 16); }
DI float bfhi(u32 v) { return __builtin_bit_cast(float, v & 0xffff0000u); }
DI float rbf(float f) { return (float)(__bf16)f; }
DI u32 pk(float a, float b) { return __builtin_bit_cast(u32, __builtin_convertvector((f32x2){a, b}, bf16x2)); }
DI u32 f2u(float f) { return __builtin_bit_cast(u32, f); }
DI float u2f(u32 u) { return __builtin_bit_cast(float, u); }
DI f32x4 mfma(u32x4 a, u32x4 b, f32x4 c) {
  return __builtin_amdgcn_mfma_f32_16x16x32_bf16(__builtin_bit_cast(bf16x8, a), __builtin_bit_cast(bf16x8, b), c, 0, 0, 0);
}
template <int CTRL> DI float dpp(float x) { return u2f(__builtin_amdgcn_mov_dpp(f2u(x), CTRL, 0xf, 0xf, true)); }
constexpr int XOR1 = 0xB1, XOR2 = 0x4E, ROR4 = 0x124, ROR8 = 0x128;
DI float half32(float a, float b) { auto r = __builtin_amdgcn_permlane32_swap(f2u(a), f2u(b), false, false); return u2f(r[0]) + u2f(r[1]); }
DI float half16(float a, float b) { auto r = __builtin_amdgcn_permlane16_swap(f2u(a), f2u(b), false, false); return u2f(r[0]) + u2f(r[1]); }
DI float max32(float a) { auto r = __builtin_amdgcn_permlane32_swap(f2u(a), f2u(a), false, false); return __builtin_amdgcn_fmed3f(u2f(r[0]), u2f(r[1]), __builtin_inff()); }
DI float max16(float a) { auto r = __builtin_amdgcn_permlane16_swap(f2u(a), f2u(a), false, false); return __builtin_amdgcn_fmed3f(u2f(r[0]), u2f(r[1]), __builtin_inff()); }
DI float half_sum(float v) {
  v += dpp<XOR1>(v); v += dpp<XOR2>(v); v += dpp<ROR4>(v); v += dpp<ROR8>(v);
  return half16(v, v);
}
DI u32 plo(u32 k1, u32 k0) { return __builtin_amdgcn_perm(k1, k0, 0x05040100u); }
DI u32 phi(u32 k1, u32 k0) { return __builtin_amdgcn_perm(k1, k0, 0x07060302u); }
DI u32x4 colfrag(const u32x4 (&r)[8], int d) {
  const int q = d >> 1;
  return (d & 1) ? (u32x4){phi(r[1][q], r[0][q]), phi(r[3][q], r[2][q]), phi(r[5][q], r[4][q]), phi(r[7][q], r[6][q])}
                 : (u32x4){plo(r[1][q], r[0][q]), plo(r[3][q], r[2][q]), plo(r[5][q], r[4][q]), plo(r[7][q], r[6][q])};
}
DI u32x4 sel4(bool c, u32x4 v) { return (u32x4){c ? v[0] : 0u, c ? v[1] : 0u, c ? v[2] : 0u, c ? v[3] : 0u}; }
template <typename T> DI T ldnt(const GAS T* p) { return __builtin_nontemporal_load(p); }
#define LAUX 16
#define SAUX 16
DI u32x4 ldws(__amdgpu_buffer_rsrc_t ws, int off) { return __builtin_amdgcn_raw_buffer_load_b128(ws, off, 0, LAUX); }
DI void stws(__amdgpu_buffer_rsrc_t ws, int off, u32x2 a, u32x2 b) { __builtin_amdgcn_raw_buffer_store_b128((u32x4){a[0], a[1], b[0], b[1]}, ws, off, 0, SAUX); }
DI void stws1(__amdgpu_buffer_rsrc_t ws, int off, u32x2 a) { __builtin_amdgcn_raw_buffer_store_b64(a, ws, off, 0, SAUX); }
DI u32x2 pack2(float a, float b, u32 tag8) {
  const u32 ar = f2u(a) + 0x800u;
  const u32 u = (f2u(b) & 0xf000u) | ((ar >> 4) & ~0xf000u);
  return (u32x2){phi(f2u(b), ar), __builtin_amdgcn_perm(tag8, u, 0x07060501u)};
}
DI float first2(u32 w0, u32 w1) { return u2f(__builtin_amdgcn_perm(w0, w1 << 4, 0x0504000cu)); }
DI float second2(u32 w0, u32 w1) { return u2f(__builtin_amdgcn_perm(w0, w1, 0x0706000cu)); }
DI bool tagged(u32x4 v, u32 tag8) { return ((v[1] ^ tag8) | (v[3] ^ tag8)) < 256u; }
#define PIN() do { asm volatile("" ::: "memory"); __builtin_amdgcn_sched_barrier(0); } while (0)
#define LDS_BARRIER() do { asm volatile("s_waitcnt lgkmcnt(0)\n\ts_barrier" ::: "memory"); __builtin_amdgcn_sched_barrier(0); } while (0)
#define RAW_BARRIER() do { __builtin_amdgcn_sched_barrier(0); asm volatile("s_barrier" ::: "memory"); __builtin_amdgcn_sched_barrier(0); } while (0)
DI void dma16(const u16* src, u32 dst) {
  u32 keep;
  asm volatile("s_mov_b32 %0, m0\n\ts_mov_b32 m0, %2\n\ts_nop 0\n\tglobal_load_lds_dwordx4 %1, off nt\n\ts_mov_b32 m0, %0"
               : "=&s"(keep) : "v"(src), "s"(dst) : "memory");
}
DI void touch(const void* src, u32 dst) {
  u32 keep;
  asm volatile("s_mov_b32 %0, m0\n\ts_mov_b32 m0, %2\n\ts_nop 0\n\tglobal_load_lds_dword %1, off\n\ts_mov_b32 m0, %0"
               : "=&s"(keep) : "v"(src), "s"(dst) : "memory");
}
DI void touch_nt(const void* src, u32 dst) {
  u32 keep;
  asm volatile("s_mov_b32 %0, m0\n\ts_mov_b32 m0, %2\n\ts_nop 0\n\tglobal_load_lds_dword %1, off nt\n\ts_mov_b32 m0, %0"
               : "=&s"(keep) : "v"(src), "s"(dst) : "memory");
}
DI void stouch8(const void* p) {
  u32 t;
  asm volatile("s_load_dword %0, %1, 0x0\n\ts_load_dword %0, %1, 0x80\n\ts_load_dword %0, %1, 0x100\n\ts_load_dword %0, %1, 0x180\n\t"
               "s_load_dword %0, %1, 0x200\n\ts_load_dword %0, %1, 0x280\n\ts_load_dword %0, %1, 0x300\n\ts_load_dword %0, %1, 0x380\n\t"
               "s_waitcnt lgkmcnt(0)" : "=&s"(t) : "s"(p) : "memory");
}
DI void stouch2x2(const void* p, const void* q) {
  u32 t;
  asm volatile("s_load_dword %0, %1, 0x0\n\ts_load_dword %0, %1, 0x80\n\ts_load_dword %0, %2, 0x0\n\ts_load_dword %0, %2, 0x80\n\t"
               "s_waitcnt lgkmcnt(0)" : "=&s"(t) : "s"(p), "s"(q) : "memory");
}
DI void set_flag(LAS unsigned char* lds, int f) {
  asm volatile("s_waitcnt lgkmcnt(0)" ::: "memory");
  *(volatile LAS u32*)(lds + L_FLAG + f * 4) = 1u;
}
DI void wait_flags(LAS unsigned char* lds, int first, int mask, int lane) {
#pragma unroll 1
  for (int spin = 0; spin < (SPIN_CAP << 4); ++spin) {
    if (__all(*(volatile LAS u32*)(lds + L_FLAG + (first + (lane & mask)) * 4) != 0u)) break;
    __builtin_amdgcn_s_sleep(1);
  }
  PIN();
}
DI u32 read_tag(const unsigned char* ws) {
  u32 e;
  asm volatile("s_load_dword %0, %1, 0x0\n\ts_waitcnt lgkmcnt(0)" : "=s"(e) : "s"(ws) : "memory");
  return (e + 1u) << 8;
}
template <int LOW, int PNT = 0> DI u32x4 poll16(__amdgpu_buffer_rsrc_t ws, int off, u32 want) {
  u32x4 v;
  u64 keep;
  u32 left = SPIN_CAP, t;
  asm volatile("s_mov_b64 %[keep], exec\n\t"
               "s_branch 2f\n"
               "1:\n\t"
               "s_sub_u32 %[left], %[left], 1\n\t"
               "s_cbranch_scc1 3f\n"
               "2:\n\t"
               ".if %[pnt]\n\tbuffer_load_dwordx4 v[100:103], %[off], %[ws], 0 offen sc1 nt\n\t.else\n\t"
               "buffer_load_dwordx4 v[100:103], %[off], %[ws], 0 offen sc1\n\t.endif\n\t"
               "s_waitcnt vmcnt(0)\n\t"
               "v_bitop3_b32 %[t], v101, v103, %[want] bitop3:0x7e\n\t"
               "v_cmp_lt_u32 vcc, %[low], %[t]\n\t"
               "s_and_b64 exec, exec, vcc\n\t"
               "s_cbranch_scc1 1b\n"
               "3:\n\t"
               "s_mov_b64 exec, %[keep]"
               : "={v[100:103]}"(v), [keep] "=&s"(keep), [left] "+s"(left), [t] "=&v"(t)
               : [off] "v"(off), [ws] "s"(ws), [want] "s"(want), [low] "i"(LOW), [pnt] "i"(PNT)
               : "vcc", "scc", "memory");
  return v;
}
DI void quad_sum4(float& a, float& b, float& c, float& d) {
  asm("s_nop 1\n\t"
      "v_add_f32_dpp %0, %0, %0 quad_perm:[1,0,3,2] row_mask:0xf bank_mask:0xf\n\t"
      "v_add_f32_dpp %1, %1, %1 quad_perm:[1,0,3,2] row_mask:0xf bank_mask:0xf\n\t"
      "v_add_f32_dpp %2, %2, %2 quad_perm:[1,0,3,2] row_mask:0xf bank_mask:0xf\n\t"
      "v_add_f32_dpp %3, %3, %3 quad_perm:[1,0,3,2] row_mask:0xf bank_mask:0xf\n\t"
      "v_add_f32_dpp %0, %0, %0 quad_perm:[2,3,0,1] row_mask:0xf bank_mask:0xf\n\t"
      "v_add_f32_dpp %1, %1, %1 quad_perm:[2,3,0,1] row_mask:0xf bank_mask:0xf\n\t"
      "v_add_f32_dpp %2, %2, %2 quad_perm:[2,3,0,1] row_mask:0xf bank_mask:0xf\n\t"
      "v_add_f32_dpp %3, %3, %3 quad_perm:[2,3,0,1] row_mask:0xf bank_mask:0xf"
      : "+v"(a), "+v"(b), "+v"(c), "+v"(d));
}
DI float sum_records(u32x4 v) {
  const float t = half16(half32(first2(v[0], v[1]), first2(v[2], v[3])), half32(second2(v[0], v[1]), second2(v[2], v[3])));
  return t + dpp<ROR8>(t);
}

DI void layernorm(u32 (&x)[8]) {
  float v[16], s = 0.f;
#pragma unroll
  for (int e = 0; e < 8; ++e) { v[2 * e] = bflo(x[e]); v[2 * e + 1] = bfhi(x[e]); s += v[2 * e] + v[2 * e + 1]; }
  const float mean = half_sum(s) * (1.f / 512.f);
  float q = 0.f;
#pragma unroll
  for (int e = 0; e < 16; ++e) { const float d = v[e] - mean; q = fmaf(d, d, q); }
  const float var = half_sum(q) * (1.f / 512.f);
  const float mb = rbf(mean), inv = __builtin_amdgcn_rcpf(rbf(__builtin_amdgcn_sqrtf(rbf(rbf(var) + 1e-5f))));
#pragma unroll
  for (int e = 0; e < 8; ++e) { const u32 d = pk(v[2 * e] - mb, v[2 * e + 1] - mb); x[e] = pk(bflo(d) * inv, bfhi(d) * inv); }
}

DI f32x2 tile_matvec(const u32x4 (&wv)[8], LAS unsigned char* xin, int g, int c) {
  const u32x4 xf = *(LAS u32x4*)(xin + ((c >> 2) & 1) * 1024 + ((c >> 3) * 32 + g * 8) * 2);
  f32x4 lo = {0.f, 0.f, 0.f, 0.f}, hi = lo;
#pragma unroll
  for (int d = 0; d < 4; ++d) {
    const u32x4 am = sel4((c & 3) == d, xf);
    lo = mfma(am, colfrag(wv, d), lo);
    hi = mfma(am, colfrag(wv, 4 + d), hi);
  }
  const bool valid = (g >> 1) == (c >> 3);
  float t[4];
#pragma unroll
  for (int e = 0; e < 4; ++e) { t[e] = half32(valid ? lo[e] : 0.f, valid ? hi[e] : 0.f); t[e] += dpp<ROR8>(t[e]); }
  return (c & 8) ? (f32x2){t[2], t[3]} : (f32x2){t[0], t[1]};
}

#define ST(i) do { if (!((STMASK >> (i)) & 1)) break; __builtin_amdgcn_sched_barrier(0); ts[i] = __builtin_amdgcn_s_memrealtime(); __builtin_amdgcn_s_waitcnt(0xC07F); __builtin_amdgcn_sched_barrier(0); } while (0)

__global__ void __launch_bounds__(1024, 4)
fused_decode(const u16* x_, const u16* k_, const u16* v_, const u16* wq_, const u16* wo_, const u16* w1_, const u16* b1_,
             const u16* w2_, const u16* b2_, u16* out_, unsigned char* ws_
             ) {
  __shared__ __attribute__((aligned(16))) unsigned char lds_raw[L_END];
  LAS unsigned char* lds = (LAS unsigned char*)lds_raw;
  u64 ts[14];
  for (int i = 0; i < 14; ++i) ts[i] = 0;
  ST(0);
  const u64 t_start = WO_MODE == 3 ? __builtin_amdgcn_s_memrealtime() : 0;
  asm volatile("" :: "s"(x_), "s"(k_), "s"(v_), "s"(wq_), "s"(wo_), "s"(w1_), "s"(b1_), "s"(w2_), "s"(b2_), "s"(out_), "s"(ws_));
  const int tid = threadIdx.x, lane = tid & 63, w = __builtin_amdgcn_readfirstlane(tid >> 6);
  const int g = lane >> 4, c = lane & 15;
  const int bid = blockIdx.x, kvh = bid & 7, j = bid >> 3, b = j >> 4, sp = j & 15, rb = j >> 2, cb = j & 3;
  const bool mlp1 = j < 14, mlp2 = j >= 14 && j < 28;
  const __amdgpu_buffer_rsrc_t ws = __builtin_amdgcn_make_buffer_rsrc(ws_, 0, WS_END, 0x00020000);
  u32 tag;
  const u32 hsh = ((u32)(bid * 16 + w) * 2654435761u) >> 20, hb = ((u32)bid * 2654435761u) >> 16;
  const int wu = (w + (ROT == 0 ? 0 : ROT == 1 || ROT == 2 || ROT == 3 ? j : (int)hb)) & 15;
  const int ro = ROT == 2 ? kvh : ROT == 3 || ROT == 4 ? (int)(hsh & 7) : 0;
  const size_t kvseq = (size_t)(b * 8 + kvh) * SK * DH;
  const __amdgpu_buffer_rsrc_t kr = __builtin_amdgcn_make_buffer_rsrc((void*)(k_ + kvseq), 0, SK * DH * 2, 0x00020000);
  const __amdgpu_buffer_rsrc_t vr = __builtin_amdgcn_make_buffer_rsrc((void*)(v_ + kvseq), 0, SK * DH * 2, 0x00020000);
  const int kvoff = (sp * 16 + wu) * 8192;
  const int klane = ((((c >> 1) ^ ro) & 1) * 4 + (c >> 2)) * 256 + (g * 2 + (c & 1)) * 16;
  u32x4 kf[8], vf[8];

  const int cq = j & 7, kq = j >> 3;
  const int mt = (mlp1 ? j : j - 14) * 8 + kvh;
  const int mhi = mt >> 2, mlo = mt & 3;
  auto pretouch = [&](int what) __attribute__((always_inline)) {
    const unsigned char* p = ws_ + WS_OP + ((kvh * 2 + b) * 16 + sp) * 2048 + c * 128;
    bool on = lane < 16 && (what & 1);
    if ((lane >> 3) == 2) { p = ws_ + WS_S2 + (cb * 64 + kvh * 8 + rb) * 1024 + (lane & 7) * 128; on = what & 16; }
    if ((lane >> 3) == 3) { p = ws_ + WS_MP + (mlo * 28 + mhi) * 1024 + (lane & 7) * 128; on = mlp2 && (what & 2); }
    if ((lane >> 2) == 8) { p = ws_ + WS_QP + ((((lane >> 1) & 1) * 8 + kvh) * 32 + cq * 4 + kq) * 256 + (lane & 1) * 128; on = what & 4; }
    if ((lane >> 2) == 9) { p = ws_ + WS_PA + mt * 512 + (lane & 3) * 128; on = mlp1 && (what & 4); }
    if (lane == 40) { p = (const unsigned char*)(out_ + ((bid >> 2) & 1) * D + (bid & 3) * 128 + ((bid >> 3) & 3) * 32); on = bid < NFIN && (what & 8); }
    if (lane == 41) { p = ws_; on = bid == 0 && (what & 32); }
    if (TV == 1 || TV == 3) on = on && (lane & 15) == 0;
    if (TV >= 2) { if (on) touch_nt(p, (u32)(size_t)(lds + L_T2)); } else
    if (on) touch(p, (u32)(size_t)(lds + L_T2));
  };
  if (w < 2 || (w < 4 && mlp1)) {
    __builtin_amdgcn_s_setprio(3);
    if (w == 0 && lane < 16) *(LAS u32*)(lds + L_FLAG + lane * 4) = 0u;
    const GAS u32x4* xp = (const GAS u32x4*)x_ + lane * 2;
    const u32x4 x0 = xp[0], x1 = xp[1];
    PIN();
    const int rs = w < 2 ? NQ / 8 : FF / 8, row0 = (w < 2 ? kq : mlo) * 128 + (w & 1) * 64;
    const u16* tile = w < 2 ? wq_ + kvh * 512 + cq * 64 : w1_ + mhi * 64;
    const GAS u32x4* wp = (const GAS u32x4*)tile + (row0 + (c >> 3) * 32 + g * 8) * rs + (c & 7);
    u32x4 wv[8];
#pragma unroll
    for (int i = 0; i < 8; ++i) wv[i] = ldnt(wp + i * rs);
    PIN();
    LDS_BARRIER();
    ST(12);
    tag = read_tag(ws_);
    PIN();
    u32 xs[8] = {x0[0], x0[1], x0[2], x0[3], x1[0], x1[1], x1[2], x1[3]};
    layernorm(xs);
    if (w >= 2) layernorm(xs);
    LAS unsigned char* xl = lds + L_XN + w * 2048;
    *(LAS u32x4*)(xl + lane * 32) = (u32x4){xs[0], xs[1], xs[2], xs[3]};
    *(LAS u32x4*)(xl + lane * 32 + 16) = (u32x4){xs[4], xs[5], xs[6], xs[7]};
    f32x2 r = tile_matvec(wv, xl + row0 * 2, g, c);
    LAS f32x2* half = (LAS f32x2*)(lds + L_HALF + (w >> 1) * 512 + lane * 8);
    if (w & 1) {
      *half = r;
      set_flag(lds, F_HALF + (w >> 1));
    } else {
      wait_flags(lds, F_HALF + (w >> 1), 0, lane);
      r += *half;
      const int rec = w == 0 ? WS_QP + ((((g & 1) * 8 + kvh) * 8 + cq) * 4 + kq) * 256 : WS_PA + mt * 512 + (g & 1) * 256;
      stws1(ws, rec + ((c & 7) * 4 + (g >> 1) * 2 + (c >> 3)) * 8, pack2(r[0], r[1], tag));
    }
    ST(2);
    __builtin_amdgcn_s_setprio(PRIO_K);
  } else {
    if (w < 4 && mlp2) {
      const u16* p = w2_ + (mhi * 64 + (w & 1) * 32 + g * 8) * D + mlo * 128 + c * 8;
#pragma unroll
      for (int i = 0; i < 8; ++i) dma16(p + i * D, (u32)(size_t)(lds + L_W2 + (w & 1) * 8192 + i * 1024));
    }
    if (CODE_LINES && w == 4) {
      const unsigned char* pc;
      asm volatile("s_getpc_b64 %0" : "=s"(pc));
      if (lane < 4 && j * 4 + lane < CODE_LINES) touch(pc + (j * 4 + lane) * 128, (u32)(size_t)(lds + L_TOUCH));
    }
    if (PRE_A && w == PREW) pretouch(PRE_A);
    if (SPRE & 16) RAW_BARRIER();
    if ((SPRE & 1) && (w >> 1) == 6) stouch8(ws_ + WS_OP + ((kvh * 2 + b) * 16 + sp) * 2048 + (w & 1) * 1024);
    if ((SPRE & 1) && w == 14) stouch8(ws_ + WS_S2 + (cb * 64 + kvh * 8 + rb) * 1024);
    if ((SPRE & 2) && w == 15 && mlp2) stouch8(ws_ + WS_MP + (mlo * 28 + mhi) * 1024);
    if ((SPRE & 4) && w == 11) { const unsigned char* q = ws_ + WS_QP + (kvh * 32 + cq * 4 + kq) * 256; stouch2x2(q, q + 65536); }
    if ((SPRE & 4) && w == 10 && mlp1) { const unsigned char* q = ws_ + WS_PA + mt * 512; stouch2x2(q, q + 256); }
    if ((SPRE & 8) && w == 9 && bid < NFIN) { const u16* q = out_ + ((bid >> 2) & 1) * D + (bid & 3) * 128; stouch2x2(q, q); }
    if (!(SPRE & 16)) RAW_BARRIER();
    if (PRE_B && w == PREW + 1) pretouch(PRE_B);
    if ((w >> 2) == 1) __builtin_amdgcn_s_setprio(2); else __builtin_amdgcn_s_setprio(PRIO_K);
  }
  PIN();

  const int slot = SLOTS == 1 ? SLOT0 : SLOTS == 2 ? SLOT0 + (j & 1) : j & 3;
  if (PRE_C && slot < 2 && w == 9 + slot * 4) pretouch(PRE_C);
#pragma unroll
  for (int i = 0; i < 8; ++i) { kf[i] = BUF ? __builtin_amdgcn_raw_buffer_load_b128(kr, klane + (i & 1) * 128, kvoff + ((i >> 1) ^ (ro >> 1)) * 2048, NT) : ldnt((const GAS u32x4*)((const GAS char*)(k_ + kvseq) + kvoff + klane + (i & 1) * 128 + (i >> 1) * 2048)); __builtin_amdgcn_sched_barrier(0); }
  if ((w >> 2) != 1) __builtin_amdgcn_s_setprio(0);
  if (PRE_C && slot >= 2 && w == 1 + (slot - 2) * 8) pretouch(PRE_C);
  PIN();
#pragma unroll
  for (int i = 0; i < 8; ++i) { vf[i] = BUF ? __builtin_amdgcn_raw_buffer_load_b128(vr, lane * 16, kvoff + (i ^ ro) * 1024, NT) : ldnt((const GAS u32x4*)((const GAS char*)(v_ + kvseq) + kvoff + lane * 16 + i * 1024)); __builtin_amdgcn_sched_barrier(0); }
  PIN();
  if ((PRE_D || WO_V) && lane == 0) __hip_atomic_fetch_add((LAS u32*)(lds + L_CNT + 4), 1u, __ATOMIC_RELAXED, __HIP_MEMORY_SCOPE_WORKGROUP);
  if (PRE_E && w == 13) pretouch(PRE_E);
  ST(4);
  tag = read_tag(ws_);
  const int paoff = WS_PA + mhi * 2048 + lane * 16;
  u32x4 p0, p1;
  u32 b1v;
  if ((w >> 2) == 1) {
    const int u = w & 3;
    const int qoff = WS_QP + (b * 8 + kvh) * 8192 + u * 2048 + lane * 16;
    asm volatile("s_waitcnt vmcnt(0)" ::: "memory");
    if (Q_DELAY) __builtin_amdgcn_s_sleep(Q_DELAY);
    u32x4 q0, q1;
#pragma unroll 1
    for (int spin = 0; spin < SPIN_CAP; ++spin) {
      q0 = ldws(ws, qoff); q1 = ldws(ws, qoff + 1024);
      if (__all(tagged(q0, tag) && tagged(q1, tag))) break;
      __builtin_amdgcn_s_sleep(SLEEP_Q);
      asm volatile("" ::: "memory");
    }
    const float qa = half16(half32(first2(q0[0], q0[1]), first2(q1[0], q1[1])), half32(first2(q0[2], q0[3]), first2(q1[2], q1[3])));
    const float qb = half16(half32(second2(q0[0], q0[1]), second2(q1[0], q1[1])), half32(second2(q0[2], q0[3]), second2(q1[2], q1[3])));
    *(LAS u32*)(lds + L_Q + u * 256 + ((g >> 1) * 32 + c * 2 + (g & 1)) * 4) = pk(qa, qb);
    set_flag(lds, F_Q + u);
    ST(3);
    if (w == 7 && mlp2) {
      p0 = ldws(ws, paoff); p1 = ldws(ws, paoff + 1024);
      b1v = B1NT ? ldnt((const GAS u32*)(b1_ + mhi * 64 + c * 4 + (g >> 1) * 2)) : *(const GAS u32*)(b1_ + mhi * 64 + c * 4 + (g >> 1) * 2);
      PIN();
    }
    __builtin_amdgcn_s_setprio(0);
  }
  wait_flags(lds, F_Q, 3, lane);

  float sc[8];
  {
    const int x0 = (c >> 2) & 1;
    const u32x4 q0 = *(LAS u32x4*)(lds + L_Q + (c & 3) * 256 + (g * 2 + x0) * 16);
    const u32x4 q1 = *(LAS u32x4*)(lds + L_Q + (c & 3) * 256 + 128 + (g * 2 + x0) * 16);
#pragma unroll
    for (int o = 0; o < 4; ++o) {
      f32x4 s = mfma(kf[2 * o], q0, (f32x4){0.f, 0.f, 0.f, 0.f});
      s = mfma(kf[2 * o + 1], q1, s);
#pragma unroll
      for (int a = 0; a < 2; ++a) {
        float e1 = s[2 * a + 1];
        asm("" : "+v"(e1));
        const float t = x0 ? e1 : s[2 * a];
        sc[2 * o + a] = t + dpp<ROR4>(t);
      }
      const u32 d = pk(sc[2 * o], sc[2 * o + 1]), e = pk(bflo(d) * 0.08838834764831845f, bfhi(d) * 0.08838834764831845f);
      sc[2 * o] = bflo(e); sc[2 * o + 1] = bfhi(e);
    }
  }
  PIN();

    float m = fmaxf(fmaxf(fmaxf(sc[0], sc[1]), fmaxf(sc[2], sc[3])), fmaxf(fmaxf(sc[4], sc[5]), fmaxf(sc[6], sc[7])));
    m = max32(max16(m)) * LOG2E;
    float p[8], l = 0.f;
#pragma unroll
    for (int e = 0; e < 8; ++e) { p[e] = __builtin_amdgcn_exp2f(fmaf(sc[e], LOG2E, -m)); l += p[e]; }
    l = half16(l, l); l = half32(l, l);
    if (lane < 4) { *(LAS float*)(lds + L_MW + (lane * 16 + w) * 4) = m; *(LAS float*)(lds + L_LW + (lane * 16 + w) * 4) = l; }
    PIN();
    if ((w >> 1) == 1) set_flag(lds, F_W2 + (w & 1));
    if (lane == 0) __hip_atomic_fetch_add((LAS u32*)(lds + L_CNT), 1u, __ATOMIC_RELAXED, __HIP_MEMORY_SCOPE_WORKGROUP);
    const u32x4 pf = {pk(p[0], p[1]), pk(p[2], p[3]), pk(p[4], p[5]), pk(p[6], p[7])};
    u32x4 am[4];
#pragma unroll
    for (int u = 0; u < 4; ++u) am[u] = sel4((c >> 2) == u, pf);
    PIN();
    u32x4 ve[4], vo[4];
    u32 slo, shi;
    asm volatile("s_mov_b32 %0, 0x05040100\n\ts_mov_b32 %1, 0x07060302" : "=s"(slo), "=s"(shi) : "v"(am[0][0]), "v"(am[1][1]), "v"(am[2][2]), "v"(am[3][3]));
#pragma unroll
    for (int r = 0; r < 4; ++r) {
#pragma unroll
      for (int u = 0; u < 4; ++u) {
        ve[u][r] = __builtin_amdgcn_perm(vf[2 * r + 1][u], vf[2 * r][u], slo);
        vo[u][r] = __builtin_amdgcn_perm(vf[2 * r + 1][u], vf[2 * r][u], shi);
      }
      if (r < 3)
        asm volatile("" : "+s"(slo), "+s"(shi) : "v"(ve[0][r]), "v"(ve[1][r]), "v"(ve[2][r]), "v"(ve[3][r]), "v"(vo[0][r]), "v"(vo[1][r]), "v"(vo[2][r]), "v"(vo[3][r]));
    }
    f32x4 oe = {0.f, 0.f, 0.f, 0.f}, oo = oe;
#pragma unroll
    for (int u = 0; u < 4; ++u) { oe = mfma(am[u], ve[u], oe); oo = mfma(am[u], vo[u], oo); }
#pragma unroll
    for (int h = 0; h < 4; ++h) *(LAS f32x2*)(lds + L_OW + w * OW_STRIDE + h * 512 + c * 32 + g * 8) = (f32x2){oe[h], oo[h]};
    PIN();
    ST(5);

  const bool gath = (w & 4) == 0;
  const int v = (w & 3) + (w >> 3) * 4;
  auto wait_ml = [&]() __attribute__((always_inline)) {
#pragma unroll 1
    for (int spin = 0; spin < (SPIN_CAP << 4); ++spin) {
      if (__builtin_amdgcn_readfirstlane(*(volatile LAS u32*)(lds + L_CNT)) == 16u) break;
      __builtin_amdgcn_s_sleep(1);
    }
    PIN();
  };
  auto load_wo = [&](u32x4 (&wt)[8]) __attribute__((always_inline)) {
    const GAS u16* p = (const GAS u16*)wo_ + (size_t)(kvh * 512 + rb * 64 + g * 4) * D + cb * 128 + (w - 4) * 64 + c * 4;
#pragma unroll
    for (int i = 0; i < 8; ++i) {
      const u32x2 t0 = ldnt((const GAS u32x2*)(p + (size_t)(32 * (i >> 2) + (i & 3)) * D));
      const u32x2 t1 = ldnt((const GAS u32x2*)(p + (size_t)(32 * (i >> 2) + 16 + (i & 3)) * D));
      wt[i] = (u32x4){t0[0], t0[1], t1[0], t1[1]};
    }
    PIN();
  };
  auto wait_v = [&](u32 n) __attribute__((always_inline)) {
#pragma unroll 1
    for (int spin = 0; spin < (SPIN_CAP << 4); ++spin) {
      if (__builtin_amdgcn_readfirstlane(*(volatile LAS u32*)(lds + L_CNT + 4)) >= n) break;
      __builtin_amdgcn_s_sleep(1);
    }
    PIN();
  };
  if (PRE_D && w == 6) { wait_v(PRE_AT); pretouch(PRE_D); }
  const bool fin = FINLAST ? bid >= 256 - NFIN : bid < NFIN;
  const int cf = bid & 3, bf = (bid >> 2) & 1, seg = (bid >> 3) & 3;
  if (w >= 12) {
    LDS_BARRIER();
    ST(6);
    if (fin) {
      const int row = (w - 12) * 8 + (lane >> 3);
      const int moff = WS_MP + (cf * 28 + min(row, 27)) * 1024 + bf * 512 + seg * 128 + (lane & 7) * 16;
      u32x4 mv;
#pragma unroll 1
      for (int spin = 0; spin < SPIN_CAP; ++spin) {
        mv = ldws(ws, moff);
        if (__all(tagged(mv, tag))) break;
        __builtin_amdgcn_s_sleep(SLEEP_M);
        asm volatile("" ::: "memory");
      }
      const float t = sum_records(sel4(row < 28, mv));
      if ((lane & 8) == 0) *(LAS float*)(lds + L_MF + (w - 12) * 128 + ((lane & 7) * 4 + g) * 4) = t;
      LDS_BARRIER();
    }
  } else if (gath) {
    {
      const int f = v * 16 + (lane >> 2), p4 = lane & 3, hd = v >> 1;
      wait_ml();
      const f32x4 mv = *(LAS f32x4*)(lds + L_MW + hd * 64 + p4 * 16), lv = *(LAS f32x4*)(lds + L_LW + hd * 64 + p4 * 16);
      float M = fmaxf(fmaxf(mv[0], mv[1]), fmaxf(mv[2], mv[3]));
      M = fmaxf(M, dpp<XOR1>(M)); M = fmaxf(M, dpp<XOR2>(M));
      float L = 0.f, fw[4];
#pragma unroll
      for (int i = 0; i < 4; ++i) { fw[i] = __builtin_amdgcn_exp2f(mv[i] - M); L = fmaf(fw[i], lv[i], L); }
      L += dpp<XOR1>(L); L += dpp<XOR2>(L);
      const float inv = __builtin_amdgcn_rcpf(L);
#pragma unroll
      for (int i = 0; i < 4; ++i) fw[i] *= inv;
      const u32 lam = f2u(M + __builtin_amdgcn_logf(L)) >> ((lane >> 5) * 16);
      u32 t0 = tag | (lam & 255u), t1 = tag | ((lam >> 8) & 255u);
      int ooff = WS_OP + ((kvh * 2 + b) * 16 + sp) * 2048 + f * 16;
      int laddr = L_OW + p4 * 4 * OW_STRIDE + f * 16;
      asm volatile("" : "+v"(fw[0]), "+v"(fw[1]), "+v"(fw[2]), "+v"(fw[3]), "+v"(t0), "+v"(t1), "+v"(ooff), "+v"(laddr));
      ST(3);
      LDS_BARRIER();
      ST(6);
      f32x4 s = fw[0] * *(LAS f32x4*)(lds + laddr);
#pragma unroll
      for (int i = 1; i < 4; ++i) s += fw[i] * *(LAS f32x4*)(lds + laddr + i * OW_STRIDE);
      float s0 = s[0], s1 = s[1], s2 = s[2], s3 = s[3];
      quad_sum4(s0, s1, s2, s3);
      if (p4 == 0) __builtin_amdgcn_raw_buffer_store_b128((u32x4){pk(s0, s1), t0, pk(s2, s3), t1}, ws, ooff, 0, OSAUX);
    }
    PIN();
    ST(7);
    {
      u32 gaddr2 = (u32)(size_t)(lds + L_GP + v * GP_STRIDE + 256), gaddr = gaddr2 - 256 + (c * 4 + g) * 4;
      u32 lsel = (c & 8) ? 0x05040100u : 0x01000504u;
      asm volatile("" : "+v"(gaddr), "+v"(gaddr2), "+v"(lsel));
      if (SLEEP_O) __builtin_amdgcn_s_sleep(SLEEP_O);
      const u32x4 ov = poll16<255, OPNT>(ws, WS_OP + (kvh * 32 + v * 4 + g) * 2048 + rb * 256 + c * 16, tag);
      ST(8);
      u32 a, lam, m1, x0, x1, x2, x3;
      f32x2 gl;
      asm volatile("v_perm_b32 %[a], %[t1], %[t0], %[sel]\n\t"
                   "v_lshlrev_b32 %[x0], 16, %[d0]\n\t"
                   "v_lshlrev_b32 %[x2], 16, %[d1]\n\t"
                   "v_mov_b32_dpp %[m1], %[a] row_ror:8 row_mask:0xf bank_mask:0xf\n\t"
                   "v_and_b32 %[x1], 0xffff0000, %[d0]\n\t"
                   "v_perm_b32 %[lam], %[a], %[m1], %[lsel]\n\t"
                   "v_mov_b32 %[m1], %[lam]\n\t"
                   "v_mov_b32 %[a], %[lam]\n\t"
                   "v_and_b32 %[x3], 0xffff0000, %[d1]\n\t"
                   "s_nop 0\n\t"
                   "v_permlane16_swap_b32 %[m1], %[a]\n\t"
                   "v_max_f32 %[m1], %[m1], %[a]\n\t"
                   "v_mov_b32 %[a], %[m1]\n\t"
                   "s_nop 1\n\t"
                   "v_permlane32_swap_b32 %[m1], %[a]\n\t"
                   "v_max_f32 v104, %[m1], %[a]\n\t"
                   "v_sub_f32 %[lam], %[lam], v104\n\t"
                   "v_exp_f32 %[lam], %[lam]\n\t"
                   "s_nop 0\n\t"
                   "v_mul_f32 %[x0], %[lam], %[x0]\n\t"
                   "v_mul_f32 %[x2], %[lam], %[x2]\n\t"
                   "v_mul_f32 %[x1], %[lam], %[x1]\n\t"
                   "v_mul_f32 %[x3], %[lam], %[x3]\n\t"
                   "v_mov_b32 %[a], %[lam]\n\t"
                   "v_permlane32_swap_b32 %[x0], %[x2]\n\t"
                   "v_permlane32_swap_b32 %[x1], %[x3]\n\t"
                   "v_permlane16_swap_b32 %[lam], %[a]\n\t"
                   "v_add_f32 %[x0], %[x0], %[x2]\n\t"
                   "v_add_f32 %[x1], %[x1], %[x3]\n\t"
                   "v_add_f32 %[lam], %[lam], %[a]\n\t"
                   "v_mov_b32 %[a], %[lam]\n\t"
                   "v_permlane16_swap_b32 %[x0], %[x1]\n\t"
                   "v_add_f32 %[x0], %[x0], %[x1]\n\t"
                   "v_permlane32_swap_b32 %[lam], %[a]\n\t"
                   "ds_write_b32 %[ga], %[x0]\n\t"
                   "v_add_f32 v105, %[lam], %[a]\n\t"
                   "ds_write_b64 %[gb], v[104:105]"
                   : [a] "=&v"(a), [lam] "=&v"(lam), [m1] "=&v"(m1), [x0] "=&v"(x0), [x1] "=&v"(x1), [x2] "=&v"(x2), [x3] "=&v"(x3), "=&{v[104:105]}"(gl)
                   : [d0] "v"(ov[0]), [t0] "v"(ov[1]), [d1] "v"(ov[2]), [t1] "v"(ov[3]), [sel] "s"(0x04000400u), [lsel] "v"(lsel), [ga] "v"(gaddr), [gb] "v"(gaddr2)
                   : "memory");
    }
    LDS_BARRIER();
    ST(9);
    if (fin) {
      int faddr = L_FP + ((lane & 7) * 4 + g) * 32 + v * 4;
      asm volatile("" : "+v"(faddr));
      if (SLEEP_S) __builtin_amdgcn_s_sleep(SLEEP_S);
      const u32x4 sv = poll16<255>(ws, WS_S2 + (cf * 64 + v * 8 + (lane >> 3)) * 1024 + bf * 512 + seg * 128 + (lane & 7) * 16, tag);
      ST(10);
      *(LAS float*)(lds + faddr) = sum_records(sv);
      LDS_BARRIER();
    }
  } else if (w < 6) {
    u32x4 wt[8], bw[8];
    if (WO_MODE == 3) {
      wait_ml();
      const bool now = __builtin_amdgcn_s_memrealtime() >= t_start + WO_AT;
      if (!now) {
        LDS_BARRIER();
        ST(6);
        while (__builtin_amdgcn_s_memrealtime() < t_start + WO_AT) __builtin_amdgcn_s_sleep(1);
      }
      load_wo(wt);
      if (now) { LDS_BARRIER(); ST(6); }
    } else {
    if (WO_MODE == 1) { if (WO_V) wait_v(16u); else wait_ml(); }
    if (WO_MODE < 2) load_wo(wt);
    LDS_BARRIER();
    ST(6);
    if (WO_MODE == 2) { if (WO_SLEEP) __builtin_amdgcn_s_sleep(WO_SLEEP); load_wo(wt); }
    }
#pragma unroll
    for (int i = 0; i < 8; ++i) bw[i] = colfrag(wt, i);
    int gp = L_GP + (c & 7) * GP_STRIDE, go = gp + ((c >> 3) * 16 + g * 4) * 4;
    int soff = WS_S2 + (cb * 64 + kvh * 8 + rb) * 1024 + (g & 1) * 512 + ((w - 4) * 32 + c * 2 + (g >> 1)) * 8;
    asm volatile("" : "+v"(gp), "+v"(go), "+v"(soff) : "v"(bw[0]), "v"(bw[1]), "v"(bw[2]), "v"(bw[3]), "v"(bw[4]), "v"(bw[5]), "v"(bw[6]), "v"(bw[7]));
    ST(1);
    LDS_BARRIER();
    ST(9);
    f32x2 gl = *(LAS f32x2*)(lds + gp + 256);
    u32x4 of = *(LAS u32x4*)(lds + go), o1 = *(LAS u32x4*)(lds + go + 128);
#define QP1(r) "v_add_f32_dpp " r ", " r ", " r " quad_perm:[1,0,3,2] row_mask:0xf bank_mask:0xf\n\t"
#define QP2(r) "v_add_f32_dpp " r ", " r ", " r " quad_perm:[2,3,0,1] row_mask:0xf bank_mask:0xf\n\t"
#define PKM(r) "v_pk_mul_f32 " r ", v[118:119], " r " op_sel_hi:[0,1]\n\t"
    asm volatile("v_max_f32_dpp v118, v116, v116 quad_perm:[1,0,3,2] row_mask:0xf bank_mask:0xf\n\t"
                 "s_nop 1\n\t"
                 "v_max_f32_dpp v118, v118, v118 quad_perm:[2,3,0,1] row_mask:0xf bank_mask:0xf\n\t"
                 "v_sub_f32 v118, v116, v118\n\t"
                 "v_exp_f32 v118, v118\n\t"
                 "s_nop 0\n\t"
                 "v_mul_f32 v117, v118, v117\n\t"
                 PKM("v[108:109]") PKM("v[110:111]") PKM("v[112:113]") PKM("v[114:115]")
                 QP1("v117") QP1("v108") QP1("v109") QP1("v110") QP1("v111") QP1("v112") QP1("v113") QP1("v114") QP1("v115")
                 QP2("v117") QP2("v108") QP2("v109")
                 "v_rcp_f32 v118, v117\n\t"
                 QP2("v110") QP2("v111") QP2("v112") QP2("v113") QP2("v114") QP2("v115")
                 PKM("v[108:109]") PKM("v[110:111]") PKM("v[112:113]") PKM("v[114:115]")
                 "v_cvt_pk_bf16_f32 v108, v108, v109\n\t"
                 "v_cvt_pk_bf16_f32 v109, v110, v111\n\t"
                 "v_cvt_pk_bf16_f32 v110, v112, v113\n\t"
                 "v_cvt_pk_bf16_f32 v111, v114, v115\n\t"
                 "s_nop 1"
                 : "+{v[108:111]}"(of), "+{v[112:115]}"(o1), "+{v[116:117]}"(gl)
                 :
                 : "v118", "v119");
    f32x4 acc[8];
#pragma unroll
    for (int i = 0; i < 8; ++i) { const int k = (i & 3) * 2 + (i >> 2); acc[k] = mfma(of, bw[k], (f32x4){0.f, 0.f, 0.f, 0.f}); }
    __builtin_amdgcn_sched_barrier(0);
    float r[4];
#pragma unroll
    for (int d = 0; d < 4; ++d) r[d] = (g >> 1) ? acc[4 + d][0] : acc[d][0];
    stws1(ws, soff, pack2(half32(r[0], r[2]), half32(r[1], r[3]), tag));
    ST(10);
  } else if (w == 7 && mlp2) {
#pragma unroll 1
    for (int spin = 0; spin < SPIN_CAP; ++spin) {
      if (__all(tagged(p0, tag) && tagged(p1, tag))) break;
      __builtin_amdgcn_s_sleep(SLEEP_Q);
      asm volatile("" ::: "memory");
      p0 = ldws(ws, paoff); p1 = ldws(ws, paoff + 1024);
    }
    const float t2[2] = {half32(first2(p0[0], p0[1]) + first2(p1[0], p1[1]), first2(p0[2], p0[3]) + first2(p1[2], p1[3])),
                         half32(second2(p0[0], p0[1]) + second2(p1[0], p1[1]), second2(p0[2], p0[3]) + second2(p1[2], p1[3]))};
    float hv[2];
#pragma unroll
    for (int e = 0; e < 2; ++e) {
      const float t = rbf(rbf(t2[e]) + (e ? bfhi(b1v) : bflo(b1v)));
      const float uu = 0.7978845608028654f * (t + 0.044715f * t * t * t);
      hv[e] = t / (1.f + __builtin_amdgcn_exp2f(-2.f * LOG2E * uu));
    }
    *(LAS u32*)(lds + L_HS + (g & 1) * 128 + (c * 2 + (g >> 1)) * 4) = pk(hv[0], hv[1]);
    wait_flags(lds, F_W2, 1, lane);
    f32x4 r = {0.f, 0.f, 0.f, 0.f};
#pragma unroll
    for (int s = 0; s < 2; ++s) {
      const u32x4 ha = *(LAS u32x4*)(lds + L_HS + ((c >> 2) & 1) * 128 + s * 64 + g * 16);
      u32x4 w2t[8];
#pragma unroll
      for (int i = 0; i < 8; ++i) w2t[i] = *(LAS u32x4*)(lds + L_W2 + s * 8192 + i * 1024 + lane * 16);
#pragma unroll
      for (int d = 0; d < 8; ++d) r = mfma(sel4((c >> 3) == (d >> 2) && (c & 3) == (d & 3), ha), colfrag(w2t, d), r);
    }
    u32x2 m0 = pack2(r[0], r[1], tag), m1 = pack2(r[2], r[3], tag);
    asm volatile("" : "+v"(m0), "+v"(m1));
    ST(2);
    if (MP_LATE) LDS_BARRIER();
    stws(ws, WS_MP + (mlo * 28 + mhi) * 1024 + (g & 1) * 512 + (c * 2 + (g >> 1)) * 16, m0, m1);
    if (!MP_LATE) LDS_BARRIER();
    ST(6);
  } else if (w == 6 && fin) {
    const int col = cf * 128 + seg * 32 + (lane & 31);
    const u32 xv = ((const GAS u16*)x_)[bf * D + col], b2v = ((const GAS u16*)b2_)[col];
    LDS_BARRIER();
    ST(6);
    LDS_BARRIER();
    LAS float* mf = (LAS float*)(lds + L_MF + (lane >> 5) * 256 + (lane & 31) * 4);
    float t = mf[0] + mf[32];
    t = half32(t, t);
    t = rbf(rbf(t) + bflo(b2v));
    float gate = rbf(t / (1.f + __builtin_amdgcn_exp2f(-LOG2E * t))), xf = bflo(xv);
    int faddr = L_FP + (lane & 31) * 32 + (lane >> 5) * 16;
    GAS u16* op = (GAS u16*)out_ + bf * D + col;
    asm volatile("" : "+v"(gate), "+v"(xf), "+v"(faddr), "+v"(op));
    LDS_BARRIER();
    ST(10);
    const f32x4 fp = *(LAS f32x4*)(lds + faddr);
    float s = (fp[0] + fp[1]) + (fp[2] + fp[3]);
    s = half32(s, s);
    const float res = xf + rbf(rbf(s) * gate);
    if (lane < 32) __hip_atomic_store(op, (u16)pk(res, res), RLX_AGENT);
    if ((bid & 31) == 0 && lane == 0) __hip_atomic_store((GAS u32*)(ws_ + WS_EPOCH), tag >> 8, RLX_AGENT);
  } else {
    LDS_BARRIER();
    ST(6);
  }
  ST(11);
  {
    GAS u64* st = (GAS u64*)(ws_ + (WS_AT_END ? -(8 << 20) : (8 << 20)));
    volatile LAS u64* ls = (volatile LAS u64*)(lds + L_TOUCH);
    if (lane == 0) { ls[w * 2] = ((PROBE_WM >> w) & 1) ? ts[PROBE] : 0; ls[w * 2 + 1] = ts[PROBE_B]; }
    if (w == 0) {
      while (__builtin_amdgcn_s_memrealtime() < ts[0] + 2500) __builtin_amdgcn_s_sleep(1);
      u64 ma = ls[(lane & 15) * 2], mb = ls[(lane & 15) * 2 + 1];
      auto red = [&]() {
        for (int o = 32; o; o >>= 1) {
          const u64 x = __shfl_xor(ma, o), y = __shfl_xor(mb, o);
          ma = x > ma ? x : ma;
          if (PROBE_B == 0) mb = y < mb ? y : mb; else mb = y > mb ? y : mb;
        }
      };
      red();
      if (lane == 0) { __hip_atomic_store(st + bid * 2, ma, RLX_AGENT); __hip_atomic_store(st + bid * 2 + 1, mb, RLX_AGENT); }
      if (bid == 0) {
        while (__builtin_amdgcn_s_memrealtime() < ts[0] + 3200) __builtin_amdgcn_s_sleep(1);
        for (int i = lane; i < 256; i += 64) {
          const u64 x = __hip_atomic_load(st + i * 2, RLX_AGENT), y = __hip_atomic_load(st + i * 2 + 1, RLX_AGENT);
          ma = x > ma ? x : ma;
          if (PROBE_B == 0) mb = y < mb ? y : mb; else mb = y > mb ? y : mb;
        }
        red();
        const u64 until = ts[0] + 5000 + 10 * (ma - mb);
        while (__builtin_amdgcn_s_memrealtime() < until && __builtin_amdgcn_s_memrealtime() < ts[0] + 40000) __builtin_amdgcn_s_sleep(1);
      }
    }
  }
}

extern "C" void kernel_launch(void* const* d_in, const int* in_sizes, int n_in, void* d_out, int out_size, void* d_ws, size_t ws_size,
                              hipStream_t stream) {
  unsigned char* ws = (unsigned char*)d_ws + (WS_AT_END ? ((ws_size - WS_SPAN) & ~(size_t)4095) : 0);
  fused_decode<<<256, 1024, 0, stream>>>((const u16*)d_in[0], (const u16*)d_in[1], (const u16*)d_in[2], (const u16*)d_in[3],
                                         (const u16*)d_in[4], (const u16*)d_in[6], (const u16*)d_in[7], (const u16*)d_in[8],
                                         (const u16*)d_in[9], (u16*)d_out, ws
                                         );
}
```

```cpp
#define STAMP 1
#define PROBE 12
#define PROBE_B 14
#define STMASK (1 | (1 << 12) | (1 << 14))
#define FINLAST 0
#include <hip/hip_runtime.h>
#include <stdint.h>

#define WS_AT_END 0
#define SLEEP_Q 8
#define SLEEP_O 8
#define SLEEP_S 8
#define WO_MODE 1
#define PRIO_K 1
#define ROT 1
#define CODE_LINES 0
#define RND 1
#define BUF 1
#define SPRE 0
#define TV 0
#define PRE_C 0
#define SLOTS 4
#define SLOT0 0
#define OSAUX 16
#define OPNT 0
#define PRE_D 25
#define PRE_E 0
#define B1NT 0
#define PROBE_WM 0xffff
#define WALK 0
#define WALKW 0
#define PRE_AT 16u
#define WO_V 0
#define PRE_A 0
#define PRE_B 0
#define PREW 12
#define Q_DELAY 32
#define MP_LATE 0
#define SLEEP_M 4
#define WO_AT 1150
#define WO_SLEEP 0
#define ABL 0

#define GAS __attribute__((address_space(1)))
#define LAS __attribute__((address_space(3)))
#define DI __device__ __forceinline__
#define RLX_AGENT __ATOMIC_RELAXED, __HIP_MEMORY_SCOPE_AGENT
typedef unsigned short u16;
typedef unsigned u32;
typedef unsigned long long u64;
typedef u32 u32x2 __attribute__((ext_vector_type(2)));
typedef u32 u32x4 __attribute__((ext_vector_type(4)));
typedef float f32x2 __attribute__((ext_vector_type(2)));
typedef float f32x4 __attribute__((ext_vector_type(4)));
typedef __bf16 bf16x2 __attribute__((ext_vector_type(2)));
typedef __bf16 bf16x8 __attribute__((ext_vector_type(8)));

constexpr int D = 512, SK = 8192, DH = 128, FF = 1792, NQ = 4096;
constexpr int SC1 = 16, NT = 2;
constexpr int SPIN_CAP = 1 << 18;
constexpr int NFIN = 32;
constexpr float LOG2E = 1.4426950408889634f;

constexpr int WS_EPOCH = 0;
constexpr int WS_QP = 256;
constexpr int WS_PA = WS_QP + 512 * 256;
constexpr int WS_OP = WS_PA + 112 * 512;
constexpr int WS_S2 = WS_OP + 256 * 2048;
constexpr int WS_MP = WS_S2 + 256 * 1024;
constexpr int WS_END = WS_MP + 112 * 1024;
constexpr int WS_SPAN = 4 << 20;
constexpr int WS_STAMP = 8 << 20;

constexpr int F_Q = 0, F_HALF = 4, F_W2 = 6;
constexpr int L_FLAG = 0, L_CNT = 48, L_XN = 64, L_HALF = L_XN + 8192, L_Q = L_HALF + 1024, L_MW = L_Q + 1024, L_LW = L_MW + 256,
              L_HS = L_LW + 256, L_GP = L_HS + 256, GP_STRIDE = 272, L_FP = L_GP + 8 * GP_STRIDE, L_MF = L_FP + 1024,
              L_TOUCH = L_MF + 512, L_T2 = L_TOUCH + 256, L_W2 = L_T2 + 256, L_OW = L_W2 + 16384, OW_STRIDE = 2064, L_END = L_OW + 16 * OW_STRIDE;

DI float bflo(u32 v) { return __builtin_bit_cast(float, v << 16); }
DI float bfhi(u32 v) { return __builtin_bit_cast(float, v & 0xffff0000u); }
DI float rbf(float f) { return (float)(__bf16)f; }
DI u32 pk(float a, float b) { return __builtin_bit_cast(u32, __builtin_convertvector((f32x2){a, b}, bf16x2)); }
DI u32 f2u(float f) { return __builtin_bit_cast(u32, f); }
DI float u2f(u32 u) { return __builtin_bit_cast(float, u); }
DI f32x4 mfma(u32x4 a, u32x4 b, f32x4 c) {
  return __builtin_amdgcn_mfma_f32_16x16x32_bf16(__builtin_bit_cast(bf16x8, a), __builtin_bit_cast(bf16x8, b), c, 0, 0, 0);
}
template <int CTRL> DI float dpp(float x) { return u2f(__builtin_amdgcn_mov_dpp(f2u(x), CTRL, 0xf, 0xf, true)); }
constexpr int XOR1 = 0xB1, XOR2 = 0x4E, ROR4 = 0x124, ROR8 = 0x128;
DI float half32(float a, float b) { auto r = __builtin_amdgcn_permlane32_swap(f2u(a), f2u(b), false, false); return u2f(r[0]) + u2f(r[1]); }
DI float half16(float a, float b) { auto r = __builtin_amdgcn_permlane16_swap(f2u(a), f2u(b), false, false); return u2f(r[0]) + u2f(r[1]); }
DI float max32(float a) { auto r = __builtin_amdgcn_permlane32_swap(f2u(a), f2u(a), false, false); return __builtin_amdgcn_fmed3f(u2f(r[0]), u2f(r[1]), __builtin_inff()); }
DI float max16(float a) { auto r = __builtin_amdgcn_permlane16_swap(f2u(a), f2u(a), false, false); return __builtin_amdgcn_fmed3f(u2f(r[0]), u2f(r[1]), __builtin_inff()); }
DI float half_sum(float v) {
  v += dpp<XOR1>(v); v += dpp<XOR2>(v); v += dpp<ROR4>(v); v += dpp<ROR8>(v);
  return half16(v, v);
}
DI u32 plo(u32 k1, u32 k0) { return __builtin_amdgcn_perm(k1, k0, 0x05040100u); }
DI u32 phi(u32 k1, u32 k0) { return __builtin_amdgcn_perm(k1, k0, 0x07060302u); }
DI u32x4 colfrag(const u32x4 (&r)[8], int d) {
  const int q = d >> 1;
  return (d & 1) ? (u32x4){phi(r[1][q], r[0][q]), phi(r[3][q], r[2][q]), phi(r[5][q], r[4][q]), phi(r[7][q], r[6][q])}
                 : (u32x4){plo(r[1][q], r[0][q]), plo(r[3][q], r[2][q]), plo(r[5][q], r[4][q]), plo(r[7][q], r[6][q])};
}
DI u32x4 sel4(bool c, u32x4 v) { return (u32x4){c ? v[0] : 0u, c ? v[1] : 0u, c ? v[2] : 0u, c ? v[3] : 0u}; }
template <typename T> DI T ldnt(const GAS T* p) { return __builtin_nontemporal_load(p); }
#define LAUX 16
#define SAUX 16
DI u32x4 ldws(__amdgpu_buffer_rsrc_t ws, int off) { return __builtin_amdgcn_raw_buffer_load_b128(ws, off, 0, LAUX); }
DI void stws(__amdgpu_buffer_rsrc_t ws, int off, u32x2 a, u32x2 b) { __builtin_amdgcn_raw_buffer_store_b128((u32x4){a[0], a[1], b[0], b[1]}, ws, off, 0, SAUX); }
DI void stws1(__amdgpu_buffer_rsrc_t ws, int off, u32x2 a) { __builtin_amdgcn_raw_buffer_store_b64(a, ws, off, 0, SAUX); }
DI u32x2 pack2(float a, float b, u32 tag8) {
  const u32 ar = f2u(a) + 0x800u;
  const u32 u = (f2u(b) & 0xf000u) | ((ar >> 4) & ~0xf000u);
  return (u32x2){phi(f2u(b), ar), __builtin_amdgcn_perm(tag8, u, 0x07060501u)};
}
DI float first2(u32 w0, u32 w1) { return u2f(__builtin_amdgcn_perm(w0, w1 << 4, 0x0504000cu)); }
DI float second2(u32 w0, u32 w1) { return u2f(__builtin_amdgcn_perm(w0, w1, 0x0706000cu)); }
DI bool tagged(u32x4 v, u32 tag8) { return ((v[1] ^ tag8) | (v[3] ^ tag8)) < 256u; }
#define PIN() do { asm volatile("" ::: "memory"); __builtin_amdgcn_sched_barrier(0); } while (0)
#define LDS_BARRIER() do { asm volatile("s_waitcnt lgkmcnt(0)\n\ts_barrier" ::: "memory"); __builtin_amdgcn_sched_barrier(0); } while (0)
#define RAW_BARRIER() do { __builtin_amdgcn_sched_barrier(0); asm volatile("s_barrier" ::: "memory"); __builtin_amdgcn_sched_barrier(0); } while (0)
DI void dma16(const u16* src, u32 dst) {
  u32 keep;
  asm volatile("s_mov_b32 %0, m0\n\ts_mov_b32 m0, %2\n\ts_nop 0\n\tglobal_load_lds_dwordx4 %1, off nt\n\ts_mov_b32 m0, %0"
               : "=&s"(keep) : "v"(src), "s"(dst) : "memory");
}
DI void touch(const void* src, u32 dst) {
  u32 keep;
  asm volatile("s_mov_b32 %0, m0\n\ts_mov_b32 m0, %2\n\ts_nop 0\n\tglobal_load_lds_dword %1, off\n\ts_mov_b32 m0, %0"
               : "=&s"(keep) : "v"(src), "s"(dst) : "memory");
}
DI void touch_nt(const void* src, u32 dst) {
  u32 keep;
  asm volatile("s_mov_b32 %0, m0\n\ts_mov_b32 m0, %2\n\ts_nop 0\n\tglobal_load_lds_dword %1, off nt\n\ts_mov_b32 m0, %0"
               : "=&s"(keep) : "v"(src), "s"(dst) : "memory");
}
DI void stouch8(const void* p) {
  u32 t;
  asm volatile("s_load_dword %0, %1, 0x0\n\ts_load_dword %0, %1, 0x80\n\ts_load_dword %0, %1, 0x100\n\ts_load_dword %0, %1, 0x180\n\t"
               "s_load_dword %0, %1, 0x200\n\ts_load_dword %0, %1, 0x280\n\ts_load_dword %0, %1, 0x300\n\ts_load_dword %0, %1, 0x380\n\t"
               "s_waitcnt lgkmcnt(0)" : "=&s"(t) : "s"(p) : "memory");
}
DI void stouch2x2(const void* p, const void* q) {
  u32 t;
  asm volatile("s_load_dword %0, %1, 0x0\n\ts_load_dword %0, %1, 0x80\n\ts_load_dword %0, %2, 0x0\n\ts_load_dword %0, %2, 0x80\n\t"
               "s_waitcnt lgkmcnt(0)" : "=&s"(t) : "s"(p), "s"(q) : "memory");
}
DI void set_flag(LAS unsigned char* lds, int f) {
  asm volatile("s_waitcnt lgkmcnt(0)" ::: "memory");
  *(volatile LAS u32*)(lds + L_FLAG + f * 4) = 1u;
}
DI void wait_flags(LAS unsigned char* lds, int first, int mask, int lane) {
#pragma unroll 1
  for (int spin = 0; spin < (SPIN_CAP << 4); ++spin) {
    if (__all(*(volatile LAS u32*)(lds + L_FLAG + (first + (lane & mask)) * 4) != 0u)) break;
    __builtin_amdgcn_s_sleep(1);
  }
  PIN();
}
DI u32 read_tag(const unsigned char* ws) {
  u32 e;
  asm volatile("s_load_dword %0, %1, 0x0\n\ts_waitcnt lgkmcnt(0)" : "=s"(e) : "s"(ws) : "memory");
  return (e + 1u) << 8;
}
template <int LOW, int PNT = 0> DI u32x4 poll16(__amdgpu_buffer_rsrc_t ws, int off, u32 want) {
  u32x4 v;
  u64 keep;
  u32 left = SPIN_CAP, t;
  asm volatile("s_mov_b64 %[keep], exec\n\t"
               "s_branch 2f\n"
               "1:\n\t"
               "s_sub_u32 %[left], %[left], 1\n\t"
               "s_cbranch_scc1 3f\n"
               "2:\n\t"
               ".if %[pnt]\n\tbuffer_load_dwordx4 v[100:103], %[off], %[ws], 0 offen sc1 nt\n\t.else\n\t"
               "buffer_load_dwordx4 v[100:103], %[off], %[ws], 0 offen sc1\n\t.endif\n\t"
               "s_waitcnt vmcnt(0)\n\t"
               "v_bitop3_b32 %[t], v101, v103, %[want] bitop3:0x7e\n\t"
               "v_cmp_lt_u32 vcc, %[low], %[t]\n\t"
               "s_and_b64 exec, exec, vcc\n\t"
               "s_cbranch_scc1 1b\n"
               "3:\n\t"
               "s_mov_b64 exec, %[keep]"
               : "={v[100:103]}"(v), [keep] "=&s"(keep), [left] "+s"(left), [t] "=&v"(t)
               : [off] "v"(off), [ws] "s"(ws), [want] "s"(want), [low] "i"(LOW), [pnt] "i"(PNT)
               : "vcc", "scc", "memory");
  return v;
}
DI void quad_sum4(float& a, float& b, float& c, float& d) {
  asm("s_nop 1\n\t"
      "v_add_f32_dpp %0, %0, %0 quad_perm:[1,0,3,2] row_mask:0xf bank_mask:0xf\n\t"
      "v_add_f32_dpp %1, %1, %1 quad_perm:[1,0,3,2] row_mask:0xf bank_mask:0xf\n\t"
      "v_add_f32_dpp %2, %2, %2 quad_perm:[1,0,3,2] row_mask:0xf bank_mask:0xf\n\t"
      "v_add_f32_dpp %3, %3, %3 quad_perm:[1,0,3,2] row_mask:0xf bank_mask:0xf\n\t"
      "v_add_f32_dpp %0, %0, %0 quad_perm:[2,3,0,1] row_mask:0xf bank_mask:0xf\n\t"
      "v_add_f32_dpp %1, %1, %1 quad_perm:[2,3,0,1] row_mask:0xf bank_mask:0xf\n\t"
      "v_add_f32_dpp %2, %2, %2 quad_perm:[2,3,0,1] row_mask:0xf bank_mask:0xf\n\t"
      "v_add_f32_dpp %3, %3, %3 quad_perm:[2,3,0,1] row_mask:0xf bank_mask:0xf"
      : "+v"(a), "+v"(b), "+v"(c), "+v"(d));
}
DI float sum_records(u32x4 v) {
  const float t = half16(half32(first2(v[0], v[1]), first2(v[2], v[3])), half32(second2(v[0], v[1]), second2(v[2], v[3])));
  return t + dpp<ROR8>(t);
}

DI void layernorm(u32 (&x)[8]) {
  float v[16], s = 0.f;
#pragma unroll
  for (int e = 0; e < 8; ++e) { v[2 * e] = bflo(x[e]); v[2 * e + 1] = bfhi(x[e]); s += v[2 * e] + v[2 * e + 1]; }
  const float mean = half_sum(s) * (1.f / 512.f);
  float q = 0.f;
#pragma unroll
  for (int e = 0; e < 16; ++e) { const float d = v[e] - mean; q = fmaf(d, d, q); }
  const float var = half_sum(q) * (1.f / 512.f);
  const float mb = rbf(mean), inv = __builtin_amdgcn_rcpf(rbf(__builtin_amdgcn_sqrtf(rbf(rbf(var) + 1e-5f))));
#pragma unroll
  for (int e = 0; e < 8; ++e) { const u32 d = pk(v[2 * e] - mb, v[2 * e + 1] - mb); x[e] = pk(bflo(d) * inv, bfhi(d) * inv); }
}

DI f32x2 tile_matvec(const u32x4 (&wv)[8], LAS unsigned char* xin, int g, int c) {
  const u32x4 xf = *(LAS u32x4*)(xin + ((c >> 2) & 1) * 1024 + ((c >> 3) * 32 + g * 8) * 2);
  f32x4 lo = {0.f, 0.f, 0.f, 0.f}, hi = lo;
#pragma unroll
  for (int d = 0; d < 4; ++d) {
    const u32x4 am = sel4((c & 3) == d, xf);
    lo = mfma(am, colfrag(wv, d), lo);
    hi = mfma(am, colfrag(wv, 4 + d), hi);
  }
  const bool valid = (g >> 1) == (c >> 3);
  float t[4];
#pragma unroll
  for (int e = 0; e < 4; ++e) { t[e] = half32(valid ? lo[e] : 0.f, valid ? hi[e] : 0.f); t[e] += dpp<ROR8>(t[e]); }
  return (c & 8) ? (f32x2){t[2], t[3]} : (f32x2){t[0], t[1]};
}

#define ST(i) do { if (!((STMASK >> (i)) & 1)) break; __builtin_amdgcn_sched_barrier(0); ts[i] = __builtin_amdgcn_s_memrealtime(); __builtin_amdgcn_s_waitcnt(0xC07F); __builtin_amdgcn_sched_barrier(0); } while (0)

__global__ void __launch_bounds__(1024, 4)
fused_decode(const u16* x_, const u16* k_, const u16* v_, const u16* wq_, const u16* wo_, const u16* w1_, const u16* b1_,
             const u16* w2_, const u16* b2_, u16* out_, unsigned char* ws_
             ) {
  __shared__ __attribute__((aligned(16))) unsigned char lds_raw[L_END];
  LAS unsigned char* lds = (LAS unsigned char*)lds_raw;
  u64 ts[15];
  for (int i = 0; i < 15; ++i) ts[i] = 0;
  ST(0);
  const u64 t_start = WO_MODE == 3 ? __builtin_amdgcn_s_memrealtime() : 0;
  asm volatile("" :: "s"(x_), "s"(k_), "s"(v_), "s"(wq_), "s"(wo_), "s"(w1_), "s"(b1_), "s"(w2_), "s"(b2_), "s"(out_), "s"(ws_));
  const int tid = threadIdx.x, lane = tid & 63, w = __builtin_amdgcn_readfirstlane(tid >> 6);
  const int g = lane >> 4, c = lane & 15;
  const int bid = blockIdx.x, kvh = bid & 7, j = bid >> 3, b = j >> 4, sp = j & 15, rb = j >> 2, cb = j & 3;
  const bool mlp1 = j < 14, mlp2 = j >= 14 && j < 28;
  const __amdgpu_buffer_rsrc_t ws = __builtin_amdgcn_make_buffer_rsrc(ws_, 0, WS_END, 0x00020000);
  u32 tag;
  const u32 hsh = ((u32)(bid * 16 + w) * 2654435761u) >> 20, hb = ((u32)bid * 2654435761u) >> 16;
  const int wu = (w + (ROT == 0 ? 0 : ROT == 1 || ROT == 2 || ROT == 3 ? j : (int)hb)) & 15;
  const int ro = ROT == 2 ? kvh : ROT == 3 || ROT == 4 ? (int)(hsh & 7) : 0;
  const size_t kvseq = (size_t)(b * 8 + kvh) * SK * DH;
  const __amdgpu_buffer_rsrc_t kr = __builtin_amdgcn_make_buffer_rsrc((void*)(k_ + kvseq), 0, SK * DH * 2, 0x00020000);
  const __amdgpu_buffer_rsrc_t vr = __builtin_amdgcn_make_buffer_rsrc((void*)(v_ + kvseq), 0, SK * DH * 2, 0x00020000);
  const int kvoff = (sp * 16 + wu) * 8192;
  const int klane = ((((c >> 1) ^ ro) & 1) * 4 + (c >> 2)) * 256 + (g * 2 + (c & 1)) * 16;
  u32x4 kf[8], vf[8];

  const int cq = j & 7, kq = j >> 3;
  const int mt = (mlp1 ? j : j - 14) * 8 + kvh;
  const int mhi = mt >> 2, mlo = mt & 3;
  auto pretouch = [&](int what) __attribute__((always_inline)) {
    const unsigned char* p = ws_ + WS_OP + ((kvh * 2 + b) * 16 + sp) * 2048 + c * 128;
    bool on = lane < 16 && (what & 1);
    if ((lane >> 3) == 2) { p = ws_ + WS_S2 + (cb * 64 + kvh * 8 + rb) * 1024 + (lane & 7) * 128; on = what & 16; }
    if ((lane >> 3) == 3) { p = ws_ + WS_MP + (mlo * 28 + mhi) * 1024 + (lane & 7) * 128; on = mlp2 && (what & 2); }
    if ((lane >> 2) == 8) { p = ws_ + WS_QP + ((((lane >> 1) & 1) * 8 + kvh) * 32 + cq * 4 + kq) * 256 + (lane & 1) * 128; on = what & 4; }
    if ((lane >> 2) == 9) { p = ws_ + WS_PA + mt * 512 + (lane & 3) * 128; on = mlp1 && (what & 4); }
    if (lane == 40) { p = (const unsigned char*)(out_ + ((bid >> 2) & 1) * D + (bid & 3) * 128 + ((bid >> 3) & 3) * 32); on = bid < NFIN && (what & 8); }
    if (lane == 41) { p = ws_; on = bid == 0 && (what & 32); }
    if (TV == 1 || TV == 3) on = on && (lane & 15) == 0;
    if (TV >= 2) { if (on) touch_nt(p, (u32)(size_t)(lds + L_T2)); } else
    if (on) touch(p, (u32)(size_t)(lds + L_T2));
  };
  if (WALK && w == WALKW) {
    const unsigned char* p;
    const int q = j & 3, xo = kvh * 128;
    switch (j >> 2) {
      case 0: p = (const unsigned char*)wq_ + (q << 20) + kvh * 1024; break;
      case 1: p = (const unsigned char*)w1_ + q * 458752 + xo; break;
      case 2: p = (const unsigned char*)w2_ + q * 458752 + xo; break;
      case 3: p = (const unsigned char*)k_ + ((size_t)((q >> 1) * 8 + kvh) << 21) + ((q & 1) << 20); break;
      case 4: p = (const unsigned char*)v_ + ((size_t)((q >> 1) * 8 + kvh) << 21) + ((q & 1) << 20); break;
      case 5: p = q == 0 ? (const unsigned char*)x_ + xo : q == 1 ? (const unsigned char*)b1_ + xo : q == 2 ? (const unsigned char*)b2_ + xo : ws_ + WS_END + xo; break;
      case 6: p = (const unsigned char*)wo_ + kvh * 524288 + q * 131072; break;
      default: p = (q & 1 ? (const unsigned char*)w2_ : (const unsigned char*)w1_) + 229376 + (q >> 1) * 917504 + xo; break;
    }
    if (lane == 0) touch_nt(p, (u32)(size_t)(lds + L_T2));
    PIN();
  }
  if (w < 2 || (w < 4 && mlp1)) {
    __builtin_amdgcn_s_setprio(3);
    if (w == 0 && lane < 16) *(LAS u32*)(lds + L_FLAG + lane * 4) = 0u;
    const GAS u32x4* xp = (const GAS u32x4*)x_ + lane * 2;
    const u32x4 x0 = xp[0], x1 = xp[1];
    PIN();
    ST(14);
    const int rs = w < 2 ? NQ / 8 : FF / 8, row0 = (w < 2 ? kq : mlo) * 128 + (w & 1) * 64;
    const u16* tile = w < 2 ? wq_ + kvh * 512 + cq * 64 : w1_ + mhi * 64;
    const GAS u32x4* wp = (const GAS u32x4*)tile + (row0 + (c >> 3) * 32 + g * 8) * rs + (c & 7);
    u32x4 wv[8];
#pragma unroll
    for (int i = 0; i < 8; ++i) wv[i] = ldnt(wp + i * rs);
    PIN();
    LDS_BARRIER();
    ST(12);
    tag = read_tag(ws_);
    PIN();
    u32 xs[8] = {x0[0], x0[1], x0[2], x0[3], x1[0], x1[1], x1[2], x1[3]};
    layernorm(xs);
    if (w >= 2) layernorm(xs);
    LAS unsigned char* xl = lds + L_XN + w * 2048;
    *(LAS u32x4*)(xl + lane * 32) = (u32x4){xs[0], xs[1], xs[2], xs[3]};
    *(LAS u32x4*)(xl + lane * 32 + 16) = (u32x4){xs[4], xs[5], xs[6], xs[7]};
    f32x2 r = tile_matvec(wv, xl + row0 * 2, g, c);
    LAS f32x2* half = (LAS f32x2*)(lds + L_HALF + (w >> 1) * 512 + lane * 8);
    if (w & 1) {
      *half = r;
      set_flag(lds, F_HALF + (w >> 1));
    } else {
      wait_flags(lds, F_HALF + (w >> 1), 0, lane);
      r += *half;
      const int rec = w == 0 ? WS_QP + ((((g & 1) * 8 + kvh) * 8 + cq) * 4 + kq) * 256 : WS_PA + mt * 512 + (g & 1) * 256;
      stws1(ws, rec + ((c & 7) * 4 + (g >> 1) * 2 + (c >> 3)) * 8, pack2(r[0], r[1], tag));
    }
    ST(2);
    __builtin_amdgcn_s_setprio(PRIO_K);
  } else {
    if (w < 4 && mlp2) {
      const u16* p = w2_ + (mhi * 64 + (w & 1) * 32 + g * 8) * D + mlo * 128 + c * 8;
#pragma unroll
      for (int i = 0; i < 8; ++i) dma16(p + i * D, (u32)(size_t)(lds + L_W2 + (w & 1) * 8192 + i * 1024));
    }
    if (CODE_LINES && w == 4) {
      const unsigned char* pc;
      asm volatile("s_getpc_b64 %0" : "=s"(pc));
      if (lane < 4 && j * 4 + lane < CODE_LINES) touch(pc + (j * 4 + lane) * 128, (u32)(size_t)(lds + L_TOUCH));
    }
    if (PRE_A && w == PREW) pretouch(PRE_A);
    if (SPRE & 16) RAW_BARRIER();
    if ((SPRE & 1) && (w >> 1) == 6) stouch8(ws_ + WS_OP + ((kvh * 2 + b) * 16 + sp) * 2048 + (w & 1) * 1024);
    if ((SPRE & 1) && w == 14) stouch8(ws_ + WS_S2 + (cb * 64 + kvh * 8 + rb) * 1024);
    if ((SPRE & 2) && w == 15 && mlp2) stouch8(ws_ + WS_MP + (mlo * 28 + mhi) * 1024);
    if ((SPRE & 4) && w == 11) { const unsigned char* q = ws_ + WS_QP + (kvh * 32 + cq * 4 + kq) * 256; stouch2x2(q, q + 65536); }
    if ((SPRE & 4) && w == 10 && mlp1) { const unsigned char* q = ws_ + WS_PA + mt * 512; stouch2x2(q, q + 256); }
    if ((SPRE & 8) && w == 9 && bid < NFIN) { const u16* q = out_ + ((bid >> 2) & 1) * D + (bid & 3) * 128; stouch2x2(q, q); }
    if (!(SPRE & 16)) RAW_BARRIER();
    if (PRE_B && w == PREW + 1) pretouch(PRE_B);
    if ((w >> 2) == 1) __builtin_amdgcn_s_setprio(2); else __builtin_amdgcn_s_setprio(PRIO_K);
  }
  PIN();

  const int slot = SLOTS == 1 ? SLOT0 : SLOTS == 2 ? SLOT0 + (j & 1) : j & 3;
  if (PRE_C && slot < 2 && w == 9 + slot * 4) pretouch(PRE_C);
#pragma unroll
  for (int i = 0; i < 8; ++i) { kf[i] = BUF ? __builtin_amdgcn_raw_buffer_load_b128(kr, klane + (i & 1) * 128, kvoff + ((i >> 1) ^ (ro >> 1)) * 2048, NT) : ldnt((const GAS u32x4*)((const GAS char*)(k_ + kvseq) + kvoff + klane + (i & 1) * 128 + (i >> 1) * 2048)); __builtin_amdgcn_sched_barrier(0); if (i == 0) ST(13); }
  if ((w >> 2) != 1) __builtin_amdgcn_s_setprio(0);
  if (PRE_C && slot >= 2 && w == 1 + (slot - 2) * 8) pretouch(PRE_C);
  PIN();
#pragma unroll
  for (int i = 0; i < 8; ++i) { vf[i] = BUF ? __builtin_amdgcn_raw_buffer_load_b128(vr, lane * 16, kvoff + (i ^ ro) * 1024, NT) : ldnt((const GAS u32x4*)((const GAS char*)(v_ + kvseq) + kvoff + lane * 16 + i * 1024)); __builtin_amdgcn_sched_barrier(0); }
  PIN();
  if ((PRE_D || WO_V) && lane == 0) __hip_atomic_fetch_add((LAS u32*)(lds + L_CNT + 4), 1u, __ATOMIC_RELAXED, __HIP_MEMORY_SCOPE_WORKGROUP);
  if (PRE_E && w == 13) pretouch(PRE_E);
  ST(4);
  tag = read_tag(ws_);
  const int paoff = WS_PA + mhi * 2048 + lane * 16;
  u32x4 p0, p1;
  u32 b1v;
  if ((w >> 2) == 1) {
    const int u = w & 3;
    const int qoff = WS_QP + (b * 8 + kvh) * 8192 + u * 2048 + lane * 16;
    asm volatile("s_waitcnt vmcnt(0)" ::: "memory");
    if (Q_DELAY) __builtin_amdgcn_s_sleep(Q_DELAY);
    u32x4 q0, q1;
#pragma unroll 1
    for (int spin = 0; spin < SPIN_CAP; ++spin) {
      q0 = ldws(ws, qoff); q1 = ldws(ws, qoff + 1024);
      if (__all(tagged(q0, tag) && tagged(q1, tag))) break;
      __builtin_amdgcn_s_sleep(SLEEP_Q);
      asm volatile("" ::: "memory");
    }
    const float qa = half16(half32(first2(q0[0], q0[1]), first2(q1[0], q1[1])), half32(first2(q0[2], q0[3]), first2(q1[2], q1[3])));
    const float qb = half16(half32(second2(q0[0], q0[1]), second2(q1[0], q1[1])), half32(second2(q0[2], q0[3]), second2(q1[2], q1[3])));
    *(LAS u32*)(lds + L_Q + u * 256 + ((g >> 1) * 32 + c * 2 + (g & 1)) * 4) = pk(qa, qb);
    set_flag(lds, F_Q + u);
    ST(3);
    if (w == 7 && mlp2) {
      p0 = ldws(ws, paoff); p1 = ldws(ws, paoff + 1024);
      b1v = B1NT ? ldnt((const GAS u32*)(b1_ + mhi * 64 + c * 4 + (g >> 1) * 2)) : *(const GAS u32*)(b1_ + mhi * 64 + c * 4 + (g >> 1) * 2);
      PIN();
    }
    __builtin_amdgcn_s_setprio(0);
  }
  wait_flags(lds, F_Q, 3, lane);

  float sc[8];
  {
    const int x0 = (c >> 2) & 1;
    const u32x4 q0 = *(LAS u32x4*)(lds + L_Q + (c & 3) * 256 + (g * 2 + x0) * 16);
    const u32x4 q1 = *(LAS u32x4*)(lds + L_Q + (c & 3) * 256 + 128 + (g * 2 + x0) * 16);
#pragma unroll
    for (int o = 0; o < 4; ++o) {
      f32x4 s = mfma(kf[2 * o], q0, (f32x4){0.f, 0.f, 0.f, 0.f});
      s = mfma(kf[2 * o + 1], q1, s);
#pragma unroll
      for (int a = 0; a < 2; ++a) {
        float e1 = s[2 * a + 1];
        asm("" : "+v"(e1));
        const float t = x0 ? e1 : s[2 * a];
        sc[2 * o + a] = t + dpp<ROR4>(t);
      }
      const u32 d = pk(sc[2 * o], sc[2 * o + 1]), e = pk(bflo(d) * 0.08838834764831845f, bfhi(d) * 0.08838834764831845f);
      sc[2 * o] = bflo(e); sc[2 * o + 1] = bfhi(e);
    }
  }
  PIN();

    float m = fmaxf(fmaxf(fmaxf(sc[0], sc[1]), fmaxf(sc[2], sc[3])), fmaxf(fmaxf(sc[4], sc[5]), fmaxf(sc[6], sc[7])));
    m = max32(max16(m)) * LOG2E;
    float p[8], l = 0.f;
#pragma unroll
    for (int e = 0; e < 8; ++e) { p[e] = __builtin_amdgcn_exp2f(fmaf(sc[e], LOG2E, -m)); l += p[e]; }
    l = half16(l, l); l = half32(l, l);
    if (lane < 4) { *(LAS float*)(lds + L_MW + (lane * 16 + w) * 4) = m; *(LAS float*)(lds + L_LW + (lane * 16 + w) * 4) = l; }
    PIN();
    if ((w >> 1) == 1) set_flag(lds, F_W2 + (w & 1));
    if (lane == 0) __hip_atomic_fetch_add((LAS u32*)(lds + L_CNT), 1u, __ATOMIC_RELAXED, __HIP_MEMORY_SCOPE_WORKGROUP);
    const u32x4 pf = {pk(p[0], p[1]), pk(p[2], p[3]), pk(p[4], p[5]), pk(p[6], p[7])};
    u32x4 am[4];
#pragma unroll
    for (int u = 0; u < 4; ++u) am[u] = sel4((c >> 2) == u, pf);
    PIN();
    u32x4 ve[4], vo[4];
    u32 slo, shi;
    asm volatile("s_mov_b32 %0, 0x05040100\n\ts_mov_b32 %1, 0x07060302" : "=s"(slo), "=s"(shi) : "v"(am[0][0]), "v"(am[1][1]), "v"(am[2][2]), "v"(am[3][3]));
#pragma unroll
    for (int r = 0; r < 4; ++r) {
#pragma unroll
      for (int u = 0; u < 4; ++u) {
        ve[u][r] = __builtin_amdgcn_perm(vf[2 * r + 1][u], vf[2 * r][u], slo);
        vo[u][r] = __builtin_amdgcn_perm(vf[2 * r + 1][u], vf[2 * r][u], shi);
      }
      if (r < 3)
        asm volatile("" : "+s"(slo), "+s"(shi) : "v"(ve[0][r]), "v"(ve[1][r]), "v"(ve[2][r]), "v"(ve[3][r]), "v"(vo[0][r]), "v"(vo[1][r]), "v"(vo[2][r]), "v"(vo[3][r]));
    }
    f32x4 oe = {0.f, 0.f, 0.f, 0.f}, oo = oe;
#pragma unroll
    for (int u = 0; u < 4; ++u) { oe = mfma(am[u], ve[u], oe); oo = mfma(am[u], vo[u], oo); }
#pragma unroll
    for (int h = 0; h < 4; ++h) *(LAS f32x2*)(lds + L_OW + w * OW_STRIDE + h * 512 + c * 32 + g * 8) = (f32x2){oe[h], oo[h]};
    PIN();
    ST(5);

  const bool gath = (w & 4) == 0;
  const int v = (w & 3) + (w >> 3) * 4;
  auto wait_ml = [&]() __attribute__((always_inline)) {
#pragma unroll 1
    for (int spin = 0; spin < (SPIN_CAP << 4); ++spin) {
      if (__builtin_amdgcn_readfirstlane(*(volatile LAS u32*)(lds + L_CNT)) == 16u) break;
      __builtin_amdgcn_s_sleep(1);
    }
    PIN();
  };
  auto load_wo = [&](u32x4 (&wt)[8]) __attribute__((always_inline)) {
    const GAS u16* p = (const GAS u16*)wo_ + (size_t)(kvh * 512 + rb * 64 + g * 4) * D + cb * 128 + (w - 4) * 64 + c * 4;
#pragma unroll
    for (int i = 0; i < 8; ++i) {
      const u32x2 t0 = ldnt((const GAS u32x2*)(p + (size_t)(32 * (i >> 2) + (i & 3)) * D));
      const u32x2 t1 = ldnt((const GAS u32x2*)(p + (size_t)(32 * (i >> 2) + 16 + (i & 3)) * D));
      wt[i] = (u32x4){t0[0], t0[1], t1[0], t1[1]};
    }
    PIN();
  };
  auto wait_v = [&](u32 n) __attribute__((always_inline)) {
#pragma unroll 1
    for (int spin = 0; spin < (SPIN_CAP << 4); ++spin) {
      if (__builtin_amdgcn_readfirstlane(*(volatile LAS u32*)(lds + L_CNT + 4)) >= n) break;
      __builtin_amdgcn_s_sleep(1);
    }
    PIN();
  };
  if (PRE_D && w == 6) { wait_v(PRE_AT); pretouch(PRE_D); }
  const bool fin = FINLAST ? bid >= 256 - NFIN : bid < NFIN;
  const int cf = bid & 3, bf = (bid >> 2) & 1, seg = (bid >> 3) & 3;
  if (w >= 12) {
    LDS_BARRIER();
    ST(6);
    if (fin) {
      const int row = (w - 12) * 8 + (lane >> 3);
      const int moff = WS_MP + (cf * 28 + min(row, 27)) * 1024 + bf * 512 + seg * 128 + (lane & 7) * 16;
      u32x4 mv;
#pragma unroll 1
      for (int spin = 0; spin < SPIN_CAP; ++spin) {
        mv = ldws(ws, moff);
        if (__all(tagged(mv, tag))) break;
        __builtin_amdgcn_s_sleep(SLEEP_M);
        asm volatile("" ::: "memory");
      }
      const float t = sum_records(sel4(row < 28, mv));
      if ((lane & 8) == 0) *(LAS float*)(lds + L_MF + (w - 12) * 128 + ((lane & 7) * 4 + g) * 4) = t;
      LDS_BARRIER();
    }
  } else if (gath) {
    {
      const int f = v * 16 + (lane >> 2), p4 = lane & 3, hd = v >> 1;
      wait_ml();
      const f32x4 mv = *(LAS f32x4*)(lds + L_MW + hd * 64 + p4 * 16), lv = *(LAS f32x4*)(lds + L_LW + hd * 64 + p4 * 16);
      float M = fmaxf(fmaxf(mv[0], mv[1]), fmaxf(mv[2], mv[3]));
      M = fmaxf(M, dpp<XOR1>(M)); M = fmaxf(M, dpp<XOR2>(M));
      float L = 0.f, fw[4];
#pragma unroll
      for (int i = 0; i < 4; ++i) { fw[i] = __builtin_amdgcn_exp2f(mv[i] - M); L = fmaf(fw[i], lv[i], L); }
      L += dpp<XOR1>(L); L += dpp<XOR2>(L);
      const float inv = __builtin_amdgcn_rcpf(L);
#pragma unroll
      for (int i = 0; i < 4; ++i) fw[i] *= inv;
      const u32 lam = f2u(M + __builtin_amdgcn_logf(L)) >> ((lane >> 5) * 16);
      u32 t0 = tag | (lam & 255u), t1 = tag | ((lam >> 8) & 255u);
      int ooff = WS_OP + ((kvh * 2 + b) * 16 + sp) * 2048 + f * 16;
      int laddr = L_OW + p4 * 4 * OW_STRIDE + f * 16;
      asm volatile("" : "+v"(fw[0]), "+v"(fw[1]), "+v"(fw[2]), "+v"(fw[3]), "+v"(t0), "+v"(t1), "+v"(ooff), "+v"(laddr));
      ST(3);
      LDS_BARRIER();
      ST(6);
      f32x4 s = fw[0] * *(LAS f32x4*)(lds + laddr);
#pragma unroll
      for (int i = 1; i < 4; ++i) s += fw[i] * *(LAS f32x4*)(lds + laddr + i * OW_STRIDE);
      float s0 = s[0], s1 = s[1], s2 = s[2], s3 = s[3];
      quad_sum4(s0, s1, s2, s3);
      if (p4 == 0) __builtin_amdgcn_raw_buffer_store_b128((u32x4){pk(s0, s1), t0, pk(s2, s3), t1}, ws, ooff, 0, OSAUX);
    }
    PIN();
    ST(7);
    {
      u32 gaddr2 = (u32)(size_t)(lds + L_GP + v * GP_STRIDE + 256), gaddr = gaddr2 - 256 + (c * 4 + g) * 4;
      u32 lsel = (c & 8) ? 0x05040100u : 0x01000504u;
      asm volatile("" : "+v"(gaddr), "+v"(gaddr2), "+v"(lsel));
      if (SLEEP_O) __builtin_amdgcn_s_sleep(SLEEP_O);
      const u32x4 ov = poll16<255, OPNT>(ws, WS_OP + (kvh * 32 + v * 4 + g) * 2048 + rb * 256 + c * 16, tag);
      ST(8);
      u32 a, lam, m1, x0, x1, x2, x3;
      f32x2 gl;
      asm volatile("v_perm_b32 %[a], %[t1], %[t0], %[sel]\n\t"
                   "v_lshlrev_b32 %[x0], 16, %[d0]\n\t"
                   "v_lshlrev_b32 %[x2], 16, %[d1]\n\t"
                   "v_mov_b32_dpp %[m1], %[a] row_ror:8 row_mask:0xf bank_mask:0xf\n\t"
                   "v_and_b32 %[x1], 0xffff0000, %[d0]\n\t"
                   "v_perm_b32 %[lam], %[a], %[m1], %[lsel]\n\t"
                   "v_mov_b32 %[m1], %[lam]\n\t"
                   "v_mov_b32 %[a], %[lam]\n\t"
                   "v_and_b32 %[x3], 0xffff0000, %[d1]\n\t"
                   "s_nop 0\n\t"
                   "v_permlane16_swap_b32 %[m1], %[a]\n\t"
                   "v_max_f32 %[m1], %[m1], %[a]\n\t"
                   "v_mov_b32 %[a], %[m1]\n\t"
                   "s_nop 1\n\t"
                   "v_permlane32_swap_b32 %[m1], %[a]\n\t"
                   "v_max_f32 v104, %[m1], %[a]\n\t"
                   "v_sub_f32 %[lam], %[lam], v104\n\t"
                   "v_exp_f32 %[lam], %[lam]\n\t"
                   "s_nop 0\n\t"
                   "v_mul_f32 %[x0], %[lam], %[x0]\n\t"
                   "v_mul_f32 %[x2], %[lam], %[x2]\n\t"
                   "v_mul_f32 %[x1], %[lam], %[x1]\n\t"
                   "v_mul_f32 %[x3], %[lam], %[x3]\n\t"
                   "v_mov_b32 %[a], %[lam]\n\t"
                   "v_permlane32_swap_b32 %[x0], %[x2]\n\t"
                   "v_permlane32_swap_b32 %[x1], %[x3]\n\t"
                   "v_permlane16_swap_b32 %[lam], %[a]\n\t"
                   "v_add_f32 %[x0], %[x0], %[x2]\n\t"
                   "v_add_f32 %[x1], %[x1], %[x3]\n\t"
                   "v_add_f32 %[lam], %[lam], %[a]\n\t"
                   "v_mov_b32 %[a], %[lam]\n\t"
                   "v_permlane16_swap_b32 %[x0], %[x1]\n\t"
                   "v_add_f32 %[x0], %[x0], %[x1]\n\t"
                   "v_permlane32_swap_b32 %[lam], %[a]\n\t"
                   "ds_write_b32 %[ga], %[x0]\n\t"
                   "v_add_f32 v105, %[lam], %[a]\n\t"
                   "ds_write_b64 %[gb], v[104:105]"
                   : [a] "=&v"(a), [lam] "=&v"(lam), [m1] "=&v"(m1), [x0] "=&v"(x0), [x1] "=&v"(x1), [x2] "=&v"(x2), [x3] "=&v"(x3), "=&{v[104:105]}"(gl)
                   : [d0] "v"(ov[0]), [t0] "v"(ov[1]), [d1] "v"(ov[2]), [t1] "v"(ov[3]), [sel] "s"(0x04000400u), [lsel] "v"(lsel), [ga] "v"(gaddr), [gb] "v"(gaddr2)
                   : "memory");
    }
    LDS_BARRIER();
    ST(9);
    if (fin) {
      int faddr = L_FP + ((lane & 7) * 4 + g) * 32 + v * 4;
      asm volatile("" : "+v"(faddr));
      if (SLEEP_S) __builtin_amdgcn_s_sleep(SLEEP_S);
      const u32x4 sv = poll16<255>(ws, WS_S2 + (cf * 64 + v * 8 + (lane >> 3)) * 1024 + bf * 512 + seg * 128 + (lane & 7) * 16, tag);
      ST(10);
      *(LAS float*)(lds + faddr) = sum_records(sv);
      LDS_BARRIER();
    }
  } else if (w < 6) {
    u32x4 wt[8], bw[8];
    if (WO_MODE == 3) {
      wait_ml();
      const bool now = __builtin_amdgcn_s_memrealtime() >= t_start + WO_AT;
      if (!now) {
        LDS_BARRIER();
        ST(6);
        while (__builtin_amdgcn_s_memrealtime() < t_start + WO_AT) __builtin_amdgcn_s_sleep(1);
      }
      load_wo(wt);
      if (now) { LDS_BARRIER(); ST(6); }
    } else {
    if (WO_MODE == 1) { if (WO_V) wait_v(16u); else wait_ml(); }
    if (WO_MODE < 2) load_wo(wt);
    LDS_BARRIER();
    ST(6);
    if (WO_MODE == 2) { if (WO_SLEEP) __builtin_amdgcn_s_sleep(WO_SLEEP); load_wo(wt); }
    }
#pragma unroll
    for (int i = 0; i < 8; ++i) bw[i] = colfrag(wt, i);
    int gp = L_GP + (c & 7) * GP_STRIDE, go = gp + ((c >> 3) * 16 + g * 4) * 4;
    int soff = WS_S2 + (cb * 64 + kvh * 8 + rb) * 1024 + (g & 1) * 512 + ((w - 4) * 32 + c * 2 + (g >> 1)) * 8;
    asm volatile("" : "+v"(gp), "+v"(go), "+v"(soff) : "v"(bw[0]), "v"(bw[1]), "v"(bw[2]), "v"(bw[3]), "v"(bw[4]), "v"(bw[5]), "v"(bw[6]), "v"(bw[7]));
    ST(1);
    LDS_BARRIER();
    ST(9);
    f32x2 gl = *(LAS f32x2*)(lds + gp + 256);
    u32x4 of = *(LAS u32x4*)(lds + go), o1 = *(LAS u32x4*)(lds + go + 128);
#define QP1(r) "v_add_f32_dpp " r ", " r ", " r " quad_perm:[1,0,3,2] row_mask:0xf bank_mask:0xf\n\t"
#define QP2(r) "v_add_f32_dpp " r ", " r ", " r " quad_perm:[2,3,0,1] row_mask:0xf bank_mask:0xf\n\t"
#define PKM(r) "v_pk_mul_f32 " r ", v[118:119], " r " op_sel_hi:[0,1]\n\t"
    asm volatile("v_max_f32_dpp v118, v116, v116 quad_perm:[1,0,3,2] row_mask:0xf bank_mask:0xf\n\t"
                 "s_nop 1\n\t"
                 "v_max_f32_dpp v118, v118, v118 quad_perm:[2,3,0,1] row_mask:0xf bank_mask:0xf\n\t"
                 "v_sub_f32 v118, v116, v118\n\t"
                 "v_exp_f32 v118, v118\n\t"
                 "s_nop 0\n\t"
                 "v_mul_f32 v117, v118, v117\n\t"
                 PKM("v[108:109]") PKM("v[110:111]") PKM("v[112:113]") PKM("v[114:115]")
                 QP1("v117") QP1("v108") QP1("v109") QP1("v110") QP1("v111") QP1("v112") QP1("v113") QP1("v114") QP1("v115")
                 QP2("v117") QP2("v108") QP2("v109")
                 "v_rcp_f32 v118, v117\n\t"
                 QP2("v110") QP2("v111") QP2("v112") QP2("v113") QP2("v114") QP2("v115")
                 PKM("v[108:109]") PKM("v[110:111]") PKM("v[112:113]") PKM("v[114:115]")
                 "v_cvt_pk_bf16_f32 v108, v108, v109\n\t"
                 "v_cvt_pk_bf16_f32 v109, v110, v111\n\t"
                 "v_cvt_pk_bf16_f32 v110, v112, v113\n\t"
                 "v_cvt_pk_bf16_f32 v111, v114, v115\n\t"
                 "s_nop 1"
                 : "+{v[108:111]}"(of), "+{v[112:115]}"(o1), "+{v[116:117]}"(gl)
                 :
                 : "v118", "v119");
    f32x4 acc[8];
#pragma unroll
    for (int i = 0; i < 8; ++i) { const int k = (i & 3) * 2 + (i >> 2); acc[k] = mfma(of, bw[k], (f32x4){0.f, 0.f, 0.f, 0.f}); }
    __builtin_amdgcn_sched_barrier(0);
    float r[4];
#pragma unroll
    for (int d = 0; d < 4; ++d) r[d] = (g >> 1) ? acc[4 + d][0] : acc[d][0];
    stws1(ws, soff, pack2(half32(r[0], r[2]), half32(r[1], r[3]), tag));
    ST(10);
  } else if (w == 7 && mlp2) {
#pragma unroll 1
    for (int spin = 0; spin < SPIN_CAP; ++spin) {
      if (__all(tagged(p0, tag) && tagged(p1, tag))) break;
      __builtin_amdgcn_s_sleep(SLEEP_Q);
      asm volatile("" ::: "memory");
      p0 = ldws(ws, paoff); p1 = ldws(ws, paoff + 1024);
    }
    const float t2[2] = {half32(first2(p0[0], p0[1]) + first2(p1[0], p1[1]), first2(p0[2], p0[3]) + first2(p1[2], p1[3])),
                         half32(second2(p0[0], p0[1]) + second2(p1[0], p1[1]), second2(p0[2], p0[3]) + second2(p1[2], p1[3]))};
    float hv[2];
#pragma unroll
    for (int e = 0; e < 2; ++e) {
      const float t = rbf(rbf(t2[e]) + (e ? bfhi(b1v) : bflo(b1v)));
      const float uu = 0.7978845608028654f * (t + 0.044715f * t * t * t);
      hv[e] = t / (1.f + __builtin_amdgcn_exp2f(-2.f * LOG2E * uu));
    }
    *(LAS u32*)(lds + L_HS + (g & 1) * 128 + (c * 2 + (g >> 1)) * 4) = pk(hv[0], hv[1]);
    wait_flags(lds, F_W2, 1, lane);
    f32x4 r = {0.f, 0.f, 0.f, 0.f};
#pragma unroll
    for (int s = 0; s < 2; ++s) {
      const u32x4 ha = *(LAS u32x4*)(lds + L_HS + ((c >> 2) & 1) * 128 + s * 64 + g * 16);
      u32x4 w2t[8];
#pragma unroll
      for (int i = 0; i < 8; ++i) w2t[i] = *(LAS u32x4*)(lds + L_W2 + s * 8192 + i * 1024 + lane * 16);
#pragma unroll
      for (int d = 0; d < 8; ++d) r = mfma(sel4((c >> 3) == (d >> 2) && (c & 3) == (d & 3), ha), colfrag(w2t, d), r);
    }
    u32x2 m0 = pack2(r[0], r[1], tag), m1 = pack2(r[2], r[3], tag);
    asm volatile("" : "+v"(m0), "+v"(m1));
    ST(2);
    if (MP_LATE) LDS_BARRIER();
    stws(ws, WS_MP + (mlo * 28 + mhi) * 1024 + (g & 1) * 512 + (c * 2 + (g >> 1)) * 16, m0, m1);
    if (!MP_LATE) LDS_BARRIER();
    ST(6);
  } else if (w == 6 && fin) {
    const int col = cf * 128 + seg * 32 + (lane & 31);
    const u32 xv = ((const GAS u16*)x_)[bf * D + col], b2v = ((const GAS u16*)b2_)[col];
    LDS_BARRIER();
    ST(6);
    LDS_BARRIER();
    LAS float* mf = (LAS float*)(lds + L_MF + (lane >> 5) * 256 + (lane & 31) * 4);
    float t = mf[0] + mf[32];
    t = half32(t, t);
    t = rbf(rbf(t) + bflo(b2v));
    float gate = rbf(t / (1.f + __builtin_amdgcn_exp2f(-LOG2E * t))), xf = bflo(xv);
    int faddr = L_FP + (lane & 31) * 32 + (lane >> 5) * 16;
    GAS u16* op = (GAS u16*)out_ + bf * D + col;
    asm volatile("" : "+v"(gate), "+v"(xf), "+v"(faddr), "+v"(op));
    LDS_BARRIER();
    ST(10);
    const f32x4 fp = *(LAS f32x4*)(lds + faddr);
    float s = (fp[0] + fp[1]) + (fp[2] + fp[3]);
    s = half32(s, s);
    const float res = xf + rbf(rbf(s) * gate);
    if (lane < 32) __hip_atomic_store(op, (u16)pk(res, res), RLX_AGENT);
    if ((bid & 31) == 0 && lane == 0) __hip_atomic_store((GAS u32*)(ws_ + WS_EPOCH), tag >> 8, RLX_AGENT);
  } else {
    LDS_BARRIER();
    ST(6);
  }
  ST(11);
  {
    GAS u64* st = (GAS u64*)(ws_ + (WS_AT_END ? -(8 << 20) : (8 << 20)));
    volatile LAS u64* ls = (volatile LAS u64*)(lds + L_TOUCH);
    if (lane == 0) { ls[w * 2] = ((PROBE_WM >> w) & 1) ? ts[PROBE] : 0; ls[w * 2 + 1] = ts[PROBE_B]; }
    if (w == 0) {
      while (__builtin_amdgcn_s_memrealtime() < ts[0] + 2500) __builtin_amdgcn_s_sleep(1);
      u64 ma = ls[(lane & 15) * 2], mb = ls[(lane & 15) * 2 + 1];
      auto red = [&]() {
        for (int o = 32; o; o >>= 1) {
          const u64 x = __shfl_xor(ma, o), y = __shfl_xor(mb, o);
          ma = x > ma ? x : ma;
          if (PROBE_B == 0) mb = y < mb ? y : mb; else mb = y > mb ? y : mb;
        }
      };
      red();
      if (lane == 0) { __hip_atomic_store(st + bid * 2, ma, RLX_AGENT); __hip_atomic_store(st + bid * 2 + 1, mb, RLX_AGENT); }
      if (bid == 0) {
        while (__builtin_amdgcn_s_memrealtime() < ts[0] + 3200) __builtin_amdgcn_s_sleep(1);
        for (int i = lane; i < 256; i += 64) {
          const u64 x = __hip_atomic_load(st + i * 2, RLX_AGENT), y = __hip_atomic_load(st + i * 2 + 1, RLX_AGENT);
          ma = x > ma ? x : ma;
          if (PROBE_B == 0) mb = y < mb ? y : mb; else mb = y > mb ? y : mb;
        }
        red();
        const u64 until = ts[0] + 5000 + 10 * (ma - mb);
        while (__builtin_amdgcn_s_memrealtime() < until && __builtin_amdgcn_s_memrealtime() < ts[0] + 40000) __builtin_amdgcn_s_sleep(1);
      }
    }
  }
}

extern "C" void kernel_launch(void* const* d_in, const int* in_sizes, int n_in, void* d_out, int out_size, void* d_ws, size_t ws_size,
                              hipStream_t stream) {
  unsigned char* ws = (unsigned char*)d_ws + (WS_AT_END ? ((ws_size - WS_SPAN) & ~(size_t)4095) : 0);
  fused_decode<<<256, 1024, 0, stream>>>((const u16*)d_in[0], (const u16*)d_in[1], (const u16*)d_in[2], (const u16*)d_in[3],
                                         (const u16*)d_in[4], (const u16*)d_in[6], (const u16*)d_in[7], (const u16*)d_in[8],
                                         (const u16*)d_in[9], (u16*)d_out, ws
                                         );
}
```

```cpp
#define FINLAST 0
#include <hip/hip_runtime.h>
#include <stdint.h>

#define WS_AT_END 0
#define SLEEP_Q 8
#define SLEEP_O 8
#define SLEEP_S 8
#define WO_MODE 1
#define PRIO_K 1
#define ROT 1
#define CODE_LINES 0
#define RND 1
#define BUF 1
#define SPRE 0
#define TV 0
#define PRE_C 0
#define SLOTS 4
#define SLOT0 0
#define OSAUX 16
#define OPNT 0
#define PRE_D 25
#define PRE_E 0
#define B1NT 0
#define PROBE_WM 0xffff
#define WALK 0
#define WALKW 0
#define SCODE 0
#define SCODEW 15
#define PRE_AT 16u
#define WO_V 0
#define PRE_A 0
#define PRE_B 0
#define PREW 12
#define Q_DELAY 32
#define MP_LATE 0
#define SLEEP_M 4
#define WO_AT 1150
#define WO_SLEEP 0
#define ABL 0
#define STAMP 0

#define GAS __attribute__((address_space(1)))
#define LAS __attribute__((address_space(3)))
#define DI __device__ __forceinline__
#define RLX_AGENT __ATOMIC_RELAXED, __HIP_MEMORY_SCOPE_AGENT
typedef unsigned short u16;
typedef unsigned u32;
typedef unsigned long long u64;
typedef u32 u32x2 __attribute__((ext_vector_type(2)));
typedef u32 u32x4 __attribute__((ext_vector_type(4)));
typedef float f32x2 __attribute__((ext_vector_type(2)));
typedef float f32x4 __attribute__((ext_vector_type(4)));
typedef __bf16 bf16x2 __attribute__((ext_vector_type(2)));
typedef __bf16 bf16x8 __attribute__((ext_vector_type(8)));

constexpr int D = 512, SK = 8192, DH = 128, FF = 1792, NQ = 4096;
constexpr int SC1 = 16, NT = 2;
constexpr int SPIN_CAP = 1 << 18;
constexpr int NFIN = 32;
constexpr float LOG2E = 1.4426950408889634f;

constexpr int WS_EPOCH = 0;
constexpr int WS_QP = 256;
constexpr int WS_PA = WS_QP + 512 * 256;
constexpr int WS_OP = WS_PA + 112 * 512;
constexpr int WS_S2 = WS_OP + 256 * 2048;
constexpr int WS_MP = WS_S2 + 256 * 1024;
constexpr int WS_END = WS_MP + 112 * 1024;
constexpr int WS_SPAN = 4 << 20;
constexpr int WS_STAMP = 8 << 20;

constexpr int F_Q = 0, F_HALF = 4, F_W2 = 6;
constexpr int L_FLAG = 0, L_CNT = 48, L_XN = 64, L_HALF = L_XN + 8192, L_Q = L_HALF + 1024, L_MW = L_Q + 1024, L_LW = L_MW + 256,
              L_HS = L_LW + 256, L_GP = L_HS + 256, GP_STRIDE = 272, L_FP = L_GP + 8 * GP_STRIDE, L_MF = L_FP + 1024,
              L_TOUCH = L_MF + 512, L_T2 = L_TOUCH + 256, L_W2 = L_T2 + 256, L_OW = L_W2 + 16384, OW_STRIDE = 2064, L_END = L_OW + 16 * OW_STRIDE;

DI float bflo(u32 v) { return __builtin_bit_cast(float, v << 16); }
DI float bfhi(u32 v) { return __builtin_bit_cast(float, v & 0xffff0000u); }
DI float rbf(float f) { return (float)(__bf16)f; }
DI u32 pk(float a, float b) { return __builtin_bit_cast(u32, __builtin_convertvector((f32x2){a, b}, bf16x2)); }
DI u32 f2u(float f) { return __builtin_bit_cast(u32, f); }
DI float u2f(u32 u) { return __builtin_bit_cast(float, u); }
DI f32x4 mfma(u32x4 a, u32x4 b, f32x4 c) {
  return __builtin_amdgcn_mfma_f32_16x16x32_bf16(__builtin_bit_cast(bf16x8, a), __builtin_bit_cast(bf16x8, b), c, 0, 0, 0);
}
template <int CTRL> DI float dpp(float x) { return u2f(__builtin_amdgcn_mov_dpp(f2u(x), CTRL, 0xf, 0xf, true)); }
constexpr int XOR1 = 0xB1, XOR2 = 0x4E, ROR4 = 0x124, ROR8 = 0x128;
DI float half32(float a, float b) { auto r = __builtin_amdgcn_permlane32_swap(f2u(a), f2u(b), false, false); return u2f(r[0]) + u2f(r[1]); }
DI float half16(float a, float b) { auto r = __builtin_amdgcn_permlane16_swap(f2u(a), f2u(b), false, false); return u2f(r[0]) + u2f(r[1]); }
DI float max32(float a) { auto r = __builtin_amdgcn_permlane32_swap(f2u(a), f2u(a), false, false); return __builtin_amdgcn_fmed3f(u2f(r[0]), u2f(r[1]), __builtin_inff()); }
DI float max16(float a) { auto r = __builtin_amdgcn_permlane16_swap(f2u(a), f2u(a), false, false); return __builtin_amdgcn_fmed3f(u2f(r[0]), u2f(r[1]), __builtin_inff()); }
DI float half_sum(float v) {
  v += dpp<XOR1>(v); v += dpp<XOR2>(v); v += dpp<ROR4>(v); v += dpp<ROR8>(v);
  return half16(v, v);
}
DI u32 plo(u32 k1, u32 k0) { return __builtin_amdgcn_perm(k1, k0, 0x05040100u); }
DI u32 phi(u32 k1, u32 k0) { return __builtin_amdgcn_perm(k1, k0, 0x07060302u); }
DI u32x4 colfrag(const u32x4 (&r)[8], int d) {
  const int q = d >> 1;
  return (d & 1) ? (u32x4){phi(r[1][q], r[0][q]), phi(r[3][q], r[2][q]), phi(r[5][q], r[4][q]), phi(r[7][q], r[6][q])}
                 : (u32x4){plo(r[1][q], r[0][q]), plo(r[3][q], r[2][q]), plo(r[5][q], r[4][q]), plo(r[7][q], r[6][q])};
}
DI u32x4 sel4(bool c, u32x4 v) { return (u32x4){c ? v[0] : 0u, c ? v[1] : 0u, c ? v[2] : 0u, c ? v[3] : 0u}; }
template <typename T> DI T ldnt(const GAS T* p) { return __builtin_nontemporal_load(p); }
#define LAUX 16
#define SAUX 16
DI u32x4 ldws(__amdgpu_buffer_rsrc_t ws, int off) { return __builtin_amdgcn_raw_buffer_load_b128(ws, off, 0, LAUX); }
DI void stws(__amdgpu_buffer_rsrc_t ws, int off, u32x2 a, u32x2 b) { __builtin_amdgcn_raw_buffer_store_b128((u32x4){a[0], a[1], b[0], b[1]}, ws, off, 0, SAUX); }
DI void stws1(__amdgpu_buffer_rsrc_t ws, int off, u32x2 a) { __builtin_amdgcn_raw_buffer_store_b64(a, ws, off, 0, SAUX); }
DI u32x2 pack2(float a, float b, u32 tag8) {
  const u32 ar = f2u(a) + 0x800u;
  const u32 u = (f2u(b) & 0xf000u) | ((ar >> 4) & ~0xf000u);
  return (u32x2){phi(f2u(b), ar), __builtin_amdgcn_perm(tag8, u, 0x07060501u)};
}
DI float first2(u32 w0, u32 w1) { return u2f(__builtin_amdgcn_perm(w0, w1 << 4, 0x0504000cu)); }
DI float second2(u32 w0, u32 w1) { return u2f(__builtin_amdgcn_perm(w0, w1, 0x0706000cu)); }
DI bool tagged(u32x4 v, u32 tag8) { return ((v[1] ^ tag8) | (v[3] ^ tag8)) < 256u; }
#define PIN() do { asm volatile("" ::: "memory"); __builtin_amdgcn_sched_barrier(0); } while (0)
#define LDS_BARRIER() do { asm volatile("s_waitcnt lgkmcnt(0)\n\ts_barrier" ::: "memory"); __builtin_amdgcn_sched_barrier(0); } while (0)
#define RAW_BARRIER() do { __builtin_amdgcn_sched_barrier(0); asm volatile("s_barrier" ::: "memory"); __builtin_amdgcn_sched_barrier(0); } while (0)
DI void dma16(const u16* src, u32 dst) {
  u32 keep;
  asm volatile("s_mov_b32 %0, m0\n\ts_mov_b32 m0, %2\n\ts_nop 0\n\tglobal_load_lds_dwordx4 %1, off nt\n\ts_mov_b32 m0, %0"
               : "=&s"(keep) : "v"(src), "s"(dst) : "memory");
}
DI void touch(const void* src, u32 dst) {
  u32 keep;
  asm volatile("s_mov_b32 %0, m0\n\ts_mov_b32 m0, %2\n\ts_nop 0\n\tglobal_load_lds_dword %1, off\n\ts_mov_b32 m0, %0"
               : "=&s"(keep) : "v"(src), "s"(dst) : "memory");
}
DI void touch_nt(const void* src, u32 dst) {
  u32 keep;
  asm volatile("s_mov_b32 %0, m0\n\ts_mov_b32 m0, %2\n\ts_nop 0\n\tglobal_load_lds_dword %1, off nt\n\ts_mov_b32 m0, %0"
               : "=&s"(keep) : "v"(src), "s"(dst) : "memory");
}
DI void stouch8(const void* p) {
  u32 t;
  asm volatile("s_load_dword %0, %1, 0x0\n\ts_load_dword %0, %1, 0x80\n\ts_load_dword %0, %1, 0x100\n\ts_load_dword %0, %1, 0x180\n\t"
               "s_load_dword %0, %1, 0x200\n\ts_load_dword %0, %1, 0x280\n\ts_load_dword %0, %1, 0x300\n\ts_load_dword %0, %1, 0x380\n\t"
               "s_waitcnt lgkmcnt(0)" : "=&s"(t) : "s"(p) : "memory");
}
DI void stouch2x2(const void* p, const void* q) {
  u32 t;
  asm volatile("s_load_dword %0, %1, 0x0\n\ts_load_dword %0, %1, 0x80\n\ts_load_dword %0, %2, 0x0\n\ts_load_dword %0, %2, 0x80\n\t"
               "s_waitcnt lgkmcnt(0)" : "=&s"(t) : "s"(p), "s"(q) : "memory");
}
DI void set_flag(LAS unsigned char* lds, int f) {
  asm volatile("s_waitcnt lgkmcnt(0)" ::: "memory");
  *(volatile LAS u32*)(lds + L_FLAG + f * 4) = 1u;
}
DI void wait_flags(LAS unsigned char* lds, int first, int mask, int lane) {
#pragma unroll 1
  for (int spin = 0; spin < (SPIN_CAP << 4); ++spin) {
    if (__all(*(volatile LAS u32*)(lds + L_FLAG + (first + (lane & mask)) * 4) != 0u)) break;
    __builtin_amdgcn_s_sleep(1);
  }
  PIN();
}
DI u32 read_tag(const unsigned char* ws) {
  u32 e;
  asm volatile("s_load_dword %0, %1, 0x0\n\ts_waitcnt lgkmcnt(0)" : "=s"(e) : "s"(ws) : "memory");
  return (e + 1u) << 8;
}
template <int LOW, int PNT = 0> DI u32x4 poll16(__amdgpu_buffer_rsrc_t ws, int off, u32 want) {
  u32x4 v;
  u64 keep;
  u32 left = SPIN_CAP, t;
  asm volatile("s_mov_b64 %[keep], exec\n\t"
               "s_branch 2f\n"
               "1:\n\t"
               "s_sub_u32 %[left], %[left], 1\n\t"
               "s_cbranch_scc1 3f\n"
               "2:\n\t"
               ".if %[pnt]\n\tbuffer_load_dwordx4 v[100:103], %[off], %[ws], 0 offen sc1 nt\n\t.else\n\t"
               "buffer_load_dwordx4 v[100:103], %[off], %[ws], 0 offen sc1\n\t.endif\n\t"
               "s_waitcnt vmcnt(0)\n\t"
               "v_bitop3_b32 %[t], v101, v103, %[want] bitop3:0x7e\n\t"
               "v_cmp_lt_u32 vcc, %[low], %[t]\n\t"
               "s_and_b64 exec, exec, vcc\n\t"
               "s_cbranch_scc1 1b\n"
               "3:\n\t"
               "s_mov_b64 exec, %[keep]"
               : "={v[100:103]}"(v), [keep] "=&s"(keep), [left] "+s"(left), [t] "=&v"(t)
               : [off] "v"(off), [ws] "s"(ws), [want] "s"(want), [low] "i"(LOW), [pnt] "i"(PNT)
               : "vcc", "scc", "memory");
  return v;
}
DI void quad_sum4(float& a, float& b, float& c, float& d) {
  asm("s_nop 1\n\t"
      "v_add_f32_dpp %0, %0, %0 quad_perm:[1,0,3,2] row_mask:0xf bank_mask:0xf\n\t"
      "v_add_f32_dpp %1, %1, %1 quad_perm:[1,0,3,2] row_mask:0xf bank_mask:0xf\n\t"
      "v_add_f32_dpp %2, %2, %2 quad_perm:[1,0,3,2] row_mask:0xf bank_mask:0xf\n\t"
      "v_add_f32_dpp %3, %3, %3 quad_perm:[1,0,3,2] row_mask:0xf bank_mask:0xf\n\t"
      "v_add_f32_dpp %0, %0, %0 quad_perm:[2,3,0,1] row_mask:0xf bank_mask:0xf\n\t"
      "v_add_f32_dpp %1, %1, %1 quad_perm:[2,3,0,1] row_mask:0xf bank_mask:0xf\n\t"
      "v_add_f32_dpp %2, %2, %2 quad_perm:[2,3,0,1] row_mask:0xf bank_mask:0xf\n\t"
      "v_add_f32_dpp %3, %3, %3 quad_perm:[2,3,0,1] row_mask:0xf bank_mask:0xf"
      : "+v"(a), "+v"(b), "+v"(c), "+v"(d));
}
DI float sum_records(u32x4 v) {
  const float t = half16(half32(first2(v[0], v[1]), first2(v[2], v[3])), half32(second2(v[0], v[1]), second2(v[2], v[3])));
  return t + dpp<ROR8>(t);
}

DI void layernorm(u32 (&x)[8]) {
  float v[16], s = 0.f;
#pragma unroll
  for (int e = 0; e < 8; ++e) { v[2 * e] = bflo(x[e]); v[2 * e + 1] = bfhi(x[e]); s += v[2 * e] + v[2 * e + 1]; }
  const float mean = half_sum(s) * (1.f / 512.f);
  float q = 0.f;
#pragma unroll
  for (int e = 0; e < 16; ++e) { const float d = v[e] - mean; q = fmaf(d, d, q); }
  const float var = half_sum(q) * (1.f / 512.f);
  const float mb = rbf(mean), inv = __builtin_amdgcn_rcpf(rbf(__builtin_amdgcn_sqrtf(rbf(rbf(var) + 1e-5f))));
#pragma unroll
  for (int e = 0; e < 8; ++e) { const u32 d = pk(v[2 * e] - mb, v[2 * e + 1] - mb); x[e] = pk(bflo(d) * inv, bfhi(d) * inv); }
}

DI f32x2 tile_matvec(const u32x4 (&wv)[8], LAS unsigned char* xin, int g, int c) {
  const u32x4 xf = *(LAS u32x4*)(xin + ((c >> 2) & 1) * 1024 + ((c >> 3) * 32 + g * 8) * 2);
  f32x4 lo = {0.f, 0.f, 0.f, 0.f}, hi = lo;
#pragma unroll
  for (int d = 0; d < 4; ++d) {
    const u32x4 am = sel4((c & 3) == d, xf);
    lo = mfma(am, colfrag(wv, d), lo);
    hi = mfma(am, colfrag(wv, 4 + d), hi);
  }
  const bool valid = (g >> 1) == (c >> 3);
  float t[4];
#pragma unroll
  for (int e = 0; e < 4; ++e) { t[e] = half32(valid ? lo[e] : 0.f, valid ? hi[e] : 0.f); t[e] += dpp<ROR8>(t[e]); }
  return (c & 8) ? (f32x2){t[2], t[3]} : (f32x2){t[0], t[1]};
}

#define ST(i)

extern "C" __global__ void __launch_bounds__(1024, 4)
fused_decode(const u16* x_, const u16* k_, const u16* v_, const u16* wq_, const u16* wo_, const u16* w1_, const u16* b1_,
             const u16* w2_, const u16* b2_, u16* out_, unsigned char* ws_
             ) {
  __shared__ __attribute__((aligned(16))) unsigned char lds_raw[L_END];
  LAS unsigned char* lds = (LAS unsigned char*)lds_raw;
  ST(0);
  const u64 t_start = WO_MODE == 3 ? __builtin_amdgcn_s_memrealtime() : 0;
  asm volatile("" :: "s"(x_), "s"(k_), "s"(v_), "s"(wq_), "s"(wo_), "s"(w1_), "s"(b1_), "s"(w2_), "s"(b2_), "s"(out_), "s"(ws_));
  const int tid = threadIdx.x, lane = tid & 63, w = __builtin_amdgcn_readfirstlane(tid >> 6);
  const int g = lane >> 4, c = lane & 15;
  const int bid = blockIdx.x, kvh = bid & 7, j = bid >> 3, b = j >> 4, sp = j & 15, rb = j >> 2, cb = j & 3;
  const bool mlp1 = j < 14, mlp2 = j >= 14 && j < 28;
  const __amdgpu_buffer_rsrc_t ws = __builtin_amdgcn_make_buffer_rsrc(ws_, 0, WS_END, 0x00020000);
  u32 tag;
  const u32 hsh = ((u32)(bid * 16 + w) * 2654435761u) >> 20, hb = ((u32)bid * 2654435761u) >> 16;
  const int wu = (w + (ROT == 0 ? 0 : ROT == 1 || ROT == 2 || ROT == 3 ? j : (int)hb)) & 15;
  const int ro = ROT == 2 ? kvh : ROT == 3 || ROT == 4 ? (int)(hsh & 7) : 0;
  const size_t kvseq = (size_t)(b * 8 + kvh) * SK * DH;
  const __amdgpu_buffer_rsrc_t kr = __builtin_amdgcn_make_buffer_rsrc((void*)(k_ + kvseq), 0, SK * DH * 2, 0x00020000);
  const __amdgpu_buffer_rsrc_t vr = __builtin_amdgcn_make_buffer_rsrc((void*)(v_ + kvseq), 0, SK * DH * 2, 0x00020000);
  const int kvoff = (sp * 16 + wu) * 8192;
  const int klane = ((((c >> 1) ^ ro) & 1) * 4 + (c >> 2)) * 256 + (g * 2 + (c & 1)) * 16;
  u32x4 kf[8], vf[8];

  const int cq = j & 7, kq = j >> 3;
  const int mt = (mlp1 ? j : j - 14) * 8 + kvh;
  const int mhi = mt >> 2, mlo = mt & 3;
  auto pretouch = [&](int what) __attribute__((always_inline)) {
    const unsigned char* p = ws_ + WS_OP + ((kvh * 2 + b) * 16 + sp) * 2048 + c * 128;
    bool on = lane < 16 && (what & 1);
    if ((lane >> 3) == 2) { p = ws_ + WS_S2 + (cb * 64 + kvh * 8 + rb) * 1024 + (lane & 7) * 128; on = what & 16; }
    if ((lane >> 3) == 3) { p = ws_ + WS_MP + (mlo * 28 + mhi) * 1024 + (lane & 7) * 128; on = mlp2 && (what & 2); }
    if ((lane >> 2) == 8) { p = ws_ + WS_QP + ((((lane >> 1) & 1) * 8 + kvh) * 32 + cq * 4 + kq) * 256 + (lane & 1) * 128; on = what & 4; }
    if ((lane >> 2) == 9) { p = ws_ + WS_PA + mt * 512 + (lane & 3) * 128; on = mlp1 && (what & 4); }
    if (lane == 40) { p = (const unsigned char*)(out_ + ((bid >> 2) & 1) * D + (bid & 3) * 128 + ((bid >> 3) & 3) * 32); on = bid < NFIN && (what & 8); }
    if (lane == 41) { p = ws_; on = bid == 0 && (what & 32); }
    if (TV == 1 || TV == 3) on = on && (lane & 15) == 0;
    if (TV >= 2) { if (on) touch_nt(p, (u32)(size_t)(lds + L_T2)); } else
    if (on) touch(p, (u32)(size_t)(lds + L_T2));
  };
  if (WALK && w == WALKW) {
    const unsigned char* p;
    const int q = j & 3, xo = kvh * 128;
    switch (j >> 2) {
      case 0: p = (const unsigned char*)wq_ + (q << 20) + kvh * 1024; break;
      case 1: p = (const unsigned char*)w1_ + q * 458752 + xo; break;
      case 2: p = (const unsigned char*)w2_ + q * 458752 + xo; break;
      case 3: p = (const unsigned char*)k_ + ((size_t)((q >> 1) * 8 + kvh) << 21) + ((q & 1) << 20); break;
      case 4: p = (const unsigned char*)v_ + ((size_t)((q >> 1) * 8 + kvh) << 21) + ((q & 1) << 20); break;
      case 5: p = q == 0 ? (const unsigned char*)x_ + xo : q == 1 ? (const unsigned char*)b1_ + xo : q == 2 ? (const unsigned char*)b2_ + xo : ws_ + WS_END + xo; break;
      case 6: p = (const unsigned char*)wo_ + kvh * 524288 + q * 131072; break;
      default: p = (q & 1 ? (const unsigned char*)w2_ : (const unsigned char*)w1_) + 229376 + (q >> 1) * 917504 + xo; break;
    }
    if (lane == 0) touch_nt(p, (u32)(size_t)(lds + L_T2));
    PIN();
  }
  if (w < 2 || (w < 4 && mlp1)) {
    __builtin_amdgcn_s_setprio(3);
    if (w == 0 && lane < 16) *(LAS u32*)(lds + L_FLAG + lane * 4) = 0u;
    const GAS u32x4* xp = (const GAS u32x4*)x_ + lane * 2;
    const u32x4 x0 = xp[0], x1 = xp[1];
    PIN();
    ST(14);
    const int rs = w < 2 ? NQ / 8 : FF / 8, row0 = (w < 2 ? kq : mlo) * 128 + (w & 1) * 64;
    const u16* tile = w < 2 ? wq_ + kvh * 512 + cq * 64 : w1_ + mhi * 64;
    const GAS u32x4* wp = (const GAS u32x4*)tile + (row0 + (c >> 3) * 32 + g * 8) * rs + (c & 7);
    u32x4 wv[8];
#pragma unroll
    for (int i = 0; i < 8; ++i) wv[i] = ldnt(wp + i * rs);
    PIN();
    LDS_BARRIER();
    ST(12);
    tag = read_tag(ws_);
    PIN();
    u32 xs[8] = {x0[0], x0[1], x0[2], x0[3], x1[0], x1[1], x1[2], x1[3]};
    layernorm(xs);
    if (w >= 2) layernorm(xs);
    LAS unsigned char* xl = lds + L_XN + w * 2048;
    *(LAS u32x4*)(xl + lane * 32) = (u32x4){xs[0], xs[1], xs[2], xs[3]};
    *(LAS u32x4*)(xl + lane * 32 + 16) = (u32x4){xs[4], xs[5], xs[6], xs[7]};
    f32x2 r = tile_matvec(wv, xl + row0 * 2, g, c);
    LAS f32x2* half = (LAS f32x2*)(lds + L_HALF + (w >> 1) * 512 + lane * 8);
    if (w & 1) {
      *half = r;
      set_flag(lds, F_HALF + (w >> 1));
    } else {
      wait_flags(lds, F_HALF + (w >> 1), 0, lane);
      r += *half;
      const int rec = w == 0 ? WS_QP + ((((g & 1) * 8 + kvh) * 8 + cq) * 4 + kq) * 256 : WS_PA + mt * 512 + (g & 1) * 256;
      stws1(ws, rec + ((c & 7) * 4 + (g >> 1) * 2 + (c >> 3)) * 8, pack2(r[0], r[1], tag));
    }
    ST(2);
    __builtin_amdgcn_s_setprio(PRIO_K);
  } else {
    if (w < 4 && mlp2) {
      const u16* p = w2_ + (mhi * 64 + (w & 1) * 32 + g * 8) * D + mlo * 128 + c * 8;
#pragma unroll
      for (int i = 0; i < 8; ++i) dma16(p + i * D, (u32)(size_t)(lds + L_W2 + (w & 1) * 8192 + i * 1024));
    }
    if (CODE_LINES && w == 4) {
      const unsigned char* pc;
      asm volatile("s_getpc_b64 %0" : "=s"(pc));
      if (lane < 4 && j * 4 + lane < CODE_LINES) touch(pc + (j * 4 + lane) * 128, (u32)(size_t)(lds + L_TOUCH));
    }
    if (PRE_A && w == PREW) pretouch(PRE_A);
    if (SPRE & 16) RAW_BARRIER();
    if ((SPRE & 1) && (w >> 1) == 6) stouch8(ws_ + WS_OP + ((kvh * 2 + b) * 16 + sp) * 2048 + (w & 1) * 1024);
    if ((SPRE & 1) && w == 14) stouch8(ws_ + WS_S2 + (cb * 64 + kvh * 8 + rb) * 1024);
    if ((SPRE & 2) && w == 15 && mlp2) stouch8(ws_ + WS_MP + (mlo * 28 + mhi) * 1024);
    if ((SPRE & 4) && w == 11) { const unsigned char* q = ws_ + WS_QP + (kvh * 32 + cq * 4 + kq) * 256; stouch2x2(q, q + 65536); }
    if ((SPRE & 4) && w == 10 && mlp1) { const unsigned char* q = ws_ + WS_PA + mt * 512; stouch2x2(q, q + 256); }
    if ((SPRE & 8) && w == 9 && bid < NFIN) { const u16* q = out_ + ((bid >> 2) & 1) * D + (bid & 3) * 128; stouch2x2(q, q); }
    if (SCODE && w == SCODEW) {
      u32 t;
      const u32 off = min(j * 512, SCODE - 512);
      __builtin_amdgcn_sched_barrier(0);
      asm volatile("s_getpc_b64 s[92:93]\n"
                   "1:\n\t"
                   "s_sub_u32 s92, s92, 1b - fused_decode\n\t"
                   "s_subb_u32 s93, s93, 0\n\t"
                   "s_add_u32 s92, s92, %1\n\t"
                   "s_addc_u32 s93, s93, 0\n\t"
                   "s_load_dword %0, s[92:93], 0x0\n\ts_load_dword %0, s[92:93], 0x40\n\ts_load_dword %0, s[92:93], 0x80\n\ts_load_dword %0, s[92:93], 0xc0\n\t"
                   "s_load_dword %0, s[92:93], 0x100\n\ts_load_dword %0, s[92:93], 0x140\n\ts_load_dword %0, s[92:93], 0x180\n\ts_load_dword %0, s[92:93], 0x1c0\n\t"
                   "s_barrier\n\t"
                   "s_waitcnt lgkmcnt(0)"
                   : "=&s"(t) : "s"(off) : "s92", "s93", "scc", "memory");
      __builtin_amdgcn_sched_barrier(0);
    } else
    if (!(SPRE & 16)) RAW_BARRIER();
    if (PRE_B && w == PREW + 1) pretouch(PRE_B);
    if ((w >> 2) == 1) __builtin_amdgcn_s_setprio(2); else __builtin_amdgcn_s_setprio(PRIO_K);
  }
  PIN();

  const int slot = SLOTS == 1 ? SLOT0 : SLOTS == 2 ? SLOT0 + (j & 1) : j & 3;
  if (PRE_C && slot < 2 && w == 9 + slot * 4) pretouch(PRE_C);
#pragma unroll
  for (int i = 0; i < 8; ++i) { kf[i] = BUF ? __builtin_amdgcn_raw_buffer_load_b128(kr, klane + (i & 1) * 128, kvoff + ((i >> 1) ^ (ro >> 1)) * 2048, NT) : ldnt((const GAS u32x4*)((const GAS char*)(k_ + kvseq) + kvoff + klane + (i & 1) * 128 + (i >> 1) * 2048)); __builtin_amdgcn_sched_barrier(0); if (i == 0) ST(13); }
  if ((w >> 2) != 1) __builtin_amdgcn_s_setprio(0);
  if (PRE_C && slot >= 2 && w == 1 + (slot - 2) * 8) pretouch(PRE_C);
  PIN();
#pragma unroll
  for (int i = 0; i < 8; ++i) { vf[i] = BUF ? __builtin_amdgcn_raw_buffer_load_b128(vr, lane * 16, kvoff + (i ^ ro) * 1024, NT) : ldnt((const GAS u32x4*)((const GAS char*)(v_ + kvseq) + kvoff + lane * 16 + i * 1024)); __builtin_amdgcn_sched_barrier(0); }
  PIN();
  if ((PRE_D || WO_V) && lane == 0) __hip_atomic_fetch_add((LAS u32*)(lds + L_CNT + 4), 1u, __ATOMIC_RELAXED, __HIP_MEMORY_SCOPE_WORKGROUP);
  if (PRE_E && w == 13) pretouch(PRE_E);
  ST(4);
  tag = read_tag(ws_);
  const int paoff = WS_PA + mhi * 2048 + lane * 16;
  u32x4 p0, p1;
  u32 b1v;
  if ((w >> 2) == 1) {
    const int u = w & 3;
    const int qoff = WS_QP + (b * 8 + kvh) * 8192 + u * 2048 + lane * 16;
    asm volatile("s_waitcnt vmcnt(0)" ::: "memory");
    if (Q_DELAY) __builtin_amdgcn_s_sleep(Q_DELAY);
    u32x4 q0, q1;
#pragma unroll 1
    for (int spin = 0; spin < SPIN_CAP; ++spin) {
      q0 = ldws(ws, qoff); q1 = ldws(ws, qoff + 1024);
      if (__all(tagged(q0, tag) && tagged(q1, tag))) break;
      __builtin_amdgcn_s_sleep(SLEEP_Q);
      asm volatile("" ::: "memory");
    }
    const float qa = half16(half32(first2(q0[0], q0[1]), first2(q1[0], q1[1])), half32(first2(q0[2], q0[3]), first2(q1[2], q1[3])));
    const float qb = half16(half32(second2(q0[0], q0[1]), second2(q1[0], q1[1])), half32(second2(q0[2], q0[3]), second2(q1[2], q1[3])));
    *(LAS u32*)(lds + L_Q + u * 256 + ((g >> 1) * 32 + c * 2 + (g & 1)) * 4) = pk(qa, qb);
    set_flag(lds, F_Q + u);
    ST(3);
    if (w == 7 && mlp2) {
      p0 = ldws(ws, paoff); p1 = ldws(ws, paoff + 1024);
      b1v = B1NT ? ldnt((const GAS u32*)(b1_ + mhi * 64 + c * 4 + (g >> 1) * 2)) : *(const GAS u32*)(b1_ + mhi * 64 + c * 4 + (g >> 1) * 2);
      PIN();
    }
    __builtin_amdgcn_s_setprio(0);
  }
  wait_flags(lds, F_Q, 3, lane);

  float sc[8];
  {
    const int x0 = (c >> 2) & 1;
    const u32x4 q0 = *(LAS u32x4*)(lds + L_Q + (c & 3) * 256 + (g * 2 + x0) * 16);
    const u32x4 q1 = *(LAS u32x4*)(lds + L_Q + (c & 3) * 256 + 128 + (g * 2 + x0) * 16);
#pragma unroll
    for (int o = 0; o < 4; ++o) {
      f32x4 s = mfma(kf[2 * o], q0, (f32x4){0.f, 0.f, 0.f, 0.f});
      s = mfma(kf[2 * o + 1], q1, s);
#pragma unroll
      for (int a = 0; a < 2; ++a) {
        float e1 = s[2 * a + 1];
        asm("" : "+v"(e1));
        const float t = x0 ? e1 : s[2 * a];
        sc[2 * o + a] = t + dpp<ROR4>(t);
      }
      const u32 d = pk(sc[2 * o], sc[2 * o + 1]), e = pk(bflo(d) * 0.08838834764831845f, bfhi(d) * 0.08838834764831845f);
      sc[2 * o] = bflo(e); sc[2 * o + 1] = bfhi(e);
    }
  }
  PIN();

    float m = fmaxf(fmaxf(fmaxf(sc[0], sc[1]), fmaxf(sc[2], sc[3])), fmaxf(fmaxf(sc[4], sc[5]), fmaxf(sc[6], sc[7])));
    m = max32(max16(m)) * LOG2E;
    float p[8], l = 0.f;
#pragma unroll
    for (int e = 0; e < 8; ++e) { p[e] = __builtin_amdgcn_exp2f(fmaf(sc[e], LOG2E, -m)); l += p[e]; }
    l = half16(l, l); l = half32(l, l);
    if (lane < 4) { *(LAS float*)(lds + L_MW + (lane * 16 + w) * 4) = m; *(LAS float*)(lds + L_LW + (lane * 16 + w) * 4) = l; }
    PIN();
    if ((w >> 1) == 1) set_flag(lds, F_W2 + (w & 1));
    if (lane == 0) __hip_atomic_fetch_add((LAS u32*)(lds + L_CNT), 1u, __ATOMIC_RELAXED, __HIP_MEMORY_SCOPE_WORKGROUP);
    const u32x4 pf = {pk(p[0], p[1]), pk(p[2], p[3]), pk(p[4], p[5]), pk(p[6], p[7])};
    u32x4 am[4];
#pragma unroll
    for (int u = 0; u < 4; ++u) am[u] = sel4((c >> 2) == u, pf);
    PIN();
    u32x4 ve[4], vo[4];
    u32 slo, shi;
    asm volatile("s_mov_b32 %0, 0x05040100\n\ts_mov_b32 %1, 0x07060302" : "=s"(slo), "=s"(shi) : "v"(am[0][0]), "v"(am[1][1]), "v"(am[2][2]), "v"(am[3][3]));
#pragma unroll
    for (int r = 0; r < 4; ++r) {
#pragma unroll
      for (int u = 0; u < 4; ++u) {
        ve[u][r] = __builtin_amdgcn_perm(vf[2 * r + 1][u], vf[2 * r][u], slo);
        vo[u][r] = __builtin_amdgcn_perm(vf[2 * r + 1][u], vf[2 * r][u], shi);
      }
      if (r < 3)
        asm volatile("" : "+s"(slo), "+s"(shi) : "v"(ve[0][r]), "v"(ve[1][r]), "v"(ve[2][r]), "v"(ve[3][r]), "v"(vo[0][r]), "v"(vo[1][r]), "v"(vo[2][r]), "v"(vo[3][r]));
    }
    f32x4 oe = {0.f, 0.f, 0.f, 0.f}, oo = oe;
#pragma unroll
    for (int u = 0; u < 4; ++u) { oe = mfma(am[u], ve[u], oe); oo = mfma(am[u], vo[u], oo); }
#pragma unroll
    for (int h = 0; h < 4; ++h) *(LAS f32x2*)(lds + L_OW + w * OW_STRIDE + h * 512 + c * 32 + g * 8) = (f32x2){oe[h], oo[h]};
    PIN();
    ST(5);

  const bool gath = (w & 4) == 0;
  const int v = (w & 3) + (w >> 3) * 4;
  auto wait_ml = [&]() __attribute__((always_inline)) {
#pragma unroll 1
    for (int spin = 0; spin < (SPIN_CAP << 4); ++spin) {
      if (__builtin_amdgcn_readfirstlane(*(volatile LAS u32*)(lds + L_CNT)) == 16u) break;
      __builtin_amdgcn_s_sleep(1);
    }
    PIN();
  };
  auto load_wo = [&](u32x4 (&wt)[8]) __attribute__((always_inline)) {
    const GAS u16* p = (const GAS u16*)wo_ + (size_t)(kvh * 512 + rb * 64 + g * 4) * D + cb * 128 + (w - 4) * 64 + c * 4;
#pragma unroll
    for (int i = 0; i < 8; ++i) {
      const u32x2 t0 = ldnt((const GAS u32x2*)(p + (size_t)(32 * (i >> 2) + (i & 3)) * D));
      const u32x2 t1 = ldnt((const GAS u32x2*)(p + (size_t)(32 * (i >> 2) + 16 + (i & 3)) * D));
      wt[i] = (u32x4){t0[0], t0[1], t1[0], t1[1]};
    }
    PIN();
  };
  auto wait_v = [&](u32 n) __attribute__((always_inline)) {
#pragma unroll 1
    for (int spin = 0; spin < (SPIN_CAP << 4); ++spin) {
      if (__builtin_amdgcn_readfirstlane(*(volatile LAS u32*)(lds + L_CNT + 4)) >= n) break;
      __builtin_amdgcn_s_sleep(1);
    }
    PIN();
  };
  if (PRE_D && w == 6) { wait_v(PRE_AT); pretouch(PRE_D); }
  const bool fin = FINLAST ? bid >= 256 - NFIN : bid < NFIN;
  const int cf = bid & 3, bf = (bid >> 2) & 1, seg = (bid >> 3) & 3;
  if (w >= 12) {
    LDS_BARRIER();
    ST(6);
    if (fin) {
      const int row = (w - 12) * 8 + (lane >> 3);
      const int moff = WS_MP + (cf * 28 + min(row, 27)) * 1024 + bf * 512 + seg * 128 + (lane & 7) * 16;
      u32x4 mv;
#pragma unroll 1
      for (int spin = 0; spin < SPIN_CAP; ++spin) {
        mv = ldws(ws, moff);
        if (__all(tagged(mv, tag))) break;
        __builtin_amdgcn_s_sleep(SLEEP_M);
        asm volatile("" ::: "memory");
      }
      const float t = sum_records(sel4(row < 28, mv));
      if ((lane & 8) == 0) *(LAS float*)(lds + L_MF + (w - 12) * 128 + ((lane & 7) * 4 + g) * 4) = t;
      LDS_BARRIER();
    }
  } else if (gath) {
    {
      const int f = v * 16 + (lane >> 2), p4 = lane & 3, hd = v >> 1;
      wait_ml();
      const f32x4 mv = *(LAS f32x4*)(lds + L_MW + hd * 64 + p4 * 16), lv = *(LAS f32x4*)(lds + L_LW + hd * 64 + p4 * 16);
      float M = fmaxf(fmaxf(mv[0], mv[1]), fmaxf(mv[2], mv[3]));
      M = fmaxf(M, dpp<XOR1>(M)); M = fmaxf(M, dpp<XOR2>(M));
      float L = 0.f, fw[4];
#pragma unroll
      for (int i = 0; i < 4; ++i) { fw[i] = __builtin_amdgcn_exp2f(mv[i] - M); L = fmaf(fw[i], lv[i], L); }
      L += dpp<XOR1>(L); L += dpp<XOR2>(L);
      const float inv = __builtin_amdgcn_rcpf(L);
#pragma unroll
      for (int i = 0; i < 4; ++i) fw[i] *= inv;
      const u32 lam = f2u(M + __builtin_amdgcn_logf(L)) >> ((lane >> 5) * 16);
      u32 t0 = tag | (lam & 255u), t1 = tag | ((lam >> 8) & 255u);
      int ooff = WS_OP + ((kvh * 2 + b) * 16 + sp) * 2048 + f * 16;
      int laddr = L_OW + p4 * 4 * OW_STRIDE + f * 16;
      asm volatile("" : "+v"(fw[0]), "+v"(fw[1]), "+v"(fw[2]), "+v"(fw[3]), "+v"(t0), "+v"(t1), "+v"(ooff), "+v"(laddr));
      ST(3);
      LDS_BARRIER();
      ST(6);
      f32x4 s = fw[0] * *(LAS f32x4*)(lds + laddr);
#pragma unroll
      for (int i = 1; i < 4; ++i) s += fw[i] * *(LAS f32x4*)(lds + laddr + i * OW_STRIDE);
      float s0 = s[0], s1 = s[1], s2 = s[2], s3 = s[3];
      quad_sum4(s0, s1, s2, s3);
      if (p4 == 0) __builtin_amdgcn_raw_buffer_store_b128((u32x4){pk(s0, s1), t0, pk(s2, s3), t1}, ws, ooff, 0, OSAUX);
    }
    PIN();
    ST(7);
    {
      u32 gaddr2 = (u32)(size_t)(lds + L_GP + v * GP_STRIDE + 256), gaddr = gaddr2 - 256 + (c * 4 + g) * 4;
      u32 lsel = (c & 8) ? 0x05040100u : 0x01000504u;
      asm volatile("" : "+v"(gaddr), "+v"(gaddr2), "+v"(lsel));
      if (SLEEP_O) __builtin_amdgcn_s_sleep(SLEEP_O);
      const u32x4 ov = poll16<255, OPNT>(ws, WS_OP + (kvh * 32 + v * 4 + g) * 2048 + rb * 256 + c * 16, tag);
      ST(8);
      u32 a, lam, m1, x0, x1, x2, x3;
      f32x2 gl;
      asm volatile("v_perm_b32 %[a], %[t1], %[t0], %[sel]\n\t"
                   "v_lshlrev_b32 %[x0], 16, %[d0]\n\t"
                   "v_lshlrev_b32 %[x2], 16, %[d1]\n\t"
                   "v_mov_b32_dpp %[m1], %[a] row_ror:8 row_mask:0xf bank_mask:0xf\n\t"
                   "v_and_b32 %[x1], 0xffff0000, %[d0]\n\t"
                   "v_perm_b32 %[lam], %[a], %[m1], %[lsel]\n\t"
                   "v_mov_b32 %[m1], %[lam]\n\t"
                   "v_mov_b32 %[a], %[lam]\n\t"
                   "v_and_b32 %[x3], 0xffff0000, %[d1]\n\t"
                   "s_nop 0\n\t"
                   "v_permlane16_swap_b32 %[m1], %[a]\n\t"
                   "v_max_f32 %[m1], %[m1], %[a]\n\t"
                   "v_mov_b32 %[a], %[m1]\n\t"
                   "s_nop 1\n\t"
                   "v_permlane32_swap_b32 %[m1], %[a]\n\t"
                   "v_max_f32 v104, %[m1], %[a]\n\t"
                   "v_sub_f32 %[lam], %[lam], v104\n\t"
                   "v_exp_f32 %[lam], %[lam]\n\t"
                   "s_nop 0\n\t"
                   "v_mul_f32 %[x0], %[lam], %[x0]\n\t"
                   "v_mul_f32 %[x2], %[lam], %[x2]\n\t"
                   "v_mul_f32 %[x1], %[lam], %[x1]\n\t"
                   "v_mul_f32 %[x3], %[lam], %[x3]\n\t"
                   "v_mov_b32 %[a], %[lam]\n\t"
                   "v_permlane32_swap_b32 %[x0], %[x2]\n\t"
                   "v_permlane32_swap_b32 %[x1], %[x3]\n\t"
                   "v_permlane16_swap_b32 %[lam], %[a]\n\t"
                   "v_add_f32 %[x0], %[x0], %[x2]\n\t"
                   "v_add_f32 %[x1], %[x1], %[x3]\n\t"
                   "v_add_f32 %[lam], %[lam], %[a]\n\t"
                   "v_mov_b32 %[a], %[lam]\n\t"
                   "v_permlane16_swap_b32 %[x0], %[x1]\n\t"
                   "v_add_f32 %[x0], %[x0], %[x1]\n\t"
                   "v_permlane32_swap_b32 %[lam], %[a]\n\t"
                   "ds_write_b32 %[ga], %[x0]\n\t"
                   "v_add_f32 v105, %[lam], %[a]\n\t"
                   "ds_write_b64 %[gb], v[104:105]"
                   : [a] "=&v"(a), [lam] "=&v"(lam), [m1] "=&v"(m1), [x0] "=&v"(x0), [x1] "=&v"(x1), [x2] "=&v"(x2), [x3] "=&v"(x3), "=&{v[104:105]}"(gl)
                   : [d0] "v"(ov[0]), [t0] "v"(ov[1]), [d1] "v"(ov[2]), [t1] "v"(ov[3]), [sel] "s"(0x04000400u), [lsel] "v"(lsel), [ga] "v"(gaddr), [gb] "v"(gaddr2)
                   : "memory");
    }
    LDS_BARRIER();
    ST(9);
    if (fin) {
      int faddr = L_FP + ((lane & 7) * 4 + g) * 32 + v * 4;
      asm volatile("" : "+v"(faddr));
      if (SLEEP_S) __builtin_amdgcn_s_sleep(SLEEP_S);
      const u32x4 sv = poll16<255>(ws, WS_S2 + (cf * 64 + v * 8 + (lane >> 3)) * 1024 + bf * 512 + seg * 128 + (lane & 7) * 16, tag);
      ST(10);
      *(LAS float*)(lds + faddr) = sum_records(sv);
      LDS_BARRIER();
    }
  } else if (w < 6) {
    u32x4 wt[8], bw[8];
    if (WO_MODE == 3) {
      wait_ml();
      const bool now = __builtin_amdgcn_s_memrealtime() >= t_start + WO_AT;
      if (!now) {
        LDS_BARRIER();
        ST(6);
        while (__builtin_amdgcn_s_memrealtime() < t_start + WO_AT) __builtin_amdgcn_s_sleep(1);
      }
      load_wo(wt);
      if (now) { LDS_BARRIER(); ST(6); }
    } else {
    if (WO_MODE == 1) { if (WO_V) wait_v(16u); else wait_ml(); }
    if (WO_MODE < 2) load_wo(wt);
    LDS_BARRIER();
    ST(6);
    if (WO_MODE == 2) { if (WO_SLEEP) __builtin_amdgcn_s_sleep(WO_SLEEP); load_wo(wt); }
    }
#pragma unroll
    for (int i = 0; i < 8; ++i) bw[i] = colfrag(wt, i);
    int gp = L_GP + (c & 7) * GP_STRIDE, go = gp + ((c >> 3) * 16 + g * 4) * 4;
    int soff = WS_S2 + (cb * 64 + kvh * 8 + rb) * 1024 + (g & 1) * 512 + ((w - 4) * 32 + c * 2 + (g >> 1)) * 8;
    asm volatile("" : "+v"(gp), "+v"(go), "+v"(soff) : "v"(bw[0]), "v"(bw[1]), "v"(bw[2]), "v"(bw[3]), "v"(bw[4]), "v"(bw[5]), "v"(bw[6]), "v"(bw[7]));
    ST(1);
    LDS_BARRIER();
    ST(9);
    f32x2 gl = *(LAS f32x2*)(lds + gp + 256);
    u32x4 of = *(LAS u32x4*)(lds + go), o1 = *(LAS u32x4*)(lds + go + 128);
#define QP1(r) "v_add_f32_dpp " r ", " r ", " r " quad_perm:[1,0,3,2] row_mask:0xf bank_mask:0xf\n\t"
#define QP2(r) "v_add_f32_dpp " r ", " r ", " r " quad_perm:[2,3,0,1] row_mask:0xf bank_mask:0xf\n\t"
#define PKM(r) "v_pk_mul_f32 " r ", v[118:119], " r " op_sel_hi:[0,1]\n\t"
    asm volatile("v_max_f32_dpp v118, v116, v116 quad_perm:[1,0,3,2] row_mask:0xf bank_mask:0xf\n\t"
                 "s_nop 1\n\t"
                 "v_max_f32_dpp v118, v118, v118 quad_perm:[2,3,0,1] row_mask:0xf bank_mask:0xf\n\t"
                 "v_sub_f32 v118, v116, v118\n\t"
                 "v_exp_f32 v118, v118\n\t"
                 "s_nop 0\n\t"
                 "v_mul_f32 v117, v118, v117\n\t"
                 PKM("v[108:109]") PKM("v[110:111]") PKM("v[112:113]") PKM("v[114:115]")
                 QP1("v117") QP1("v108") QP1("v109") QP1("v110") QP1("v111") QP1("v112") QP1("v113") QP1("v114") QP1("v115")
                 QP2("v117") QP2("v108") QP2("v109")
                 "v_rcp_f32 v118, v117\n\t"
                 QP2("v110") QP2("v111") QP2("v112") QP2("v113") QP2("v114") QP2("v115")
                 PKM("v[108:109]") PKM("v[110:111]") PKM("v[112:113]") PKM("v[114:115]")
                 "v_cvt_pk_bf16_f32 v108, v108, v109\n\t"
                 "v_cvt_pk_bf16_f32 v109, v110, v111\n\t"
                 "v_cvt_pk_bf16_f32 v110, v112, v113\n\t"
                 "v_cvt_pk_bf16_f32 v111, v114, v115\n\t"
                 "s_nop 1"
                 : "+{v[108:111]}"(of), "+{v[112:115]}"(o1), "+{v[116:117]}"(gl)
                 :
                 : "v118", "v119");
    f32x4 acc[8];
#pragma unroll
    for (int i = 0; i < 8; ++i) { const int k = (i & 3) * 2 + (i >> 2); acc[k] = mfma(of, bw[k], (f32x4){0.f, 0.f, 0.f, 0.f}); }
    __builtin_amdgcn_sched_barrier(0);
    float r[4];
#pragma unroll
    for (int d = 0; d < 4; ++d) r[d] = (g >> 1) ? acc[4 + d][0] : acc[d][0];
    stws1(ws, soff, pack2(half32(r[0], r[2]), half32(r[1], r[3]), tag));
    ST(10);
  } else if (w == 7 && mlp2) {
#pragma unroll 1
    for (int spin = 0; spin < SPIN_CAP; ++spin) {
      if (__all(tagged(p0, tag) && tagged(p1, tag))) break;
      __builtin_amdgcn_s_sleep(SLEEP_Q);
      asm volatile("" ::: "memory");
      p0 = ldws(ws, paoff); p1 = ldws(ws, paoff + 1024);
    }
    const float t2[2] = {half32(first2(p0[0], p0[1]) + first2(p1[0], p1[1]), first2(p0[2], p0[3]) + first2(p1[2], p1[3])),
                         half32(second2(p0[0], p0[1]) + second2(p1[0], p1[1]), second2(p0[2], p0[3]) + second2(p1[2], p1[3]))};
    float hv[2];
#pragma unroll
    for (int e = 0; e < 2; ++e) {
      const float t = rbf(rbf(t2[e]) + (e ? bfhi(b1v) : bflo(b1v)));
      const float uu = 0.7978845608028654f * (t + 0.044715f * t * t * t);
      hv[e] = t / (1.f + __builtin_amdgcn_exp2f(-2.f * LOG2E * uu));
    }
    *(LAS u32*)(lds + L_HS + (g & 1) * 128 + (c * 2 + (g >> 1)) * 4) = pk(hv[0], hv[1]);
    wait_flags(lds, F_W2, 1, lane);
    f32x4 r = {0.f, 0.f, 0.f, 0.f};
#pragma unroll
    for (int s = 0; s < 2; ++s) {
      const u32x4 ha = *(LAS u32x4*)(lds + L_HS + ((c >> 2) & 1) * 128 + s * 64 + g * 16);
      u32x4 w2t[8];
#pragma unroll
      for (int i = 0; i < 8; ++i) w2t[i] = *(LAS u32x4*)(lds + L_W2 + s * 8192 + i * 1024 + lane * 16);
#pragma unroll
      for (int d = 0; d < 8; ++d) r = mfma(sel4((c >> 3) == (d >> 2) && (c & 3) == (d & 3), ha), colfrag(w2t, d), r);
    }
    u32x2 m0 = pack2(r[0], r[1], tag), m1 = pack2(r[2], r[3], tag);
    asm volatile("" : "+v"(m0), "+v"(m1));
    ST(2);
    if (MP_LATE) LDS_BARRIER();
    stws(ws, WS_MP + (mlo * 28 + mhi) * 1024 + (g & 1) * 512 + (c * 2 + (g >> 1)) * 16, m0, m1);
    if (!MP_LATE) LDS_BARRIER();
    ST(6);
  } else if (w == 6 && fin) {
    const int col = cf * 128 + seg * 32 + (lane & 31);
    const u32 xv = ((const GAS u16*)x_)[bf * D + col], b2v = ((const GAS u16*)b2_)[col];
    LDS_BARRIER();
    ST(6);
    LDS_BARRIER();
    LAS float* mf = (LAS float*)(lds + L_MF + (lane >> 5) * 256 + (lane & 31) * 4);
    float t = mf[0] + mf[32];
    t = half32(t, t);
    t = rbf(rbf(t) + bflo(b2v));
    float gate = rbf(t / (1.f + __builtin_amdgcn_exp2f(-LOG2E * t))), xf = bflo(xv);
    int faddr = L_FP + (lane & 31) * 32 + (lane >> 5) * 16;
    GAS u16* op = (GAS u16*)out_ + bf * D + col;
    asm volatile("" : "+v"(gate), "+v"(xf), "+v"(faddr), "+v"(op));
    LDS_BARRIER();
    ST(10);
    const f32x4 fp = *(LAS f32x4*)(lds + faddr);
    float s = (fp[0] + fp[1]) + (fp[2] + fp[3]);
    s = half32(s, s);
    const float res = xf + rbf(rbf(s) * gate);
    if (lane < 32) __hip_atomic_store(op, (u16)pk(res, res), RLX_AGENT);
    if ((bid & 31) == 0 && lane == 0) __hip_atomic_store((GAS u32*)(ws_ + WS_EPOCH), tag >> 8, RLX_AGENT);
  } else {
    LDS_BARRIER();
    ST(6);
  }
  ST(11);
}

extern "C" void kernel_launch(void* const* d_in, const int* in_sizes, int n_in, void* d_out, int out_size, void* d_ws, size_t ws_size,
                              hipStream_t stream) {
  unsigned char* ws = (unsigned char*)d_ws + (WS_AT_END ? ((ws_size - WS_SPAN) & ~(size_t)4095) : 0);
  fused_decode<<<256, 1024, 0, stream>>>((const u16*)d_in[0], (const u16*)d_in[1], (const u16*)d_in[2], (const u16*)d_in[3],
                                         (const u16*)d_in[4], (const u16*)d_in[6], (const u16*)d_in[7], (const u16*)d_in[8],
                                         (const u16*)d_in[9], (u16*)d_out, ws
                                         );
}
```

```cpp
#define STAMP 1
#define PROBE 1
#define PROBE_B 6
#define STMASK (1 | (1 << 1) | (1 << 6))
#define FINLAST 0
#define WO_V 1
#include <hip/hip_runtime.h>
#include <stdint.h>

#define WS_AT_END 0
#define SLEEP_Q 8
#define SLEEP_O 8
#define SLEEP_S 8
#define WO_MODE 1
#define PRIO_K 1
#define ROT 1
#define CODE_LINES 0
#define RND 1
#define BUF 1
#define SPRE 0
#define TV 0
#define PRE_C 0
#define SLOTS 4
#define SLOT0 0
#define OSAUX 16
#define OPNT 0
#define PRE_D 25
#define PRE_E 0
#define B1NT 0
#define PROBE_WM 0xffff
#define WALK 0
#define WALKW 0
#define SCODE 0
#define SCODEW 15
#define NOBUMP 0
#define ENDWAIT 0
#define MSA 16
#define MLA 16
#define TSC " sc1"
#define Q_AT 0
#define YPRIO 0
#define LIN 0
#define PRE_AT 16u
#define PRE_A 0
#define PRE_B 0
#define PREW 12
#define Q_DELAY 32
#define MP_LATE 0
#define SLEEP_M 4
#define WO_AT 1150
#define WO_SLEEP 0
#define ABL 0

#define GAS __attribute__((address_space(1)))
#define LAS __attribute__((address_space(3)))
#define DI __device__ __forceinline__
#define RLX_AGENT __ATOMIC_RELAXED, __HIP_MEMORY_SCOPE_AGENT
typedef unsigned short u16;
typedef unsigned u32;
typedef unsigned long long u64;
typedef u32 u32x2 __attribute__((ext_vector_type(2)));
typedef u32 u32x4 __attribute__((ext_vector_type(4)));
typedef float f32x2 __attribute__((ext_vector_type(2)));
typedef float f32x4 __attribute__((ext_vector_type(4)));
typedef __bf16 bf16x2 __attribute__((ext_vector_type(2)));
typedef __bf16 bf16x8 __attribute__((ext_vector_type(8)));

constexpr int D = 512, SK = 8192, DH = 128, FF = 1792, NQ = 4096;
constexpr int SC1 = 16, NT = 2;
constexpr int SPIN_CAP = 1 << 18;
constexpr int NFIN = 32;
constexpr float LOG2E = 1.4426950408889634f;

constexpr int WS_EPOCH = 0;
constexpr int WS_QP = 256;
constexpr int WS_PA = WS_QP + 512 * 256;
constexpr int WS_OP = WS_PA + 112 * 512;
constexpr int WS_S2 = WS_OP + 256 * 2048;
constexpr int WS_MP = WS_S2 + 256 * 1024;
constexpr int WS_END = WS_MP + 112 * 1024;
constexpr int WS_SPAN = 4 << 20;
constexpr int WS_STAMP = 8 << 20;

constexpr int F_Q = 0, F_HALF = 4, F_W2 = 6;
constexpr int L_FLAG = 0, L_CNT = 48, L_XN = 64, L_HALF = L_XN + 8192, L_Q = L_HALF + 1024, L_MW = L_Q + 1024, L_LW = L_MW + 256,
              L_HS = L_LW + 256, L_GP = L_HS + 256, GP_STRIDE = 272, L_FP = L_GP + 8 * GP_STRIDE, L_MF = L_FP + 1024,
              L_TOUCH = L_MF + 512, L_T2 = L_TOUCH + 256, L_W2 = L_T2 + 256, L_OW = L_W2 + 16384, OW_STRIDE = 2064, L_END = L_OW + 16 * OW_STRIDE;

DI float bflo(u32 v) { return __builtin_bit_cast(float, v << 16); }
DI float bfhi(u32 v) { return __builtin_bit_cast(float, v & 0xffff0000u); }
DI float rbf(float f) { return (float)(__bf16)f; }
DI u32 pk(float a, float b) { return __builtin_bit_cast(u32, __builtin_convertvector((f32x2){a, b}, bf16x2)); }
DI u32 f2u(float f) { return __builtin_bit_cast(u32, f); }
DI float u2f(u32 u) { return __builtin_bit_cast(float, u); }
DI f32x4 mfma(u32x4 a, u32x4 b, f32x4 c) {
  return __builtin_amdgcn_mfma_f32_16x16x32_bf16(__builtin_bit_cast(bf16x8, a), __builtin_bit_cast(bf16x8, b), c, 0, 0, 0);
}
template <int CTRL> DI float dpp(float x) { return u2f(__builtin_amdgcn_mov_dpp(f2u(x), CTRL, 0xf, 0xf, true)); }
constexpr int XOR1 = 0xB1, XOR2 = 0x4E, ROR4 = 0x124, ROR8 = 0x128;
DI float half32(float a, float b) { auto r = __builtin_amdgcn_permlane32_swap(f2u(a), f2u(b), false, false); return u2f(r[0]) + u2f(r[1]); }
DI float half16(float a, float b) { auto r = __builtin_amdgcn_permlane16_swap(f2u(a), f2u(b), false, false); return u2f(r[0]) + u2f(r[1]); }
DI float max32(float a) { auto r = __builtin_amdgcn_permlane32_swap(f2u(a), f2u(a), false, false); return __builtin_amdgcn_fmed3f(u2f(r[0]), u2f(r[1]), __builtin_inff()); }
DI float max16(float a) { auto r = __builtin_amdgcn_permlane16_swap(f2u(a), f2u(a), false, false); return __builtin_amdgcn_fmed3f(u2f(r[0]), u2f(r[1]), __builtin_inff()); }
DI float half_sum(float v) {
  v += dpp<XOR1>(v); v += dpp<XOR2>(v); v += dpp<ROR4>(v); v += dpp<ROR8>(v);
  return half16(v, v);
}
DI u32 plo(u32 k1, u32 k0) { return __builtin_amdgcn_perm(k1, k0, 0x05040100u); }
DI u32 phi(u32 k1, u32 k0) { return __builtin_amdgcn_perm(k1, k0, 0x07060302u); }
DI u32x4 colfrag(const u32x4 (&r)[8], int d) {
  const int q = d >> 1;
  return (d & 1) ? (u32x4){phi(r[1][q], r[0][q]), phi(r[3][q], r[2][q]), phi(r[5][q], r[4][q]), phi(r[7][q], r[6][q])}
                 : (u32x4){plo(r[1][q], r[0][q]), plo(r[3][q], r[2][q]), plo(r[5][q], r[4][q]), plo(r[7][q], r[6][q])};
}
DI u32x4 sel4(bool c, u32x4 v) { return (u32x4){c ? v[0] : 0u, c ? v[1] : 0u, c ? v[2] : 0u, c ? v[3] : 0u}; }
template <typename T> DI T ldnt(const GAS T* p) { return __builtin_nontemporal_load(p); }
#define LAUX 16
#define SAUX 16
DI u32x4 ldws(__amdgpu_buffer_rsrc_t ws, int off) { return __builtin_amdgcn_raw_buffer_load_b128(ws, off, 0, LAUX); }
DI void stws(__amdgpu_buffer_rsrc_t ws, int off, u32x2 a, u32x2 b) { __builtin_amdgcn_raw_buffer_store_b128((u32x4){a[0], a[1], b[0], b[1]}, ws, off, 0, SAUX); }
DI void stws1(__amdgpu_buffer_rsrc_t ws, int off, u32x2 a) { __builtin_amdgcn_raw_buffer_store_b64(a, ws, off, 0, SAUX); }
DI u32x2 pack2(float a, float b, u32 tag8) {
  const u32 ar = f2u(a) + 0x800u;
  const u32 u = (f2u(b) & 0xf000u) | ((ar >> 4) & ~0xf000u);
  return (u32x2){phi(f2u(b), ar), __builtin_amdgcn_perm(tag8, u, 0x07060501u)};
}
DI float first2(u32 w0, u32 w1) { return u2f(__builtin_amdgcn_perm(w0, w1 << 4, 0x0504000cu)); }
DI float second2(u32 w0, u32 w1) { return u2f(__builtin_amdgcn_perm(w0, w1, 0x0706000cu)); }
DI bool tagged(u32x4 v, u32 tag8) { return ((v[1] ^ tag8) | (v[3] ^ tag8)) < 256u; }
#define PIN() do { asm volatile("" ::: "memory"); __builtin_amdgcn_sched_barrier(0); } while (0)
#define LDS_BARRIER() do { asm volatile("s_waitcnt lgkmcnt(0)\n\ts_barrier" ::: "memory"); __builtin_amdgcn_sched_barrier(0); } while (0)
#define RAW_BARRIER() do { __builtin_amdgcn_sched_barrier(0); asm volatile("s_barrier" ::: "memory"); __builtin_amdgcn_sched_barrier(0); } while (0)
DI void dma16(const u16* src, u32 dst) {
  u32 keep;
  asm volatile("s_mov_b32 %0, m0\n\ts_mov_b32 m0, %2\n\ts_nop 0\n\tglobal_load_lds_dwordx4 %1, off nt\n\ts_mov_b32 m0, %0"
               : "=&s"(keep) : "v"(src), "s"(dst) : "memory");
}
DI void touch(const void* src, u32 dst) {
  u32 keep;
  asm volatile("s_mov_b32 %0, m0\n\ts_mov_b32 m0, %2\n\ts_nop 0\n\tglobal_load_lds_dword %1, off" TSC "\n\ts_mov_b32 m0, %0"
               : "=&s"(keep) : "v"(src), "s"(dst) : "memory");
}
DI void touch_nt(const void* src, u32 dst) {
  u32 keep;
  asm volatile("s_mov_b32 %0, m0\n\ts_mov_b32 m0, %2\n\ts_nop 0\n\tglobal_load_lds_dword %1, off nt\n\ts_mov_b32 m0, %0"
               : "=&s"(keep) : "v"(src), "s"(dst) : "memory");
}
DI void stouch8(const void* p) {
  u32 t;
  asm volatile("s_load_dword %0, %1, 0x0\n\ts_load_dword %0, %1, 0x80\n\ts_load_dword %0, %1, 0x100\n\ts_load_dword %0, %1, 0x180\n\t"
               "s_load_dword %0, %1, 0x200\n\ts_load_dword %0, %1, 0x280\n\ts_load_dword %0, %1, 0x300\n\ts_load_dword %0, %1, 0x380\n\t"
               "s_waitcnt lgkmcnt(0)" : "=&s"(t) : "s"(p) : "memory");
}
DI void stouch2x2(const void* p, const void* q) {
  u32 t;
  asm volatile("s_load_dword %0, %1, 0x0\n\ts_load_dword %0, %1, 0x80\n\ts_load_dword %0, %2, 0x0\n\ts_load_dword %0, %2, 0x80\n\t"
               "s_waitcnt lgkmcnt(0)" : "=&s"(t) : "s"(p), "s"(q) : "memory");
}
DI void set_flag(LAS unsigned char* lds, int f) {
  asm volatile("s_waitcnt lgkmcnt(0)" ::: "memory");
  *(volatile LAS u32*)(lds + L_FLAG + f * 4) = 1u;
}
DI void wait_flags(LAS unsigned char* lds, int first, int mask, int lane) {
#pragma unroll 1
  for (int spin = 0; spin < (SPIN_CAP << 4); ++spin) {
    if (__all(*(volatile LAS u32*)(lds + L_FLAG + (first + (lane & mask)) * 4) != 0u)) break;
    __builtin_amdgcn_s_sleep(1);
  }
  PIN();
}
DI u32 read_tag(const unsigned char* ws) {
  u32 e;
  asm volatile("s_load_dword %0, %1, 0x0\n\ts_waitcnt lgkmcnt(0)" : "=s"(e) : "s"(ws) : "memory");
  return (e + 1u) << 8;
}
template <int LOW, int PNT = 0> DI u32x4 poll16(__amdgpu_buffer_rsrc_t ws, int off, u32 want) {
  u32x4 v;
  u64 keep;
  u32 left = SPIN_CAP, t;
  asm volatile("s_mov_b64 %[keep], exec\n\t"
               "s_branch 2f\n"
               "1:\n\t"
               "s_sub_u32 %[left], %[left], 1\n\t"
               "s_cbranch_scc1 3f\n"
               "2:\n\t"
               ".if %[pnt]\n\tbuffer_load_dwordx4 v[100:103], %[off], %[ws], 0 offen sc1 nt\n\t.else\n\t"
               "buffer_load_dwordx4 v[100:103], %[off], %[ws], 0 offen sc1\n\t.endif\n\t"
               "s_waitcnt vmcnt(0)\n\t"
               "v_bitop3_b32 %[t], v101, v103, %[want] bitop3:0x7e\n\t"
               "v_cmp_lt_u32 vcc, %[low], %[t]\n\t"
               "s_and_b64 exec, exec, vcc\n\t"
               "s_cbranch_scc1 1b\n"
               "3:\n\t"
               "s_mov_b64 exec, %[keep]"
               : "={v[100:103]}"(v), [keep] "=&s"(keep), [left] "+s"(left), [t] "=&v"(t)
               : [off] "v"(off), [ws] "s"(ws), [want] "s"(want), [low] "i"(LOW), [pnt] "i"(PNT)
               : "vcc", "scc", "memory");
  return v;
}
DI void quad_sum4(float& a, float& b, float& c, float& d) {
  asm("s_nop 1\n\t"
      "v_add_f32_dpp %0, %0, %0 quad_perm:[1,0,3,2] row_mask:0xf bank_mask:0xf\n\t"
      "v_add_f32_dpp %1, %1, %1 quad_perm:[1,0,3,2] row_mask:0xf bank_mask:0xf\n\t"
      "v_add_f32_dpp %2, %2, %2 quad_perm:[1,0,3,2] row_mask:0xf bank_mask:0xf\n\t"
      "v_add_f32_dpp %3, %3, %3 quad_perm:[1,0,3,2] row_mask:0xf bank_mask:0xf\n\t"
      "v_add_f32_dpp %0, %0, %0 quad_perm:[2,3,0,1] row_mask:0xf bank_mask:0xf\n\t"
      "v_add_f32_dpp %1, %1, %1 quad_perm:[2,3,0,1] row_mask:0xf bank_mask:0xf\n\t"
      "v_add_f32_dpp %2, %2, %2 quad_perm:[2,3,0,1] row_mask:0xf bank_mask:0xf\n\t"
      "v_add_f32_dpp %3, %3, %3 quad_perm:[2,3,0,1] row_mask:0xf bank_mask:0xf"
      : "+v"(a), "+v"(b), "+v"(c), "+v"(d));
}
DI float sum_records(u32x4 v) {
  const float t = half16(half32(first2(v[0], v[1]), first2(v[2], v[3])), half32(second2(v[0], v[1]), second2(v[2], v[3])));
  return t + dpp<ROR8>(t);
}

DI int pairpos(int c, int e) { return ((2 * c + e) & 7) * 4 + (c >> 3) * 2 + ((c >> 2) & 1); }

DI void layernorm(u32 (&x)[8]) {
  float v[16], s = 0.f;
#pragma unroll
  for (int e = 0; e < 8; ++e) { v[2 * e] = bflo(x[e]); v[2 * e + 1] = bfhi(x[e]); s += v[2 * e] + v[2 * e + 1]; }
  const float mean = half_sum(s) * (1.f / 512.f);
  float q = 0.f;
#pragma unroll
  for (int e = 0; e < 16; ++e) { const float d = v[e] - mean; q = fmaf(d, d, q); }
  const float var = half_sum(q) * (1.f / 512.f);
  const float mb = rbf(mean), inv = __builtin_amdgcn_rcpf(rbf(__builtin_amdgcn_sqrtf(rbf(rbf(var) + 1e-5f))));
#pragma unroll
  for (int e = 0; e < 8; ++e) { const u32 d = pk(v[2 * e] - mb, v[2 * e + 1] - mb); x[e] = pk(bflo(d) * inv, bfhi(d) * inv); }
}

DI f32x2 tile_matvec(const u32x4 (&wv)[8], LAS unsigned char* xin, int g, int c) {
  const u32x4 xf = *(LAS u32x4*)(xin + ((c >> 2) & 1) * 1024 + ((c >> 3) * 32 + g * 8) * 2);
  f32x4 lo = {0.f, 0.f, 0.f, 0.f}, hi = lo;
#pragma unroll
  for (int d = 0; d < 4; ++d) {
    const u32x4 am = sel4((c & 3) == d, xf);
    lo = mfma(am, colfrag(wv, d), lo);
    hi = mfma(am, colfrag(wv, 4 + d), hi);
  }
  const bool valid = (g >> 1) == (c >> 3);
  float t[4];
#pragma unroll
  for (int e = 0; e < 4; ++e) { t[e] = half32(valid ? lo[e] : 0.f, valid ? hi[e] : 0.f); t[e] += dpp<ROR8>(t[e]); }
  return (c & 8) ? (f32x2){t[2], t[3]} : (f32x2){t[0], t[1]};
}

#define ST(i) do { if (!((STMASK >> (i)) & 1)) break; __builtin_amdgcn_sched_barrier(0); ts[i] = __builtin_amdgcn_s_memrealtime(); __builtin_amdgcn_s_waitcnt(0xC07F); __builtin_amdgcn_sched_barrier(0); } while (0)

extern "C" __global__ void __launch_bounds__(1024, 4)
fused_decode(const u16* x_, const u16* k_, const u16* v_, const u16* wq_, const u16* wo_, const u16* w1_, const u16* b1_,
             const u16* w2_, const u16* b2_, u16* out_, unsigned char* ws_
             ) {
  __shared__ __attribute__((aligned(16))) unsigned char lds_raw[L_END];
  LAS unsigned char* lds = (LAS unsigned char*)lds_raw;
  u64 ts[15];
  for (int i = 0; i < 15; ++i) ts[i] = 0;
  ST(0);
  const u64 t_start = WO_MODE == 3 ? __builtin_amdgcn_s_memrealtime() : 0;
  asm volatile("" :: "s"(x_), "s"(k_), "s"(v_), "s"(wq_), "s"(wo_), "s"(w1_), "s"(b1_), "s"(w2_), "s"(b2_), "s"(out_), "s"(ws_));
  const int tid = threadIdx.x, lane = tid & 63, w = __builtin_amdgcn_readfirstlane(tid >> 6);
  const int g = lane >> 4, c = lane & 15;
  const int bid = blockIdx.x, kvh = bid & 7, j = bid >> 3, b = j >> 4, sp = j & 15, rb = j >> 2, cb = j & 3;
  const bool mlp1 = j < 14, mlp2 = j >= 14 && j < 28;
  const __amdgpu_buffer_rsrc_t ws = __builtin_amdgcn_make_buffer_rsrc(ws_, 0, WS_END, 0x00020000);
  u32 tag;
  const u32 hsh = ((u32)(bid * 16 + w) * 2654435761u) >> 20, hb = ((u32)bid * 2654435761u) >> 16;
  const int wu = (w + (ROT == 0 ? 0 : ROT == 1 || ROT == 2 || ROT == 3 ? j : (int)hb)) & 15;
  const int ro = ROT == 2 ? kvh : ROT == 3 || ROT == 4 ? (int)(hsh & 7) : 0;
  const size_t kvseq = (size_t)(b * 8 + kvh) * SK * DH;
  const __amdgpu_buffer_rsrc_t kr = __builtin_amdgcn_make_buffer_rsrc((void*)(k_ + kvseq), 0, SK * DH * 2, 0x00020000);
  const __amdgpu_buffer_rsrc_t vr = __builtin_amdgcn_make_buffer_rsrc((void*)(v_ + kvseq), 0, SK * DH * 2, 0x00020000);
  const int kvoff = (sp * 16 + wu) * 8192;
  const int klane = ((((c >> 1) ^ ro) & 1) * 4 + (c >> 2)) * 256 + (g * 2 + (c & 1)) * 16;
  u32x4 kf[8], vf[8];

  const int cq = j & 7, kq = j >> 3;
  const int mt = (mlp1 ? j : j - 14) * 8 + kvh;
  const int mhi = mt >> 2, mlo = mt & 3;
  auto pretouch = [&](int what) __attribute__((always_inline)) {
    const unsigned char* p = ws_ + WS_OP + ((kvh * 2 + b) * 16 + sp) * 2048 + c * 128;
    bool on = lane < 16 && (what & 1);
    if ((lane >> 3) == 2) { p = ws_ + WS_S2 + (cb * 64 + kvh * 8 + rb) * 1024 + (lane & 7) * 128; on = what & 16; }
    if ((lane >> 3) == 3) { p = ws_ + WS_MP + (mlo * 28 + mhi) * 1024 + (lane & 7) * 128; on = mlp2 && (what & 2); }
    if ((lane >> 2) == 8) { p = ws_ + WS_QP + ((((lane >> 1) & 1) * 8 + kvh) * 32 + cq * 4 + kq) * 256 + (lane & 1) * 128; on = what & 4; }
    if ((lane >> 2) == 9) { p = ws_ + WS_PA + mt * 512 + (lane & 3) * 128; on = mlp1 && (what & 4); }
    if (lane == 40) { p = (const unsigned char*)(out_ + ((bid >> 2) & 1) * D + (bid & 3) * 128 + ((bid >> 3) & 3) * 32); on = bid < NFIN && (what & 8); }
    if (lane == 41) { p = ws_; on = bid == 0 && (what & 32); }
    if (TV == 1 || TV == 3) on = on && (lane & 15) == 0;
    if (TV >= 2) { if (on) touch_nt(p, (u32)(size_t)(lds + L_T2)); } else
    if (on) touch(p, (u32)(size_t)(lds + L_T2));
  };
  if (WALK && w == WALKW) {
    const unsigned char* p;
    const int q = j & 3, xo = kvh * 128;
    switch (j >> 2) {
      case 0: p = (const unsigned char*)wq_ + (q << 20) + kvh * 1024; break;
      case 1: p = (const unsigned char*)w1_ + q * 458752 + xo; break;
      case 2: p = (const unsigned char*)w2_ + q * 458752 + xo; break;
      case 3: p = (const unsigned char*)k_ + ((size_t)((q >> 1) * 8 + kvh) << 21) + ((q & 1) << 20); break;
      case 4: p = (const unsigned char*)v_ + ((size_t)((q >> 1) * 8 + kvh) << 21) + ((q & 1) << 20); break;
      case 5: p = q == 0 ? (const unsigned char*)x_ + xo : q == 1 ? (const unsigned char*)b1_ + xo : q == 2 ? (const unsigned char*)b2_ + xo : ws_ + WS_END + xo; break;
      case 6: p = (const unsigned char*)wo_ + kvh * 524288 + q * 131072; break;
      default: p = (q & 1 ? (const unsigned char*)w2_ : (const unsigned char*)w1_) + 229376 + (q >> 1) * 917504 + xo; break;
    }
    if (lane == 0) touch_nt(p, (u32)(size_t)(lds + L_T2));
    PIN();
  }
  if (w < 2 || (w < 4 && mlp1)) {
    __builtin_amdgcn_s_setprio(3);
    if (w == 0 && lane < 16) *(LAS u32*)(lds + L_FLAG + lane * 4) = 0u;
    const GAS u32x4* xp = (const GAS u32x4*)x_ + lane * 2;
    const u32x4 x0 = xp[0], x1 = xp[1];
    PIN();
    ST(14);
    const int rs = w < 2 ? NQ / 8 : FF / 8, row0 = (w < 2 ? kq : mlo) * 128 + (w & 1) * 64;
    const u16* tile = w < 2 ? wq_ + kvh * 512 + cq * 64 : w1_ + mhi * 64;
    const GAS u32x4* wp = (const GAS u32x4*)tile + (row0 + (c >> 3) * 32 + g * 8) * rs + (c & 7);
    u32x4 wv[8];
#pragma unroll
    for (int i = 0; i < 8; ++i) wv[i] = ldnt(wp + i * rs);
    PIN();
    LDS_BARRIER();
    ST(12);
    tag = read_tag(ws_);
    PIN();
    u32 xs[8] = {x0[0], x0[1], x0[2], x0[3], x1[0], x1[1], x1[2], x1[3]};
    layernorm(xs);
    if (w >= 2) layernorm(xs);
    LAS unsigned char* xl = lds + L_XN + w * 2048;
    *(LAS u32x4*)(xl + lane * 32) = (u32x4){xs[0], xs[1], xs[2], xs[3]};
    *(LAS u32x4*)(xl + lane * 32 + 16) = (u32x4){xs[4], xs[5], xs[6], xs[7]};
    f32x2 r = tile_matvec(wv, xl + row0 * 2, g, c);
    LAS f32x2* half = (LAS f32x2*)(lds + L_HALF + (w >> 1) * 512 + lane * 8);
    if (w & 1) {
      *half = r;
      set_flag(lds, F_HALF + (w >> 1));
    } else {
      wait_flags(lds, F_HALF + (w >> 1), 0, lane);
      r += *half;
      const int rec = w == 0 ? WS_QP + ((((g & 1) * 8 + kvh) * 8 + cq) * 4 + kq) * 256 : WS_PA + mt * 512 + (g & 1) * 256;
      if (!(ABL & 4)) __builtin_amdgcn_raw_buffer_store_b64(pack2(r[0], r[1], tag), ws, rec + (LIN ? (g >> 1) * 16 + c : (c & 7) * 4 + (g >> 1) * 2 + (c >> 3)) * 8, 0, MSA); else asm volatile("" :: "v"(r), "v"(rec));
    }
    ST(2);
    __builtin_amdgcn_s_setprio(PRIO_K);
  } else {
    if (w < 4 && mlp2) {
      const u16* p = w2_ + (mhi * 64 + (w & 1) * 32 + g * 8) * D + mlo * 128 + c * 8;
#pragma unroll
      for (int i = 0; i < 8; ++i) dma16(p + i * D, (u32)(size_t)(lds + L_W2 + (w & 1) * 8192 + i * 1024));
    }
    if (CODE_LINES && w == 4) {
      const unsigned char* pc;
      asm volatile("s_getpc_b64 %0" : "=s"(pc));
      if (lane < 4 && j * 4 + lane < CODE_LINES) touch(pc + (j * 4 + lane) * 128, (u32)(size_t)(lds + L_TOUCH));
    }
    if (PRE_A && w == PREW) pretouch(PRE_A);
    if (SPRE & 16) RAW_BARRIER();
    if ((SPRE & 1) && (w >> 1) == 6) stouch8(ws_ + WS_OP + ((kvh * 2 + b) * 16 + sp) * 2048 + (w & 1) * 1024);
    if ((SPRE & 1) && w == 14) stouch8(ws_ + WS_S2 + (cb * 64 + kvh * 8 + rb) * 1024);
    if ((SPRE & 2) && w == 15 && mlp2) stouch8(ws_ + WS_MP + (mlo * 28 + mhi) * 1024);
    if ((SPRE & 4) && w == 11) { const unsigned char* q = ws_ + WS_QP + (kvh * 32 + cq * 4 + kq) * 256; stouch2x2(q, q + 65536); }
    if ((SPRE & 4) && w == 10 && mlp1) { const unsigned char* q = ws_ + WS_PA + mt * 512; stouch2x2(q, q + 256); }
    if ((SPRE & 8) && w == 9 && bid < NFIN) { const u16* q = out_ + ((bid >> 2) & 1) * D + (bid & 3) * 128; stouch2x2(q, q); }
    if (SCODE && w == SCODEW) {
      u32 t;
      const u32 off = min(j * 512, SCODE - 512);
      __builtin_amdgcn_sched_barrier(0);
      asm volatile("s_getpc_b64 s[92:93]\n"
                   "1:\n\t"
                   "s_sub_u32 s92, s92, 1b - fused_decode\n\t"
                   "s_subb_u32 s93, s93, 0\n\t"
                   "s_add_u32 s92, s92, %1\n\t"
                   "s_addc_u32 s93, s93, 0\n\t"
                   "s_load_dword %0, s[92:93], 0x0\n\ts_load_dword %0, s[92:93], 0x40\n\ts_load_dword %0, s[92:93], 0x80\n\ts_load_dword %0, s[92:93], 0xc0\n\t"
                   "s_load_dword %0, s[92:93], 0x100\n\ts_load_dword %0, s[92:93], 0x140\n\ts_load_dword %0, s[92:93], 0x180\n\ts_load_dword %0, s[92:93], 0x1c0\n\t"
                   "s_barrier\n\t"
                   "s_waitcnt lgkmcnt(0)"
                   : "=&s"(t) : "s"(off) : "s92", "s93", "scc", "memory");
      __builtin_amdgcn_sched_barrier(0);
    } else
    if (!(SPRE & 16)) RAW_BARRIER();
    if (PRE_B && w == PREW + 1) pretouch(PRE_B);
    if ((w >> 2) == 1) __builtin_amdgcn_s_setprio(2); else __builtin_amdgcn_s_setprio(PRIO_K);
  }
  PIN();

  const u64 t_bar = Q_AT ? __builtin_amdgcn_s_memrealtime() : 0;
  const int slot = SLOTS == 1 ? SLOT0 : SLOTS == 2 ? SLOT0 + (j & 1) : j & 3;
  if (PRE_C && slot < 2 && w == 9 + slot * 4) pretouch(PRE_C);
#pragma unroll
  for (int i = 0; i < 8; ++i) { kf[i] = BUF ? __builtin_amdgcn_raw_buffer_load_b128(kr, klane + (i & 1) * 128, kvoff + ((i >> 1) ^ (ro >> 1)) * 2048, NT) : ldnt((const GAS u32x4*)((const GAS char*)(k_ + kvseq) + kvoff + klane + (i & 1) * 128 + (i >> 1) * 2048)); __builtin_amdgcn_sched_barrier(0); if (i == 0) ST(13); }
  if ((w >> 2) != 1) __builtin_amdgcn_s_setprio(0);
  if (PRE_C && slot >= 2 && w == 1 + (slot - 2) * 8) pretouch(PRE_C);
  PIN();
#pragma unroll
  for (int i = 0; i < 8; ++i) { vf[i] = BUF ? __builtin_amdgcn_raw_buffer_load_b128(vr, lane * 16, kvoff + (i ^ ro) * 1024, NT) : ldnt((const GAS u32x4*)((const GAS char*)(v_ + kvseq) + kvoff + lane * 16 + i * 1024)); __builtin_amdgcn_sched_barrier(0); }
  PIN();
  if ((PRE_D || WO_V) && lane == 0) __hip_atomic_fetch_add((LAS u32*)(lds + L_CNT + 4), 1u, __ATOMIC_RELAXED, __HIP_MEMORY_SCOPE_WORKGROUP);
  if (PRE_E && w == 13) pretouch(PRE_E);
  ST(4);
  tag = read_tag(ws_);
  const int paoff = WS_PA + mhi * 2048 + lane * 16;
  u32x4 p0, p1;
  u32 b1v;
  if ((w >> 2) == 1) {
    const int u = w & 3;
    const int qoff = WS_QP + (b * 8 + kvh) * 8192 + u * 2048 + lane * 16;
    if (Q_AT) {
      while (__builtin_amdgcn_s_memrealtime() < t_bar + Q_AT) __builtin_amdgcn_s_sleep(2);
    } else {
    asm volatile("s_waitcnt vmcnt(0)" ::: "memory");
    if (Q_DELAY) __builtin_amdgcn_s_sleep(Q_DELAY);
    }
    u32x4 q0, q1;
#pragma unroll 1
    for (int spin = 0; spin < SPIN_CAP; ++spin) {
      q0 = __builtin_amdgcn_raw_buffer_load_b128(ws, qoff, 0, MLA); q1 = __builtin_amdgcn_raw_buffer_load_b128(ws, qoff + 1024, 0, MLA);
      if ((ABL & 4) || __all(tagged(q0, tag) && tagged(q1, tag))) break;
      __builtin_amdgcn_s_sleep(SLEEP_Q);
      asm volatile("" ::: "memory");
    }
    const float qa = half16(half32(first2(q0[0], q0[1]), first2(q1[0], q1[1])), half32(first2(q0[2], q0[3]), first2(q1[2], q1[3])));
    const float qb = half16(half32(second2(q0[0], q0[1]), second2(q1[0], q1[1])), half32(second2(q0[2], q0[3]), second2(q1[2], q1[3])));
    *(LAS u32*)(lds + L_Q + u * 256 + ((g >> 1) * 32 + (LIN ? pairpos(c, g & 1) : c * 2 + (g & 1))) * 4) = pk(qa, qb);
    set_flag(lds, F_Q + u);
    ST(3);
    if (w == 7 && mlp2) {
      p0 = ldws(ws, paoff); p1 = ldws(ws, paoff + 1024);
      b1v = B1NT ? ldnt((const GAS u32*)(b1_ + mhi * 64 + (LIN ? pairpos(c, g >> 1) * 2 : c * 4 + (g >> 1) * 2))) : *(const GAS u32*)(b1_ + mhi * 64 + (LIN ? pairpos(c, g >> 1) * 2 : c * 4 + (g >> 1) * 2));
      PIN();
    }
    __builtin_amdgcn_s_setprio(0);
  }
  wait_flags(lds, F_Q, 3, lane);
  if (YPRIO) { if (w >= 12) __builtin_amdgcn_s_setprio(3); else if (w >= 8) __builtin_amdgcn_s_setprio(2); else if (w < 4) __builtin_amdgcn_s_setprio(1); }

  float sc[8];
  {
    const int x0 = (c >> 2) & 1;
    const u32x4 q0 = *(LAS u32x4*)(lds + L_Q + (c & 3) * 256 + (g * 2 + x0) * 16);
    const u32x4 q1 = *(LAS u32x4*)(lds + L_Q + (c & 3) * 256 + 128 + (g * 2 + x0) * 16);
#pragma unroll
    for (int o = 0; o < 4; ++o) {
      f32x4 s = mfma(kf[2 * o], q0, (f32x4){0.f, 0.f, 0.f, 0.f});
      s = mfma(kf[2 * o + 1], q1, s);
#pragma unroll
      for (int a = 0; a < 2; ++a) {
        float e1 = s[2 * a + 1];
        asm("" : "+v"(e1));
        const float t = x0 ? e1 : s[2 * a];
        sc[2 * o + a] = t + dpp<ROR4>(t);
      }
      const u32 d = pk(sc[2 * o], sc[2 * o + 1]), e = pk(bflo(d) * 0.08838834764831845f, bfhi(d) * 0.08838834764831845f);
      sc[2 * o] = bflo(e); sc[2 * o + 1] = bfhi(e);
    }
  }
  PIN();

    float m = fmaxf(fmaxf(fmaxf(sc[0], sc[1]), fmaxf(sc[2], sc[3])), fmaxf(fmaxf(sc[4], sc[5]), fmaxf(sc[6], sc[7])));
    m = max32(max16(m)) * LOG2E;
    float p[8], l = 0.f;
#pragma unroll
    for (int e = 0; e < 8; ++e) { p[e] = __builtin_amdgcn_exp2f(fmaf(sc[e], LOG2E, -m)); l += p[e]; }
    l = half16(l, l); l = half32(l, l);
    if (lane < 4) { *(LAS float*)(lds + L_MW + (lane * 16 + w) * 4) = m; *(LAS float*)(lds + L_LW + (lane * 16 + w) * 4) = l; }
    PIN();
    if ((w >> 1) == 1) set_flag(lds, F_W2 + (w & 1));
    if (lane == 0) __hip_atomic_fetch_add((LAS u32*)(lds + L_CNT), 1u, __ATOMIC_RELAXED, __HIP_MEMORY_SCOPE_WORKGROUP);
    const u32x4 pf = {pk(p[0], p[1]), pk(p[2], p[3]), pk(p[4], p[5]), pk(p[6], p[7])};
    u32x4 am[4];
#pragma unroll
    for (int u = 0; u < 4; ++u) am[u] = sel4((c >> 2) == u, pf);
    PIN();
    u32x4 ve[4], vo[4];
    u32 slo, shi;
    asm volatile("s_mov_b32 %0, 0x05040100\n\ts_mov_b32 %1, 0x07060302" : "=s"(slo), "=s"(shi) : "v"(am[0][0]), "v"(am[1][1]), "v"(am[2][2]), "v"(am[3][3]));
#pragma unroll
    for (int r = 0; r < 4; ++r) {
#pragma unroll
      for (int u = 0; u < 4; ++u) {
        ve[u][r] = __builtin_amdgcn_perm(vf[2 * r + 1][u], vf[2 * r][u], slo);
        vo[u][r] = __builtin_amdgcn_perm(vf[2 * r + 1][u], vf[2 * r][u], shi);
      }
      if (r < 3)
        asm volatile("" : "+s"(slo), "+s"(shi) : "v"(ve[0][r]), "v"(ve[1][r]), "v"(ve[2][r]), "v"(ve[3][r]), "v"(vo[0][r]), "v"(vo[1][r]), "v"(vo[2][r]), "v"(vo[3][r]));
    }
    f32x4 oe = {0.f, 0.f, 0.f, 0.f}, oo = oe;
#pragma unroll
    for (int u = 0; u < 4; ++u) { oe = mfma(am[u], ve[u], oe); oo = mfma(am[u], vo[u], oo); }
#pragma unroll
    for (int h = 0; h < 4; ++h) *(LAS f32x2*)(lds + L_OW + w * OW_STRIDE + h * 512 + c * 32 + g * 8) = (f32x2){oe[h], oo[h]};
    PIN();
    ST(5);

  const bool gath = (w & 4) == 0;
  const int v = (w & 3) + (w >> 3) * 4;
  auto wait_ml = [&]() __attribute__((always_inline)) {
#pragma unroll 1
    for (int spin = 0; spin < (SPIN_CAP << 4); ++spin) {
      if (__builtin_amdgcn_readfirstlane(*(volatile LAS u32*)(lds + L_CNT)) == 16u) break;
      __builtin_amdgcn_s_sleep(1);
    }
    PIN();
  };
  auto load_wo = [&](u32x4 (&wt)[8]) __attribute__((always_inline)) {
    const GAS u16* p = (const GAS u16*)wo_ + (size_t)(kvh * 512 + rb * 64 + g * 4) * D + cb * 128 + (w - 4) * 64 + c * 4;
#pragma unroll
    for (int i = 0; i < 8; ++i) {
      const u32x2 t0 = ldnt((const GAS u32x2*)(p + (size_t)(32 * (i >> 2) + (i & 3)) * D));
      const u32x2 t1 = ldnt((const GAS u32x2*)(p + (size_t)(32 * (i >> 2) + 16 + (i & 3)) * D));
      wt[i] = (u32x4){t0[0], t0[1], t1[0], t1[1]};
    }
    PIN();
  };
  auto wait_v = [&](u32 n) __attribute__((always_inline)) {
#pragma unroll 1
    for (int spin = 0; spin < (SPIN_CAP << 4); ++spin) {
      if (__builtin_amdgcn_readfirstlane(*(volatile LAS u32*)(lds + L_CNT + 4)) >= n) break;
      __builtin_amdgcn_s_sleep(1);
    }
    PIN();
  };
  if (PRE_D && w == 6) { wait_v(PRE_AT); pretouch(PRE_D); }
  const bool fin = FINLAST ? bid >= 256 - NFIN : bid < NFIN;
  const int cf = bid & 3, bf = (bid >> 2) & 1, seg = (bid >> 3) & 3;
  if (w >= 12) {
    LDS_BARRIER();
    ST(6);
    if (fin) {
      const int row = (w - 12) * 8 + (lane >> 3);
      const int moff = WS_MP + (cf * 28 + min(row, 27)) * 1024 + bf * 512 + seg * 128 + (lane & 7) * 16;
      u32x4 mv;
#pragma unroll 1
      for (int spin = 0; spin < SPIN_CAP; ++spin) {
        mv = ldws(ws, moff);
        if (__all(tagged(mv, tag))) break;
        __builtin_amdgcn_s_sleep(SLEEP_M);
        asm volatile("" ::: "memory");
      }
      const float t = sum_records(sel4(row < 28, mv));
      if ((lane & 8) == 0) *(LAS float*)(lds + L_MF + (w - 12) * 128 + ((lane & 7) * 4 + g) * 4) = t;
      LDS_BARRIER();
    }
  } else if (gath) {
    {
      const int f = v * 16 + (lane >> 2), p4 = lane & 3, hd = v >> 1;
      wait_ml();
      const f32x4 mv = *(LAS f32x4*)(lds + L_MW + hd * 64 + p4 * 16), lv = *(LAS f32x4*)(lds + L_LW + hd * 64 + p4 * 16);
      float M = fmaxf(fmaxf(mv[0], mv[1]), fmaxf(mv[2], mv[3]));
      M = fmaxf(M, dpp<XOR1>(M)); M = fmaxf(M, dpp<XOR2>(M));
      float L = 0.f, fw[4];
#pragma unroll
      for (int i = 0; i < 4; ++i) { fw[i] = __builtin_amdgcn_exp2f(mv[i] - M); L = fmaf(fw[i], lv[i], L); }
      L += dpp<XOR1>(L); L += dpp<XOR2>(L);
      const float inv = __builtin_amdgcn_rcpf(L);
#pragma unroll
      for (int i = 0; i < 4; ++i) fw[i] *= inv;
      const u32 lam = f2u(M + __builtin_amdgcn_logf(L)) >> ((lane >> 5) * 16);
      u32 t0 = tag | (lam & 255u), t1 = tag | ((lam >> 8) & 255u);
      int ooff = WS_OP + ((kvh * 2 + b) * 16 + sp) * 2048 + f * 16;
      int laddr = L_OW + p4 * 4 * OW_STRIDE + f * 16;
      asm volatile("" : "+v"(fw[0]), "+v"(fw[1]), "+v"(fw[2]), "+v"(fw[3]), "+v"(t0), "+v"(t1), "+v"(ooff), "+v"(laddr));
      ST(3);
      LDS_BARRIER();
      ST(6);
      f32x4 s = fw[0] * *(LAS f32x4*)(lds + laddr);
#pragma unroll
      for (int i = 1; i < 4; ++i) s += fw[i] * *(LAS f32x4*)(lds + laddr + i * OW_STRIDE);
      float s0 = s[0], s1 = s[1], s2 = s[2], s3 = s[3];
      quad_sum4(s0, s1, s2, s3);
      if (p4 == 0) __builtin_amdgcn_raw_buffer_store_b128((u32x4){pk(s0, s1), t0, pk(s2, s3), t1}, ws, ooff, 0, OSAUX);
    }
    PIN();
    ST(7);
    {
      u32 gaddr2 = (u32)(size_t)(lds + L_GP + v * GP_STRIDE + 256), gaddr = gaddr2 - 256 + (c * 4 + g) * 4;
      u32 lsel = (c & 8) ? 0x05040100u : 0x01000504u;
      asm volatile("" : "+v"(gaddr), "+v"(gaddr2), "+v"(lsel));
      if (SLEEP_O) __builtin_amdgcn_s_sleep(SLEEP_O);
      const u32x4 ov = poll16<255, OPNT>(ws, WS_OP + (kvh * 32 + v * 4 + g) * 2048 + rb * 256 + c * 16, tag);
      ST(8);
      u32 a, lam, m1, x0, x1, x2, x3;
      f32x2 gl;
      asm volatile("v_perm_b32 %[a], %[t1], %[t0], %[sel]\n\t"
                   "v_lshlrev_b32 %[x0], 16, %[d0]\n\t"
                   "v_lshlrev_b32 %[x2], 16, %[d1]\n\t"
                   "v_mov_b32_dpp %[m1], %[a] row_ror:8 row_mask:0xf bank_mask:0xf\n\t"
                   "v_and_b32 %[x1], 0xffff0000, %[d0]\n\t"
                   "v_perm_b32 %[lam], %[a], %[m1], %[lsel]\n\t"
                   "v_mov_b32 %[m1], %[lam]\n\t"
                   "v_mov_b32 %[a], %[lam]\n\t"
                   "v_and_b32 %[x3], 0xffff0000, %[d1]\n\t"
                   "s_nop 0\n\t"
                   "v_permlane16_swap_b32 %[m1], %[a]\n\t"
                   "v_max_f32 %[m1], %[m1], %[a]\n\t"
                   "v_mov_b32 %[a], %[m1]\n\t"
                   "s_nop 1\n\t"
                   "v_permlane32_swap_b32 %[m1], %[a]\n\t"
                   "v_max_f32 v104, %[m1], %[a]\n\t"
                   "v_sub_f32 %[lam], %[lam], v104\n\t"
                   "v_exp_f32 %[lam], %[lam]\n\t"
                   "s_nop 0\n\t"
                   "v_mul_f32 %[x0], %[lam], %[x0]\n\t"
                   "v_mul_f32 %[x2], %[lam], %[x2]\n\t"
                   "v_mul_f32 %[x1], %[lam], %[x1]\n\t"
                   "v_mul_f32 %[x3], %[lam], %[x3]\n\t"
                   "v_mov_b32 %[a], %[lam]\n\t"
                   "v_permlane32_swap_b32 %[x0], %[x2]\n\t"
                   "v_permlane32_swap_b32 %[x1], %[x3]\n\t"
                   "v_permlane16_swap_b32 %[lam], %[a]\n\t"
                   "v_add_f32 %[x0], %[x0], %[x2]\n\t"
                   "v_add_f32 %[x1], %[x1], %[x3]\n\t"
                   "v_add_f32 %[lam], %[lam], %[a]\n\t"
                   "v_mov_b32 %[a], %[lam]\n\t"
                   "v_permlane16_swap_b32 %[x0], %[x1]\n\t"
                   "v_add_f32 %[x0], %[x0], %[x1]\n\t"
                   "v_permlane32_swap_b32 %[lam], %[a]\n\t"
                   "ds_write_b32 %[ga], %[x0]\n\t"
                   "v_add_f32 v105, %[lam], %[a]\n\t"
                   "ds_write_b64 %[gb], v[104:105]"
                   : [a] "=&v"(a), [lam] "=&v"(lam), [m1] "=&v"(m1), [x0] "=&v"(x0), [x1] "=&v"(x1), [x2] "=&v"(x2), [x3] "=&v"(x3), "=&{v[104:105]}"(gl)
                   : [d0] "v"(ov[0]), [t0] "v"(ov[1]), [d1] "v"(ov[2]), [t1] "v"(ov[3]), [sel] "s"(0x04000400u), [lsel] "v"(lsel), [ga] "v"(gaddr), [gb] "v"(gaddr2)
                   : "memory");
    }
    LDS_BARRIER();
    ST(9);
    if (fin) {
      int faddr = L_FP + ((lane & 7) * 4 + g) * 32 + v * 4;
      asm volatile("" : "+v"(faddr));
      if (SLEEP_S) __builtin_amdgcn_s_sleep(SLEEP_S);
      const u32x4 sv = poll16<255>(ws, WS_S2 + (cf * 64 + v * 8 + (lane >> 3)) * 1024 + bf * 512 + seg * 128 + (lane & 7) * 16, tag);
      ST(10);
      *(LAS float*)(lds + faddr) = sum_records(sv);
      LDS_BARRIER();
    }
  } else if (w < 6) {
    u32x4 wt[8], bw[8];
    if (WO_MODE == 3) {
      wait_ml();
      const bool now = __builtin_amdgcn_s_memrealtime() >= t_start + WO_AT;
      if (!now) {
        LDS_BARRIER();
        ST(6);
        while (__builtin_amdgcn_s_memrealtime() < t_start + WO_AT) __builtin_amdgcn_s_sleep(1);
      }
      load_wo(wt);
      if (now) { LDS_BARRIER(); ST(6); }
    } else {
    if (WO_MODE == 1) { if (WO_V) wait_v(16u); else wait_ml(); }
    if (WO_MODE < 2) load_wo(wt);
    LDS_BARRIER();
    ST(6);
    if (WO_MODE == 2) { if (WO_SLEEP) __builtin_amdgcn_s_sleep(WO_SLEEP); load_wo(wt); }
    }
#pragma unroll
    for (int i = 0; i < 8; ++i) bw[i] = colfrag(wt, i);
    int gp = L_GP + (c & 7) * GP_STRIDE, go = gp + ((c >> 3) * 16 + g * 4) * 4;
    int soff = WS_S2 + (cb * 64 + kvh * 8 + rb) * 1024 + (g & 1) * 512 + ((w - 4) * 32 + c * 2 + (g >> 1)) * 8;
    asm volatile("" : "+v"(gp), "+v"(go), "+v"(soff) : "v"(bw[0]), "v"(bw[1]), "v"(bw[2]), "v"(bw[3]), "v"(bw[4]), "v"(bw[5]), "v"(bw[6]), "v"(bw[7]));
    ST(1);
    LDS_BARRIER();
    ST(9);
    f32x2 gl = *(LAS f32x2*)(lds + gp + 256);
    u32x4 of = *(LAS u32x4*)(lds + go), o1 = *(LAS u32x4*)(lds + go + 128);
#define QP1(r) "v_add_f32_dpp " r ", " r ", " r " quad_perm:[1,0,3,2] row_mask:0xf bank_mask:0xf\n\t"
#define QP2(r) "v_add_f32_dpp " r ", " r ", " r " quad_perm:[2,3,0,1] row_mask:0xf bank_mask:0xf\n\t"
#define PKM(r) "v_pk_mul_f32 " r ", v[118:119], " r " op_sel_hi:[0,1]\n\t"
    asm volatile("v_max_f32_dpp v118, v116, v116 quad_perm:[1,0,3,2] row_mask:0xf bank_mask:0xf\n\t"
                 "s_nop 1\n\t"
                 "v_max_f32_dpp v118, v118, v118 quad_perm:[2,3,0,1] row_mask:0xf bank_mask:0xf\n\t"
                 "v_sub_f32 v118, v116, v118\n\t"
                 "v_exp_f32 v118, v118\n\t"
                 "s_nop 0\n\t"
                 "v_mul_f32 v117, v118, v117\n\t"
                 PKM("v[108:109]") PKM("v[110:111]") PKM("v[112:113]") PKM("v[114:115]")
                 QP1("v117") QP1("v108") QP1("v109") QP1("v110") QP1("v111") QP1("v112") QP1("v113") QP1("v114") QP1("v115")
                 QP2("v117") QP2("v108") QP2("v109")
                 "v_rcp_f32 v118, v117\n\t"
                 QP2("v110") QP2("v111") QP2("v112") QP2("v113") QP2("v114") QP2("v115")
                 PKM("v[108:109]") PKM("v[110:111]") PKM("v[112:113]") PKM("v[114:115]")
                 "v_cvt_pk_bf16_f32 v108, v108, v109\n\t"
                 "v_cvt_pk_bf16_f32 v109, v110, v111\n\t"
                 "v_cvt_pk_bf16_f32 v110, v112, v113\n\t"
                 "v_cvt_pk_bf16_f32 v111, v114, v115\n\t"
                 "s_nop 1"
                 : "+{v[108:111]}"(of), "+{v[112:115]}"(o1), "+{v[116:117]}"(gl)
                 :
                 : "v118", "v119");
    f32x4 acc[8];
#pragma unroll
    for (int i = 0; i < 8; ++i) { const int k = (i & 3) * 2 + (i >> 2); acc[k] = mfma(of, bw[k], (f32x4){0.f, 0.f, 0.f, 0.f}); }
    __builtin_amdgcn_sched_barrier(0);
    float r[4];
#pragma unroll
    for (int d = 0; d < 4; ++d) r[d] = (g >> 1) ? acc[4 + d][0] : acc[d][0];
    stws1(ws, soff, pack2(half32(r[0], r[2]), half32(r[1], r[3]), tag));
    ST(10);
  } else if (w == 7 && mlp2) {
#pragma unroll 1
    for (int spin = 0; spin < SPIN_CAP; ++spin) {
      if ((ABL & 4) || __all(tagged(p0, tag) && tagged(p1, tag))) break;
      __builtin_amdgcn_s_sleep(SLEEP_Q);
      asm volatile("" ::: "memory");
      p0 = ldws(ws, paoff); p1 = ldws(ws, paoff + 1024);
    }
    const float t2[2] = {half32(first2(p0[0], p0[1]) + first2(p1[0], p1[1]), first2(p0[2], p0[3]) + first2(p1[2], p1[3])),
                         half32(second2(p0[0], p0[1]) + second2(p1[0], p1[1]), second2(p0[2], p0[3]) + second2(p1[2], p1[3]))};
    float hv[2];
#pragma unroll
    for (int e = 0; e < 2; ++e) {
      const float t = rbf(rbf(t2[e]) + (e ? bfhi(b1v) : bflo(b1v)));
      const float uu = 0.7978845608028654f * (t + 0.044715f * t * t * t);
      hv[e] = t / (1.f + __builtin_amdgcn_exp2f(-2.f * LOG2E * uu));
    }
    *(LAS u32*)(lds + L_HS + (g & 1) * 128 + (LIN ? pairpos(c, g >> 1) : c * 2 + (g >> 1)) * 4) = pk(hv[0], hv[1]);
    wait_flags(lds, F_W2, 1, lane);
    f32x4 r = {0.f, 0.f, 0.f, 0.f};
#pragma unroll
    for (int s = 0; s < 2; ++s) {
      const u32x4 ha = *(LAS u32x4*)(lds + L_HS + ((c >> 2) & 1) * 128 + s * 64 + g * 16);
      u32x4 w2t[8];
#pragma unroll
      for (int i = 0; i < 8; ++i) w2t[i] = *(LAS u32x4*)(lds + L_W2 + s * 8192 + i * 1024 + lane * 16);
#pragma unroll
      for (int d = 0; d < 8; ++d) r = mfma(sel4((c >> 3) == (d >> 2) && (c & 3) == (d & 3), ha), colfrag(w2t, d), r);
    }
    u32x2 m0 = pack2(r[0], r[1], tag), m1 = pack2(r[2], r[3], tag);
    asm volatile("" : "+v"(m0), "+v"(m1));
    ST(2);
    if (MP_LATE) LDS_BARRIER();
    stws(ws, WS_MP + (mlo * 28 + mhi) * 1024 + (g & 1) * 512 + (c * 2 + (g >> 1)) * 16, m0, m1);
    if (!MP_LATE) LDS_BARRIER();
    ST(6);
  } else if (w == 6 && fin) {
    const int col = cf * 128 + seg * 32 + (lane & 31);
    const u32 xv = ((const GAS u16*)x_)[bf * D + col], b2v = ((const GAS u16*)b2_)[col];
    LDS_BARRIER();
    ST(6);
    LDS_BARRIER();
    LAS float* mf = (LAS float*)(lds + L_MF + (lane >> 5) * 256 + (lane & 31) * 4);
    float t = mf[0] + mf[32];
    t = half32(t, t);
    t = rbf(rbf(t) + bflo(b2v));
    float gate = rbf(t / (1.f + __builtin_amdgcn_exp2f(-LOG2E * t))), xf = bflo(xv);
    int faddr = L_FP + (lane & 31) * 32 + (lane >> 5) * 16;
    GAS u16* op = (GAS u16*)out_ + bf * D + col;
    asm volatile("" : "+v"(gate), "+v"(xf), "+v"(faddr), "+v"(op));
    LDS_BARRIER();
    ST(10);
    const f32x4 fp = *(LAS f32x4*)(lds + faddr);
    float s = (fp[0] + fp[1]) + (fp[2] + fp[3]);
    s = half32(s, s);
    const float res = xf + rbf(rbf(s) * gate);
    if (lane < 32) __hip_atomic_store(op, (u16)pk(res, res), RLX_AGENT);
    if (!NOBUMP && (bid & 31) == 0 && lane == 0) __hip_atomic_store((GAS u32*)(ws_ + WS_EPOCH), tag >> 8, RLX_AGENT);
    if (ENDWAIT) { ST(8); asm volatile("s_waitcnt vmcnt(0)" ::: "memory"); ST(2); }
  } else {
    LDS_BARRIER();
    ST(6);
  }
  ST(11);
  {
    GAS u64* st = (GAS u64*)(ws_ + (WS_AT_END ? -(8 << 20) : (8 << 20)));
    volatile LAS u64* ls = (volatile LAS u64*)(lds + L_TOUCH);
    if (lane == 0) { ls[w * 2] = ((PROBE_WM >> w) & 1) ? ts[PROBE] : 0; ls[w * 2 + 1] = ts[PROBE_B]; }
    if (w == 0) {
      while (__builtin_amdgcn_s_memrealtime() < ts[0] + 2500) __builtin_amdgcn_s_sleep(1);
      u64 ma = ls[(lane & 15) * 2], mb = ls[(lane & 15) * 2 + 1];
      auto red = [&]() {
        for (int o = 32; o; o >>= 1) {
          const u64 x = __shfl_xor(ma, o), y = __shfl_xor(mb, o);
          ma = x > ma ? x : ma;
          if (PROBE_B == 0) mb = y < mb ? y : mb; else mb = y > mb ? y : mb;
        }
      };
      red();
      if (lane == 0) { __hip_atomic_store(st + bid * 2, ma, RLX_AGENT); __hip_atomic_store(st + bid * 2 + 1, mb, RLX_AGENT); }
      if (bid == 0) {
        while (__builtin_amdgcn_s_memrealtime() < ts[0] + 3200) __builtin_amdgcn_s_sleep(1);
        for (int i = lane; i < 256; i += 64) {
          const u64 x = __hip_atomic_load(st + i * 2, RLX_AGENT), y = __hip_atomic_load(st + i * 2 + 1, RLX_AGENT);
          ma = x > ma ? x : ma;
          if (PROBE_B == 0) mb = y < mb ? y : mb; else mb = y > mb ? y : mb;
        }
        red();
        const u64 until = ts[0] + 5000 + 10 * (ma - mb);
        while (__builtin_amdgcn_s_memrealtime() < until && __builtin_amdgcn_s_memrealtime() < ts[0] + 40000) __builtin_amdgcn_s_sleep(1);
      }
    }
  }
}

extern "C" void kernel_launch(void* const* d_in, const int* in_sizes, int n_in, void* d_out, int out_size, void* d_ws, size_t ws_size,
                              hipStream_t stream) {
  unsigned char* ws = (unsigned char*)d_ws + (WS_AT_END ? ((ws_size - WS_SPAN) & ~(size_t)4095) : 0);
  fused_decode<<<256, 1024, 0, stream>>>((const u16*)d_in[0], (const u16*)d_in[1], (const u16*)d_in[2], (const u16*)d_in[3],
                                         (const u16*)d_in[4], (const u16*)d_in[6], (const u16*)d_in[7], (const u16*)d_in[8],
                                         (const u16*)d_in[9], (u16*)d_out, ws
                                         );
}
```

```cpp
#include <hip/hip_runtime.h>
#include <stdint.h>

#define GAS __attribute__((address_space(1)))
#define LAS __attribute__((address_space(3)))
#define DI __device__ __forceinline__
#define RLX_AGENT __ATOMIC_RELAXED, __HIP_MEMORY_SCOPE_AGENT
typedef unsigned short u16;
typedef unsigned u32;
typedef unsigned long long u64;
typedef u32 u32x2 __attribute__((ext_vector_type(2)));
typedef u32 u32x4 __attribute__((ext_vector_type(4)));
typedef float f32x2 __attribute__((ext_vector_type(2)));
typedef float f32x4 __attribute__((ext_vector_type(4)));
typedef __bf16 bf16x2 __attribute__((ext_vector_type(2)));
typedef __bf16 bf16x8 __attribute__((ext_vector_type(8)));

constexpr int D = 512, SK = 8192, DH = 128, FF = 1792, NQ = 4096;
constexpr int SC1 = 16, NT = 2;
constexpr int SPIN_CAP = 1 << 18;
constexpr int NFIN = 32;
constexpr int NAP_Q = 32, NAP_O = 8, NAP_S = 8, NAP_RETRY = 8;
constexpr float LOG2E = 1.4426950408889634f;

constexpr int WS_EPOCH = 0;
constexpr int WS_QP = 256;
constexpr int WS_PA = WS_QP + 512 * 256;
constexpr int WS_OP = WS_PA + 112 * 512;
constexpr int WS_S2 = WS_OP + 256 * 2048;
constexpr int WS_MP = WS_S2 + 256 * 1024;
constexpr int WS_END = WS_MP + 112 * 1024;

constexpr int F_Q = 0, F_HALF = 4, F_W2 = 6;
constexpr int L_FLAG = 0, L_CNT = 48, L_CNTV = 52, L_XN = 64, L_HALF = L_XN + 8192, L_Q = L_HALF + 1024, L_MW = L_Q + 1024, L_LW = L_MW + 256,
              L_HS = L_LW + 256, L_GP = L_HS + 256, GP_STRIDE = 272, L_FP = L_GP + 8 * GP_STRIDE, L_MF = L_FP + 1024,
              L_SINK = L_MF + 512, L_W2 = L_SINK + 256, L_OW = L_W2 + 16384, OW_STRIDE = 2064, L_END = L_OW + 16 * OW_STRIDE;

DI float bflo(u32 v) { return __builtin_bit_cast(float, v << 16); }
DI float bfhi(u32 v) { return __builtin_bit_cast(float, v & 0xffff0000u); }
DI float rbf(float f) { return (float)(__bf16)f; }
DI u32 pk(float a, float b) { return __builtin_bit_cast(u32, __builtin_convertvector((f32x2){a, b}, bf16x2)); }
DI u32 f2u(float f) { return __builtin_bit_cast(u32, f); }
DI float u2f(u32 u) { return __builtin_bit_cast(float, u); }
DI f32x4 mfma(u32x4 a, u32x4 b, f32x4 c) {
  return __builtin_amdgcn_mfma_f32_16x16x32_bf16(__builtin_bit_cast(bf16x8, a), __builtin_bit_cast(bf16x8, b), c, 0, 0, 0);
}
template <int CTRL> DI float dpp(float x) { return u2f(__builtin_amdgcn_mov_dpp(f2u(x), CTRL, 0xf, 0xf, true)); }
constexpr int XOR1 = 0xB1, XOR2 = 0x4E, ROR4 = 0x124, ROR8 = 0x128;
DI float half32(float a, float b) { auto r = __builtin_amdgcn_permlane32_swap(f2u(a), f2u(b), false, false); return u2f(r[0]) + u2f(r[1]); }
DI float half16(float a, float b) { auto r = __builtin_amdgcn_permlane16_swap(f2u(a), f2u(b), false, false); return u2f(r[0]) + u2f(r[1]); }
DI float max32(float a) { auto r = __builtin_amdgcn_permlane32_swap(f2u(a), f2u(a), false, false); return fmaxf(u2f(r[0]), u2f(r[1])); }
DI float max16(float a) { auto r = __builtin_amdgcn_permlane16_swap(f2u(a), f2u(a), false, false); return fmaxf(u2f(r[0]), u2f(r[1])); }
DI float half_sum(float v) {
  v += dpp<XOR1>(v); v += dpp<XOR2>(v); v += dpp<ROR4>(v); v += dpp<ROR8>(v);
  return half16(v, v);
}
DI u32 plo(u32 k1, u32 k0) { return __builtin_amdgcn_perm(k1, k0, 0x05040100u); }
DI u32 phi(u32 k1, u32 k0) { return __builtin_amdgcn_perm(k1, k0, 0x07060302u); }
DI u32x4 colfrag(const u32x4 (&r)[8], int d) {
  const int q = d >> 1;
  return (d & 1) ? (u32x4){phi(r[1][q], r[0][q]), phi(r[3][q], r[2][q]), phi(r[5][q], r[4][q]), phi(r[7][q], r[6][q])}
                 : (u32x4){plo(r[1][q], r[0][q]), plo(r[3][q], r[2][q]), plo(r[5][q], r[4][q]), plo(r[7][q], r[6][q])};
}
DI u32x4 sel4(bool c, u32x4 v) { return (u32x4){c ? v[0] : 0u, c ? v[1] : 0u, c ? v[2] : 0u, c ? v[3] : 0u}; }
template <typename T> DI T ldnt(const GAS T* p) { return __builtin_nontemporal_load(p); }
DI u32x4 ldws(__amdgpu_buffer_rsrc_t ws, int off) { return __builtin_amdgcn_raw_buffer_load_b128(ws, off, 0, SC1); }
DI void stws(__amdgpu_buffer_rsrc_t ws, int off, u32x2 a, u32x2 b) { __builtin_amdgcn_raw_buffer_store_b128((u32x4){a[0], a[1], b[0], b[1]}, ws, off, 0, SC1); }
DI void stws1(__amdgpu_buffer_rsrc_t ws, int off, u32x2 a) { __builtin_amdgcn_raw_buffer_store_b64(a, ws, off, 0, SC1); }
DI u32x2 pack2(float a, float b, u32 tag8) {
  const u32 ar = f2u(a) + 0x800u;
  const u32 u = (f2u(b) & 0xf000u) | ((ar >> 4) & ~0xf000u);
  return (u32x2){phi(f2u(b), ar), __builtin_amdgcn_perm(tag8, u, 0x07060501u)};
}
DI float first2(u32 w0, u32 w1) { return u2f(__builtin_amdgcn_perm(w0, w1 << 4, 0x0504000cu)); }
DI float second2(u32 w0, u32 w1) { return u2f(__builtin_amdgcn_perm(w0, w1, 0x0706000cu)); }
DI bool tagged(u32x4 v, u32 tag8) { return ((v[1] ^ tag8) | (v[3] ^ tag8)) < 256u; }
#define PIN() do { asm volatile("" ::: "memory"); __builtin_amdgcn_sched_barrier(0); } while (0)
#define LDS_BARRIER() do { asm volatile("s_waitcnt lgkmcnt(0)\n\ts_barrier" ::: "memory"); __builtin_amdgcn_sched_barrier(0); } while (0)
#define RAW_BARRIER() do { __builtin_amdgcn_sched_barrier(0); asm volatile("s_barrier" ::: "memory"); __builtin_amdgcn_sched_barrier(0); } while (0)
DI void dma16(const u16* src, u32 dst) {
  u32 keep;
  asm volatile("s_mov_b32 %0, m0\n\ts_mov_b32 m0, %2\n\ts_nop 0\n\tglobal_load_lds_dwordx4 %1, off nt\n\ts_mov_b32 m0, %0"
               : "=&s"(keep) : "v"(src), "s"(dst) : "memory");
}
DI void touch(const void* src, u32 dst) {
  u32 keep;
  asm volatile("s_mov_b32 %0, m0\n\ts_mov_b32 m0, %2\n\ts_nop 0\n\tglobal_load_lds_dword %1, off sc1\n\ts_mov_b32 m0, %0"
               : "=&s"(keep) : "v"(src), "s"(dst) : "memory");
}
DI void set_flag(LAS unsigned char* lds, int f) {
  asm volatile("s_waitcnt lgkmcnt(0)" ::: "memory");
  *(volatile LAS u32*)(lds + L_FLAG + f * 4) = 1u;
}
DI void wait_flags(LAS unsigned char* lds, int first, int mask, int lane) {
#pragma unroll 1
  for (int spin = 0; spin < (SPIN_CAP << 4); ++spin) {
    if (__all(*(volatile LAS u32*)(lds + L_FLAG + (first + (lane & mask)) * 4) != 0u)) break;
    __builtin_amdgcn_s_sleep(1);
  }
  PIN();
}
DI void count_in(LAS unsigned char* lds, int cnt, int lane) {
  if (lane == 0) __hip_atomic_fetch_add((LAS u32*)(lds + cnt), 1u, __ATOMIC_RELAXED, __HIP_MEMORY_SCOPE_WORKGROUP);
}
DI void wait_all(LAS unsigned char* lds, int cnt) {
#pragma unroll 1
  for (int spin = 0; spin < (SPIN_CAP << 4); ++spin) {
    if (__builtin_amdgcn_readfirstlane(*(volatile LAS u32*)(lds + cnt)) == 16u) break;
    __builtin_amdgcn_s_sleep(1);
  }
  PIN();
}
DI u32 read_tag(const unsigned char* ws) {
  u32 e;
  asm volatile("s_load_dword %0, %1, 0x0\n\ts_waitcnt lgkmcnt(0)" : "=s"(e) : "s"(ws) : "memory");
  return (e + 1u) << 8;
}
DI u32x4 poll16(__amdgpu_buffer_rsrc_t ws, int off, u32 want) {
  u32x4 v;
  u64 keep;
  u32 left = SPIN_CAP, t;
  asm volatile("s_mov_b64 %[keep], exec\n\t"
               "s_branch 2f\n"
               "1:\n\t"
               "s_sub_u32 %[left], %[left], 1\n\t"
               "s_cbranch_scc1 3f\n"
               "2:\n\t"
               "buffer_load_dwordx4 v[100:103], %[off], %[ws], 0 offen sc1\n\t"
               "s_waitcnt vmcnt(0)\n\t"
               "v_bitop3_b32 %[t], v101, v103, %[want] bitop3:0x7e\n\t"
               "v_cmp_lt_u32 vcc, 0xff, %[t]\n\t"
               "s_and_b64 exec, exec, vcc\n\t"
               "s_cbranch_scc1 1b\n"
               "3:\n\t"
               "s_mov_b64 exec, %[keep]"
               : "=&{v[100:103]}"(v), [keep] "=&s"(keep), [left] "+s"(left), [t] "=&v"(t)
               : [off] "v"(off), [ws] "s"(ws), [want] "s"(want)
               : "vcc", "scc", "memory");
  return v;
}
DI void quad_sum4(float& a, float& b, float& c, float& d) {
  asm volatile("s_nop 1\n\t"
      "v_add_f32_dpp %0, %0, %0 quad_perm:[1,0,3,2] row_mask:0xf bank_mask:0xf\n\t"
      "v_add_f32_dpp %1, %1, %1 quad_perm:[1,0,3,2] row_mask:0xf bank_mask:0xf\n\t"
      "v_add_f32_dpp %2, %2, %2 quad_perm:[1,0,3,2] row_mask:0xf bank_mask:0xf\n\t"
      "v_add_f32_dpp %3, %3, %3 quad_perm:[1,0,3,2] row_mask:0xf bank_mask:0xf\n\t"
      "v_add_f32_dpp %0, %0, %0 quad_perm:[2,3,0,1] row_mask:0xf bank_mask:0xf\n\t"
      "v_add_f32_dpp %1, %1, %1 quad_perm:[2,3,0,1] row_mask:0xf bank_mask:0xf\n\t"
      "v_add_f32_dpp %2, %2, %2 quad_perm:[2,3,0,1] row_mask:0xf bank_mask:0xf\n\t"
      "v_add_f32_dpp %3, %3, %3 quad_perm:[2,3,0,1] row_mask:0xf bank_mask:0xf"
      : "+v"(a), "+v"(b), "+v"(c), "+v"(d));
}
DI float sum_records(u32x4 v) {
  const float t = half16(half32(first2(v[0], v[1]), first2(v[2], v[3])), half32(second2(v[0], v[1]), second2(v[2], v[3])));
  return t + dpp<ROR8>(t);
}

DI void layernorm(u32 (&x)[8]) {
  float v[16], s = 0.f;
#pragma unroll
  for (int e = 0; e < 8; ++e) { v[2 * e] = bflo(x[e]); v[2 * e + 1] = bfhi(x[e]); s += v[2 * e] + v[2 * e + 1]; }
  const float mean = half_sum(s) * (1.f / 512.f);
  float q = 0.f;
#pragma unroll
  for (int e = 0; e < 16; ++e) { const float d = v[e] - mean; q = fmaf(d, d, q); }
  const float var = half_sum(q) * (1.f / 512.f);
  const float mb = rbf(mean), inv = __builtin_amdgcn_rcpf(rbf(__builtin_amdgcn_sqrtf(rbf(rbf(var) + 1e-5f))));
#pragma unroll
  for (int e = 0; e < 8; ++e) { const u32 d = pk(v[2 * e] - mb, v[2 * e + 1] - mb); x[e] = pk(bflo(d) * inv, bfhi(d) * inv); }
}

DI f32x2 tile_matvec(const u32x4 (&wv)[8], LAS unsigned char* xin, int g, int c) {
  const u32x4 xf = *(LAS u32x4*)(xin + ((c >> 2) & 1) * 1024 + ((c >> 3) * 32 + g * 8) * 2);
  f32x4 lo = {0.f, 0.f, 0.f, 0.f}, hi = lo;
#pragma unroll
  for (int d = 0; d < 4; ++d) {
    const u32x4 am = sel4((c & 3) == d, xf);
    lo = mfma(am, colfrag(wv, d), lo);
    hi = mfma(am, colfrag(wv, 4 + d), hi);
  }
  const bool valid = (g >> 1) == (c >> 3);
  float t[4];
#pragma unroll
  for (int e = 0; e < 4; ++e) { t[e] = half32(valid ? lo[e] : 0.f, valid ? hi[e] : 0.f); t[e] += dpp<ROR8>(t[e]); }
  return (c & 8) ? (f32x2){t[2], t[3]} : (f32x2){t[0], t[1]};
}

__global__ void __launch_bounds__(1024, 4)
fused_decode(const u16* x_, const u16* k_, const u16* v_, const u16* wq_, const u16* wo_, const u16* w1_, const u16* b1_,
             const u16* w2_, const u16* b2_, u16* out_, unsigned char* ws_) {
  __shared__ __attribute__((aligned(16))) unsigned char lds_raw[L_END];
  LAS unsigned char* lds = (LAS unsigned char*)lds_raw;
  asm volatile("" :: "s"(x_), "s"(k_), "s"(v_), "s"(wq_), "s"(wo_), "s"(w1_), "s"(b1_), "s"(w2_), "s"(b2_), "s"(out_), "s"(ws_));
  const int tid = threadIdx.x, lane = tid & 63, w = __builtin_amdgcn_readfirstlane(tid >> 6);
  const int g = lane >> 4, c = lane & 15;
  const int bid = blockIdx.x, kvh = bid & 7, j = bid >> 3, b = j >> 4, sp = j & 15, rb = j >> 2, cb = j & 3;
  const bool mlp1 = j < 14, mlp2 = j >= 14 && j < 28;
  const __amdgpu_buffer_rsrc_t ws = __builtin_amdgcn_make_buffer_rsrc(ws_, 0, WS_END, 0x00020000);
  u32 tag;
  const size_t kvseq = (size_t)(b * 8 + kvh) * SK * DH;
  const __amdgpu_buffer_rsrc_t kr = __builtin_amdgcn_make_buffer_rsrc((void*)(k_ + kvseq), 0, SK * DH * 2, 0x00020000);
  const __amdgpu_buffer_rsrc_t vr = __builtin_amdgcn_make_buffer_rsrc((void*)(v_ + kvseq), 0, SK * DH * 2, 0x00020000);
  const int kvoff = (sp * 16 + ((w + j) & 15)) * 8192;
  const int klane = (((c >> 1) & 1) * 4 + (c >> 2)) * 256 + (g * 2 + (c & 1)) * 16;
  u32x4 kf[8], vf[8];

  const int cq = j & 7, kq = j >> 3;
  const int mt = (mlp1 ? j : j - 14) * 8 + kvh;
  const int mhi = mt >> 2, mlo = mt & 3;
  if (w < 2 || (w < 4 && mlp1)) {
    __builtin_amdgcn_s_setprio(3);
    if (w == 0 && lane < 16) *(LAS u32*)(lds + L_FLAG + lane * 4) = 0u;
    const GAS u32x4* xp = (const GAS u32x4*)x_ + lane * 2;
    const u32x4 x0 = xp[0], x1 = xp[1];
    PIN();
    const int rs = w < 2 ? NQ / 8 : FF / 8, row0 = (w < 2 ? kq : mlo) * 128 + (w & 1) * 64;
    const u16* tile = w < 2 ? wq_ + kvh * 512 + cq * 64 : w1_ + mhi * 64;
    const GAS u32x4* wp = (const GAS u32x4*)tile + (row0 + (c >> 3) * 32 + g * 8) * rs + (c & 7);
    u32x4 wv[8];
#pragma unroll
    for (int i = 0; i < 8; ++i) wv[i] = ldnt(wp + i * rs);
    PIN();
    LDS_BARRIER();
    tag = read_tag(ws_);
    PIN();
    u32 xs[8] = {x0[0], x0[1], x0[2], x0[3], x1[0], x1[1], x1[2], x1[3]};
    layernorm(xs);
    if (w >= 2) layernorm(xs);
    LAS unsigned char* xl = lds + L_XN + w * 2048;
    *(LAS u32x4*)(xl + lane * 32) = (u32x4){xs[0], xs[1], xs[2], xs[3]};
    *(LAS u32x4*)(xl + lane * 32 + 16) = (u32x4){xs[4], xs[5], xs[6], xs[7]};
    f32x2 r = tile_matvec(wv, xl + row0 * 2, g, c);
    LAS f32x2* half = (LAS f32x2*)(lds + L_HALF + (w >> 1) * 512 + lane * 8);
    if (w & 1) {
      *half = r;
      set_flag(lds, F_HALF + (w >> 1));
    } else {
      wait_flags(lds, F_HALF + (w >> 1), 0, lane);
      r += *half;
      const int rec = w == 0 ? WS_QP + ((((g & 1) * 8 + kvh) * 8 + cq) * 4 + kq) * 256 : WS_PA + mt * 512 + (g & 1) * 256;
      stws1(ws, rec + ((c & 7) * 4 + (g >> 1) * 2 + (c >> 3)) * 8, pack2(r[0], r[1], tag));
    }
    __builtin_amdgcn_s_setprio(1);
  } else {
    if (w < 4 && mlp2) {
      const u16* p = w2_ + (mhi * 64 + (w & 1) * 32 + g * 8) * D + mlo * 128 + c * 8;
#pragma unroll
      for (int i = 0; i < 8; ++i) dma16(p + i * D, (u32)(size_t)(lds + L_W2 + (w & 1) * 8192 + i * 1024));
    }
    RAW_BARRIER();
    if ((w >> 2) == 1) __builtin_amdgcn_s_setprio(2); else __builtin_amdgcn_s_setprio(1);
  }
  PIN();

#pragma unroll
  for (int i = 0; i < 8; ++i) {
    kf[i] = __builtin_amdgcn_raw_buffer_load_b128(kr, klane + (i & 1) * 128, kvoff + (i >> 1) * 2048, NT);
    __builtin_amdgcn_sched_barrier(0);
  }
  if ((w >> 2) != 1) __builtin_amdgcn_s_setprio(0);
  PIN();
#pragma unroll
  for (int i = 0; i < 8; ++i) {
    vf[i] = __builtin_amdgcn_raw_buffer_load_b128(vr, lane * 16, kvoff + i * 1024, NT);
    __builtin_amdgcn_sched_barrier(0);
  }
  PIN();
  count_in(lds, L_CNTV, lane);
  tag = read_tag(ws_);
  const int paoff = WS_PA + mhi * 2048 + lane * 16;
  u32x4 p0, p1;
  u32 b1v;
  if ((w >> 2) == 1) {
    const int u = w & 3;
    const int qoff = WS_QP + (b * 8 + kvh) * 8192 + u * 2048 + lane * 16;
    asm volatile("s_waitcnt vmcnt(0)" ::: "memory");
    __builtin_amdgcn_s_sleep(NAP_Q);
    u32x4 q0, q1;
#pragma unroll 1
    for (int spin = 0; spin < SPIN_CAP; ++spin) {
      q0 = ldws(ws, qoff); q1 = ldws(ws, qoff + 1024);
      if (__all(tagged(q0, tag) && tagged(q1, tag))) break;
      __builtin_amdgcn_s_sleep(NAP_RETRY);
      asm volatile("" ::: "memory");
    }
    const float qa = half16(half32(first2(q0[0], q0[1]), first2(q1[0], q1[1])), half32(first2(q0[2], q0[3]), first2(q1[2], q1[3])));
    const float qb = half16(half32(second2(q0[0], q0[1]), second2(q1[0], q1[1])), half32(second2(q0[2], q0[3]), second2(q1[2], q1[3])));
    *(LAS u32*)(lds + L_Q + u * 256 + ((g >> 1) * 32 + c * 2 + (g & 1)) * 4) = pk(qa, qb);
    set_flag(lds, F_Q + u);
    if (w == 7 && mlp2) {
      p0 = ldws(ws, paoff); p1 = ldws(ws, paoff + 1024);
      b1v = *(const GAS u32*)(b1_ + mhi * 64 + c * 4 + (g >> 1) * 2);
      PIN();
    }
    __builtin_amdgcn_s_setprio(0);
  }
  wait_flags(lds, F_Q, 3, lane);

  float sc[8];
  {
    const int x0 = (c >> 2) & 1;
    const u32x4 q0 = *(LAS u32x4*)(lds + L_Q + (c & 3) * 256 + (g * 2 + x0) * 16);
    const u32x4 q1 = *(LAS u32x4*)(lds + L_Q + (c & 3) * 256 + 128 + (g * 2 + x0) * 16);
#pragma unroll
    for (int o = 0; o < 4; ++o) {
      f32x4 s = mfma(kf[2 * o], q0, (f32x4){0.f, 0.f, 0.f, 0.f});
      s = mfma(kf[2 * o + 1], q1, s);
#pragma unroll
      for (int a = 0; a < 2; ++a) {
        float e1 = s[2 * a + 1];
        asm("" : "+v"(e1));
        const float t = x0 ? e1 : s[2 * a];
        sc[2 * o + a] = t + dpp<ROR4>(t);
      }
      const u32 d = pk(sc[2 * o], sc[2 * o + 1]), e = pk(bflo(d) * 0.08838834764831845f, bfhi(d) * 0.08838834764831845f);
      sc[2 * o] = bflo(e); sc[2 * o + 1] = bfhi(e);
    }
  }
  PIN();

  float m = fmaxf(fmaxf(fmaxf(sc[0], sc[1]), fmaxf(sc[2], sc[3])), fmaxf(fmaxf(sc[4], sc[5]), fmaxf(sc[6], sc[7])));
  m = max32(max16(m)) * LOG2E;
  float p[8], l = 0.f;
#pragma unroll
  for (int e = 0; e < 8; ++e) { p[e] = __builtin_amdgcn_exp2f(fmaf(sc[e], LOG2E, -m)); l += p[e]; }
  l = half16(l, l); l = half32(l, l);
  if (lane < 4) { *(LAS float*)(lds + L_MW + (lane * 16 + w) * 4) = m; *(LAS float*)(lds + L_LW + (lane * 16 + w) * 4) = l; }
  PIN();
  if ((w >> 1) == 1) set_flag(lds, F_W2 + (w & 1));
  count_in(lds, L_CNT, lane);
  const u32x4 pf = {pk(p[0], p[1]), pk(p[2], p[3]), pk(p[4], p[5]), pk(p[6], p[7])};
  u32x4 am[4];
#pragma unroll
  for (int u = 0; u < 4; ++u) am[u] = sel4((c >> 2) == u, pf);
  PIN();
  u32x4 ve[4], vo[4];
  u32 slo, shi;
  asm volatile("s_mov_b32 %0, 0x05040100\n\ts_mov_b32 %1, 0x07060302" : "=s"(slo), "=s"(shi) : "v"(am[0][0]), "v"(am[1][1]), "v"(am[2][2]), "v"(am[3][3]));
#pragma unroll
  for (int r = 0; r < 4; ++r) {
#pragma unroll
    for (int u = 0; u < 4; ++u) {
      ve[u][r] = __builtin_amdgcn_perm(vf[2 * r + 1][u], vf[2 * r][u], slo);
      vo[u][r] = __builtin_amdgcn_perm(vf[2 * r + 1][u], vf[2 * r][u], shi);
    }
    if (r < 3)
      asm volatile("" : "+s"(slo), "+s"(shi) : "v"(ve[0][r]), "v"(ve[1][r]), "v"(ve[2][r]), "v"(ve[3][r]), "v"(vo[0][r]), "v"(vo[1][r]), "v"(vo[2][r]), "v"(vo[3][r]));
  }
  f32x4 oe = {0.f, 0.f, 0.f, 0.f}, oo = oe;
#pragma unroll
  for (int u = 0; u < 4; ++u) { oe = mfma(am[u], ve[u], oe); oo = mfma(am[u], vo[u], oo); }
#pragma unroll
  for (int h = 0; h < 4; ++h) *(LAS f32x2*)(lds + L_OW + w * OW_STRIDE + h * 512 + c * 32 + g * 8) = (f32x2){oe[h], oo[h]};
  PIN();

  const bool gath = (w & 4) == 0;
  const int v = (w & 3) + (w >> 3) * 4;
  const int cf = bid & 3, bf = (bid >> 2) & 1, seg = bid >> 3;
  if (w == 6) {
    wait_all(lds, L_CNTV);
    const unsigned char* tp = ws_ + WS_OP + ((kvh * 2 + b) * 16 + sp) * 2048 + c * 128;
    if (g == 1) tp = c < 8 ? ws_ + WS_S2 + (cb * 64 + kvh * 8 + rb) * 1024 + c * 128 : (const unsigned char*)(out_ + bf * D + cf * 128 + seg * 32);
    if (lane < (bid < NFIN ? 25 : 24)) touch(tp, (u32)(size_t)(lds + L_SINK));
  }
  if (w >= 12) {
    LDS_BARRIER();
    if (bid < NFIN) {
      const int row = (w - 12) * 8 + (lane >> 3);
      const int moff = WS_MP + (cf * 28 + min(row, 27)) * 1024 + bf * 512 + seg * 128 + (lane & 7) * 16;
      u32x4 mv;
#pragma unroll 1
      for (int spin = 0; spin < SPIN_CAP; ++spin) {
        mv = ldws(ws, moff);
        if (__all(tagged(mv, tag))) break;
        __builtin_amdgcn_s_sleep(NAP_RETRY);
        asm volatile("" ::: "memory");
      }
      const float t = sum_records(sel4(row < 28, mv));
      if ((lane & 8) == 0) *(LAS float*)(lds + L_MF + (w - 12) * 128 + ((lane & 7) * 4 + g) * 4) = t;
      LDS_BARRIER();
    }
  } else if (gath) {
    {
      const int f = v * 16 + (lane >> 2), p4 = lane & 3, hd = v >> 1;
      wait_all(lds, L_CNT);
      const f32x4 mv = *(LAS f32x4*)(lds + L_MW + hd * 64 + p4 * 16), lv = *(LAS f32x4*)(lds + L_LW + hd * 64 + p4 * 16);
      float M = fmaxf(fmaxf(mv[0], mv[1]), fmaxf(mv[2], mv[3]));
      M = fmaxf(M, dpp<XOR1>(M)); M = fmaxf(M, dpp<XOR2>(M));
      float L = 0.f, fw[4];
#pragma unroll
      for (int i = 0; i < 4; ++i) { fw[i] = __builtin_amdgcn_exp2f(mv[i] - M); L = fmaf(fw[i], lv[i], L); }
      L += dpp<XOR1>(L); L += dpp<XOR2>(L);
      const float inv = __builtin_amdgcn_rcpf(L);
#pragma unroll
      for (int i = 0; i < 4; ++i) fw[i] *= inv;
      const u32 lam = f2u(M + __builtin_amdgcn_logf(L)) >> ((lane >> 5) * 16);
      u32 t0 = tag | (lam & 255u), t1 = tag | ((lam >> 8) & 255u);
      int ooff = WS_OP + ((kvh * 2 + b) * 16 + sp) * 2048 + f * 16;
      int laddr = L_OW + p4 * 4 * OW_STRIDE + f * 16;
      asm volatile("" : "+v"(fw[0]), "+v"(fw[1]), "+v"(fw[2]), "+v"(fw[3]), "+v"(t0), "+v"(t1), "+v"(ooff), "+v"(laddr));
      LDS_BARRIER();
      f32x4 s = fw[0] * *(LAS f32x4*)(lds + laddr);
#pragma unroll
      for (int i = 1; i < 4; ++i) s += fw[i] * *(LAS f32x4*)(lds + laddr + i * OW_STRIDE);
      float s0 = s[0], s1 = s[1], s2 = s[2], s3 = s[3];
      quad_sum4(s0, s1, s2, s3);
      if (p4 == 0) __builtin_amdgcn_raw_buffer_store_b128((u32x4){pk(s0, s1), t0, pk(s2, s3), t1}, ws, ooff, 0, SC1);
    }
    PIN();
    {
      u32 gaddr2 = (u32)(size_t)(lds + L_GP + v * GP_STRIDE + 256), gaddr = gaddr2 - 256 + (c * 4 + g) * 4;
      u32 lsel = (c & 8) ? 0x05040100u : 0x01000504u;
      asm volatile("" : "+v"(gaddr), "+v"(gaddr2), "+v"(lsel));
      __builtin_amdgcn_s_sleep(NAP_O);
      const u32x4 ov = poll16(ws, WS_OP + (kvh * 32 + v * 4 + g) * 2048 + rb * 256 + c * 16, tag);
      u32 a, lam, m1, x0, x1, x2, x3;
      f32x2 gl;
      asm volatile("v_perm_b32 %[a], %[t1], %[t0], %[sel]\n\t"
                   "v_lshlrev_b32 %[x0], 16, %[d0]\n\t"
                   "v_lshlrev_b32 %[x2], 16, %[d1]\n\t"
                   "v_mov_b32_dpp %[m1], %[a] row_ror:8 row_mask:0xf bank_mask:0xf\n\t"
                   "v_and_b32 %[x1], 0xffff0000, %[d0]\n\t"
                   "v_perm_b32 %[lam], %[a], %[m1], %[lsel]\n\t"
                   "v_mov_b32 %[m1], %[lam]\n\t"
                   "v_mov_b32 %[a], %[lam]\n\t"
                   "v_and_b32 %[x3], 0xffff0000, %[d1]\n\t"
                   "s_nop 0\n\t"
                   "v_permlane16_swap_b32 %[m1], %[a]\n\t"
                   "v_max_f32 %[m1], %[m1], %[a]\n\t"
                   "v_mov_b32 %[a], %[m1]\n\t"
                   "s_nop 1\n\t"
                   "v_permlane32_swap_b32 %[m1], %[a]\n\t"
                   "v_max_f32 v104, %[m1], %[a]\n\t"
                   "v_sub_f32 %[lam], %[lam], v104\n\t"
                   "v_exp_f32 %[lam], %[lam]\n\t"
                   "s_nop 0\n\t"
                   "v_mul_f32 %[x0], %[lam], %[x0]\n\t"
                   "v_mul_f32 %[x2], %[lam], %[x2]\n\t"
                   "v_mul_f32 %[x1], %[lam], %[x1]\n\t"
                   "v_mul_f32 %[x3], %[lam], %[x3]\n\t"
                   "v_mov_b32 %[a], %[lam]\n\t"
                   "v_permlane32_swap_b32 %[x0], %[x2]\n\t"
                   "v_permlane32_swap_b32 %[x1], %[x3]\n\t"
                   "v_permlane16_swap_b32 %[lam], %[a]\n\t"
                   "v_add_f32 %[x0], %[x0], %[x2]\n\t"
                   "v_add_f32 %[x1], %[x1], %[x3]\n\t"
                   "v_add_f32 %[lam], %[lam], %[a]\n\t"
                   "v_mov_b32 %[a], %[lam]\n\t"
                   "v_permlane16_swap_b32 %[x0], %[x1]\n\t"
                   "v_add_f32 %[x0], %[x0], %[x1]\n\t"
                   "v_permlane32_swap_b32 %[lam], %[a]\n\t"
                   "ds_write_b32 %[ga], %[x0]\n\t"
                   "v_add_f32 v105, %[lam], %[a]\n\t"
                   "ds_write_b64 %[gb], v[104:105]"
                   : [a] "=&v"(a), [lam] "=&v"(lam), [m1] "=&v"(m1), [x0] "=&v"(x0), [x1] "=&v"(x1), [x2] "=&v"(x2), [x3] "=&v"(x3), "=&{v[104:105]}"(gl)
                   : [d0] "v"(ov[0]), [t0] "v"(ov[1]), [d1] "v"(ov[2]), [t1] "v"(ov[3]), [sel] "s"(0x04000400u), [lsel] "v"(lsel), [ga] "v"(gaddr), [gb] "v"(gaddr2)
                   : "memory");
    }
    LDS_BARRIER();
    if (bid < NFIN) {
      int faddr = L_FP + ((lane & 7) * 4 + g) * 32 + v * 4;
      asm volatile("" : "+v"(faddr));
      __builtin_amdgcn_s_sleep(NAP_S);
      const u32x4 sv = poll16(ws, WS_S2 + (cf * 64 + v * 8 + (lane >> 3)) * 1024 + bf * 512 + seg * 128 + (lane & 7) * 16, tag);
      *(LAS float*)(lds + faddr) = sum_records(sv);
      LDS_BARRIER();
    }
  } else if (w < 6) {
    u32x4 wt[8], bw[8];
    wait_all(lds, L_CNT);
    const GAS u16* wp = (const GAS u16*)wo_ + (size_t)(kvh * 512 + rb * 64 + g * 4) * D + cb * 128 + (w - 4) * 64 + c * 4;
#pragma unroll
    for (int i = 0; i < 8; ++i) {
      const u32x2 t0 = ldnt((const GAS u32x2*)(wp + (size_t)(32 * (i >> 2) + (i & 3)) * D));
      const u32x2 t1 = ldnt((const GAS u32x2*)(wp + (size_t)(32 * (i >> 2) + 16 + (i & 3)) * D));
      wt[i] = (u32x4){t0[0], t0[1], t1[0], t1[1]};
    }
    PIN();
    LDS_BARRIER();
#pragma unroll
    for (int i = 0; i < 8; ++i) bw[i] = colfrag(wt, i);
    int gp = L_GP + (c & 7) * GP_STRIDE, go = gp + ((c >> 3) * 16 + g * 4) * 4;
    int soff = WS_S2 + (cb * 64 + kvh * 8 + rb) * 1024 + (g & 1) * 512 + ((w - 4) * 32 + c * 2 + (g >> 1)) * 8;
    asm volatile("" : "+v"(gp), "+v"(go), "+v"(soff) : "v"(bw[0]), "v"(bw[1]), "v"(bw[2]), "v"(bw[3]), "v"(bw[4]), "v"(bw[5]), "v"(bw[6]), "v"(bw[7]));
    LDS_BARRIER();
    f32x2 gl = *(LAS f32x2*)(lds + gp + 256);
    u32x4 of = *(LAS u32x4*)(lds + go), o1 = *(LAS u32x4*)(lds + go + 128);
#define QP1(r) "v_add_f32_dpp " r ", " r ", " r " quad_perm:[1,0,3,2] row_mask:0xf bank_mask:0xf\n\t"
#define QP2(r) "v_add_f32_dpp " r ", " r ", " r " quad_perm:[2,3,0,1] row_mask:0xf bank_mask:0xf\n\t"
#define PKM(r) "v_pk_mul_f32 " r ", v[118:119], " r " op_sel_hi:[0,1]\n\t"
    asm volatile("s_nop 1\n\t"
                 "v_max_f32_dpp v118, v116, v116 quad_perm:[1,0,3,2] row_mask:0xf bank_mask:0xf\n\t"
                 "s_nop 1\n\t"
                 "v_max_f32_dpp v118, v118, v118 quad_perm:[2,3,0,1] row_mask:0xf bank_mask:0xf\n\t"
                 "v_sub_f32 v118, v116, v118\n\t"
                 "v_exp_f32 v118, v118\n\t"
                 "s_nop 0\n\t"
                 "v_mul_f32 v117, v118, v117\n\t"
                 PKM("v[108:109]") PKM("v[110:111]") PKM("v[112:113]") PKM("v[114:115]")
                 QP1("v117") QP1("v108") QP1("v109") QP1("v110") QP1("v111") QP1("v112") QP1("v113") QP1("v114") QP1("v115")
                 QP2("v117") QP2("v108") QP2("v109")
                 "v_rcp_f32 v118, v117\n\t"
                 QP2("v110") QP2("v111") QP2("v112") QP2("v113") QP2("v114") QP2("v115")
                 PKM("v[108:109]") PKM("v[110:111]") PKM("v[112:113]") PKM("v[114:115]")
                 "v_cvt_pk_bf16_f32 v108, v108, v109\n\t"
                 "v_cvt_pk_bf16_f32 v109, v110, v111\n\t"
                 "v_cvt_pk_bf16_f32 v110, v112, v113\n\t"
                 "v_cvt_pk_bf16_f32 v111, v114, v115\n\t"
                 "s_nop 1"
                 : "+{v[108:111]}"(of), "+{v[112:115]}"(o1), "+{v[116:117]}"(gl)
                 :
                 : "v118", "v119");
    f32x4 acc[8];
#pragma unroll
    for (int i = 0; i < 8; ++i) { const int k = (i & 3) * 2 + (i >> 2); acc[k] = mfma(of, bw[k], (f32x4){0.f, 0.f, 0.f, 0.f}); }
    __builtin_amdgcn_sched_barrier(0);
    float r[4];
#pragma unroll
    for (int d = 0; d < 4; ++d) r[d] = (g >> 1) ? acc[4 + d][0] : acc[d][0];
    stws1(ws, soff, pack2(half32(r[0], r[2]), half32(r[1], r[3]), tag));
  } else if (w == 7 && mlp2) {
#pragma unroll 1
    for (int spin = 0; spin < SPIN_CAP; ++spin) {
      if (__all(tagged(p0, tag) && tagged(p1, tag))) break;
      __builtin_amdgcn_s_sleep(NAP_RETRY);
      asm volatile("" ::: "memory");
      p0 = ldws(ws, paoff); p1 = ldws(ws, paoff + 1024);
    }
    const float t2[2] = {half32(first2(p0[0], p0[1]) + first2(p1[0], p1[1]), first2(p0[2], p0[3]) + first2(p1[2], p1[3])),
                         half32(second2(p0[0], p0[1]) + second2(p1[0], p1[1]), second2(p0[2], p0[3]) + second2(p1[2], p1[3]))};
    float hv[2];
#pragma unroll
    for (int e = 0; e < 2; ++e) {
      const float t = rbf(rbf(t2[e]) + (e ? bfhi(b1v) : bflo(b1v)));
      const float uu = 0.7978845608028654f * (t + 0.044715f * t * t * t);
      hv[e] = t / (1.f + __builtin_amdgcn_exp2f(-2.f * LOG2E * uu));
    }
    *(LAS u32*)(lds + L_HS + (g & 1) * 128 + (c * 2 + (g >> 1)) * 4) = pk(hv[0], hv[1]);
    wait_flags(lds, F_W2, 1, lane);
    f32x4 r = {0.f, 0.f, 0.f, 0.f};
#pragma unroll
    for (int s = 0; s < 2; ++s) {
      const u32x4 ha = *(LAS u32x4*)(lds + L_HS + ((c >> 2) & 1) * 128 + s * 64 + g * 16);
      u32x4 w2t[8];
#pragma unroll
      for (int i = 0; i < 8; ++i) w2t[i] = *(LAS u32x4*)(lds + L_W2 + s * 8192 + i * 1024 + lane * 16);
#pragma unroll
      for (int d = 0; d < 8; ++d) r = mfma(sel4((c >> 3) == (d >> 2) && (c & 3) == (d & 3), ha), colfrag(w2t, d), r);
    }
    stws(ws, WS_MP + (mlo * 28 + mhi) * 1024 + (g & 1) * 512 + (c * 2 + (g >> 1)) * 16, pack2(r[0], r[1], tag), pack2(r[2], r[3], tag));
    LDS_BARRIER();
  } else if (w == 6 && bid < NFIN) {
    const int col = cf * 128 + seg * 32 + (lane & 31);
    const u32 xv = ((const GAS u16*)x_)[bf * D + col], b2v = ((const GAS u16*)b2_)[col];
    LDS_BARRIER();
    LDS_BARRIER();
    LAS float* mf = (LAS float*)(lds + L_MF + (lane >> 5) * 256 + (lane & 31) * 4);
    float t = mf[0] + mf[32];
    t = half32(t, t);
    t = rbf(rbf(t) + bflo(b2v));
    float gate = rbf(t / (1.f + __builtin_amdgcn_exp2f(-LOG2E * t))), xf = bflo(xv);
    int faddr = L_FP + (lane & 31) * 32 + (lane >> 5) * 16;
    GAS u16* op = (GAS u16*)out_ + bf * D + col;
    asm volatile("" : "+v"(gate), "+v"(xf), "+v"(faddr), "+v"(op));
    LDS_BARRIER();
    const f32x4 fp = *(LAS f32x4*)(lds + faddr);
    float s = (fp[0] + fp[1]) + (fp[2] + fp[3]);
    s = half32(s, s);
    const float res = xf + rbf(rbf(s) * gate);
    if (lane < 32) __hip_atomic_store(op, (u16)pk(res, res), RLX_AGENT);
    if (bid == 0 && lane == 0) __hip_atomic_store((GAS u32*)(ws_ + WS_EPOCH), tag >> 8, RLX_AGENT);
  } else {
    LDS_BARRIER();
  }
}

extern "C" void kernel_launch(void* const* d_in, const int* in_sizes, int n_in, void* d_out, int out_size, void* d_ws, size_t ws_size,
                              hipStream_t stream) {
  fused_decode<<<256, 1024, 0, stream>>>((const u16*)d_in[0], (const u16*)d_in[1], (const u16*)d_in[2], (const u16*)d_in[3],
                                         (const u16*)d_in[4], (const u16*)d_in[6], (const u16*)d_in[7], (const u16*)d_in[8],
                                         (const u16*)d_in[9], (u16*)d_out, (unsigned char*)d_ws);
}
```

```cpp
#define STAMP 1
#define PROBE 12
#define PROBE_B 0
#define STMASK (1 | (1 << 12) | (1 << 0))
#define FINLAST 0
#define WALK 1
#include <hip/hip_runtime.h>
#include <stdint.h>

#define WS_AT_END 0
#define SLEEP_Q 8
#define SLEEP_O 8
#define SLEEP_S 8
#define WO_MODE 1
#define PRIO_K 1
#define ROT 1
#define CODE_LINES 0
#define RND 1
#define BUF 1
#define SPRE 0
#define TV 0
#define PRE_C 0
#define SLOTS 4
#define SLOT0 0
#define OSAUX 16
#define OPNT 0
#define PRE_D 25
#define PRE_E 0
#define B1NT 0
#define PROBE_WM 0xffff
#define WALKW 0
#define SCODE 0
#define SCODEW 15
#define NOBUMP 0
#define ENDWAIT 0
#define MSA 16
#define MLA 16
#define TSC " sc1"
#define Q_AT 0
#define YPRIO 0
#define LIN 0
#define PRE_AT 16u
#define WO_V 0
#define PRE_A 0
#define PRE_B 0
#define PREW 12
#define Q_DELAY 32
#define MP_LATE 0
#define SLEEP_M 4
#define WO_AT 1150
#define WO_SLEEP 0
#define ABL 0

#define GAS __attribute__((address_space(1)))
#define LAS __attribute__((address_space(3)))
#define DI __device__ __forceinline__
#define RLX_AGENT __ATOMIC_RELAXED, __HIP_MEMORY_SCOPE_AGENT
typedef unsigned short u16;
typedef unsigned u32;
typedef unsigned long long u64;
typedef u32 u32x2 __attribute__((ext_vector_type(2)));
typedef u32 u32x4 __attribute__((ext_vector_type(4)));
typedef float f32x2 __attribute__((ext_vector_type(2)));
typedef float f32x4 __attribute__((ext_vector_type(4)));
typedef __bf16 bf16x2 __attribute__((ext_vector_type(2)));
typedef __bf16 bf16x8 __attribute__((ext_vector_type(8)));

constexpr int D = 512, SK = 8192, DH = 128, FF = 1792, NQ = 4096;
constexpr int SC1 = 16, NT = 2;
constexpr int SPIN_CAP = 1 << 18;
constexpr int NFIN = 32;
constexpr float LOG2E = 1.4426950408889634f;

constexpr int WS_EPOCH = 0;
constexpr int WS_QP = 256;
constexpr int WS_PA = WS_QP + 512 * 256;
constexpr int WS_OP = WS_PA + 112 * 512;
constexpr int WS_S2 = WS_OP + 256 * 2048;
constexpr int WS_MP = WS_S2 + 256 * 1024;
constexpr int WS_END = WS_MP + 112 * 1024;
constexpr int WS_SPAN = 4 << 20;
constexpr int WS_STAMP = 8 << 20;

constexpr int F_Q = 0, F_HALF = 4, F_W2 = 6;
constexpr int L_FLAG = 0, L_CNT = 48, L_XN = 64, L_HALF = L_XN + 8192, L_Q = L_HALF + 1024, L_MW = L_Q + 1024, L_LW = L_MW + 256,
              L_HS = L_LW + 256, L_GP = L_HS + 256, GP_STRIDE = 272, L_FP = L_GP + 8 * GP_STRIDE, L_MF = L_FP + 1024,
              L_TOUCH = L_MF + 512, L_T2 = L_TOUCH + 256, L_W2 = L_T2 + 256, L_OW = L_W2 + 16384, OW_STRIDE = 2064, L_END = L_OW + 16 * OW_STRIDE;

DI float bflo(u32 v) { return __builtin_bit_cast(float, v << 16); }
DI float bfhi(u32 v) { return __builtin_bit_cast(float, v & 0xffff0000u); }
DI float rbf(float f) { return (float)(__bf16)f; }
DI u32 pk(float a, float b) { return __builtin_bit_cast(u32, __builtin_convertvector((f32x2){a, b}, bf16x2)); }
DI u32 f2u(float f) { return __builtin_bit_cast(u32, f); }
DI float u2f(u32 u) { return __builtin_bit_cast(float, u); }
DI f32x4 mfma(u32x4 a, u32x4 b, f32x4 c) {
  return __builtin_amdgcn_mfma_f32_16x16x32_bf16(__builtin_bit_cast(bf16x8, a), __builtin_bit_cast(bf16x8, b), c, 0, 0, 0);
}
template <int CTRL> DI float dpp(float x) { return u2f(__builtin_amdgcn_mov_dpp(f2u(x), CTRL, 0xf, 0xf, true)); }
constexpr int XOR1 = 0xB1, XOR2 = 0x4E, ROR4 = 0x124, ROR8 = 0x128;
DI float half32(float a, float b) { auto r = __builtin_amdgcn_permlane32_swap(f2u(a), f2u(b), false, false); return u2f(r[0]) + u2f(r[1]); }
DI float half16(float a, float b) { auto r = __builtin_amdgcn_permlane16_swap(f2u(a), f2u(b), false, false); return u2f(r[0]) + u2f(r[1]); }
DI float max32(float a) { auto r = __builtin_amdgcn_permlane32_swap(f2u(a), f2u(a), false, false); return __builtin_amdgcn_fmed3f(u2f(r[0]), u2f(r[1]), __builtin_inff()); }
DI float max16(float a) { auto r = __builtin_amdgcn_permlane16_swap(f2u(a), f2u(a), false, false); return __builtin_amdgcn_fmed3f(u2f(r[0]), u2f(r[1]), __builtin_inff()); }
DI float half_sum(float v) {
  v += dpp<XOR1>(v); v += dpp<XOR2>(v); v += dpp<ROR4>(v); v += dpp<ROR8>(v);
  return half16(v, v);
}
DI u32 plo(u32 k1, u32 k0) { return __builtin_amdgcn_perm(k1, k0, 0x05040100u); }
DI u32 phi(u32 k1, u32 k0) { return __builtin_amdgcn_perm(k1, k0, 0x07060302u); }
DI u32x4 colfrag(const u32x4 (&r)[8], int d) {
  const int q = d >> 1;
  return (d & 1) ? (u32x4){phi(r[1][q], r[0][q]), phi(r[3][q], r[2][q]), phi(r[5][q], r[4][q]), phi(r[7][q], r[6][q])}
                 : (u32x4){plo(r[1][q], r[0][q]), plo(r[3][q], r[2][q]), plo(r[5][q], r[4][q]), plo(r[7][q], r[6][q])};
}
DI u32x4 sel4(bool c, u32x4 v) { return (u32x4){c ? v[0] : 0u, c ? v[1] : 0u, c ? v[2] : 0u, c ? v[3] : 0u}; }
template <typename T> DI T ldnt(const GAS T* p) { return __builtin_nontemporal_load(p); }
#define LAUX 16
#define SAUX 16
DI u32x4 ldws(__amdgpu_buffer_rsrc_t ws, int off) { return __builtin_amdgcn_raw_buffer_load_b128(ws, off, 0, LAUX); }
DI void stws(__amdgpu_buffer_rsrc_t ws, int off, u32x2 a, u32x2 b) { __builtin_amdgcn_raw_buffer_store_b128((u32x4){a[0], a[1], b[0], b[1]}, ws, off, 0, SAUX); }
DI void stws1(__amdgpu_buffer_rsrc_t ws, int off, u32x2 a) { __builtin_amdgcn_raw_buffer_store_b64(a, ws, off, 0, SAUX); }
DI u32x2 pack2(float a, float b, u32 tag8) {
  const u32 ar = f2u(a) + 0x800u;
  const u32 u = (f2u(b) & 0xf000u) | ((ar >> 4) & ~0xf000u);
  return (u32x2){phi(f2u(b), ar), __builtin_amdgcn_perm(tag8, u, 0x07060501u)};
}
DI float first2(u32 w0, u32 w1) { return u2f(__builtin_amdgcn_perm(w0, w1 << 4, 0x0504000cu)); }
DI float second2(u32 w0, u32 w1) { return u2f(__builtin_amdgcn_perm(w0, w1, 0x0706000cu)); }
DI bool tagged(u32x4 v, u32 tag8) { return ((v[1] ^ tag8) | (v[3] ^ tag8)) < 256u; }
#define PIN() do { asm volatile("" ::: "memory"); __builtin_amdgcn_sched_barrier(0); } while (0)
#define LDS_BARRIER() do { asm volatile("s_waitcnt lgkmcnt(0)\n\ts_barrier" ::: "memory"); __builtin_amdgcn_sched_barrier(0); } while (0)
#define RAW_BARRIER() do { __builtin_amdgcn_sched_barrier(0); asm volatile("s_barrier" ::: "memory"); __builtin_amdgcn_sched_barrier(0); } while (0)
DI void dma16(const u16* src, u32 dst) {
  u32 keep;
  asm volatile("s_mov_b32 %0, m0\n\ts_mov_b32 m0, %2\n\ts_nop 0\n\tglobal_load_lds_dwordx4 %1, off nt\n\ts_mov_b32 m0, %0"
               : "=&s"(keep) : "v"(src), "s"(dst) : "memory");
}
DI void touch(const void* src, u32 dst) {
  u32 keep;
  asm volatile("s_mov_b32 %0, m0\n\ts_mov_b32 m0, %2\n\ts_nop 0\n\tglobal_load_lds_dword %1, off" TSC "\n\ts_mov_b32 m0, %0"
               : "=&s"(keep) : "v"(src), "s"(dst) : "memory");
}
DI void touch_nt(const void* src, u32 dst) {
  u32 keep;
  asm volatile("s_mov_b32 %0, m0\n\ts_mov_b32 m0, %2\n\ts_nop 0\n\tglobal_load_lds_dword %1, off nt\n\ts_mov_b32 m0, %0"
               : "=&s"(keep) : "v"(src), "s"(dst) : "memory");
}
DI void stouch8(const void* p) {
  u32 t;
  asm volatile("s_load_dword %0, %1, 0x0\n\ts_load_dword %0, %1, 0x80\n\ts_load_dword %0, %1, 0x100\n\ts_load_dword %0, %1, 0x180\n\t"
               "s_load_dword %0, %1, 0x200\n\ts_load_dword %0, %1, 0x280\n\ts_load_dword %0, %1, 0x300\n\ts_load_dword %0, %1, 0x380\n\t"
               "s_waitcnt lgkmcnt(0)" : "=&s"(t) : "s"(p) : "memory");
}
DI void stouch2x2(const void* p, const void* q) {
  u32 t;
  asm volatile("s_load_dword %0, %1, 0x0\n\ts_load_dword %0, %1, 0x80\n\ts_load_dword %0, %2, 0x0\n\ts_load_dword %0, %2, 0x80\n\t"
               "s_waitcnt lgkmcnt(0)" : "=&s"(t) : "s"(p), "s"(q) : "memory");
}
DI void set_flag(LAS unsigned char* lds, int f) {
  asm volatile("s_waitcnt lgkmcnt(0)" ::: "memory");
  *(volatile LAS u32*)(lds + L_FLAG + f * 4) = 1u;
}
DI void wait_flags(LAS unsigned char* lds, int first, int mask, int lane) {
#pragma unroll 1
  for (int spin = 0; spin < (SPIN_CAP << 4); ++spin) {
    if (__all(*(volatile LAS u32*)(lds + L_FLAG + (first + (lane & mask)) * 4) != 0u)) break;
    __builtin_amdgcn_s_sleep(1);
  }
  PIN();
}
DI u32 read_tag(const unsigned char* ws) {
  u32 e;
  asm volatile("s_load_dword %0, %1, 0x0\n\ts_waitcnt lgkmcnt(0)" : "=s"(e) : "s"(ws) : "memory");
  return (e + 1u) << 8;
}
template <int LOW, int PNT = 0> DI u32x4 poll16(__amdgpu_buffer_rsrc_t ws, int off, u32 want) {
  u32x4 v;
  u64 keep;
  u32 left = SPIN_CAP, t;
  asm volatile("s_mov_b64 %[keep], exec\n\t"
               "s_branch 2f\n"
               "1:\n\t"
               "s_sub_u32 %[left], %[left], 1\n\t"
               "s_cbranch_scc1 3f\n"
               "2:\n\t"
               ".if %[pnt]\n\tbuffer_load_dwordx4 v[100:103], %[off], %[ws], 0 offen sc1 nt\n\t.else\n\t"
               "buffer_load_dwordx4 v[100:103], %[off], %[ws], 0 offen sc1\n\t.endif\n\t"
               "s_waitcnt vmcnt(0)\n\t"
               "v_bitop3_b32 %[t], v101, v103, %[want] bitop3:0x7e\n\t"
               "v_cmp_lt_u32 vcc, %[low], %[t]\n\t"
               "s_and_b64 exec, exec, vcc\n\t"
               "s_cbranch_scc1 1b\n"
               "3:\n\t"
               "s_mov_b64 exec, %[keep]"
               : "={v[100:103]}"(v), [keep] "=&s"(keep), [left] "+s"(left), [t] "=&v"(t)
               : [off] "v"(off), [ws] "s"(ws), [want] "s"(want), [low] "i"(LOW), [pnt] "i"(PNT)
               : "vcc", "scc", "memory");
  return v;
}
DI void quad_sum4(float& a, float& b, float& c, float& d) {
  asm("s_nop 1\n\t"
      "v_add_f32_dpp %0, %0, %0 quad_perm:[1,0,3,2] row_mask:0xf bank_mask:0xf\n\t"
      "v_add_f32_dpp %1, %1, %1 quad_perm:[1,0,3,2] row_mask:0xf bank_mask:0xf\n\t"
      "v_add_f32_dpp %2, %2, %2 quad_perm:[1,0,3,2] row_mask:0xf bank_mask:0xf\n\t"
      "v_add_f32_dpp %3, %3, %3 quad_perm:[1,0,3,2] row_mask:0xf bank_mask:0xf\n\t"
      "v_add_f32_dpp %0, %0, %0 quad_perm:[2,3,0,1] row_mask:0xf bank_mask:0xf\n\t"
      "v_add_f32_dpp %1, %1, %1 quad_perm:[2,3,0,1] row_mask:0xf bank_mask:0xf\n\t"
      "v_add_f32_dpp %2, %2, %2 quad_perm:[2,3,0,1] row_mask:0xf bank_mask:0xf\n\t"
      "v_add_f32_dpp %3, %3, %3 quad_perm:[2,3,0,1] row_mask:0xf bank_mask:0xf"
      : "+v"(a), "+v"(b), "+v"(c), "+v"(d));
}
DI float sum_records(u32x4 v) {
  const float t = half16(half32(first2(v[0], v[1]), first2(v[2], v[3])), half32(second2(v[0], v[1]), second2(v[2], v[3])));
  return t + dpp<ROR8>(t);
}

DI int pairpos(int c, int e) { return ((2 * c + e) & 7) * 4 + (c >> 3) * 2 + ((c >> 2) & 1); }

DI void layernorm(u32 (&x)[8]) {
  float v[16], s = 0.f;
#pragma unroll
  for (int e = 0; e < 8; ++e) { v[2 * e] = bflo(x[e]); v[2 * e + 1] = bfhi(x[e]); s += v[2 * e] + v[2 * e + 1]; }
  const float mean = half_sum(s) * (1.f / 512.f);
  float q = 0.f;
#pragma unroll
  for (int e = 0; e < 16; ++e) { const float d = v[e] - mean; q = fmaf(d, d, q); }
  const float var = half_sum(q) * (1.f / 512.f);
  const float mb = rbf(mean), inv = __builtin_amdgcn_rcpf(rbf(__builtin_amdgcn_sqrtf(rbf(rbf(var) + 1e-5f))));
#pragma unroll
  for (int e = 0; e < 8; ++e) { const u32 d = pk(v[2 * e] - mb, v[2 * e + 1] - mb); x[e] = pk(bflo(d) * inv, bfhi(d) * inv); }
}

DI f32x2 tile_matvec(const u32x4 (&wv)[8], LAS unsigned char* xin, int g, int c) {
  const u32x4 xf = *(LAS u32x4*)(xin + ((c >> 2) & 1) * 1024 + ((c >> 3) * 32 + g * 8) * 2);
  f32x4 lo = {0.f, 0.f, 0.f, 0.f}, hi = lo;
#pragma unroll
  for (int d = 0; d < 4; ++d) {
    const u32x4 am = sel4((c & 3) == d, xf);
    lo = mfma(am, colfrag(wv, d), lo);
    hi = mfma(am, colfrag(wv, 4 + d), hi);
  }
  const bool valid = (g >> 1) == (c >> 3);
  float t[4];
#pragma unroll
  for (int e = 0; e < 4; ++e) { t[e] = half32(valid ? lo[e] : 0.f, valid ? hi[e] : 0.f); t[e] += dpp<ROR8>(t[e]); }
  return (c & 8) ? (f32x2){t[2], t[3]} : (f32x2){t[0], t[1]};
}

#define ST(i) do { if (!((STMASK >> (i)) & 1)) break; __builtin_amdgcn_sched_barrier(0); ts[i] = __builtin_amdgcn_s_memrealtime(); __builtin_amdgcn_s_waitcnt(0xC07F); __builtin_amdgcn_sched_barrier(0); } while (0)

extern "C" __global__ void __launch_bounds__(1024, 4)
fused_decode(const u16* x_, const u16* k_, const u16* v_, const u16* wq_, const u16* wo_, const u16* w1_, const u16* b1_,
             const u16* w2_, const u16* b2_, u16* out_, unsigned char* ws_
             ) {
  __shared__ __attribute__((aligned(16))) unsigned char lds_raw[L_END];
  LAS unsigned char* lds = (LAS unsigned char*)lds_raw;
  u64 ts[15];
  for (int i = 0; i < 15; ++i) ts[i] = 0;
  ST(0);
  const u64 t_start = WO_MODE == 3 ? __builtin_amdgcn_s_memrealtime() : 0;
  asm volatile("" :: "s"(x_), "s"(k_), "s"(v_), "s"(wq_), "s"(wo_), "s"(w1_), "s"(b1_), "s"(w2_), "s"(b2_), "s"(out_), "s"(ws_));
  const int tid = threadIdx.x, lane = tid & 63, w = __builtin_amdgcn_readfirstlane(tid >> 6);
  const int g = lane >> 4, c = lane & 15;
  const int bid = blockIdx.x, kvh = bid & 7, j = bid >> 3, b = j >> 4, sp = j & 15, rb = j >> 2, cb = j & 3;
  const bool mlp1 = j < 14, mlp2 = j >= 14 && j < 28;
  const __amdgpu_buffer_rsrc_t ws = __builtin_amdgcn_make_buffer_rsrc(ws_, 0, WS_END, 0x00020000);
  u32 tag;
  const u32 hsh = ((u32)(bid * 16 + w) * 2654435761u) >> 20, hb = ((u32)bid * 2654435761u) >> 16;
  const int wu = (w + (ROT == 0 ? 0 : ROT == 1 || ROT == 2 || ROT == 3 ? j : (int)hb)) & 15;
  const int ro = ROT == 2 ? kvh : ROT == 3 || ROT == 4 ? (int)(hsh & 7) : 0;
  const size_t kvseq = (size_t)(b * 8 + kvh) * SK * DH;
  const __amdgpu_buffer_rsrc_t kr = __builtin_amdgcn_make_buffer_rsrc((void*)(k_ + kvseq), 0, SK * DH * 2, 0x00020000);
  const __amdgpu_buffer_rsrc_t vr = __builtin_amdgcn_make_buffer_rsrc((void*)(v_ + kvseq), 0, SK * DH * 2, 0x00020000);
  const int kvoff = (sp * 16 + wu) * 8192;
  const int klane = ((((c >> 1) ^ ro) & 1) * 4 + (c >> 2)) * 256 + (g * 2 + (c & 1)) * 16;
  u32x4 kf[8], vf[8];

  const int cq = j & 7, kq = j >> 3;
  const int mt = (mlp1 ? j : j - 14) * 8 + kvh;
  const int mhi = mt >> 2, mlo = mt & 3;
  auto pretouch = [&](int what) __attribute__((always_inline)) {
    const unsigned char* p = ws_ + WS_OP + ((kvh * 2 + b) * 16 + sp) * 2048 + c * 128;
    bool on = lane < 16 && (what & 1);
    if ((lane >> 3) == 2) { p = ws_ + WS_S2 + (cb * 64 + kvh * 8 + rb) * 1024 + (lane & 7) * 128; on = what & 16; }
    if ((lane >> 3) == 3) { p = ws_ + WS_MP + (mlo * 28 + mhi) * 1024 + (lane & 7) * 128; on = mlp2 && (what & 2); }
    if ((lane >> 2) == 8) { p = ws_ + WS_QP + ((((lane >> 1) & 1) * 8 + kvh) * 32 + cq * 4 + kq) * 256 + (lane & 1) * 128; on = what & 4; }
    if ((lane >> 2) == 9) { p = ws_ + WS_PA + mt * 512 + (lane & 3) * 128; on = mlp1 && (what & 4); }
    if (lane == 40) { p = (const unsigned char*)(out_ + ((bid >> 2) & 1) * D + (bid & 3) * 128 + ((bid >> 3) & 3) * 32); on = bid < NFIN && (what & 8); }
    if (lane == 41) { p = ws_; on = bid == 0 && (what & 32); }
    if (TV == 1 || TV == 3) on = on && (lane & 15) == 0;
    if (TV >= 2) { if (on) touch_nt(p, (u32)(size_t)(lds + L_T2)); } else
    if (on) touch(p, (u32)(size_t)(lds + L_T2));
  };
  if (w < 2 || (w < 4 && mlp1)) {
    __builtin_amdgcn_s_setprio(3);
    if (w == 0 && lane < 16) *(LAS u32*)(lds + L_FLAG + lane * 4) = 0u;
    const GAS u32x4* xp = (const GAS u32x4*)x_ + lane * 2;
    const u32x4 x0 = xp[0], x1 = xp[1];
    PIN();
    ST(14);
    const int rs = w < 2 ? NQ / 8 : FF / 8, row0 = (w < 2 ? kq : mlo) * 128 + (w & 1) * 64;
    const u16* tile = w < 2 ? wq_ + kvh * 512 + cq * 64 : w1_ + mhi * 64;
    const GAS u32x4* wp = (const GAS u32x4*)tile + (row0 + (c >> 3) * 32 + g * 8) * rs + (c & 7);
    u32x4 wv[8];
#pragma unroll
    for (int i = 0; i < 8; ++i) wv[i] = ldnt(wp + i * rs);
    PIN();
    LDS_BARRIER();
    ST(12);
    tag = read_tag(ws_);
    PIN();
    u32 xs[8] = {x0[0], x0[1], x0[2], x0[3], x1[0], x1[1], x1[2], x1[3]};
    layernorm(xs);
    if (w >= 2) layernorm(xs);
    LAS unsigned char* xl = lds + L_XN + w * 2048;
    *(LAS u32x4*)(xl + lane * 32) = (u32x4){xs[0], xs[1], xs[2], xs[3]};
    *(LAS u32x4*)(xl + lane * 32 + 16) = (u32x4){xs[4], xs[5], xs[6], xs[7]};
    f32x2 r = tile_matvec(wv, xl + row0 * 2, g, c);
    LAS f32x2* half = (LAS f32x2*)(lds + L_HALF + (w >> 1) * 512 + lane * 8);
    if (w & 1) {
      *half = r;
      set_flag(lds, F_HALF + (w >> 1));
    } else {
      wait_flags(lds, F_HALF + (w >> 1), 0, lane);
      r += *half;
      const int rec = w == 0 ? WS_QP + ((((g & 1) * 8 + kvh) * 8 + cq) * 4 + kq) * 256 : WS_PA + mt * 512 + (g & 1) * 256;
      if (!(ABL & 4)) __builtin_amdgcn_raw_buffer_store_b64(pack2(r[0], r[1], tag), ws, rec + (LIN ? (g >> 1) * 16 + c : (c & 7) * 4 + (g >> 1) * 2 + (c >> 3)) * 8, 0, MSA); else asm volatile("" :: "v"(r), "v"(rec));
    }
    ST(2);
    __builtin_amdgcn_s_setprio(PRIO_K);
  } else {
    if (w < 4 && mlp2) {
      const u16* p = w2_ + (mhi * 64 + (w & 1) * 32 + g * 8) * D + mlo * 128 + c * 8;
#pragma unroll
      for (int i = 0; i < 8; ++i) dma16(p + i * D, (u32)(size_t)(lds + L_W2 + (w & 1) * 8192 + i * 1024));
    }
    if (CODE_LINES && w == 4) {
      const unsigned char* pc;
      asm volatile("s_getpc_b64 %0" : "=s"(pc));
      if (lane < 4 && j * 4 + lane < CODE_LINES) touch(pc + (j * 4 + lane) * 128, (u32)(size_t)(lds + L_TOUCH));
    }
    if (WALK && w == 4) {
      const u32 gq = j >> 2, q = j & 3, xo = kvh * 128, mo = q * 458752 + xo, ko = (((q >> 1) * 8 + kvh) << 21) + ((q & 1) << 20);
      u64 base = (u64)wq_;
      u32 off = (q << 20) + kvh * 1024;
#define PICK(KEY, N, B, O) asm volatile("s_cmp_eq_u32 %2, " #N "\n\ts_cselect_b64 %0, %3, %0\n\ts_cselect_b32 %1, %4, %1" : "+s"(base), "+s"(off) : "s"(KEY), "s"((u64)(B)), "s"((u32)(O)) : "scc")
      PICK(gq, 1, w1_, mo); PICK(gq, 2, w2_, mo); PICK(gq, 3, k_, ko); PICK(gq, 4, v_, ko); PICK(gq, 6, wo_, kvh * 524288 + q * 131072);
      PICK(gq, 7, w1_, mo + 229376); PICK(j, 20, x_, xo); PICK(j, 21, b1_, xo); PICK(j, 22, b2_, xo); PICK(j, 23, ws_, xo + WS_END);
      touch_nt((const unsigned char*)(base + off), (u32)(size_t)(lds + L_T2));
    }
    if (PRE_A && w == PREW) pretouch(PRE_A);
    if (SPRE & 16) RAW_BARRIER();
    if ((SPRE & 1) && (w >> 1) == 6) stouch8(ws_ + WS_OP + ((kvh * 2 + b) * 16 + sp) * 2048 + (w & 1) * 1024);
    if ((SPRE & 1) && w == 14) stouch8(ws_ + WS_S2 + (cb * 64 + kvh * 8 + rb) * 1024);
    if ((SPRE & 2) && w == 15 && mlp2) stouch8(ws_ + WS_MP + (mlo * 28 + mhi) * 1024);
    if ((SPRE & 4) && w == 11) { const unsigned char* q = ws_ + WS_QP + (kvh * 32 + cq * 4 + kq) * 256; stouch2x2(q, q + 65536); }
    if ((SPRE & 4) && w == 10 && mlp1) { const unsigned char* q = ws_ + WS_PA + mt * 512; stouch2x2(q, q + 256); }
    if ((SPRE & 8) && w == 9 && bid < NFIN) { const u16* q = out_ + ((bid >> 2) & 1) * D + (bid & 3) * 128; stouch2x2(q, q); }
    if (SCODE && w == SCODEW) {
      u32 t;
      const u32 off = min(j * 512, SCODE - 512);
      __builtin_amdgcn_sched_barrier(0);
      asm volatile("s_getpc_b64 s[92:93]\n"
                   "1:\n\t"
                   "s_sub_u32 s92, s92, 1b - fused_decode\n\t"
                   "s_subb_u32 s93, s93, 0\n\t"
                   "s_add_u32 s92, s92, %1\n\t"
                   "s_addc_u32 s93, s93, 0\n\t"
                   "s_load_dword %0, s[92:93], 0x0\n\ts_load_dword %0, s[92:93], 0x40\n\ts_load_dword %0, s[92:93], 0x80\n\ts_load_dword %0, s[92:93], 0xc0\n\t"
                   "s_load_dword %0, s[92:93], 0x100\n\ts_load_dword %0, s[92:93], 0x140\n\ts_load_dword %0, s[92:93], 0x180\n\ts_load_dword %0, s[92:93], 0x1c0\n\t"
                   "s_barrier\n\t"
                   "s_waitcnt lgkmcnt(0)"
                   : "=&s"(t) : "s"(off) : "s92", "s93", "scc", "memory");
      __builtin_amdgcn_sched_barrier(0);
    } else
    if (!(SPRE & 16)) RAW_BARRIER();
    if (PRE_B && w == PREW + 1) pretouch(PRE_B);
    if ((w >> 2) == 1) __builtin_amdgcn_s_setprio(2); else __builtin_amdgcn_s_setprio(PRIO_K);
  }
  PIN();

  const u64 t_bar = Q_AT ? __builtin_amdgcn_s_memrealtime() : 0;
  const int slot = SLOTS == 1 ? SLOT0 : SLOTS == 2 ? SLOT0 + (j & 1) : j & 3;
  if (PRE_C && slot < 2 && w == 9 + slot * 4) pretouch(PRE_C);
#pragma unroll
  for (int i = 0; i < 8; ++i) { kf[i] = BUF ? __builtin_amdgcn_raw_buffer_load_b128(kr, klane + (i & 1) * 128, kvoff + ((i >> 1) ^ (ro >> 1)) * 2048, NT) : ldnt((const GAS u32x4*)((const GAS char*)(k_ + kvseq) + kvoff + klane + (i & 1) * 128 + (i >> 1) * 2048)); __builtin_amdgcn_sched_barrier(0); if (i == 0) ST(13); }
  if ((w >> 2) != 1) __builtin_amdgcn_s_setprio(0);
  if (PRE_C && slot >= 2 && w == 1 + (slot - 2) * 8) pretouch(PRE_C);
  PIN();
#pragma unroll
  for (int i = 0; i < 8; ++i) { vf[i] = BUF ? __builtin_amdgcn_raw_buffer_load_b128(vr, lane * 16, kvoff + (i ^ ro) * 1024, NT) : ldnt((const GAS u32x4*)((const GAS char*)(v_ + kvseq) + kvoff + lane * 16 + i * 1024)); __builtin_amdgcn_sched_barrier(0); }
  PIN();
  if ((PRE_D || WO_V) && lane == 0) __hip_atomic_fetch_add((LAS u32*)(lds + L_CNT + 4), 1u, __ATOMIC_RELAXED, __HIP_MEMORY_SCOPE_WORKGROUP);
  if (PRE_E && w == 13) pretouch(PRE_E);
  ST(4);
  tag = read_tag(ws_);
  const int paoff = WS_PA + mhi * 2048 + lane * 16;
  u32x4 p0, p1;
  u32 b1v;
  if ((w >> 2) == 1) {
    const int u = w & 3;
    const int qoff = WS_QP + (b * 8 + kvh) * 8192 + u * 2048 + lane * 16;
    if (Q_AT) {
      while (__builtin_amdgcn_s_memrealtime() < t_bar + Q_AT) __builtin_amdgcn_s_sleep(2);
    } else {
    asm volatile("s_waitcnt vmcnt(0)" ::: "memory");
    if (Q_DELAY) __builtin_amdgcn_s_sleep(Q_DELAY);
    }
    u32x4 q0, q1;
#pragma unroll 1
    for (int spin = 0; spin < SPIN_CAP; ++spin) {
      q0 = __builtin_amdgcn_raw_buffer_load_b128(ws, qoff, 0, MLA); q1 = __builtin_amdgcn_raw_buffer_load_b128(ws, qoff + 1024, 0, MLA);
      if ((ABL & 4) || __all(tagged(q0, tag) && tagged(q1, tag))) break;
      __builtin_amdgcn_s_sleep(SLEEP_Q);
      asm volatile("" ::: "memory");
    }
    const float qa = half16(half32(first2(q0[0], q0[1]), first2(q1[0], q1[1])), half32(first2(q0[2], q0[3]), first2(q1[2], q1[3])));
    const float qb = half16(half32(second2(q0[0], q0[1]), second2(q1[0], q1[1])), half32(second2(q0[2], q0[3]), second2(q1[2], q1[3])));
    *(LAS u32*)(lds + L_Q + u * 256 + ((g >> 1) * 32 + (LIN ? pairpos(c, g & 1) : c * 2 + (g & 1))) * 4) = pk(qa, qb);
    set_flag(lds, F_Q + u);
    ST(3);
    if (w == 7 && mlp2) {
      p0 = ldws(ws, paoff); p1 = ldws(ws, paoff + 1024);
      b1v = B1NT ? ldnt((const GAS u32*)(b1_ + mhi * 64 + (LIN ? pairpos(c, g >> 1) * 2 : c * 4 + (g >> 1) * 2))) : *(const GAS u32*)(b1_ + mhi * 64 + (LIN ? pairpos(c, g >> 1) * 2 : c * 4 + (g >> 1) * 2));
      PIN();
    }
    __builtin_amdgcn_s_setprio(0);
  }
  wait_flags(lds, F_Q, 3, lane);
  if (YPRIO) { if (w >= 12) __builtin_amdgcn_s_setprio(3); else if (w >= 8) __builtin_amdgcn_s_setprio(2); else if (w < 4) __builtin_amdgcn_s_setprio(1); }

  float sc[8];
  {
    const int x0 = (c >> 2) & 1;
    const u32x4 q0 = *(LAS u32x4*)(lds + L_Q + (c & 3) * 256 + (g * 2 + x0) * 16);
    const u32x4 q1 = *(LAS u32x4*)(lds + L_Q + (c & 3) * 256 + 128 + (g * 2 + x0) * 16);
#pragma unroll
    for (int o = 0; o < 4; ++o) {
      f32x4 s = mfma(kf[2 * o], q0, (f32x4){0.f, 0.f, 0.f, 0.f});
      s = mfma(kf[2 * o + 1], q1, s);
#pragma unroll
      for (int a = 0; a < 2; ++a) {
        float e1 = s[2 * a + 1];
        asm("" : "+v"(e1));
        const float t = x0 ? e1 : s[2 * a];
        sc[2 * o + a] = t + dpp<ROR4>(t);
      }
      const u32 d = pk(sc[2 * o], sc[2 * o + 1]), e = pk(bflo(d) * 0.08838834764831845f, bfhi(d) * 0.08838834764831845f);
      sc[2 * o] = bflo(e); sc[2 * o + 1] = bfhi(e);
    }
  }
  PIN();

    float m = fmaxf(fmaxf(fmaxf(sc[0], sc[1]), fmaxf(sc[2], sc[3])), fmaxf(fmaxf(sc[4], sc[5]), fmaxf(sc[6], sc[7])));
    m = max32(max16(m)) * LOG2E;
    float p[8], l = 0.f;
#pragma unroll
    for (int e = 0; e < 8; ++e) { p[e] = __builtin_amdgcn_exp2f(fmaf(sc[e], LOG2E, -m)); l += p[e]; }
    l = half16(l, l); l = half32(l, l);
    if (lane < 4) { *(LAS float*)(lds + L_MW + (lane * 16 + w) * 4) = m; *(LAS float*)(lds + L_LW + (lane * 16 + w) * 4) = l; }
    PIN();
    if ((w >> 1) == 1) set_flag(lds, F_W2 + (w & 1));
    if (lane == 0) __hip_atomic_fetch_add((LAS u32*)(lds + L_CNT), 1u, __ATOMIC_RELAXED, __HIP_MEMORY_SCOPE_WORKGROUP);
    const u32x4 pf = {pk(p[0], p[1]), pk(p[2], p[3]), pk(p[4], p[5]), pk(p[6], p[7])};
    u32x4 am[4];
#pragma unroll
    for (int u = 0; u < 4; ++u) am[u] = sel4((c >> 2) == u, pf);
    PIN();
    u32x4 ve[4], vo[4];
    u32 slo, shi;
    asm volatile("s_mov_b32 %0, 0x05040100\n\ts_mov_b32 %1, 0x07060302" : "=s"(slo), "=s"(shi) : "v"(am[0][0]), "v"(am[1][1]), "v"(am[2][2]), "v"(am[3][3]));
#pragma unroll
    for (int r = 0; r < 4; ++r) {
#pragma unroll
      for (int u = 0; u < 4; ++u) {
        ve[u][r] = __builtin_amdgcn_perm(vf[2 * r + 1][u], vf[2 * r][u], slo);
        vo[u][r] = __builtin_amdgcn_perm(vf[2 * r + 1][u], vf[2 * r][u], shi);
      }
      if (r < 3)
        asm volatile("" : "+s"(slo), "+s"(shi) : "v"(ve[0][r]), "v"(ve[1][r]), "v"(ve[2][r]), "v"(ve[3][r]), "v"(vo[0][r]), "v"(vo[1][r]), "v"(vo[2][r]), "v"(vo[3][r]));
    }
    f32x4 oe = {0.f, 0.f, 0.f, 0.f}, oo = oe;
#pragma unroll
    for (int u = 0; u < 4; ++u) { oe = mfma(am[u], ve[u], oe); oo = mfma(am[u], vo[u], oo); }
#pragma unroll
    for (int h = 0; h < 4; ++h) *(LAS f32x2*)(lds + L_OW + w * OW_STRIDE + h * 512 + c * 32 + g * 8) = (f32x2){oe[h], oo[h]};
    PIN();
    ST(5);

  const bool gath = (w & 4) == 0;
  const int v = (w & 3) + (w >> 3) * 4;
  auto wait_ml = [&]() __attribute__((always_inline)) {
#pragma unroll 1
    for (int spin = 0; spin < (SPIN_CAP << 4); ++spin) {
      if (__builtin_amdgcn_readfirstlane(*(volatile LAS u32*)(lds + L_CNT)) == 16u) break;
      __builtin_amdgcn_s_sleep(1);
    }
    PIN();
  };
  auto load_wo = [&](u32x4 (&wt)[8]) __attribute__((always_inline)) {
    const GAS u16* p = (const GAS u16*)wo_ + (size_t)(kvh * 512 + rb * 64 + g * 4) * D + cb * 128 + (w - 4) * 64 + c * 4;
#pragma unroll
    for (int i = 0; i < 8; ++i) {
      const u32x2 t0 = ldnt((const GAS u32x2*)(p + (size_t)(32 * (i >> 2) + (i & 3)) * D));
      const u32x2 t1 = ldnt((const GAS u32x2*)(p + (size_t)(32 * (i >> 2) + 16 + (i & 3)) * D));
      wt[i] = (u32x4){t0[0], t0[1], t1[0], t1[1]};
    }
    PIN();
  };
  auto wait_v = [&](u32 n) __attribute__((always_inline)) {
#pragma unroll 1
    for (int spin = 0; spin < (SPIN_CAP << 4); ++spin) {
      if (__builtin_amdgcn_readfirstlane(*(volatile LAS u32*)(lds + L_CNT + 4)) >= n) break;
      __builtin_amdgcn_s_sleep(1);
    }
    PIN();
  };
  if (PRE_D && w == 6) { wait_v(PRE_AT); pretouch(PRE_D); }
  const bool fin = FINLAST ? bid >= 256 - NFIN : bid < NFIN;
  const int cf = bid & 3, bf = (bid >> 2) & 1, seg = (bid >> 3) & 3;
  if (w >= 12) {
    LDS_BARRIER();
    ST(6);
    if (fin) {
      const int row = (w - 12) * 8 + (lane >> 3);
      const int moff = WS_MP + (cf * 28 + min(row, 27)) * 1024 + bf * 512 + seg * 128 + (lane & 7) * 16;
      u32x4 mv;
#pragma unroll 1
      for (int spin = 0; spin < SPIN_CAP; ++spin) {
        mv = ldws(ws, moff);
        if (__all(tagged(mv, tag))) break;
        __builtin_amdgcn_s_sleep(SLEEP_M);
        asm volatile("" ::: "memory");
      }
      const float t = sum_records(sel4(row < 28, mv));
      if ((lane & 8) == 0) *(LAS float*)(lds + L_MF + (w - 12) * 128 + ((lane & 7) * 4 + g) * 4) = t;
      LDS_BARRIER();
    }
  } else if (gath) {
    {
      const int f = v * 16 + (lane >> 2), p4 = lane & 3, hd = v >> 1;
      wait_ml();
      const f32x4 mv = *(LAS f32x4*)(lds + L_MW + hd * 64 + p4 * 16), lv = *(LAS f32x4*)(lds + L_LW + hd * 64 + p4 * 16);
      float M = fmaxf(fmaxf(mv[0], mv[1]), fmaxf(mv[2], mv[3]));
      M = fmaxf(M, dpp<XOR1>(M)); M = fmaxf(M, dpp<XOR2>(M));
      float L = 0.f, fw[4];
#pragma unroll
      for (int i = 0; i < 4; ++i) { fw[i] = __builtin_amdgcn_exp2f(mv[i] - M); L = fmaf(fw[i], lv[i], L); }
      L += dpp<XOR1>(L); L += dpp<XOR2>(L);
      const float inv = __builtin_amdgcn_rcpf(L);
#pragma unroll
      for (int i = 0; i < 4; ++i) fw[i] *= inv;
      const u32 lam = f2u(M + __builtin_amdgcn_logf(L)) >> ((lane >> 5) * 16);
      u32 t0 = tag | (lam & 255u), t1 = tag | ((lam >> 8) & 255u);
      int ooff = WS_OP + ((kvh * 2 + b) * 16 + sp) * 2048 + f * 16;
      int laddr = L_OW + p4 * 4 * OW_STRIDE + f * 16;
      asm volatile("" : "+v"(fw[0]), "+v"(fw[1]), "+v"(fw[2]), "+v"(fw[3]), "+v"(t0), "+v"(t1), "+v"(ooff), "+v"(laddr));
      ST(3);
      LDS_BARRIER();
      ST(6);
      f32x4 s = fw[0] * *(LAS f32x4*)(lds + laddr);
#pragma unroll
      for (int i = 1; i < 4; ++i) s += fw[i] * *(LAS f32x4*)(lds + laddr + i * OW_STRIDE);
      float s0 = s[0], s1 = s[1], s2 = s[2], s3 = s[3];
      quad_sum4(s0, s1, s2, s3);
      if (p4 == 0) __builtin_amdgcn_raw_buffer_store_b128((u32x4){pk(s0, s1), t0, pk(s2, s3), t1}, ws, ooff, 0, OSAUX);
    }
    PIN();
    ST(7);
    {
      u32 gaddr2 = (u32)(size_t)(lds + L_GP + v * GP_STRIDE + 256), gaddr = gaddr2 - 256 + (c * 4 + g) * 4;
      u32 lsel = (c & 8) ? 0x05040100u : 0x01000504u;
      asm volatile("" : "+v"(gaddr), "+v"(gaddr2), "+v"(lsel));
      if (SLEEP_O) __builtin_amdgcn_s_sleep(SLEEP_O);
      const u32x4 ov = poll16<255, OPNT>(ws, WS_OP + (kvh * 32 + v * 4 + g) * 2048 + rb * 256 + c * 16, tag);
      ST(8);
      u32 a, lam, m1, x0, x1, x2, x3;
      f32x2 gl;
      asm volatile("v_perm_b32 %[a], %[t1], %[t0], %[sel]\n\t"
                   "v_lshlrev_b32 %[x0], 16, %[d0]\n\t"
                   "v_lshlrev_b32 %[x2], 16, %[d1]\n\t"
                   "v_mov_b32_dpp %[m1], %[a] row_ror:8 row_mask:0xf bank_mask:0xf\n\t"
                   "v_and_b32 %[x1], 0xffff0000, %[d0]\n\t"
                   "v_perm_b32 %[lam], %[a], %[m1], %[lsel]\n\t"
                   "v_mov_b32 %[m1], %[lam]\n\t"
                   "v_mov_b32 %[a], %[lam]\n\t"
                   "v_and_b32 %[x3], 0xffff0000, %[d1]\n\t"
                   "s_nop 0\n\t"
                   "v_permlane16_swap_b32 %[m1], %[a]\n\t"
                   "v_max_f32 %[m1], %[m1], %[a]\n\t"
                   "v_mov_b32 %[a], %[m1]\n\t"
                   "s_nop 1\n\t"
                   "v_permlane32_swap_b32 %[m1], %[a]\n\t"
                   "v_max_f32 v104, %[m1], %[a]\n\t"
                   "v_sub_f32 %[lam], %[lam], v104\n\t"
                   "v_exp_f32 %[lam], %[lam]\n\t"
                   "s_nop 0\n\t"
                   "v_mul_f32 %[x0], %[lam], %[x0]\n\t"
                   "v_mul_f32 %[x2], %[lam], %[x2]\n\t"
                   "v_mul_f32 %[x1], %[lam], %[x1]\n\t"
                   "v_mul_f32 %[x3], %[lam], %[x3]\n\t"
                   "v_mov_b32 %[a], %[lam]\n\t"
                   "v_permlane32_swap_b32 %[x0], %[x2]\n\t"
                   "v_permlane32_swap_b32 %[x1], %[x3]\n\t"
                   "v_permlane16_swap_b32 %[lam], %[a]\n\t"
                   "v_add_f32 %[x0], %[x0], %[x2]\n\t"
                   "v_add_f32 %[x1], %[x1], %[x3]\n\t"
                   "v_add_f32 %[lam], %[lam], %[a]\n\t"
                   "v_mov_b32 %[a], %[lam]\n\t"
                   "v_permlane16_swap_b32 %[x0], %[x1]\n\t"
                   "v_add_f32 %[x0], %[x0], %[x1]\n\t"
                   "v_permlane32_swap_b32 %[lam], %[a]\n\t"
                   "ds_write_b32 %[ga], %[x0]\n\t"
                   "v_add_f32 v105, %[lam], %[a]\n\t"
                   "ds_write_b64 %[gb], v[104:105]"
                   : [a] "=&v"(a), [lam] "=&v"(lam), [m1] "=&v"(m1), [x0] "=&v"(x0), [x1] "=&v"(x1), [x2] "=&v"(x2), [x3] "=&v"(x3), "=&{v[104:105]}"(gl)
                   : [d0] "v"(ov[0]), [t0] "v"(ov[1]), [d1] "v"(ov[2]), [t1] "v"(ov[3]), [sel] "s"(0x04000400u), [lsel] "v"(lsel), [ga] "v"(gaddr), [gb] "v"(gaddr2)
                   : "memory");
    }
    LDS_BARRIER();
    ST(9);
    if (fin) {
      int faddr = L_FP + ((lane & 7) * 4 + g) * 32 + v * 4;
      asm volatile("" : "+v"(faddr));
      if (SLEEP_S) __builtin_amdgcn_s_sleep(SLEEP_S);
      const u32x4 sv = poll16<255>(ws, WS_S2 + (cf * 64 + v * 8 + (lane >> 3)) * 1024 + bf * 512 + seg * 128 + (lane & 7) * 16, tag);
      ST(10);
      *(LAS float*)(lds + faddr) = sum_records(sv);
      LDS_BARRIER();
    }
  } else if (w < 6) {
    u32x4 wt[8], bw[8];
    if (WO_MODE == 3) {
      wait_ml();
      const bool now = __builtin_amdgcn_s_memrealtime() >= t_start + WO_AT;
      if (!now) {
        LDS_BARRIER();
        ST(6);
        while (__builtin_amdgcn_s_memrealtime() < t_start + WO_AT) __builtin_amdgcn_s_sleep(1);
      }
      load_wo(wt);
      if (now) { LDS_BARRIER(); ST(6); }
    } else {
    if (WO_MODE == 1) { if (WO_V) wait_v(16u); else wait_ml(); }
    if (WO_MODE < 2) load_wo(wt);
    LDS_BARRIER();
    ST(6);
    if (WO_MODE == 2) { if (WO_SLEEP) __builtin_amdgcn_s_sleep(WO_SLEEP); load_wo(wt); }
    }
#pragma unroll
    for (int i = 0; i < 8; ++i) bw[i] = colfrag(wt, i);
    int gp = L_GP + (c & 7) * GP_STRIDE, go = gp + ((c >> 3) * 16 + g * 4) * 4;
    int soff = WS_S2 + (cb * 64 + kvh * 8 + rb) * 1024 + (g & 1) * 512 + ((w - 4) * 32 + c * 2 + (g >> 1)) * 8;
    asm volatile("" : "+v"(gp), "+v"(go), "+v"(soff) : "v"(bw[0]), "v"(bw[1]), "v"(bw[2]), "v"(bw[3]), "v"(bw[4]), "v"(bw[5]), "v"(bw[6]), "v"(bw[7]));
    ST(1);
    LDS_BARRIER();
    ST(9);
    f32x2 gl = *(LAS f32x2*)(lds + gp + 256);
    u32x4 of = *(LAS u32x4*)(lds + go), o1 = *(LAS u32x4*)(lds + go + 128);
#define QP1(r) "v_add_f32_dpp " r ", " r ", " r " quad_perm:[1,0,3,2] row_mask:0xf bank_mask:0xf\n\t"
#define QP2(r) "v_add_f32_dpp " r ", " r ", " r " quad_perm:[2,3,0,1] row_mask:0xf bank_mask:0xf\n\t"
#define PKM(r) "v_pk_mul_f32 " r ", v[118:119], " r " op_sel_hi:[0,1]\n\t"
    asm volatile("v_max_f32_dpp v118, v116, v116 quad_perm:[1,0,3,2] row_mask:0xf bank_mask:0xf\n\t"
                 "s_nop 1\n\t"
                 "v_max_f32_dpp v118, v118, v118 quad_perm:[2,3,0,1] row_mask:0xf bank_mask:0xf\n\t"
                 "v_sub_f32 v118, v116, v118\n\t"
                 "v_exp_f32 v118, v118\n\t"
                 "s_nop 0\n\t"
                 "v_mul_f32 v117, v118, v117\n\t"
                 PKM("v[108:109]") PKM("v[110:111]") PKM("v[112:113]") PKM("v[114:115]")
                 QP1("v117") QP1("v108") QP1("v109") QP1("v110") QP1("v111") QP1("v112") QP1("v113") QP1("v114") QP1("v115")
                 QP2("v117") QP2("v108") QP2("v109")
                 "v_rcp_f32 v118, v117\n\t"
                 QP2("v110") QP2("v111") QP2("v112") QP2("v113") QP2("v114") QP2("v115")
                 PKM("v[108:109]") PKM("v[110:111]") PKM("v[112:113]") PKM("v[114:115]")
                 "v_cvt_pk_bf16_f32 v108, v108, v109\n\t"
                 "v_cvt_pk_bf16_f32 v109, v110, v111\n\t"
                 "v_cvt_pk_bf16_f32 v110, v112, v113\n\t"
                 "v_cvt_pk_bf16_f32 v111, v114, v115\n\t"
                 "s_nop 1"
                 : "+{v[108:111]}"(of), "+{v[112:115]}"(o1), "+{v[116:117]}"(gl)
                 :
                 : "v118", "v119");
    f32x4 acc[8];
#pragma unroll
    for (int i = 0; i < 8; ++i) { const int k = (i & 3) * 2 + (i >> 2); acc[k] = mfma(of, bw[k], (f32x4){0.f, 0.f, 0.f, 0.f}); }
    __builtin_amdgcn_sched_barrier(0);
    float r[4];
#pragma unroll
    for (int d = 0; d < 4; ++d) r[d] = (g >> 1) ? acc[4 + d][0] : acc[d][0];
    stws1(ws, soff, pack2(half32(r[0], r[2]), half32(r[1], r[3]), tag));
    ST(10);
  } else if (w == 7 && mlp2) {
#pragma unroll 1
    for (int spin = 0; spin < SPIN_CAP; ++spin) {
      if ((ABL & 4) || __all(tagged(p0, tag) && tagged(p1, tag))) break;
      __builtin_amdgcn_s_sleep(SLEEP_Q);
      asm volatile("" ::: "memory");
      p0 = ldws(ws, paoff); p1 = ldws(ws, paoff + 1024);
    }
    const float t2[2] = {half32(first2(p0[0], p0[1]) + first2(p1[0], p1[1]), first2(p0[2], p0[3]) + first2(p1[2], p1[3])),
                         half32(second2(p0[0], p0[1]) + second2(p1[0], p1[1]), second2(p0[2], p0[3]) + second2(p1[2], p1[3]))};
    float hv[2];
#pragma unroll
    for (int e = 0; e < 2; ++e) {
      const float t = rbf(rbf(t2[e]) + (e ? bfhi(b1v) : bflo(b1v)));
      const float uu = 0.7978845608028654f * (t + 0.044715f * t * t * t);
      hv[e] = t / (1.f + __builtin_amdgcn_exp2f(-2.f * LOG2E * uu));
    }
    *(LAS u32*)(lds + L_HS + (g & 1) * 128 + (LIN ? pairpos(c, g >> 1) : c * 2 + (g >> 1)) * 4) = pk(hv[0], hv[1]);
    wait_flags(lds, F_W2, 1, lane);
    f32x4 r = {0.f, 0.f, 0.f, 0.f};
#pragma unroll
    for (int s = 0; s < 2; ++s) {
      const u32x4 ha = *(LAS u32x4*)(lds + L_HS + ((c >> 2) & 1) * 128 + s * 64 + g * 16);
      u32x4 w2t[8];
#pragma unroll
      for (int i = 0; i < 8; ++i) w2t[i] = *(LAS u32x4*)(lds + L_W2 + s * 8192 + i * 1024 + lane * 16);
#pragma unroll
      for (int d = 0; d < 8; ++d) r = mfma(sel4((c >> 3) == (d >> 2) && (c & 3) == (d & 3), ha), colfrag(w2t, d), r);
    }
    u32x2 m0 = pack2(r[0], r[1], tag), m1 = pack2(r[2], r[3], tag);
    asm volatile("" : "+v"(m0), "+v"(m1));
    ST(2);
    if (MP_LATE) LDS_BARRIER();
    stws(ws, WS_MP + (mlo * 28 + mhi) * 1024 + (g & 1) * 512 + (c * 2 + (g >> 1)) * 16, m0, m1);
    if (!MP_LATE) LDS_BARRIER();
    ST(6);
  } else if (w == 6 && fin) {
    const int col = cf * 128 + seg * 32 + (lane & 31);
    const u32 xv = ((const GAS u16*)x_)[bf * D + col], b2v = ((const GAS u16*)b2_)[col];
    LDS_BARRIER();
    ST(6);
    LDS_BARRIER();
    LAS float* mf = (LAS float*)(lds + L_MF + (lane >> 5) * 256 + (lane & 31) * 4);
    float t = mf[0] + mf[32];
    t = half32(t, t);
    t = rbf(rbf(t) + bflo(b2v));
    float gate = rbf(t / (1.f + __builtin_amdgcn_exp2f(-LOG2E * t))), xf = bflo(xv);
    int faddr = L_FP + (lane & 31) * 32 + (lane >> 5) * 16;
    GAS u16* op = (GAS u16*)out_ + bf * D + col;
    asm volatile("" : "+v"(gate), "+v"(xf), "+v"(faddr), "+v"(op));
    LDS_BARRIER();
    ST(10);
    const f32x4 fp = *(LAS f32x4*)(lds + faddr);
    float s = (fp[0] + fp[1]) + (fp[2] + fp[3]);
    s = half32(s, s);
    const float res = xf + rbf(rbf(s) * gate);
    if (lane < 32) __hip_atomic_store(op, (u16)pk(res, res), RLX_AGENT);
    if (!NOBUMP && (bid & 31) == 0 && lane == 0) __hip_atomic_store((GAS u32*)(ws_ + WS_EPOCH), tag >> 8, RLX_AGENT);
    if (ENDWAIT) { ST(8); asm volatile("s_waitcnt vmcnt(0)" ::: "memory"); ST(2); }
  } else {
    LDS_BARRIER();
    ST(6);
  }
  ST(11);
  {
    GAS u64* st = (GAS u64*)(ws_ + (WS_AT_END ? -(8 << 20) : (8 << 20)));
    volatile LAS u64* ls = (volatile LAS u64*)(lds + L_TOUCH);
    if (lane == 0) { ls[w * 2] = ((PROBE_WM >> w) & 1) ? ts[PROBE] : 0; ls[w * 2 + 1] = ts[PROBE_B]; }
    if (w == 0) {
      while (__builtin_amdgcn_s_memrealtime() < ts[0] + 2500) __builtin_amdgcn_s_sleep(1);
      u64 ma = ls[(lane & 15) * 2], mb = ls[(lane & 15) * 2 + 1];
      auto red = [&]() {
        for (int o = 32; o; o >>= 1) {
          const u64 x = __shfl_xor(ma, o), y = __shfl_xor(mb, o);
          ma = x > ma ? x : ma;
          if (PROBE_B == 0) mb = y < mb ? y : mb; else mb = y > mb ? y : mb;
        }
      };
      red();
      if (lane == 0) { __hip_atomic_store(st + bid * 2, ma, RLX_AGENT); __hip_atomic_store(st + bid * 2 + 1, mb, RLX_AGENT); }
      if (bid == 0) {
        while (__builtin_amdgcn_s_memrealtime() < ts[0] + 3200) __builtin_amdgcn_s_sleep(1);
        for (int i = lane; i < 256; i += 64) {
          const u64 x = __hip_atomic_load(st + i * 2, RLX_AGENT), y = __hip_atomic_load(st + i * 2 + 1, RLX_AGENT);
          ma = x > ma ? x : ma;
          if (PROBE_B == 0) mb = y < mb ? y : mb; else mb = y > mb ? y : mb;
        }
        red();
        const u64 until = ts[0] + 5000 + 10 * (ma - mb);
        while (__builtin_amdgcn_s_memrealtime() < until && __builtin_amdgcn_s_memrealtime() < ts[0] + 40000) __builtin_amdgcn_s_sleep(1);
      }
    }
  }
}

extern "C" void kernel_launch(void* const* d_in, const int* in_sizes, int n_in, void* d_out, int out_size, void* d_ws, size_t ws_size,
                              hipStream_t stream) {
  unsigned char* ws = (unsigned char*)d_ws + (WS_AT_END ? ((ws_size - WS_SPAN) & ~(size_t)4095) : 0);
  fused_decode<<<256, 1024, 0, stream>>>((const u16*)d_in[0], (const u16*)d_in[1], (const u16*)d_in[2], (const u16*)d_in[3],
                                         (const u16*)d_in[4], (const u16*)d_in[6], (const u16*)d_in[7], (const u16*)d_in[8],
                                         (const u16*)d_in[9], (u16*)d_out, ws
                                         );
}
```

```cpp
#include <hip/hip_runtime.h>
#include <stdint.h>

#define GAS __attribute__((address_space(1)))
#define LAS __attribute__((address_space(3)))
#define DI __device__ __forceinline__
#define RLX_AGENT __ATOMIC_RELAXED, __HIP_MEMORY_SCOPE_AGENT
typedef unsigned short u16;
typedef unsigned u32;
typedef unsigned long long u64;
typedef u32 u32x2 __attribute__((ext_vector_type(2)));
typedef u32 u32x4 __attribute__((ext_vector_type(4)));
typedef float f32x2 __attribute__((ext_vector_type(2)));
typedef float f32x4 __attribute__((ext_vector_type(4)));
typedef __bf16 bf16x2 __attribute__((ext_vector_type(2)));
typedef __bf16 bf16x8 __attribute__((ext_vector_type(8)));

constexpr int D = 512, SK = 8192, DH = 128, FF = 1792, NQ = 4096;
constexpr int SC1 = 16, NT = 2;
constexpr int SPIN_CAP = 1 << 20;
constexpr int NFIN = 32;
constexpr int NAP_Q = 32, NAP_O = 8, NAP_S = 8, NAP_RETRY = 8;
constexpr float LOG2E = 1.4426950408889634f;

constexpr int WS_EPOCH = 0;
constexpr int WS_QP = 256;
constexpr int WS_PA = WS_QP + 512 * 256;
constexpr int WS_OP = WS_PA + 112 * 512;
constexpr int WS_S2 = WS_OP + 256 * 2048;
constexpr int WS_MP = WS_S2 + 256 * 1024;
constexpr int WS_END = WS_MP + 112 * 1024;

constexpr int F_Q = 0, F_HALF = 4, F_W2 = 6;
constexpr int L_FLAG = 0, L_CNT = 48, L_CNTV = 52, L_XN = 64, L_HALF = L_XN + 8192, L_Q = L_HALF + 1024, L_MW = L_Q + 1024, L_LW = L_MW + 256,
              L_HS = L_LW + 256, L_GP = L_HS + 256, GP_STRIDE = 272, L_FP = L_GP + 8 * GP_STRIDE, L_MF = L_FP + 1024,
              L_SINK = L_MF + 512, L_W2 = L_SINK + 256, L_OW = L_W2 + 16384, OW_STRIDE = 2064, L_END = L_OW + 16 * OW_STRIDE;

DI float bflo(u32 v) { return __builtin_bit_cast(float, v << 16); }
DI float bfhi(u32 v) { return __builtin_bit_cast(float, v & 0xffff0000u); }
DI float rbf(float f) { return (float)(__bf16)f; }
DI u32 pk(float a, float b) { return __builtin_bit_cast(u32, __builtin_convertvector((f32x2){a, b}, bf16x2)); }
DI u32 f2u(float f) { return __builtin_bit_cast(u32, f); }
DI float u2f(u32 u) { return __builtin_bit_cast(float, u); }
DI f32x4 mfma(u32x4 a, u32x4 b, f32x4 c) {
  return __builtin_amdgcn_mfma_f32_16x16x32_bf16(__builtin_bit_cast(bf16x8, a), __builtin_bit_cast(bf16x8, b), c, 0, 0, 0);
}
template <int CTRL> DI float dpp(float x) { return u2f(__builtin_amdgcn_mov_dpp(f2u(x), CTRL, 0xf, 0xf, true)); }
constexpr int XOR1 = 0xB1, XOR2 = 0x4E, ROR4 = 0x124, ROR8 = 0x128;
DI float half32(float a, float b) { auto r = __builtin_amdgcn_permlane32_swap(f2u(a), f2u(b), false, false); return u2f(r[0]) + u2f(r[1]); }
DI float half16(float a, float b) { auto r = __builtin_amdgcn_permlane16_swap(f2u(a), f2u(b), false, false); return u2f(r[0]) + u2f(r[1]); }
DI float max32(float a) { auto r = __builtin_amdgcn_permlane32_swap(f2u(a), f2u(a), false, false); return fmaxf(u2f(r[0]), u2f(r[1])); }
DI float max16(float a) { auto r = __builtin_amdgcn_permlane16_swap(f2u(a), f2u(a), false, false); return fmaxf(u2f(r[0]), u2f(r[1])); }
DI float half_sum(float v) {
  v += dpp<XOR1>(v); v += dpp<XOR2>(v); v += dpp<ROR4>(v); v += dpp<ROR8>(v);
  return half16(v, v);
}
DI u32 plo(u32 k1, u32 k0) { return __builtin_amdgcn_perm(k1, k0, 0x05040100u); }
DI u32 phi(u32 k1, u32 k0) { return __builtin_amdgcn_perm(k1, k0, 0x07060302u); }
DI u32x4 colfrag(const u32x4 (&r)[8], int d) {
  const int q = d >> 1;
  return (d & 1) ? (u32x4){phi(r[1][q], r[0][q]), phi(r[3][q], r[2][q]), phi(r[5][q], r[4][q]), phi(r[7][q], r[6][q])}
                 : (u32x4){plo(r[1][q], r[0][q]), plo(r[3][q], r[2][q]), plo(r[5][q], r[4][q]), plo(r[7][q], r[6][q])};
}
DI u32x4 sel4(bool c, u32x4 v) { return (u32x4){c ? v[0] : 0u, c ? v[1] : 0u, c ? v[2] : 0u, c ? v[3] : 0u}; }
template <typename T> DI T ldnt(const GAS T* p) { return __builtin_nontemporal_load(p); }
DI u32x4 ldws(__amdgpu_buffer_rsrc_t ws, int off) { return __builtin_amdgcn_raw_buffer_load_b128(ws, off, 0, SC1); }
DI void stws(__amdgpu_buffer_rsrc_t ws, int off, u32x2 a, u32x2 b) { __builtin_amdgcn_raw_buffer_store_b128((u32x4){a[0], a[1], b[0], b[1]}, ws, off, 0, SC1); }
DI void stws1(__amdgpu_buffer_rsrc_t ws, int off, u32x2 a) { __builtin_amdgcn_raw_buffer_store_b64(a, ws, off, 0, SC1); }
DI u32x2 pack2(float a, float b, u32 tag8) {
  const u32 ar = f2u(a) + 0x800u;
  const u32 u = (f2u(b) & 0xf000u) | ((ar >> 4) & ~0xf000u);
  return (u32x2){phi(f2u(b), ar), __builtin_amdgcn_perm(tag8, u, 0x07060501u)};
}
DI float first2(u32 w0, u32 w1) { return u2f(__builtin_amdgcn_perm(w0, w1 << 4, 0x0504000cu)); }
DI float second2(u32 w0, u32 w1) { return u2f(__builtin_amdgcn_perm(w0, w1, 0x0706000cu)); }
DI bool tagged(u32x4 v, u32 tag8) { return ((v[1] ^ tag8) | (v[3] ^ tag8)) < 256u; }
#define PIN() do { asm volatile("" ::: "memory"); __builtin_amdgcn_sched_barrier(0); } while (0)
#define LDS_BARRIER() do { asm volatile("s_waitcnt lgkmcnt(0)\n\ts_barrier" ::: "memory"); __builtin_amdgcn_sched_barrier(0); } while (0)
#define RAW_BARRIER() do { __builtin_amdgcn_sched_barrier(0); asm volatile("s_barrier" ::: "memory"); __builtin_amdgcn_sched_barrier(0); } while (0)
DI void dma16(const u16* src, u32 dst) {
  u32 keep;
  asm volatile("s_mov_b32 %0, m0\n\ts_mov_b32 m0, %2\n\ts_nop 0\n\tglobal_load_lds_dwordx4 %1, off nt\n\ts_mov_b32 m0, %0"
               : "=&s"(keep) : "v"(src), "s"(dst) : "memory");
}
DI void touch(const void* src, u32 dst) {
  u32 keep;
  asm volatile("s_mov_b32 %0, m0\n\ts_mov_b32 m0, %2\n\ts_nop 0\n\tglobal_load_lds_dword %1, off sc1\n\ts_mov_b32 m0, %0"
               : "=&s"(keep) : "v"(src), "s"(dst) : "memory");
}
DI void set_flag(LAS unsigned char* lds, int f) {
  asm volatile("s_waitcnt lgkmcnt(0)" ::: "memory");
  *(volatile LAS u32*)(lds + L_FLAG + f * 4) = 1u;
}
DI void wait_flags(LAS unsigned char* lds, int first, int mask, int lane) {
#pragma unroll 1
  for (int spin = 0; spin < (SPIN_CAP << 4); ++spin) {
    if (__all(*(volatile LAS u32*)(lds + L_FLAG + (first + (lane & mask)) * 4) != 0u)) break;
    __builtin_amdgcn_s_sleep(1);
  }
  PIN();
}
DI void count_in(LAS unsigned char* lds, int cnt, int lane) {
  if (lane == 0) __hip_atomic_fetch_add((LAS u32*)(lds + cnt), 1u, __ATOMIC_RELAXED, __HIP_MEMORY_SCOPE_WORKGROUP);
}
DI void wait_all(LAS unsigned char* lds, int cnt) {
#pragma unroll 1
  for (int spin = 0; spin < (SPIN_CAP << 4); ++spin) {
    if (__builtin_amdgcn_readfirstlane(*(volatile LAS u32*)(lds + cnt)) == 16u) break;
    __builtin_amdgcn_s_sleep(1);
  }
  PIN();
}
DI u32 read_tag(const unsigned char* ws) {
  u32 e;
  asm volatile("s_load_dword %0, %1, 0x0\n\ts_waitcnt lgkmcnt(0)" : "=s"(e) : "s"(ws) : "memory");
  return (e + 1u) << 8;
}
DI u32x4 poll16(__amdgpu_buffer_rsrc_t ws, int off, u32 want) {
  u32x4 v;
  u64 keep;
  u32 left = SPIN_CAP, t;
  asm volatile("s_mov_b64 %[keep], exec\n\t"
               "s_branch 2f\n"
               "1:\n\t"
               "s_sub_u32 %[left], %[left], 1\n\t"
               "s_cbranch_scc1 3f\n"
               "2:\n\t"
               "buffer_load_dwordx4 v[100:103], %[off], %[ws], 0 offen sc1\n\t"
               "s_waitcnt vmcnt(0)\n\t"
               "v_bitop3_b32 %[t], v101, v103, %[want] bitop3:0x7e\n\t"
               "v_cmp_lt_u32 vcc, 0xff, %[t]\n\t"
               "s_and_b64 exec, exec, vcc\n\t"
               "s_cbranch_scc1 1b\n"
               "3:\n\t"
               "s_mov_b64 exec, %[keep]"
               : "=&{v[100:103]}"(v), [keep] "=&s"(keep), [left] "+s"(left), [t] "=&v"(t)
               : [off] "v"(off), [ws] "s"(ws), [want] "s"(want)
               : "vcc", "scc", "memory");
  return v;
}
DI void quad_sum4(float& a, float& b, float& c, float& d) {
  asm volatile("s_nop 1\n\t"
      "v_add_f32_dpp %0, %0, %0 quad_perm:[1,0,3,2] row_mask:0xf bank_mask:0xf\n\t"
      "v_add_f32_dpp %1, %1, %1 quad_perm:[1,0,3,2] row_mask:0xf bank_mask:0xf\n\t"
      "v_add_f32_dpp %2, %2, %2 quad_perm:[1,0,3,2] row_mask:0xf bank_mask:0xf\n\t"
      "v_add_f32_dpp %3, %3, %3 quad_perm:[1,0,3,2] row_mask:0xf bank_mask:0xf\n\t"
      "v_add_f32_dpp %0, %0, %0 quad_perm:[2,3,0,1] row_mask:0xf bank_mask:0xf\n\t"
      "v_add_f32_dpp %1, %1, %1 quad_perm:[2,3,0,1] row_mask:0xf bank_mask:0xf\n\t"
      "v_add_f32_dpp %2, %2, %2 quad_perm:[2,3,0,1] row_mask:0xf bank_mask:0xf\n\t"
      "v_add_f32_dpp %3, %3, %3 quad_perm:[2,3,0,1] row_mask:0xf bank_mask:0xf"
      : "+v"(a), "+v"(b), "+v"(c), "+v"(d));
}
DI float sum_records(u32x4 v) {
  const float t = half16(half32(first2(v[0], v[1]), first2(v[2], v[3])), half32(second2(v[0], v[1]), second2(v[2], v[3])));
  return t + dpp<ROR8>(t);
}

DI void layernorm(u32 (&x)[8]) {
  float v[16], s = 0.f;
#pragma unroll
  for (int e = 0; e < 8; ++e) { v[2 * e] = bflo(x[e]); v[2 * e + 1] = bfhi(x[e]); s += v[2 * e] + v[2 * e + 1]; }
  const float mean = half_sum(s) * (1.f / 512.f);
  float q = 0.f;
#pragma unroll
  for (int e = 0; e < 16; ++e) { const float d = v[e] - mean; q = fmaf(d, d, q); }
  const float var = half_sum(q) * (1.f / 512.f);
  const float mb = rbf(mean), inv = __builtin_amdgcn_rcpf(rbf(__builtin_amdgcn_sqrtf(rbf(rbf(var) + 1e-5f))));
#pragma unroll
  for (int e = 0; e < 8; ++e) { const u32 d = pk(v[2 * e] - mb, v[2 * e + 1] - mb); x[e] = pk(bflo(d) * inv, bfhi(d) * inv); }
}

DI f32x2 tile_matvec(const u32x4 (&wv)[8], LAS unsigned char* xin, int g, int c) {
  const u32x4 xf = *(LAS u32x4*)(xin + ((c >> 2) & 1) * 1024 + ((c >> 3) * 32 + g * 8) * 2);
  f32x4 lo = {0.f, 0.f, 0.f, 0.f}, hi = lo;
#pragma unroll
  for (int d = 0; d < 4; ++d) {
    const u32x4 am = sel4((c & 3) == d, xf);
    lo = mfma(am, colfrag(wv, d), lo);
    hi = mfma(am, colfrag(wv, 4 + d), hi);
  }
  const bool valid = (g >> 1) == (c >> 3);
  float t[4];
#pragma unroll
  for (int e = 0; e < 4; ++e) { t[e] = half32(valid ? lo[e] : 0.f, valid ? hi[e] : 0.f); t[e] += dpp<ROR8>(t[e]); }
  return (c & 8) ? (f32x2){t[2], t[3]} : (f32x2){t[0], t[1]};
}

__global__ void __launch_bounds__(1024, 4)
fused_decode(const u16* x_, const u16* k_, const u16* v_, const u16* wq_, const u16* wo_, const u16* w1_, const u16* b1_,
             const u16* w2_, const u16* b2_, u16* out_, unsigned char* ws_) {
  __shared__ __attribute__((aligned(16))) unsigned char lds_raw[L_END];
  LAS unsigned char* lds = (LAS unsigned char*)lds_raw;
  asm volatile("" :: "s"(x_), "s"(k_), "s"(v_), "s"(wq_), "s"(wo_), "s"(w1_), "s"(b1_), "s"(w2_), "s"(b2_), "s"(out_), "s"(ws_));
  const int tid = threadIdx.x, lane = tid & 63, w = __builtin_amdgcn_readfirstlane(tid >> 6);
  const int g = lane >> 4, c = lane & 15;
  const int bid = blockIdx.x, kvh = bid & 7, j = bid >> 3, b = j >> 4, sp = j & 15, rb = j >> 2, cb = j & 3;
  const bool mlp1 = j < 14, mlp2 = j >= 14 && j < 28;
  const __amdgpu_buffer_rsrc_t ws = __builtin_amdgcn_make_buffer_rsrc(ws_, 0, WS_END, 0x00020000);
  u32 tag;
  const size_t kvseq = (size_t)(b * 8 + kvh) * SK * DH;
  const __amdgpu_buffer_rsrc_t kr = __builtin_amdgcn_make_buffer_rsrc((void*)(k_ + kvseq), 0, SK * DH * 2, 0x00020000);
  const __amdgpu_buffer_rsrc_t vr = __builtin_amdgcn_make_buffer_rsrc((void*)(v_ + kvseq), 0, SK * DH * 2, 0x00020000);
  const int kvoff = (sp * 16 + ((w + j) & 15)) * 8192;
  const int klane = (((c >> 1) & 1) * 4 + (c >> 2)) * 256 + (g * 2 + (c & 1)) * 16;
  u32x4 kf[8], vf[8];

  const int cq = j & 7, kq = j >> 3;
  const int mt = (mlp1 ? j : j - 14) * 8 + kvh;
  const int mhi = mt >> 2, mlo = mt & 3;
  if (w < 2 || (w < 4 && mlp1)) {
    __builtin_amdgcn_s_setprio(3);
    if (w == 0 && lane < 16) *(LAS u32*)(lds + L_FLAG + lane * 4) = 0u;
    const GAS u32x4* xp = (const GAS u32x4*)x_ + lane * 2;
    const u32x4 x0 = xp[0], x1 = xp[1];
    PIN();
    const int rs = w < 2 ? NQ / 8 : FF / 8, row0 = (w < 2 ? kq : mlo) * 128 + (w & 1) * 64;
    const u16* tile = w < 2 ? wq_ + kvh * 512 + cq * 64 : w1_ + mhi * 64;
    const GAS u32x4* wp = (const GAS u32x4*)tile + (row0 + (c >> 3) * 32 + g * 8) * rs + (c & 7);
    u32x4 wv[8];
#pragma unroll
    for (int i = 0; i < 8; ++i) wv[i] = ldnt(wp + i * rs);
    PIN();
    LDS_BARRIER();
    tag = read_tag(ws_);
    PIN();
    u32 xs[8] = {x0[0], x0[1], x0[2], x0[3], x1[0], x1[1], x1[2], x1[3]};
    layernorm(xs);
    if (w >= 2) layernorm(xs);
    LAS unsigned char* xl = lds + L_XN + w * 2048;
    *(LAS u32x4*)(xl + lane * 32) = (u32x4){xs[0], xs[1], xs[2], xs[3]};
    *(LAS u32x4*)(xl + lane * 32 + 16) = (u32x4){xs[4], xs[5], xs[6], xs[7]};
    f32x2 r = tile_matvec(wv, xl + row0 * 2, g, c);
    LAS f32x2* half = (LAS f32x2*)(lds + L_HALF + (w >> 1) * 512 + lane * 8);
    if (w & 1) {
      *half = r;
      set_flag(lds, F_HALF + (w >> 1));
    } else {
      wait_flags(lds, F_HALF + (w >> 1), 0, lane);
      r += *half;
      const int rec = w == 0 ? WS_QP + ((((g & 1) * 8 + kvh) * 8 + cq) * 4 + kq) * 256 : WS_PA + mt * 512 + (g & 1) * 256;
      stws1(ws, rec + ((c & 7) * 4 + (g >> 1) * 2 + (c >> 3)) * 8, pack2(r[0], r[1], tag));
    }
    __builtin_amdgcn_s_setprio(1);
  } else {
    if (w < 4 && mlp2) {
      const u16* p = w2_ + (mhi * 64 + (w & 1) * 32 + g * 8) * D + mlo * 128 + c * 8;
#pragma unroll
      for (int i = 0; i < 8; ++i) dma16(p + i * D, (u32)(size_t)(lds + L_W2 + (w & 1) * 8192 + i * 1024));
    }
    RAW_BARRIER();
    if ((w >> 2) == 1) __builtin_amdgcn_s_setprio(2); else __builtin_amdgcn_s_setprio(1);
  }
  PIN();

#pragma unroll
  for (int i = 0; i < 8; ++i) {
    kf[i] = __builtin_amdgcn_raw_buffer_load_b128(kr, klane + (i & 1) * 128, kvoff + (i >> 1) * 2048, NT);
    __builtin_amdgcn_sched_barrier(0);
  }
  if ((w >> 2) != 1) __builtin_amdgcn_s_setprio(0);
  PIN();
#pragma unroll
  for (int i = 0; i < 8; ++i) {
    vf[i] = __builtin_amdgcn_raw_buffer_load_b128(vr, lane * 16, kvoff + i * 1024, NT);
    __builtin_amdgcn_sched_barrier(0);
  }
  PIN();
  count_in(lds, L_CNTV, lane);
  tag = read_tag(ws_);
  const int paoff = WS_PA + mhi * 2048 + lane * 16;
  u32x4 p0, p1;
  u32 b1v;
  if ((w >> 2) == 1) {
    const int u = w & 3;
    const int qoff = WS_QP + (b * 8 + kvh) * 8192 + u * 2048 + lane * 16;
    asm volatile("s_waitcnt vmcnt(0)" ::: "memory");
    __builtin_amdgcn_s_sleep(NAP_Q);
    u32x4 q0, q1;
#pragma unroll 1
    for (int spin = 0; spin < SPIN_CAP; ++spin) {
      q0 = ldws(ws, qoff); q1 = ldws(ws, qoff + 1024);
      if (__all(tagged(q0, tag) && tagged(q1, tag))) break;
      __builtin_amdgcn_s_sleep(NAP_RETRY);
      asm volatile("" ::: "memory");
    }
    const float qa = half16(half32(first2(q0[0], q0[1]), first2(q1[0], q1[1])), half32(first2(q0[2], q0[3]), first2(q1[2], q1[3])));
    const float qb = half16(half32(second2(q0[0], q0[1]), second2(q1[0], q1[1])), half32(second2(q0[2], q0[3]), second2(q1[2], q1[3])));
    *(LAS u32*)(lds + L_Q + u * 256 + ((g >> 1) * 32 + c * 2 + (g & 1)) * 4) = pk(qa, qb);
    set_flag(lds, F_Q + u);
    if (w == 7 && mlp2) {
      p0 = ldws(ws, paoff); p1 = ldws(ws, paoff + 1024);
      b1v = *(const GAS u32*)(b1_ + mhi * 64 + c * 4 + (g >> 1) * 2);
      PIN();
    }
    __builtin_amdgcn_s_setprio(0);
  }
  wait_flags(lds, F_Q, 3, lane);

  float sc[8];
  {
    const int x0 = (c >> 2) & 1;
    const u32x4 q0 = *(LAS u32x4*)(lds + L_Q + (c & 3) * 256 + (g * 2 + x0) * 16);
    const u32x4 q1 = *(LAS u32x4*)(lds + L_Q + (c & 3) * 256 + 128 + (g * 2 + x0) * 16);
#pragma unroll
    for (int o = 0; o < 4; ++o) {
      f32x4 s = mfma(kf[2 * o], q0, (f32x4){0.f, 0.f, 0.f, 0.f});
      s = mfma(kf[2 * o + 1], q1, s);
#pragma unroll
      for (int a = 0; a < 2; ++a) {
        float e1 = s[2 * a + 1];
        asm("" : "+v"(e1));
        const float t = x0 ? e1 : s[2 * a];
        sc[2 * o + a] = t + dpp<ROR4>(t);
      }
      const u32 d = pk(sc[2 * o], sc[2 * o + 1]), e = pk(bflo(d) * 0.08838834764831845f, bfhi(d) * 0.08838834764831845f);
      sc[2 * o] = bflo(e); sc[2 * o + 1] = bfhi(e);
    }
  }
  PIN();

  float m = fmaxf(fmaxf(fmaxf(sc[0], sc[1]), fmaxf(sc[2], sc[3])), fmaxf(fmaxf(sc[4], sc[5]), fmaxf(sc[6], sc[7])));
  m = max32(max16(m)) * LOG2E;
  float p[8], l = 0.f;
#pragma unroll
  for (int e = 0; e < 8; ++e) { p[e] = __builtin_amdgcn_exp2f(fmaf(sc[e], LOG2E, -m)); l += p[e]; }
  l = half16(l, l); l = half32(l, l);
  if (lane < 4) { *(LAS float*)(lds + L_MW + (lane * 16 + w) * 4) = m; *(LAS float*)(lds + L_LW + (lane * 16 + w) * 4) = l; }
  PIN();
  if ((w >> 1) == 1) set_flag(lds, F_W2 + (w & 1));
  count_in(lds, L_CNT, lane);
  const u32x4 pf = {pk(p[0], p[1]), pk(p[2], p[3]), pk(p[4], p[5]), pk(p[6], p[7])};
  u32x4 am[4];
#pragma unroll
  for (int u = 0; u < 4; ++u) am[u] = sel4((c >> 2) == u, pf);
  PIN();
  u32x4 ve[4], vo[4];
  u32 slo, shi;
  asm volatile("s_mov_b32 %0, 0x05040100\n\ts_mov_b32 %1, 0x07060302" : "=s"(slo), "=s"(shi) : "v"(am[0][0]), "v"(am[1][1]), "v"(am[2][2]), "v"(am[3][3]));
#pragma unroll
  for (int r = 0; r < 4; ++r) {
#pragma unroll
    for (int u = 0; u < 4; ++u) {
      ve[u][r] = __builtin_amdgcn_perm(vf[2 * r + 1][u], vf[2 * r][u], slo);
      vo[u][r] = __builtin_amdgcn_perm(vf[2 * r + 1][u], vf[2 * r][u], shi);
    }
    if (r < 3)
      asm volatile("" : "+s"(slo), "+s"(shi) : "v"(ve[0][r]), "v"(ve[1][r]), "v"(ve[2][r]), "v"(ve[3][r]), "v"(vo[0][r]), "v"(vo[1][r]), "v"(vo[2][r]), "v"(vo[3][r]));
  }
  f32x4 oe = {0.f, 0.f, 0.f, 0.f}, oo = oe;
#pragma unroll
  for (int u = 0; u < 4; ++u) { oe = mfma(am[u], ve[u], oe); oo = mfma(am[u], vo[u], oo); }
#pragma unroll
  for (int h = 0; h < 4; ++h) *(LAS f32x2*)(lds + L_OW + w * OW_STRIDE + h * 512 + c * 32 + g * 8) = (f32x2){oe[h], oo[h]};
  PIN();

  const bool gath = (w & 4) == 0;
  const int v = (w & 3) + (w >> 3) * 4;
  const int cf = bid & 3, bf = (bid >> 2) & 1, seg = bid >> 3;
  if (w == 6) {
    wait_all(lds, L_CNTV);
    const unsigned char* tp = ws_ + WS_OP + ((kvh * 2 + b) * 16 + sp) * 2048 + c * 128;
    if (g == 1) tp = c < 8 ? ws_ + WS_S2 + (cb * 64 + kvh * 8 + rb) * 1024 + c * 128 : (const unsigned char*)(out_ + bf * D + cf * 128 + seg * 32);
    if (lane < (bid < NFIN ? 25 : 24)) touch(tp, (u32)(size_t)(lds + L_SINK));
  }
  if (w >= 12) {
    LDS_BARRIER();
    if (bid < NFIN) {
      const int row = (w - 12) * 8 + (lane >> 3);
      const int moff = WS_MP + (cf * 28 + min(row, 27)) * 1024 + bf * 512 + seg * 128 + (lane & 7) * 16;
      u32x4 mv;
#pragma unroll 1
      for (int spin = 0; spin < SPIN_CAP; ++spin) {
        mv = ldws(ws, moff);
        if (__all(tagged(mv, tag))) break;
        __builtin_amdgcn_s_sleep(NAP_RETRY);
        asm volatile("" ::: "memory");
      }
      const float t = sum_records(sel4(row < 28, mv));
      if ((lane & 8) == 0) *(LAS float*)(lds + L_MF + (w - 12) * 128 + ((lane & 7) * 4 + g) * 4) = t;
      LDS_BARRIER();
    }
  } else if (gath) {
    {
      const int f = v * 16 + (lane >> 2), p4 = lane & 3, hd = v >> 1;
      wait_all(lds, L_CNT);
      const f32x4 mv = *(LAS f32x4*)(lds + L_MW + hd * 64 + p4 * 16), lv = *(LAS f32x4*)(lds + L_LW + hd * 64 + p4 * 16);
      float M = fmaxf(fmaxf(mv[0], mv[1]), fmaxf(mv[2], mv[3]));
      M = fmaxf(M, dpp<XOR1>(M)); M = fmaxf(M, dpp<XOR2>(M));
      float L = 0.f, fw[4];
#pragma unroll
      for (int i = 0; i < 4; ++i) { fw[i] = __builtin_amdgcn_exp2f(mv[i] - M); L = fmaf(fw[i], lv[i], L); }
      L += dpp<XOR1>(L); L += dpp<XOR2>(L);
      const float inv = __builtin_amdgcn_rcpf(L);
#pragma unroll
      for (int i = 0; i < 4; ++i) fw[i] *= inv;
      const u32 lam = f2u(M + __builtin_amdgcn_logf(L)) >> ((lane >> 5) * 16);
      u32 t0 = tag | (lam & 255u), t1 = tag | ((lam >> 8) & 255u);
      int ooff = WS_OP + ((kvh * 2 + b) * 16 + sp) * 2048 + f * 16;
      int laddr = L_OW + p4 * 4 * OW_STRIDE + f * 16;
      asm volatile("" : "+v"(fw[0]), "+v"(fw[1]), "+v"(fw[2]), "+v"(fw[3]), "+v"(t0), "+v"(t1), "+v"(ooff), "+v"(laddr));
      LDS_BARRIER();
      f32x4 s = fw[0] * *(LAS f32x4*)(lds + laddr);
#pragma unroll
      for (int i = 1; i < 4; ++i) s += fw[i] * *(LAS f32x4*)(lds + laddr + i * OW_STRIDE);
      float s0 = s[0], s1 = s[1], s2 = s[2], s3 = s[3];
      quad_sum4(s0, s1, s2, s3);
      if (p4 == 0) __builtin_amdgcn_raw_buffer_store_b128((u32x4){pk(s0, s1), t0, pk(s2, s3), t1}, ws, ooff, 0, SC1);
    }
    PIN();
    {
      u32 gaddr2 = (u32)(size_t)(lds + L_GP + v * GP_STRIDE + 256), gaddr = gaddr2 - 256 + (c * 4 + g) * 4;
      u32 lsel = (c & 8) ? 0x05040100u : 0x01000504u;
      asm volatile("" : "+v"(gaddr), "+v"(gaddr2), "+v"(lsel));
      __builtin_amdgcn_s_sleep(NAP_O);
      const u32x4 ov = poll16(ws, WS_OP + (kvh * 32 + v * 4 + g) * 2048 + rb * 256 + c * 16, tag);
      u32 a, lam, m1, x0, x1, x2, x3;
      f32x2 gl;
      asm volatile("v_perm_b32 %[a], %[t1], %[t0], %[sel]\n\t"
                   "v_lshlrev_b32 %[x0], 16, %[d0]\n\t"
                   "v_lshlrev_b32 %[x2], 16, %[d1]\n\t"
                   "v_mov_b32_dpp %[m1], %[a] row_ror:8 row_mask:0xf bank_mask:0xf\n\t"
                   "v_and_b32 %[x1], 0xffff0000, %[d0]\n\t"
                   "v_perm_b32 %[lam], %[a], %[m1], %[lsel]\n\t"
                   "v_mov_b32 %[m1], %[lam]\n\t"
                   "v_mov_b32 %[a], %[lam]\n\t"
                   "v_and_b32 %[x3], 0xffff0000, %[d1]\n\t"
                   "s_nop 0\n\t"
                   "v_permlane16_swap_b32 %[m1], %[a]\n\t"
                   "v_max_f32 %[m1], %[m1], %[a]\n\t"
                   "v_mov_b32 %[a], %[m1]\n\t"
                   "s_nop 1\n\t"
                   "v_permlane32_swap_b32 %[m1], %[a]\n\t"
                   "v_max_f32 v104, %[m1], %[a]\n\t"
                   "v_sub_f32 %[lam], %[lam], v104\n\t"
                   "v_exp_f32 %[lam], %[lam]\n\t"
                   "s_nop 0\n\t"
                   "v_mul_f32 %[x0], %[lam], %[x0]\n\t"
                   "v_mul_f32 %[x2], %[lam], %[x2]\n\t"
                   "v_mul_f32 %[x1], %[lam], %[x1]\n\t"
                   "v_mul_f32 %[x3], %[lam], %[x3]\n\t"
                   "v_mov_b32 %[a], %[lam]\n\t"
                   "v_permlane32_swap_b32 %[x0], %[x2]\n\t"
                   "v_permlane32_swap_b32 %[x1], %[x3]\n\t"
                   "v_permlane16_swap_b32 %[lam], %[a]\n\t"
                   "v_add_f32 %[x0], %[x0], %[x2]\n\t"
                   "v_add_f32 %[x1], %[x1], %[x3]\n\t"
                   "v_add_f32 %[lam], %[lam], %[a]\n\t"
                   "v_mov_b32 %[a], %[lam]\n\t"
                   "v_permlane16_swap_b32 %[x0], %[x1]\n\t"
                   "v_add_f32 %[x0], %[x0], %[x1]\n\t"
                   "v_permlane32_swap_b32 %[lam], %[a]\n\t"
                   "ds_write_b32 %[ga], %[x0]\n\t"
                   "v_add_f32 v105, %[lam], %[a]\n\t"
                   "ds_write_b64 %[gb], v[104:105]"
                   : [a] "=&v"(a), [lam] "=&v"(lam), [m1] "=&v"(m1), [x0] "=&v"(x0), [x1] "=&v"(x1), [x2] "=&v"(x2), [x3] "=&v"(x3), "=&{v[104:105]}"(gl)
                   : [d0] "v"(ov[0]), [t0] "v"(ov[1]), [d1] "v"(ov[2]), [t1] "v"(ov[3]), [sel] "s"(0x04000400u), [lsel] "v"(lsel), [ga] "v"(gaddr), [gb] "v"(gaddr2)
                   : "memory");
    }
    LDS_BARRIER();
    if (bid < NFIN) {
      int faddr = L_FP + ((lane & 7) * 4 + g) * 32 + v * 4;
      asm volatile("" : "+v"(faddr));
      __builtin_amdgcn_s_sleep(NAP_S);
      const u32x4 sv = poll16(ws, WS_S2 + (cf * 64 + v * 8 + (lane >> 3)) * 1024 + bf * 512 + seg * 128 + (lane & 7) * 16, tag);
      *(LAS float*)(lds + faddr) = sum_records(sv);
      LDS_BARRIER();
    }
  } else if (w < 6) {
    u32x4 wt[8], bw[8];
    wait_all(lds, L_CNT);
    const GAS u16* wp = (const GAS u16*)wo_ + (size_t)(kvh * 512 + rb * 64 + g * 4) * D + cb * 128 + (w - 4) * 64 + c * 4;
#pragma unroll
    for (int i = 0; i < 8; ++i) {
      const u32x2 t0 = ldnt((const GAS u32x2*)(wp + (size_t)(32 * (i >> 2) + (i & 3)) * D));
      const u32x2 t1 = ldnt((const GAS u32x2*)(wp + (size_t)(32 * (i >> 2) + 16 + (i & 3)) * D));
      wt[i] = (u32x4){t0[0], t0[1], t1[0], t1[1]};
    }
    PIN();
    LDS_BARRIER();
#pragma unroll
    for (int i = 0; i < 8; ++i) bw[i] = colfrag(wt, i);
    int gp = L_GP + (c & 7) * GP_STRIDE, go = gp + ((c >> 3) * 16 + g * 4) * 4;
    int soff = WS_S2 + (cb * 64 + kvh * 8 + rb) * 1024 + (g & 1) * 512 + ((w - 4) * 32 + c * 2 + (g >> 1)) * 8;
    asm volatile("" : "+v"(gp), "+v"(go), "+v"(soff) : "v"(bw[0]), "v"(bw[1]), "v"(bw[2]), "v"(bw[3]), "v"(bw[4]), "v"(bw[5]), "v"(bw[6]), "v"(bw[7]));
    LDS_BARRIER();
    f32x2 gl = *(LAS f32x2*)(lds + gp + 256);
    u32x4 of = *(LAS u32x4*)(lds + go), o1 = *(LAS u32x4*)(lds + go + 128);
#define QP1(r) "v_add_f32_dpp " r ", " r ", " r " quad_perm:[1,0,3,2] row_mask:0xf bank_mask:0xf\n\t"
#define QP2(r) "v_add_f32_dpp " r ", " r ", " r " quad_perm:[2,3,0,1] row_mask:0xf bank_mask:0xf\n\t"
#define PKM(r) "v_pk_mul_f32 " r ", v[118:119], " r " op_sel_hi:[0,1]\n\t"
    asm volatile("s_nop 1\n\t"
                 "v_max_f32_dpp v118, v116, v116 quad_perm:[1,0,3,2] row_mask:0xf bank_mask:0xf\n\t"
                 "s_nop 1\n\t"
                 "v_max_f32_dpp v118, v118, v118 quad_perm:[2,3,0,1] row_mask:0xf bank_mask:0xf\n\t"
                 "v_sub_f32 v118, v116, v118\n\t"
                 "v_exp_f32 v118, v118\n\t"
                 "s_nop 0\n\t"
                 "v_mul_f32 v117, v118, v117\n\t"
                 PKM("v[108:109]") PKM("v[110:111]") PKM("v[112:113]") PKM("v[114:115]")
                 QP1("v117") QP1("v108") QP1("v109") QP1("v110") QP1("v111") QP1("v112") QP1("v113") QP1("v114") QP1("v115")
                 QP2("v117") QP2("v108") QP2("v109")
                 "v_rcp_f32 v118, v117\n\t"
                 QP2("v110") QP2("v111") QP2("v112") QP2("v113") QP2("v114") QP2("v115")
                 PKM("v[108:109]") PKM("v[110:111]") PKM("v[112:113]") PKM("v[114:115]")
                 "v_cvt_pk_bf16_f32 v108, v108, v109\n\t"
                 "v_cvt_pk_bf16_f32 v109, v110, v111\n\t"
                 "v_cvt_pk_bf16_f32 v110, v112, v113\n\t"
                 "v_cvt_pk_bf16_f32 v111, v114, v115\n\t"
                 "s_nop 1"
                 : "+{v[108:111]}"(of), "+{v[112:115]}"(o1), "+{v[116:117]}"(gl)
                 :
                 : "v118", "v119");
    f32x4 acc[8];
#pragma unroll
    for (int i = 0; i < 8; ++i) { const int k = (i & 3) * 2 + (i >> 2); acc[k] = mfma(of, bw[k], (f32x4){0.f, 0.f, 0.f, 0.f}); }
    __builtin_amdgcn_sched_barrier(0);
    float r[4];
#pragma unroll
    for (int d = 0; d < 4; ++d) r[d] = (g >> 1) ? acc[4 + d][0] : acc[d][0];
    stws1(ws, soff, pack2(half32(r[0], r[2]), half32(r[1], r[3]), tag));
  } else if (w == 7 && mlp2) {
#pragma unroll 1
    for (int spin = 0; spin < SPIN_CAP; ++spin) {
      if (__all(tagged(p0, tag) && tagged(p1, tag))) break;
      __builtin_amdgcn_s_sleep(NAP_RETRY);
      asm volatile("" ::: "memory");
      p0 = ldws(ws, paoff); p1 = ldws(ws, paoff + 1024);
    }
    const float t2[2] = {half32(first2(p0[0], p0[1]) + first2(p1[0], p1[1]), first2(p0[2], p0[3]) + first2(p1[2], p1[3])),
                         half32(second2(p0[0], p0[1]) + second2(p1[0], p1[1]), second2(p0[2], p0[3]) + second2(p1[2], p1[3]))};
    float hv[2];
#pragma unroll
    for (int e = 0; e < 2; ++e) {
      const float t = rbf(rbf(t2[e]) + (e ? bfhi(b1v) : bflo(b1v)));
      const float uu = 0.7978845608028654f * (t + 0.044715f * t * t * t);
      hv[e] = t / (1.f + __builtin_amdgcn_exp2f(-2.f * LOG2E * uu));
    }
    *(LAS u32*)(lds + L_HS + (g & 1) * 128 + (c * 2 + (g >> 1)) * 4) = pk(hv[0], hv[1]);
    wait_flags(lds, F_W2, 1, lane);
    f32x4 r = {0.f, 0.f, 0.f, 0.f};
#pragma unroll
    for (int s = 0; s < 2; ++s) {
      const u32x4 ha = *(LAS u32x4*)(lds + L_HS + ((c >> 2) & 1) * 128 + s * 64 + g * 16);
      u32x4 w2t[8];
#pragma unroll
      for (int i = 0; i < 8; ++i) w2t[i] = *(LAS u32x4*)(lds + L_W2 + s * 8192 + i * 1024 + lane * 16);
#pragma unroll
      for (int d = 0; d < 8; ++d) r = mfma(sel4((c >> 3) == (d >> 2) && (c & 3) == (d & 3), ha), colfrag(w2t, d), r);
    }
    stws(ws, WS_MP + (mlo * 28 + mhi) * 1024 + (g & 1) * 512 + (c * 2 + (g >> 1)) * 16, pack2(r[0], r[1], tag), pack2(r[2], r[3], tag));
    LDS_BARRIER();
  } else if (w == 6 && bid < NFIN) {
    const int col = cf * 128 + seg * 32 + (lane & 31);
    const u32 xv = ((const GAS u16*)x_)[bf * D + col], b2v = ((const GAS u16*)b2_)[col];
    LDS_BARRIER();
    LDS_BARRIER();
    LAS float* mf = (LAS float*)(lds + L_MF + (lane >> 5) * 256 + (lane & 31) * 4);
    float t = mf[0] + mf[32];
    t = half32(t, t);
    t = rbf(rbf(t) + bflo(b2v));
    float gate = rbf(t / (1.f + __builtin_amdgcn_exp2f(-LOG2E * t))), xf = bflo(xv);
    int faddr = L_FP + (lane & 31) * 32 + (lane >> 5) * 16;
    GAS u16* op = (GAS u16*)out_ + bf * D + col;
    asm volatile("" : "+v"(gate), "+v"(xf), "+v"(faddr), "+v"(op));
    LDS_BARRIER();
    const f32x4 fp = *(LAS f32x4*)(lds + faddr);
    float s = (fp[0] + fp[1]) + (fp[2] + fp[3]);
    s = half32(s, s);
    const float res = xf + rbf(rbf(s) * gate);
    if (lane < 32) __hip_atomic_store(op, (u16)pk(res, res), RLX_AGENT);
    if (bid == 0 && lane == 0) __hip_atomic_store((GAS u32*)(ws_ + WS_EPOCH), tag >> 8, RLX_AGENT);
  } else {
    LDS_BARRIER();
  }
}

extern "C" void kernel_launch(void* const* d_in, const int* in_sizes, int n_in, void* d_out, int out_size, void* d_ws, size_t ws_size,
                              hipStream_t stream) {
  fused_decode<<<256, 1024, 0, stream>>>((const u16*)d_in[0], (const u16*)d_in[1], (const u16*)d_in[2], (const u16*)d_in[3],
                                         (const u16*)d_in[4], (const u16*)d_in[6], (const u16*)d_in[7], (const u16*)d_in[8],
                                         (const u16*)d_in[9], (u16*)d_out, (unsigned char*)d_ws);
}
```
